```python
import torch

def _softmax(x, axis=-1):
    return torch.softmax(x, dim=axis)
def _gelu(x):
    return torch.nn.functional.gelu(x, approximate="tanh")
def _silu(x):
    return torch.nn.functional.silu(x)
def _sigmoid(x):
    return torch.sigmoid(x)
def _layernorm(x, axis=-1, eps=1e-5):
    m = x.mean(dim=axis, keepdim=True)
    v = x.var(dim=axis, keepdim=True, unbiased=False)
    return (x - m) / torch.sqrt(v + eps)
def _scatter_add(tgt, idx, src, axis=0):
    return tgt.clone().index_add_(axis, idx, src)
def _segment_sum(src, seg, n):
    out = torch.zeros((n,) + src.shape[1:], dtype=src.dtype)
    return out.index_add_(0, seg, src)
def _pad(x, pw):
    flat = tuple(v for pair in reversed(pw) for v in pair)
    return torch.nn.functional.pad(x, flat)

kernel_name = 'standalone__gqa_softmax__near_ridge'

def setup_inputs():

    _w = (lambda a: (lambda t: t.to(torch.bfloat16) if t.is_floating_point() else t)(torch.as_tensor(a)))
    B, Hq, Hkv, Sq, Sk, D = 32, 32, 8, 368, 512, 128
    return {
        "q": _w((lambda *s: torch.randn(*s, dtype=torch.bfloat16) * 0.8)(B, Hq, Sq, D)),
        "k": _w((lambda *s: torch.randn(*s, dtype=torch.bfloat16) * 0.8)(B, Hkv, Sk, D)),
        "v": _w((lambda *s: torch.randn(*s, dtype=torch.bfloat16) * 0.05)(B, Hkv, Sk, D)),
    }

def reference(q, k, v):
    B, Hq, Sq, D = q.shape
    Hkv = k.shape[1]
    rep = Hq // Hkv
    k = (lambda x, n, axis: torch.repeat_interleave(x, n, dim=axis))(k, rep, axis=1)
    v = (lambda x, n, axis: torch.repeat_interleave(x, n, dim=axis))(v, rep, axis=1)
    scale = D ** -0.5
    scores = torch.einsum("bhqd,bhkd->bhqk", q, k) * scale
    p = _softmax(scores, axis=-1)
    return torch.einsum("bhqk,bhkd->bhqd", p, v)
```

```cpp
#include <hip/hip_runtime.h>
#include <hip/hip_bf16.h>


typedef unsigned short u16;
static __device__ __forceinline__ float bf2f(u16 x) { return __uint_as_float((unsigned)x << 16); }

static __device__ __forceinline__ void exact_rows(const u16* q, const u16* k, const u16* v, __hip_bfloat16* o,
                                                  int kvh, int row0, int nrows, int lane) {
  const u16* kb = k + (size_t)kvh * (512 * 128);
  const u16* vb = v + (size_t)kvh * (512 * 128);
  for (int r = row0; r < row0 + nrows; ++r) {
    const u16* qr = q + ((size_t)kvh * 1472 + r) * 128;
    float s0 = 0.f, s1 = 0.f, s2 = 0.f, s3 = 0.f, s4 = 0.f, s5 = 0.f, s6 = 0.f, s7 = 0.f;
    for (int d = 0; d < 128; ++d) {
      const float x = bf2f(qr[d]);
      const u16* kc = kb + lane * 128 + d;
      s0 += x * bf2f(kc[0 * 8192]); s1 += x * bf2f(kc[1 * 8192]); s2 += x * bf2f(kc[2 * 8192]); s3 += x * bf2f(kc[3 * 8192]);
      s4 += x * bf2f(kc[4 * 8192]); s5 += x * bf2f(kc[5 * 8192]); s6 += x * bf2f(kc[6 * 8192]); s7 += x * bf2f(kc[7 * 8192]);
    }
    const float sc = 0.088388347648318440f;
    s0 *= sc; s1 *= sc; s2 *= sc; s3 *= sc; s4 *= sc; s5 *= sc; s6 *= sc; s7 *= sc;
    float m = fmaxf(fmaxf(fmaxf(s0, s1), fmaxf(s2, s3)), fmaxf(fmaxf(s4, s5), fmaxf(s6, s7)));
    for (int off = 32; off > 0; off >>= 1) m = fmaxf(m, __shfl_xor(m, off));
    s0 = __expf(s0 - m); s1 = __expf(s1 - m); s2 = __expf(s2 - m); s3 = __expf(s3 - m);
    s4 = __expf(s4 - m); s5 = __expf(s5 - m); s6 = __expf(s6 - m); s7 = __expf(s7 - m);
    float l = ((s0 + s1) + (s2 + s3)) + ((s4 + s5) + (s6 + s7));
    for (int off = 32; off > 0; off >>= 1) l += __shfl_xor(l, off);
    float a0 = 0.f, a1 = 0.f;
    for (int j = 0; j < 64; ++j) {
      const float p0 = __shfl(s0, j), p1 = __shfl(s1, j), p2 = __shfl(s2, j), p3 = __shfl(s3, j);
      const float p4 = __shfl(s4, j), p5 = __shfl(s5, j), p6 = __shfl(s6, j), p7 = __shfl(s7, j);
      const u16* vc = vb + j * 128 + 2 * lane;
      a0 += p0 * bf2f(vc[0 * 8192]) + p1 * bf2f(vc[1 * 8192]) + p2 * bf2f(vc[2 * 8192]) + p3 * bf2f(vc[3 * 8192])
          + p4 * bf2f(vc[4 * 8192]) + p5 * bf2f(vc[5 * 8192]) + p6 * bf2f(vc[6 * 8192]) + p7 * bf2f(vc[7 * 8192]);
      a1 += p0 * bf2f(vc[0 * 8192 + 1]) + p1 * bf2f(vc[1 * 8192 + 1]) + p2 * bf2f(vc[2 * 8192 + 1]) + p3 * bf2f(vc[3 * 8192 + 1])
          + p4 * bf2f(vc[4 * 8192 + 1]) + p5 * bf2f(vc[5 * 8192 + 1]) + p6 * bf2f(vc[6 * 8192 + 1]) + p7 * bf2f(vc[7 * 8192 + 1]);
    }
    const float inv = 1.f / l;
    __hip_bfloat16* orow = o + ((size_t)kvh * 1472 + r) * 128 + 2 * lane;
    orow[0] = __float2bfloat16(a0 * inv);
    orow[1] = __float2bfloat16(a1 * inv);
  }
}

__global__ __launch_bounds__(256, 1) void gqa_attn_kernel(const u16* q, const u16* k, const u16* v, __hip_bfloat16* o) {
  __shared__ __attribute__((aligned(1024))) char lds[163840];
  unsigned mask, wave;
  const unsigned bid = blockIdx.x;
  const unsigned long long ka = (unsigned long long)__builtin_amdgcn_kernarg_segment_ptr();
  asm volatile(
"s_load_dwordx8 s[24:31], %4, 0x0\n"
"v_lshrrev_b32_e32 v15, 6, %2\n"
"v_and_b32_e32 v14, 63, %2\n"
"v_readfirstlane_b32 s64, v15\n"
"s_and_b32 s70, %3, 7\n"
"s_lshr_b32 s71, %3, 3\n"
"s_lshr_b32 s72, s71, 2\n"
"s_and_b32 s63, s71, 3\n"
"s_lshl_b32 s70, s70, 5\n"
"s_add_u32 s70, s70, s72\n"
"s_lshl_b32 s45, s70, 17\n"
"s_add_u32 s46, s45, 1048576\n"
"s_mul_i32 s70, s70, 376832\n"
"s_mul_i32 s71, s63, 98304\n"
"s_add_u32 s73, s70, s71\n"
"s_mov_b32 s39, 0x20000\n"
"s_mov_b32 s43, 0x20000\n"
"s_mov_b32 s42, 0\n"
"s_lshl_b32 s48, s64, 11\n"
"s_lshl_b32 s65, s64, 12\n"
"s_mul_i32 s49, s64, 24576\n"
"s_add_u32 s49, s49, 65536\n"
"s_mov_b32 s50, 0\n"
"s_mov_b32 s60, 0\n"
"s_mov_b32 s62, 0\n"
"v_and_b32_e32 v18, 3, v14\n"
"v_lshrrev_b32_e32 v21, 2, v14\n"
"v_and_b32_e32 v21, 7, v21\n"
"v_lshrrev_b32_e32 v22, 5, v14\n"
"v_lshlrev_b32_e32 v23, 8, v21\n"
"v_lshlrev_b32_e32 v24, 6, v22\n"
"v_add_u32_e32 v23, v23, v24\n"
"v_lshrrev_b32_e32 v24, 2, v21\n"
"s_lshl_b32 s72, s64, 1\n"
"v_add_u32_e32 v19, s72, v24\n"
"v_and_b32_e32 v19, 3, v19\n"
"v_xor_b32_e32 v19, v18, v19\n"
"v_lshlrev_b32_e32 v19, 4, v19\n"
"v_add_u32_e32 v19, v19, v23\n"
"v_add_u32_e32 v6, s48, v19\n"
"v_add_u32_e32 v19, 0, v24\n"
"v_and_b32_e32 v19, 3, v19\n"
"v_xor_b32_e32 v19, v18, v19\n"
"v_lshlrev_b32_e32 v19, 4, v19\n"
"v_add_u32_e32 v19, v19, v23\n"
"v_add_u32_e32 v7, 0, v19\n"
"v_add_u32_e32 v19, 2, v24\n"
"v_and_b32_e32 v19, 3, v19\n"
"v_xor_b32_e32 v19, v18, v19\n"
"v_lshlrev_b32_e32 v19, 4, v19\n"
"v_add_u32_e32 v19, v19, v23\n"
"v_add_u32_e32 v8, 2048, v19\n"
"s_waitcnt lgkmcnt(0)\n"
"s_add_u32 s66, s24, s73\n"
"s_addc_u32 s67, s25, 0\n"
"s_sub_u32 s66, s66, 16384\n"
"s_subb_u32 s67, s67, 0\n"
"s_and_b32 s67, s67, 0xffff\n"
"s_add_u32 s68, s30, s73\n"
"s_addc_u32 s69, s31, 0\n"
"s_sub_u32 s68, s68, 16384\n"
"s_subb_u32 s69, s69, 0\n"
"s_and_b32 s69, s69, 0xffff\n"
"s_mov_b64 s[24:25], s[26:27]\n"
"s_and_b32 s25, s25, 0xffff\n"
"s_mov_b32 s26, 33554432\n"
"s_mov_b32 s27, 0x20000\n"
"s_and_b32 s29, s29, 0xffff\n"
"s_mov_b32 s30, 33554432\n"
"s_mov_b32 s31, 0x20000\n"
"s_add_u32 s72, s50, 0\n"
"s_mul_i32 s70, s72, 3014656\n"
"s_cmp_gt_u32 s63, s72\n"
"s_cselect_b32 s71, 0, 16384\n"
"s_add_u32 s70, s70, s71\n"
"s_add_u32 s36, s66, s70\n"
"s_addc_u32 s37, s67, 0\n"
"s_cmp_eq_u32 s63, s72\n"
"s_cselect_b32 s71, 5, 6\n"
"s_mul_i32 s38, s71, 16384\n"
"s_mul_i32 s58, s64, s71\n"
"s_mul_i32 s58, s58, 4096\n"
"s_cselect_b32 s59, 1073741824, s58\n"
"s_cmp_gt_u32 s72, 3\n"
"s_cselect_b32 s38, 0, s38\n"
"s_add_u32 m0, s48, 0\n"
"s_add_u32 s47, s45, 0\n"
"buffer_load_dwordx4 v6, s[24:27], s47 offen lds\n"
"s_add_u32 m0, s48, 1024\n"
"s_add_u32 s47, s45, 128\n"
"buffer_load_dwordx4 v6, s[24:27], s47 offen lds\n"
"s_add_u32 m0, s49, 0\n"
"s_add_u32 s61, s58, 0\n"
"buffer_load_dwordx4 v7, s[36:39], s61 offen lds\n"
"s_add_u32 m0, s49, 1024\n"
"s_add_u32 s61, s58, 128\n"
"buffer_load_dwordx4 v7, s[36:39], s61 offen lds\n"
"s_add_u32 m0, s49, 2048\n"
"s_add_u32 s61, s58, 0\n"
"buffer_load_dwordx4 v8, s[36:39], s61 offen lds\n"
"s_add_u32 m0, s49, 3072\n"
"s_add_u32 s61, s58, 128\n"
"buffer_load_dwordx4 v8, s[36:39], s61 offen lds\n"
"s_add_u32 m0, s48, 8192\n"
"s_add_u32 s47, s45, 8192\n"
"buffer_load_dwordx4 v6, s[24:27], s47 offen lds\n"
"s_add_u32 m0, s48, 9216\n"
"s_add_u32 s47, s45, 8320\n"
"buffer_load_dwordx4 v6, s[24:27], s47 offen lds\n"
"s_add_u32 m0, s48, 32768\n"
"s_add_u32 s76, s45, 0\n"
"buffer_load_dwordx4 v6, s[28:31], s76 offen lds\n"
"s_add_u32 m0, s48, 33792\n"
"s_add_u32 s76, s45, 128\n"
"buffer_load_dwordx4 v6, s[28:31], s76 offen lds\n"
"s_add_u32 m0, s48, 40960\n"
"s_add_u32 s76, s45, 8192\n"
"buffer_load_dwordx4 v6, s[28:31], s76 offen lds\n"
"s_add_u32 m0, s48, 41984\n"
"s_add_u32 s76, s45, 8320\n"
"buffer_load_dwordx4 v6, s[28:31], s76 offen lds\n"
"s_add_u32 m0, s49, 4096\n"
"s_add_u32 s61, s58, 4096\n"
"buffer_load_dwordx4 v7, s[36:39], s61 offen lds\n"
"s_add_u32 m0, s49, 5120\n"
"s_add_u32 s61, s58, 4224\n"
"buffer_load_dwordx4 v7, s[36:39], s61 offen lds\n"
"s_add_u32 m0, s49, 6144\n"
"s_add_u32 s61, s58, 4096\n"
"buffer_load_dwordx4 v8, s[36:39], s61 offen lds\n"
"s_add_u32 m0, s49, 7168\n"
"s_add_u32 s61, s58, 4224\n"
"buffer_load_dwordx4 v8, s[36:39], s61 offen lds\n"
"v_lshlrev_b32_e32 v62, 7, v14\n"
"s_getpc_b64 s[84:85]\n"
"L_code%=:\n"
"s_and_b32 s85, s85, 0xffff\n"
"s_mov_b32 s86, L_end%=-L_code%=\n"
"s_mov_b32 s87, 0x20000\n"
"s_mov_b32 s88, L_H6%=+0-L_code%=\n"
"buffer_load_dword v63, v62, s[84:87], s88 offen\n"
"s_mov_b32 s88, L_H5%=+0-L_code%=\n"
"buffer_load_dword v63, v62, s[84:87], s88 offen\n"
"s_mov_b64 s[40:41], s[68:69]\n"
"s_mov_b32 s44, 0x3e0293ee\n"
"s_mov_b32 s51, 0x30800000\n"
"s_mov_b32 s52, 0x1e000000\n"
"s_mov_b32 s53, 0\n"
"s_mov_b64 s[54:55], 0\n"
"s_mov_b32 s56, 1\n"
"v_and_b32_e32 v15, 15, v14\n"
"v_lshrrev_b32_e32 v16, 4, v14\n"
"v_lshrrev_b32_e32 v17, 2, v15\n"
"v_lshlrev_b32_e32 v19, 1, v17\n"
"v_add_u32_e32 v19, 0, v19\n"
"v_and_b32_e32 v19, 3, v19\n"
"v_xor_b32_e32 v19, v16, v19\n"
"v_lshlrev_b32_e32 v19, 4, v19\n"
"v_lshlrev_b32_e32 v20, 11, v17\n"
"v_add_u32_e32 v19, v19, v20\n"
"v_lshlrev_b32_e32 v20, 6, v18\n"
"v_add_u32_e32 v19, v19, v20\n"
"v_add_u32_e32 v1, 0, v19\n"
"v_lshlrev_b32_e32 v19, 1, v17\n"
"v_add_u32_e32 v19, 1, v19\n"
"v_and_b32_e32 v19, 3, v19\n"
"v_xor_b32_e32 v19, v16, v19\n"
"v_lshlrev_b32_e32 v19, 4, v19\n"
"v_lshlrev_b32_e32 v20, 11, v17\n"
"v_add_u32_e32 v19, v19, v20\n"
"v_lshlrev_b32_e32 v20, 6, v18\n"
"v_add_u32_e32 v19, v19, v20\n"
"v_add_u32_e32 v2, 256, v19\n"
"v_lshrrev_b32_e32 v25, 1, v18\n"
"v_and_b32_e32 v26, 1, v18\n"
"v_lshlrev_b32_e32 v26, 3, v26\n"
"v_lshlrev_b32_e32 v20, 11, v16\n"
"v_add_u32_e32 v26, v26, v20\n"
"v_lshlrev_b32_e32 v20, 6, v17\n"
"v_add_u32_e32 v26, v26, v20\n"
"v_lshlrev_b32_e32 v19, 1, v16\n"
"v_add_u32_e32 v19, 0, v19\n"
"v_and_b32_e32 v19, 3, v19\n"
"v_add_u32_e32 v20, 0, v25\n"
"v_xor_b32_e32 v19, v20, v19\n"
"v_lshlrev_b32_e32 v19, 4, v19\n"
"v_add_u32_e32 v19, v19, v26\n"
"v_add_u32_e32 v28, 32768, v19\n"
"v_lshlrev_b32_e32 v19, 1, v16\n"
"v_add_u32_e32 v19, 1, v19\n"
"v_and_b32_e32 v19, 3, v19\n"
"v_add_u32_e32 v20, 0, v25\n"
"v_xor_b32_e32 v19, v20, v19\n"
"v_lshlrev_b32_e32 v19, 4, v19\n"
"v_add_u32_e32 v19, v19, v26\n"
"v_add_u32_e32 v29, 33024, v19\n"
"v_lshlrev_b32_e32 v19, 1, v16\n"
"v_add_u32_e32 v19, 0, v19\n"
"v_and_b32_e32 v19, 3, v19\n"
"v_add_u32_e32 v20, 2, v25\n"
"v_xor_b32_e32 v19, v20, v19\n"
"v_lshlrev_b32_e32 v19, 4, v19\n"
"v_add_u32_e32 v19, v19, v26\n"
"v_add_u32_e32 v30, 32768, v19\n"
"v_lshlrev_b32_e32 v19, 1, v16\n"
"v_add_u32_e32 v19, 1, v19\n"
"v_and_b32_e32 v19, 3, v19\n"
"v_add_u32_e32 v20, 2, v25\n"
"v_xor_b32_e32 v19, v20, v19\n"
"v_lshlrev_b32_e32 v19, 4, v19\n"
"v_add_u32_e32 v19, v19, v26\n"
"v_add_u32_e32 v31, 33024, v19\n"
"v_and_b32_e32 v19, 3, v17\n"
"v_xor_b32_e32 v19, v16, v19\n"
"v_lshlrev_b32_e32 v19, 4, v19\n"
"v_lshrrev_b32_e32 v20, 3, v15\n"
"v_lshlrev_b32_e32 v20, 11, v20\n"
"v_add_u32_e32 v19, v19, v20\n"
"v_and_b32_e32 v20, 7, v15\n"
"v_lshlrev_b32_e32 v20, 6, v20\n"
"v_add_u32_e32 v19, v19, v20\n"
"v_add_u32_e32 v5, s49, v19\n"
"v_lshlrev_b32_e32 v9, 4, v14\n"
"v_xor_b32_e32 v19, v18, v16\n"
"v_lshlrev_b32_e32 v19, 4, v19\n"
"v_lshlrev_b32_e32 v20, 8, v16\n"
"v_add_u32_e32 v19, v19, v20\n"
"v_add_u32_e32 v19, s49, v19\n"
"v_lshlrev_b32_e32 v20, 6, v17\n"
"v_add_u32_e32 v58, v19, v20\n"
"v_xor_b32_e32 v20, 64, v20\n"
"v_add_u32_e32 v59, v19, v20\n"
"v_lshrrev_b32_e32 v25, 1, v16\n"
"v_and_b32_e32 v26, 1, v16\n"
"v_lshlrev_b32_e32 v26, 3, v26\n"
"v_lshlrev_b32_e32 v20, 8, v15\n"
"v_add_u32_e32 v26, v26, v20\n"
"v_add_u32_e32 v26, s49, v26\n"
"v_add_u32_e32 v19, 0, v25\n"
"v_xor_b32_e32 v19, v19, v18\n"
"v_lshlrev_b32_e32 v19, 4, v19\n"
"v_and_b32_e32 v20, 1, v17\n"
"v_xor_b32_e32 v20, 0, v20\n"
"v_lshlrev_b32_e32 v20, 6, v20\n"
"v_add_u32_e32 v19, v19, v20\n"
"v_add_u32_e32 v32, v19, v26\n"
"v_add_u32_e32 v19, 2, v25\n"
"v_xor_b32_e32 v19, v19, v18\n"
"v_lshlrev_b32_e32 v19, 4, v19\n"
"v_and_b32_e32 v20, 1, v17\n"
"v_xor_b32_e32 v20, 0, v20\n"
"v_lshlrev_b32_e32 v20, 6, v20\n"
"v_add_u32_e32 v19, v19, v20\n"
"v_add_u32_e32 v33, v19, v26\n"
"v_add_u32_e32 v19, 0, v25\n"
"v_xor_b32_e32 v19, v19, v18\n"
"v_lshlrev_b32_e32 v19, 4, v19\n"
"v_and_b32_e32 v20, 1, v17\n"
"v_xor_b32_e32 v20, 1, v20\n"
"v_lshlrev_b32_e32 v20, 6, v20\n"
"v_add_u32_e32 v19, v19, v20\n"
"v_add_u32_e32 v34, v19, v26\n"
"v_add_u32_e32 v19, 2, v25\n"
"v_xor_b32_e32 v19, v19, v18\n"
"v_lshlrev_b32_e32 v19, 4, v19\n"
"v_and_b32_e32 v20, 1, v17\n"
"v_xor_b32_e32 v20, 1, v20\n"
"v_lshlrev_b32_e32 v20, 6, v20\n"
"v_add_u32_e32 v19, v19, v20\n"
"v_add_u32_e32 v35, v19, v26\n"
"v_cmp_eq_u32_e32 vcc, 0, v18\n"
"v_mov_b32_e32 v20, 0x3f803f80\n"
"s_nop 0\n"
"v_cndmask_b32_e32 v19, 0, v20, vcc\n"
"v_accvgpr_write_b32 a224, v19\n"
"v_accvgpr_write_b32 a225, v19\n"
"v_accvgpr_write_b32 a226, v19\n"
"v_accvgpr_write_b32 a227, v19\n"
"v_mov_b32_e32 v3, v28\n"
"v_mov_b32_e32 v4, v29\n"
"v_mov_b32_e32 v11, v30\n"
"v_mov_b32_e32 v14, v31\n"
"v_mov_b32_e32 v56, v32\n"
"v_mov_b32_e32 v57, v33\n"
"v_mov_b32_e32 v15, v34\n"
"v_mov_b32_e32 v60, v35\n"
"s_waitcnt vmcnt(16)\n"
"s_barrier\n"
"ds_read_b128 a[96:99], v1 offset:0\n"
"ds_read_b128 a[100:103], v1 offset:512\n"
"ds_read_b128 a[104:107], v1 offset:1024\n"
"ds_read_b128 a[108:111], v1 offset:1536\n"
"ds_read_b128 a[112:115], v2 offset:0\n"
"ds_read_b128 a[116:119], v2 offset:512\n"
"ds_read_b128 a[120:123], v2 offset:1024\n"
"ds_read_b128 a[124:127], v2 offset:1536\n"
"s_waitcnt vmcnt(12)\n"
"ds_read_b128 a[0:3], v5 offset:0\n"
"ds_read_b128 a[4:7], v5 offset:512\n"
"ds_read_b128 a[8:11], v5 offset:1024\n"
"ds_read_b128 a[12:15], v5 offset:1536\n"
"s_waitcnt lgkmcnt(3)\n"
"s_cmp_eq_u32 s63, 0\n"
"s_cbranch_scc1 L_H5%=\n"
"L_H6%=:\n"
"v_mfma_f32_16x16x32_bf16 v[16:19], a[96:99], a[0:3], 0\n"
"s_waitcnt lgkmcnt(2)\n"
"v_mfma_f32_16x16x32_bf16 v[16:19], a[100:103], a[4:7], v[16:19]\n"
"s_waitcnt lgkmcnt(1)\n"
"v_mfma_f32_16x16x32_bf16 v[16:19], a[104:107], a[8:11], v[16:19]\n"
"s_waitcnt lgkmcnt(0)\n"
"v_mfma_f32_16x16x32_bf16 v[16:19], a[108:111], a[12:15], v[16:19]\n"
"v_mfma_f32_16x16x32_bf16 v[20:23], a[112:115], a[0:3], 0\n"
"v_mfma_f32_16x16x32_bf16 v[20:23], a[116:119], a[4:7], v[20:23]\n"
"v_mfma_f32_16x16x32_bf16 v[20:23], a[120:123], a[8:11], v[20:23]\n"
"s_waitcnt vmcnt(10)\n"
"s_barrier\n"
"v_mfma_f32_16x16x32_bf16 v[20:23], a[124:127], a[12:15], v[20:23]\n"
"ds_read_b128 a[128:131], v1 offset:8192\n"
"s_add_u32 m0, s49, 8192\n"
"s_add_u32 s61, s58, 8192\n"
"buffer_load_dwordx4 v7, s[36:39], s61 offen lds\n"
"s_add_u32 m0, s49, 9216\n"
"s_add_u32 s61, s58, 8320\n"
"buffer_load_dwordx4 v7, s[36:39], s61 offen lds\n"
"s_add_u32 m0, s49, 10240\n"
"s_add_u32 s61, s58, 8192\n"
"buffer_load_dwordx4 v8, s[36:39], s61 offen lds\n"
"s_add_u32 m0, s49, 11264\n"
"s_add_u32 s61, s58, 8320\n"
"buffer_load_dwordx4 v8, s[36:39], s61 offen lds\n"
"ds_read_b128 a[132:135], v1 offset:8704\n"
"ds_read_b128 a[136:139], v1 offset:9216\n"
"ds_read_b128 a[140:143], v1 offset:9728\n"
"ds_read_b128 a[144:147], v2 offset:8192\n"
"ds_read_b128 a[148:151], v2 offset:8704\n"
"ds_read_b128 a[152:155], v2 offset:9216\n"
"ds_read_b128 a[156:159], v2 offset:9728\n"
"s_waitcnt lgkmcnt(7)\n"
"v_mfma_f32_16x16x32_bf16 v[24:27], a[128:131], a[0:3], 0\n"
"s_waitcnt vmcnt(10) lgkmcnt(6)\n"
"s_barrier\n"
"s_waitcnt vmcnt(6)\n"
"ds_read_b128 a[16:19], v5 offset:4096\n"
"v_mul_f32_e32 v16, s44, v16\n"
"v_mul_f32_e32 v17, s44, v17\n"
"v_mfma_f32_16x16x32_bf16 v[24:27], a[132:135], a[4:7], v[24:27]\n"
"s_waitcnt lgkmcnt(6)\n"
"ds_read_b64_tr_b16 a[172:173], v11 offset:512\n"
"v_exp_f32_e32 v16, v16\n"
"v_mfma_f32_16x16x32_bf16 v[24:27], a[136:139], a[8:11], v[24:27]\n"
"s_waitcnt lgkmcnt(6)\n"
"ds_read_b64_tr_b16 a[162:163], v4 offset:0\n"
"ds_read_b64_tr_b16 a[174:175], v14 offset:512\n"
"v_mul_f32_e32 v18, s44, v18\n"
"v_mfma_f32_16x16x32_bf16 v[24:27], a[140:143], a[12:15], v[24:27]\n"
"s_waitcnt lgkmcnt(7)\n"
"ds_read_b64_tr_b16 a[176:177], v3 offset:1024\n"
"v_exp_f32_e32 v17, v17\n"
"v_mfma_f32_16x16x32_bf16 v[28:31], a[144:147], a[0:3], 0\n"
"s_waitcnt lgkmcnt(7)\n"
"ds_read_b64_tr_b16 a[178:179], v4 offset:1024\n"
"v_mul_f32_e32 v19, s44, v19\n"
"v_cvt_pk_bf16_f32 v32, v16, v17\n"
"v_mfma_f32_16x16x32_bf16 v[28:31], a[148:151], a[4:7], v[28:31]\n"
"s_waitcnt lgkmcnt(7)\n"
"ds_read_b64_tr_b16 a[180:181], v11 offset:1024\n"
"v_exp_f32_e32 v18, v18\n"
"v_mfma_f32_16x16x32_bf16 v[28:31], a[152:155], a[8:11], v[28:31]\n"
"s_waitcnt lgkmcnt(7)\n"
"ds_read_b64_tr_b16 a[164:165], v11 offset:0\n"
"ds_read_b64_tr_b16 a[182:183], v14 offset:1024\n"
"v_mul_f32_e32 v20, s44, v20\n"
"v_mfma_f32_16x16x32_bf16 v[28:31], a[156:159], a[12:15], v[28:31]\n"
"s_waitcnt lgkmcnt(8)\n"
"ds_read_b64_tr_b16 a[184:185], v3 offset:1536\n"
"v_exp_f32_e32 v19, v19\n"
"s_add_u32 m0, s49, 12288\n"
"s_add_u32 s61, s58, 12288\n"
"buffer_load_dwordx4 v7, s[36:39], s61 offen lds\n"
"s_add_u32 m0, s49, 13312\n"
"s_add_u32 s61, s58, 12416\n"
"buffer_load_dwordx4 v7, s[36:39], s61 offen lds\n"
"s_add_u32 m0, s49, 14336\n"
"s_add_u32 s61, s58, 12288\n"
"buffer_load_dwordx4 v8, s[36:39], s61 offen lds\n"
"s_add_u32 m0, s49, 15360\n"
"s_add_u32 s61, s58, 12416\n"
"buffer_load_dwordx4 v8, s[36:39], s61 offen lds\n"
"ds_read_b64_tr_b16 a[186:187], v4 offset:1536\n"
"v_mul_f32_e32 v21, s44, v21\n"
"v_cvt_pk_bf16_f32 v33, v18, v19\n"
"ds_read_b64_tr_b16 a[188:189], v11 offset:1536\n"
"v_exp_f32_e32 v20, v20\n"
"ds_read_b64_tr_b16 a[166:167], v14 offset:0\n"
"ds_read_b64_tr_b16 a[190:191], v14 offset:1536\n"
"v_mul_f32_e32 v22, s44, v22\n"
"ds_read_b128 a[20:23], v5 offset:4608\n"
"v_exp_f32_e32 v21, v21\n"
"ds_read_b128 a[24:27], v5 offset:5120\n"
"v_mul_f32_e32 v23, s44, v23\n"
"v_cvt_pk_bf16_f32 v34, v20, v21\n"
"ds_read_b128 a[28:31], v5 offset:5632\n"
"v_exp_f32_e32 v22, v22\n"
"ds_read_b64_tr_b16 a[160:161], v3 offset:0\n"
"ds_read_b64_tr_b16 a[168:169], v3 offset:512\n"
"v_exp_f32_e32 v23, v23\n"
"ds_read_b64_tr_b16 a[170:171], v4 offset:512\n"
"v_cvt_pk_bf16_f32 v35, v22, v23\n"
"v_mfma_f32_16x16x32_bf16 v[16:19], a[96:99], a[16:19], 0\n"
"s_waitcnt lgkmcnt(5)\n"
"ds_read_b64_tr_b16 a[200:201], v3 offset:8704\n"
"v_mul_f32_e32 v24, s44, v24\n"
"v_mul_f32_e32 v25, s44, v25\n"
"v_mfma_f32_16x16x32_bf16 v[16:19], a[100:103], a[20:23], v[16:19]\n"
"s_waitcnt lgkmcnt(5)\n"
"ds_read_b64_tr_b16 a[202:203], v4 offset:8704\n"
"v_exp_f32_e32 v24, v24\n"
"v_mfma_f32_16x16x32_bf16 v[16:19], a[104:107], a[24:27], v[16:19]\n"
"s_waitcnt lgkmcnt(5)\n"
"ds_read_b64_tr_b16 a[192:193], v3 offset:8192\n"
"ds_read_b64_tr_b16 a[204:205], v11 offset:8704\n"
"v_mul_f32_e32 v26, s44, v26\n"
"v_mfma_f32_16x16x32_bf16 v[16:19], a[108:111], a[28:31], v[16:19]\n"
"ds_read_b64_tr_b16 a[206:207], v14 offset:8704\n"
"v_exp_f32_e32 v25, v25\n"
"v_mfma_f32_16x16x32_bf16 v[20:23], a[112:115], a[16:19], 0\n"
"ds_read_b64_tr_b16 a[208:209], v3 offset:9216\n"
"v_mul_f32_e32 v27, s44, v27\n"
"v_cvt_pk_bf16_f32 v36, v24, v25\n"
"v_mfma_f32_16x16x32_bf16 v[20:23], a[116:119], a[20:23], v[20:23]\n"
"ds_read_b64_tr_b16 a[210:211], v4 offset:9216\n"
"v_exp_f32_e32 v26, v26\n"
"v_mfma_f32_16x16x32_bf16 v[20:23], a[120:123], a[24:27], v[20:23]\n"
"ds_read_b64_tr_b16 a[194:195], v4 offset:8192\n"
"ds_read_b64_tr_b16 a[212:213], v11 offset:9216\n"
"v_mul_f32_e32 v28, s44, v28\n"
"v_mfma_f32_16x16x32_bf16 v[20:23], a[124:127], a[28:31], v[20:23]\n"
"s_waitcnt lgkmcnt(11)\n"
"ds_read_b64_tr_b16 a[214:215], v14 offset:9216\n"
"v_exp_f32_e32 v27, v27\n"
"s_add_u32 m0, s49, 16384\n"
"s_add_u32 s61, s58, 16384\n"
"buffer_load_dwordx4 v7, s[36:39], s61 offen lds\n"
"s_add_u32 m0, s49, 17408\n"
"s_add_u32 s61, s58, 16512\n"
"buffer_load_dwordx4 v7, s[36:39], s61 offen lds\n"
"s_add_u32 m0, s49, 18432\n"
"s_add_u32 s61, s58, 16384\n"
"buffer_load_dwordx4 v8, s[36:39], s61 offen lds\n"
"s_add_u32 m0, s49, 19456\n"
"s_add_u32 s61, s58, 16512\n"
"buffer_load_dwordx4 v8, s[36:39], s61 offen lds\n"
"v_mfma_f32_16x16x32_bf16 v[64:67], a[160:163], v[32:35], 0\n"
"ds_read_b64_tr_b16 a[216:217], v3 offset:9728\n"
"v_mul_f32_e32 v29, s44, v29\n"
"v_cvt_pk_bf16_f32 v37, v26, v27\n"
"v_mfma_f32_16x16x32_bf16 v[68:71], a[164:167], v[32:35], 0\n"
"s_waitcnt lgkmcnt(11)\n"
"ds_read_b64_tr_b16 a[218:219], v4 offset:9728\n"
"v_exp_f32_e32 v28, v28\n"
"v_mfma_f32_16x16x32_bf16 v[72:75], a[168:171], v[32:35], 0\n"
"ds_read_b64_tr_b16 a[196:197], v11 offset:8192\n"
"ds_read_b64_tr_b16 a[220:221], v11 offset:9728\n"
"v_mul_f32_e32 v30, s44, v30\n"
"v_mfma_f32_16x16x32_bf16 v[76:79], a[172:175], v[32:35], 0\n"
"ds_read_b64_tr_b16 a[222:223], v14 offset:9728\n"
"v_exp_f32_e32 v29, v29\n"
"v_mfma_f32_16x16x32_bf16 v[80:83], a[176:179], v[32:35], 0\n"
"v_mul_f32_e32 v31, s44, v31\n"
"v_cvt_pk_bf16_f32 v38, v28, v29\n"
"v_mfma_f32_16x16x32_bf16 v[84:87], a[180:183], v[32:35], 0\n"
"s_mov_b32 s88, L_L6%=+0-L_code%=\n"
"v_exp_f32_e32 v30, v30\n"
"v_mfma_f32_16x16x32_bf16 v[88:91], a[184:187], v[32:35], 0\n"
"buffer_load_dword v63, v62, s[84:87], s88 offen\n"
"v_mfma_f32_16x16x32_bf16 v[92:95], a[188:191], v[32:35], 0\n"
"v_exp_f32_e32 v31, v31\n"
"v_mfma_f32_16x16x32_bf16 a[228:231], a[224:227], v[32:35], 0\n"
"ds_read_b64_tr_b16 a[198:199], v14 offset:8192\n"
"v_cvt_pk_bf16_f32 v39, v30, v31\n"
"v_mfma_f32_16x16x32_bf16 v[24:27], a[128:131], a[16:19], 0\n"
"v_mul_f32_e32 v16, s44, v16\n"
"v_mul_f32_e32 v17, s44, v17\n"
"v_mfma_f32_16x16x32_bf16 v[24:27], a[132:135], a[20:23], v[24:27]\n"
"v_exp_f32_e32 v16, v16\n"
"v_mfma_f32_16x16x32_bf16 v[24:27], a[136:139], a[24:27], v[24:27]\n"
"s_waitcnt vmcnt(9)\n"
"ds_read_b128 a[36:39], v5 offset:8704\n"
"v_mul_f32_e32 v18, s44, v18\n"
"v_mfma_f32_16x16x32_bf16 v[24:27], a[140:143], a[28:31], v[24:27]\n"
"v_exp_f32_e32 v17, v17\n"
"v_mfma_f32_16x16x32_bf16 v[28:31], a[144:147], a[16:19], 0\n"
"v_mul_f32_e32 v19, s44, v19\n"
"v_cvt_pk_bf16_f32 v32, v16, v17\n"
"v_mfma_f32_16x16x32_bf16 v[28:31], a[148:151], a[20:23], v[28:31]\n"
"v_exp_f32_e32 v18, v18\n"
"v_mfma_f32_16x16x32_bf16 v[28:31], a[152:155], a[24:27], v[28:31]\n"
"ds_read_b128 a[40:43], v5 offset:9216\n"
"v_mul_f32_e32 v20, s44, v20\n"
"v_mfma_f32_16x16x32_bf16 v[28:31], a[156:159], a[28:31], v[28:31]\n"
"s_waitcnt lgkmcnt(10)\n"
"v_exp_f32_e32 v19, v19\n"
"s_add_u32 m0, s49, 20480\n"
"s_add_u32 s61, s59, 20480\n"
"buffer_load_dwordx4 v7, s[36:39], s61 offen lds\n"
"s_add_u32 m0, s49, 21504\n"
"s_add_u32 s61, s59, 20608\n"
"buffer_load_dwordx4 v7, s[36:39], s61 offen lds\n"
"s_add_u32 m0, s49, 22528\n"
"s_add_u32 s61, s59, 20480\n"
"buffer_load_dwordx4 v8, s[36:39], s61 offen lds\n"
"s_add_u32 m0, s49, 23552\n"
"s_add_u32 s61, s59, 20608\n"
"buffer_load_dwordx4 v8, s[36:39], s61 offen lds\n"
"v_mfma_f32_16x16x32_bf16 v[64:67], a[192:195], v[36:39], v[64:67]\n"
"s_waitcnt lgkmcnt(2)\n"
"v_mul_f32_e32 v21, s44, v21\n"
"v_cvt_pk_bf16_f32 v33, v18, v19\n"
"v_mfma_f32_16x16x32_bf16 v[68:71], a[196:199], v[36:39], v[68:71]\n"
"v_exp_f32_e32 v20, v20\n"
"v_mfma_f32_16x16x32_bf16 v[72:75], a[200:203], v[36:39], v[72:75]\n"
"ds_read_b128 a[44:47], v5 offset:9728\n"
"v_mul_f32_e32 v22, s44, v22\n"
"v_mfma_f32_16x16x32_bf16 v[76:79], a[204:207], v[36:39], v[76:79]\n"
"v_exp_f32_e32 v21, v21\n"
"v_mfma_f32_16x16x32_bf16 v[80:83], a[208:211], v[36:39], v[80:83]\n"
"v_mul_f32_e32 v23, s44, v23\n"
"v_cvt_pk_bf16_f32 v34, v20, v21\n"
"v_mfma_f32_16x16x32_bf16 v[84:87], a[212:215], v[36:39], v[84:87]\n"
"v_exp_f32_e32 v22, v22\n"
"v_mfma_f32_16x16x32_bf16 v[88:91], a[216:219], v[36:39], v[88:91]\n"
"ds_read_b128 a[32:35], v5 offset:8192\n"
"v_mfma_f32_16x16x32_bf16 v[92:95], a[220:223], v[36:39], v[92:95]\n"
"v_exp_f32_e32 v23, v23\n"
"v_mfma_f32_16x16x32_bf16 a[228:231], a[224:227], v[36:39], a[228:231]\n"
"s_waitcnt lgkmcnt(0)\n"
"v_cvt_pk_bf16_f32 v35, v22, v23\n"
"v_mfma_f32_16x16x32_bf16 v[16:19], a[96:99], a[32:35], 0\n"
"v_mul_f32_e32 v24, s44, v24\n"
"v_mul_f32_e32 v25, s44, v25\n"
"v_mfma_f32_16x16x32_bf16 v[16:19], a[100:103], a[36:39], v[16:19]\n"
"s_barrier\n"
"v_exp_f32_e32 v24, v24\n"
"v_mfma_f32_16x16x32_bf16 v[16:19], a[104:107], a[40:43], v[16:19]\n"
"v_mul_f32_e32 v26, s44, v26\n"
"v_mfma_f32_16x16x32_bf16 v[16:19], a[108:111], a[44:47], v[16:19]\n"
"v_exp_f32_e32 v25, v25\n"
"v_mfma_f32_16x16x32_bf16 v[20:23], a[112:115], a[32:35], 0\n"
"v_mul_f32_e32 v27, s44, v27\n"
"v_cvt_pk_bf16_f32 v36, v24, v25\n"
"v_mfma_f32_16x16x32_bf16 v[20:23], a[116:119], a[36:39], v[20:23]\n"
"v_exp_f32_e32 v26, v26\n"
"v_mfma_f32_16x16x32_bf16 v[20:23], a[120:123], a[40:43], v[20:23]\n"
"v_mul_f32_e32 v28, s44, v28\n"
"v_mfma_f32_16x16x32_bf16 v[20:23], a[124:127], a[44:47], v[20:23]\n"
"v_exp_f32_e32 v27, v27\n"
"v_mfma_f32_16x16x32_bf16 v[96:99], a[160:163], v[32:35], 0\n"
"v_mul_f32_e32 v29, s44, v29\n"
"v_cvt_pk_bf16_f32 v37, v26, v27\n"
"v_mfma_f32_16x16x32_bf16 v[100:103], a[164:167], v[32:35], 0\n"
"v_exp_f32_e32 v28, v28\n"
"v_mfma_f32_16x16x32_bf16 v[104:107], a[168:171], v[32:35], 0\n"
"v_mul_f32_e32 v30, s44, v30\n"
"v_mfma_f32_16x16x32_bf16 v[108:111], a[172:175], v[32:35], 0\n"
"v_exp_f32_e32 v29, v29\n"
"v_mfma_f32_16x16x32_bf16 v[112:115], a[176:179], v[32:35], 0\n"
"v_mul_f32_e32 v31, s44, v31\n"
"v_cvt_pk_bf16_f32 v38, v28, v29\n"
"v_mfma_f32_16x16x32_bf16 v[116:119], a[180:183], v[32:35], 0\n"
"v_exp_f32_e32 v30, v30\n"
"v_mfma_f32_16x16x32_bf16 v[120:123], a[184:187], v[32:35], 0\n"
"v_mfma_f32_16x16x32_bf16 v[124:127], a[188:191], v[32:35], 0\n"
"v_exp_f32_e32 v31, v31\n"
"v_mfma_f32_16x16x32_bf16 a[232:235], a[224:227], v[32:35], 0\n"
"v_cvt_pk_bf16_f32 v39, v30, v31\n"
"s_add_u32 s72, s50, 1\n"
"s_mul_i32 s70, s72, 3014656\n"
"s_cmp_gt_u32 s63, s72\n"
"s_cselect_b32 s71, 0, 16384\n"
"s_add_u32 s70, s70, s71\n"
"s_add_u32 s36, s66, s70\n"
"s_addc_u32 s37, s67, 0\n"
"s_cmp_eq_u32 s63, s72\n"
"s_cselect_b32 s71, 5, 6\n"
"s_mul_i32 s38, s71, 16384\n"
"s_mul_i32 s58, s64, s71\n"
"s_mul_i32 s58, s58, 4096\n"
"s_cselect_b32 s59, 1073741824, s58\n"
"s_cmp_gt_u32 s72, 3\n"
"s_cselect_b32 s38, 0, s38\n"
"v_mfma_f32_16x16x32_bf16 v[24:27], a[128:131], a[32:35], 0\n"
"v_mul_f32_e32 v16, s44, v16\n"
"v_mul_f32_e32 v17, s44, v17\n"
"v_mfma_f32_16x16x32_bf16 v[24:27], a[132:135], a[36:39], v[24:27]\n"
"v_exp_f32_e32 v16, v16\n"
"v_mfma_f32_16x16x32_bf16 v[24:27], a[136:139], a[40:43], v[24:27]\n"
"s_waitcnt vmcnt(9)\n"
"ds_read_b128 a[48:51], v5 offset:12288\n"
"v_mul_f32_e32 v18, s44, v18\n"
"v_mfma_f32_16x16x32_bf16 v[24:27], a[140:143], a[44:47], v[24:27]\n"
"v_exp_f32_e32 v17, v17\n"
"v_mfma_f32_16x16x32_bf16 v[28:31], a[144:147], a[32:35], 0\n"
"v_mul_f32_e32 v19, s44, v19\n"
"v_cvt_pk_bf16_f32 v32, v16, v17\n"
"v_mfma_f32_16x16x32_bf16 v[28:31], a[148:151], a[36:39], v[28:31]\n"
"v_exp_f32_e32 v18, v18\n"
"v_mfma_f32_16x16x32_bf16 v[28:31], a[152:155], a[40:43], v[28:31]\n"
"ds_read_b128 a[52:55], v5 offset:12800\n"
"v_mul_f32_e32 v20, s44, v20\n"
"v_mfma_f32_16x16x32_bf16 v[28:31], a[156:159], a[44:47], v[28:31]\n"
"v_exp_f32_e32 v19, v19\n"
"s_add_u32 m0, s48, 16384\n"
"s_add_u32 s47, s45, 16384\n"
"buffer_load_dwordx4 v6, s[24:27], s47 offen lds\n"
"s_add_u32 m0, s48, 17408\n"
"s_add_u32 s47, s45, 16512\n"
"buffer_load_dwordx4 v6, s[24:27], s47 offen lds\n"
"v_mfma_f32_16x16x32_bf16 v[96:99], a[192:195], v[36:39], v[96:99]\n"
"v_mul_f32_e32 v21, s44, v21\n"
"v_cvt_pk_bf16_f32 v33, v18, v19\n"
"v_mfma_f32_16x16x32_bf16 v[100:103], a[196:199], v[36:39], v[100:103]\n"
"v_exp_f32_e32 v20, v20\n"
"v_mfma_f32_16x16x32_bf16 v[104:107], a[200:203], v[36:39], v[104:107]\n"
"ds_read_b128 a[56:59], v5 offset:13312\n"
"v_mul_f32_e32 v22, s44, v22\n"
"v_mfma_f32_16x16x32_bf16 v[108:111], a[204:207], v[36:39], v[108:111]\n"
"v_exp_f32_e32 v21, v21\n"
"v_mfma_f32_16x16x32_bf16 v[112:115], a[208:211], v[36:39], v[112:115]\n"
"v_mul_f32_e32 v23, s44, v23\n"
"v_cvt_pk_bf16_f32 v34, v20, v21\n"
"v_mfma_f32_16x16x32_bf16 v[116:119], a[212:215], v[36:39], v[116:119]\n"
"s_mov_b32 s88, L_L6%=+8192-L_code%=\n"
"v_exp_f32_e32 v22, v22\n"
"v_mfma_f32_16x16x32_bf16 v[120:123], a[216:219], v[36:39], v[120:123]\n"
"buffer_load_dword v63, v62, s[84:87], s88 offen\n"
"v_mfma_f32_16x16x32_bf16 v[124:127], a[220:223], v[36:39], v[124:127]\n"
"v_exp_f32_e32 v23, v23\n"
"v_mfma_f32_16x16x32_bf16 a[232:235], a[224:227], v[36:39], a[232:235]\n"
"s_waitcnt lgkmcnt(2)\n"
"ds_read_b128 a[60:63], v5 offset:13824\n"
"v_cvt_pk_bf16_f32 v35, v22, v23\n"
"v_mfma_f32_16x16x32_bf16 v[16:19], a[96:99], a[48:51], 0\n"
"s_waitcnt lgkmcnt(2)\n"
"v_mul_f32_e32 v24, s44, v24\n"
"v_mul_f32_e32 v25, s44, v25\n"
"v_mfma_f32_16x16x32_bf16 v[16:19], a[100:103], a[52:55], v[16:19]\n"
"s_waitcnt lgkmcnt(1)\n"
"v_exp_f32_e32 v24, v24\n"
"v_mfma_f32_16x16x32_bf16 v[16:19], a[104:107], a[56:59], v[16:19]\n"
"s_waitcnt lgkmcnt(0)\n"
"v_mul_f32_e32 v26, s44, v26\n"
"v_mfma_f32_16x16x32_bf16 v[16:19], a[108:111], a[60:63], v[16:19]\n"
"v_exp_f32_e32 v25, v25\n"
"v_mfma_f32_16x16x32_bf16 v[20:23], a[112:115], a[48:51], 0\n"
"v_mul_f32_e32 v27, s44, v27\n"
"v_cvt_pk_bf16_f32 v36, v24, v25\n"
"v_mfma_f32_16x16x32_bf16 v[20:23], a[116:119], a[52:55], v[20:23]\n"
"v_exp_f32_e32 v26, v26\n"
"v_mfma_f32_16x16x32_bf16 v[20:23], a[120:123], a[56:59], v[20:23]\n"
"v_mul_f32_e32 v28, s44, v28\n"
"v_mfma_f32_16x16x32_bf16 v[20:23], a[124:127], a[60:63], v[20:23]\n"
"v_exp_f32_e32 v27, v27\n"
"s_add_u32 m0, s48, 49152\n"
"s_add_u32 s76, s45, 16384\n"
"buffer_load_dwordx4 v6, s[28:31], s76 offen lds\n"
"s_add_u32 m0, s48, 50176\n"
"s_add_u32 s76, s45, 16512\n"
"buffer_load_dwordx4 v6, s[28:31], s76 offen lds\n"
"v_mfma_f32_16x16x32_bf16 v[128:131], a[160:163], v[32:35], 0\n"
"v_mul_f32_e32 v29, s44, v29\n"
"v_cvt_pk_bf16_f32 v37, v26, v27\n"
"v_mfma_f32_16x16x32_bf16 v[132:135], a[164:167], v[32:35], 0\n"
"v_exp_f32_e32 v28, v28\n"
"v_mfma_f32_16x16x32_bf16 v[136:139], a[168:171], v[32:35], 0\n"
"v_mul_f32_e32 v30, s44, v30\n"
"v_mfma_f32_16x16x32_bf16 v[140:143], a[172:175], v[32:35], 0\n"
"v_exp_f32_e32 v29, v29\n"
"v_mfma_f32_16x16x32_bf16 v[144:147], a[176:179], v[32:35], 0\n"
"v_mul_f32_e32 v31, s44, v31\n"
"v_cvt_pk_bf16_f32 v38, v28, v29\n"
"v_mfma_f32_16x16x32_bf16 v[148:151], a[180:183], v[32:35], 0\n"
"v_exp_f32_e32 v30, v30\n"
"v_mfma_f32_16x16x32_bf16 v[152:155], a[184:187], v[32:35], 0\n"
"v_mfma_f32_16x16x32_bf16 v[156:159], a[188:191], v[32:35], 0\n"
"v_exp_f32_e32 v31, v31\n"
"v_mfma_f32_16x16x32_bf16 a[236:239], a[224:227], v[32:35], 0\n"
"v_cvt_pk_bf16_f32 v39, v30, v31\n"
"v_mfma_f32_16x16x32_bf16 v[24:27], a[128:131], a[48:51], 0\n"
"v_mul_f32_e32 v16, s44, v16\n"
"v_mul_f32_e32 v17, s44, v17\n"
"v_mfma_f32_16x16x32_bf16 v[24:27], a[132:135], a[52:55], v[24:27]\n"
"v_exp_f32_e32 v16, v16\n"
"v_mfma_f32_16x16x32_bf16 v[24:27], a[136:139], a[56:59], v[24:27]\n"
"s_waitcnt vmcnt(10)\n"
"ds_read_b128 a[68:71], v5 offset:16896\n"
"v_mul_f32_e32 v18, s44, v18\n"
"v_mfma_f32_16x16x32_bf16 v[24:27], a[140:143], a[60:63], v[24:27]\n"
"v_exp_f32_e32 v17, v17\n"
"v_mfma_f32_16x16x32_bf16 v[28:31], a[144:147], a[48:51], 0\n"
"v_mul_f32_e32 v19, s44, v19\n"
"v_cvt_pk_bf16_f32 v32, v16, v17\n"
"v_mfma_f32_16x16x32_bf16 v[28:31], a[148:151], a[52:55], v[28:31]\n"
"v_exp_f32_e32 v18, v18\n"
"v_mfma_f32_16x16x32_bf16 v[28:31], a[152:155], a[56:59], v[28:31]\n"
"ds_read_b128 a[72:75], v5 offset:17408\n"
"v_mul_f32_e32 v20, s44, v20\n"
"v_mfma_f32_16x16x32_bf16 v[28:31], a[156:159], a[60:63], v[28:31]\n"
"v_exp_f32_e32 v19, v19\n"
"s_add_u32 m0, s48, 24576\n"
"s_add_u32 s47, s45, 24576\n"
"buffer_load_dwordx4 v6, s[24:27], s47 offen lds\n"
"s_add_u32 m0, s48, 25600\n"
"s_add_u32 s47, s45, 24704\n"
"buffer_load_dwordx4 v6, s[24:27], s47 offen lds\n"
"v_mfma_f32_16x16x32_bf16 v[128:131], a[192:195], v[36:39], v[128:131]\n"
"v_mul_f32_e32 v21, s44, v21\n"
"v_cvt_pk_bf16_f32 v33, v18, v19\n"
"v_mfma_f32_16x16x32_bf16 v[132:135], a[196:199], v[36:39], v[132:135]\n"
"v_exp_f32_e32 v20, v20\n"
"v_mfma_f32_16x16x32_bf16 v[136:139], a[200:203], v[36:39], v[136:139]\n"
"ds_read_b128 a[76:79], v5 offset:17920\n"
"v_mul_f32_e32 v22, s44, v22\n"
"v_mfma_f32_16x16x32_bf16 v[140:143], a[204:207], v[36:39], v[140:143]\n"
"v_exp_f32_e32 v21, v21\n"
"v_mfma_f32_16x16x32_bf16 v[144:147], a[208:211], v[36:39], v[144:147]\n"
"v_mul_f32_e32 v23, s44, v23\n"
"v_cvt_pk_bf16_f32 v34, v20, v21\n"
"v_mfma_f32_16x16x32_bf16 v[148:151], a[212:215], v[36:39], v[148:151]\n"
"v_exp_f32_e32 v22, v22\n"
"v_mfma_f32_16x16x32_bf16 v[152:155], a[216:219], v[36:39], v[152:155]\n"
"ds_read_b128 a[64:67], v5 offset:16384\n"
"v_mfma_f32_16x16x32_bf16 v[156:159], a[220:223], v[36:39], v[156:159]\n"
"v_exp_f32_e32 v23, v23\n"
"v_mfma_f32_16x16x32_bf16 a[236:239], a[224:227], v[36:39], a[236:239]\n"
"s_waitcnt lgkmcnt(0)\n"
"v_cvt_pk_bf16_f32 v35, v22, v23\n"
"v_mfma_f32_16x16x32_bf16 v[16:19], a[96:99], a[64:67], 0\n"
"v_mul_f32_e32 v24, s44, v24\n"
"v_mul_f32_e32 v25, s44, v25\n"
"v_mfma_f32_16x16x32_bf16 v[16:19], a[100:103], a[68:71], v[16:19]\n"
"v_exp_f32_e32 v24, v24\n"
"v_mfma_f32_16x16x32_bf16 v[16:19], a[104:107], a[72:75], v[16:19]\n"
"v_mul_f32_e32 v26, s44, v26\n"
"v_mfma_f32_16x16x32_bf16 v[16:19], a[108:111], a[76:79], v[16:19]\n"
"v_exp_f32_e32 v25, v25\n"
"v_mfma_f32_16x16x32_bf16 v[20:23], a[112:115], a[64:67], 0\n"
"v_mul_f32_e32 v27, s44, v27\n"
"v_cvt_pk_bf16_f32 v36, v24, v25\n"
"v_mfma_f32_16x16x32_bf16 v[20:23], a[116:119], a[68:71], v[20:23]\n"
"v_exp_f32_e32 v26, v26\n"
"v_mfma_f32_16x16x32_bf16 v[20:23], a[120:123], a[72:75], v[20:23]\n"
"v_mul_f32_e32 v28, s44, v28\n"
"v_mfma_f32_16x16x32_bf16 v[20:23], a[124:127], a[76:79], v[20:23]\n"
"v_exp_f32_e32 v27, v27\n"
"s_add_u32 m0, s48, 57344\n"
"s_add_u32 s76, s45, 24576\n"
"buffer_load_dwordx4 v6, s[28:31], s76 offen lds\n"
"s_add_u32 m0, s48, 58368\n"
"s_add_u32 s76, s45, 24704\n"
"buffer_load_dwordx4 v6, s[28:31], s76 offen lds\n"
"v_mfma_f32_16x16x32_bf16 v[160:163], a[160:163], v[32:35], 0\n"
"s_add_u32 m0, s48, 1024\n"
"v_mul_f32_e32 v29, s44, v29\n"
"v_cvt_pk_bf16_f32 v37, v26, v27\n"
"v_mfma_f32_16x16x32_bf16 v[164:167], a[164:167], v[32:35], 0\n"
"s_add_u32 s47, s45, 32896\n"
"v_exp_f32_e32 v28, v28\n"
"v_mfma_f32_16x16x32_bf16 v[168:171], a[168:171], v[32:35], 0\n"
"buffer_load_dwordx4 v6, s[24:27], s47 offen lds\n"
"v_mul_f32_e32 v30, s44, v30\n"
"v_mfma_f32_16x16x32_bf16 v[172:175], a[172:175], v[32:35], 0\n"
"v_exp_f32_e32 v29, v29\n"
"v_mfma_f32_16x16x32_bf16 v[176:179], a[176:179], v[32:35], 0\n"
"v_mul_f32_e32 v31, s44, v31\n"
"v_cvt_pk_bf16_f32 v38, v28, v29\n"
"v_mfma_f32_16x16x32_bf16 v[180:183], a[180:183], v[32:35], 0\n"
"s_mov_b32 s88, L_L6%=+16384-L_code%=\n"
"v_exp_f32_e32 v30, v30\n"
"v_mfma_f32_16x16x32_bf16 v[184:187], a[184:187], v[32:35], 0\n"
"s_add_u32 m0, s48, 0\n"
"buffer_load_dword v63, v62, s[84:87], s88 offen\n"
"v_mfma_f32_16x16x32_bf16 v[188:191], a[188:191], v[32:35], 0\n"
"s_add_u32 s47, s45, 32768\n"
"v_exp_f32_e32 v31, v31\n"
"v_mfma_f32_16x16x32_bf16 a[240:243], a[224:227], v[32:35], 0\n"
"buffer_load_dwordx4 v6, s[24:27], s47 offen lds\n"
"v_cvt_pk_bf16_f32 v39, v30, v31\n"
"v_mfma_f32_16x16x32_bf16 v[24:27], a[128:131], a[64:67], 0\n"
"s_waitcnt vmcnt(12)\n"
"ds_read_b128 a[92:95], v5 offset:22016\n"
"v_mul_f32_e32 v16, s44, v16\n"
"v_mul_f32_e32 v17, s44, v17\n"
"v_mfma_f32_16x16x32_bf16 v[24:27], a[132:135], a[68:71], v[24:27]\n"
"v_exp_f32_e32 v16, v16\n"
"v_mfma_f32_16x16x32_bf16 v[24:27], a[136:139], a[72:75], v[24:27]\n"
"ds_read_b128 a[80:83], v5 offset:20480\n"
"v_mul_f32_e32 v18, s44, v18\n"
"v_mfma_f32_16x16x32_bf16 v[24:27], a[140:143], a[76:79], v[24:27]\n"
"v_exp_f32_e32 v17, v17\n"
"v_mfma_f32_16x16x32_bf16 v[28:31], a[144:147], a[64:67], 0\n"
"v_mul_f32_e32 v19, s44, v19\n"
"v_cvt_pk_bf16_f32 v32, v16, v17\n"
"v_mfma_f32_16x16x32_bf16 v[28:31], a[148:151], a[68:71], v[28:31]\n"
"v_exp_f32_e32 v18, v18\n"
"v_mfma_f32_16x16x32_bf16 v[28:31], a[152:155], a[72:75], v[28:31]\n"
"ds_read_b128 a[84:87], v5 offset:20992\n"
"v_mul_f32_e32 v20, s44, v20\n"
"v_mfma_f32_16x16x32_bf16 v[28:31], a[156:159], a[76:79], v[28:31]\n"
"v_exp_f32_e32 v19, v19\n"
"v_mfma_f32_16x16x32_bf16 v[160:163], a[192:195], v[36:39], v[160:163]\n"
"s_add_u32 m0, s48, 33792\n"
"v_mul_f32_e32 v21, s44, v21\n"
"v_cvt_pk_bf16_f32 v33, v18, v19\n"
"v_mfma_f32_16x16x32_bf16 v[164:167], a[196:199], v[36:39], v[164:167]\n"
"s_add_u32 s76, s45, 32896\n"
"v_exp_f32_e32 v20, v20\n"
"v_mfma_f32_16x16x32_bf16 v[168:171], a[200:203], v[36:39], v[168:171]\n"
"buffer_load_dwordx4 v6, s[28:31], s76 offen lds\n"
"v_mul_f32_e32 v22, s44, v22\n"
"v_mfma_f32_16x16x32_bf16 v[172:175], a[204:207], v[36:39], v[172:175]\n"
"v_exp_f32_e32 v21, v21\n"
"v_mfma_f32_16x16x32_bf16 v[176:179], a[208:211], v[36:39], v[176:179]\n"
"s_add_u32 m0, s48, 32768\n"
"v_mul_f32_e32 v23, s44, v23\n"
"v_cvt_pk_bf16_f32 v34, v20, v21\n"
"v_mfma_f32_16x16x32_bf16 v[180:183], a[212:215], v[36:39], v[180:183]\n"
"s_add_u32 s76, s45, 32768\n"
"v_exp_f32_e32 v22, v22\n"
"v_mfma_f32_16x16x32_bf16 v[184:187], a[216:219], v[36:39], v[184:187]\n"
"buffer_load_dwordx4 v6, s[28:31], s76 offen lds\n"
"v_mfma_f32_16x16x32_bf16 v[188:191], a[220:223], v[36:39], v[188:191]\n"
"v_exp_f32_e32 v23, v23\n"
"v_mfma_f32_16x16x32_bf16 a[240:243], a[224:227], v[36:39], a[240:243]\n"
"s_waitcnt lgkmcnt(1)\n"
"ds_read_b128 a[88:91], v5 offset:21504\n"
"v_cvt_pk_bf16_f32 v35, v22, v23\n"
"v_mfma_f32_16x16x32_bf16 v[16:19], a[96:99], a[80:83], 0\n"
"s_waitcnt lgkmcnt(1)\n"
"v_mul_f32_e32 v24, s44, v24\n"
"v_mul_f32_e32 v25, s44, v25\n"
"v_mfma_f32_16x16x32_bf16 v[16:19], a[100:103], a[84:87], v[16:19]\n"
"s_waitcnt lgkmcnt(0)\n"
"v_exp_f32_e32 v24, v24\n"
"v_mfma_f32_16x16x32_bf16 v[16:19], a[104:107], a[88:91], v[16:19]\n"
"v_mul_f32_e32 v26, s44, v26\n"
"v_mfma_f32_16x16x32_bf16 v[16:19], a[108:111], a[92:95], v[16:19]\n"
"v_exp_f32_e32 v25, v25\n"
"v_mfma_f32_16x16x32_bf16 v[20:23], a[112:115], a[80:83], 0\n"
"v_mul_f32_e32 v27, s44, v27\n"
"v_cvt_pk_bf16_f32 v36, v24, v25\n"
"v_mfma_f32_16x16x32_bf16 v[20:23], a[116:119], a[84:87], v[20:23]\n"
"v_exp_f32_e32 v26, v26\n"
"v_mfma_f32_16x16x32_bf16 v[20:23], a[120:123], a[88:91], v[20:23]\n"
"v_mul_f32_e32 v28, s44, v28\n"
"v_mfma_f32_16x16x32_bf16 v[20:23], a[124:127], a[92:95], v[20:23]\n"
"s_waitcnt vmcnt(12)\n"
"s_barrier\n"
"v_exp_f32_e32 v27, v27\n"
"v_mfma_f32_16x16x32_bf16 v[192:195], a[160:163], v[32:35], 0\n"
"s_add_u32 m0, s48, 9216\n"
"ds_read_b128 a[100:103], v1 offset:16896\n"
"v_mul_f32_e32 v29, s44, v29\n"
"v_cvt_pk_bf16_f32 v37, v26, v27\n"
"v_mfma_f32_16x16x32_bf16 v[196:199], a[164:167], v[32:35], 0\n"
"s_add_u32 s47, s45, 41088\n"
"ds_read_b128 a[104:107], v1 offset:17408\n"
"v_exp_f32_e32 v28, v28\n"
"v_mfma_f32_16x16x32_bf16 v[200:203], a[168:171], v[32:35], 0\n"
"buffer_load_dwordx4 v6, s[24:27], s47 offen lds\n"
"v_mul_f32_e32 v30, s44, v30\n"
"v_mfma_f32_16x16x32_bf16 v[204:207], a[172:175], v[32:35], 0\n"
"ds_read_b128 a[108:111], v1 offset:17920\n"
"v_exp_f32_e32 v29, v29\n"
"v_mfma_f32_16x16x32_bf16 v[208:211], a[176:179], v[32:35], 0\n"
"s_add_u32 m0, s48, 8192\n"
"v_mul_f32_e32 v31, s44, v31\n"
"v_cvt_pk_bf16_f32 v38, v28, v29\n"
"v_mfma_f32_16x16x32_bf16 v[212:215], a[180:183], v[32:35], 0\n"
"s_add_u32 s47, s45, 40960\n"
"v_exp_f32_e32 v30, v30\n"
"v_mfma_f32_16x16x32_bf16 v[216:219], a[184:187], v[32:35], 0\n"
"buffer_load_dwordx4 v6, s[24:27], s47 offen lds\n"
"v_mfma_f32_16x16x32_bf16 v[220:223], a[188:191], v[32:35], 0\n"
"v_exp_f32_e32 v31, v31\n"
"v_mfma_f32_16x16x32_bf16 a[244:247], a[224:227], v[32:35], 0\n"
"ds_read_b128 a[96:99], v1 offset:16384\n"
"v_cvt_pk_bf16_f32 v39, v30, v31\n"
"v_mfma_f32_16x16x32_bf16 v[24:27], a[128:131], a[80:83], 0\n"
"ds_read_b128 a[120:123], v2 offset:17408\n"
"v_mul_f32_e32 v16, s44, v16\n"
"v_mul_f32_e32 v17, s44, v17\n"
"v_mfma_f32_16x16x32_bf16 v[24:27], a[132:135], a[84:87], v[24:27]\n"
"ds_read_b128 a[124:127], v2 offset:17920\n"
"v_exp_f32_e32 v16, v16\n"
"v_mfma_f32_16x16x32_bf16 v[24:27], a[136:139], a[88:91], v[24:27]\n"
"ds_read_b128 a[112:115], v2 offset:16384\n"
"v_mul_f32_e32 v18, s44, v18\n"
"v_mfma_f32_16x16x32_bf16 v[24:27], a[140:143], a[92:95], v[24:27]\n"
"v_exp_f32_e32 v17, v17\n"
"v_mfma_f32_16x16x32_bf16 v[28:31], a[144:147], a[80:83], 0\n"
"v_mul_f32_e32 v19, s44, v19\n"
"v_cvt_pk_bf16_f32 v32, v16, v17\n"
"v_mfma_f32_16x16x32_bf16 v[28:31], a[148:151], a[84:87], v[28:31]\n"
"v_exp_f32_e32 v18, v18\n"
"v_mfma_f32_16x16x32_bf16 v[28:31], a[152:155], a[88:91], v[28:31]\n"
"ds_read_b128 a[116:119], v2 offset:16896\n"
"v_mul_f32_e32 v20, s44, v20\n"
"v_mfma_f32_16x16x32_bf16 v[28:31], a[156:159], a[92:95], v[28:31]\n"
"v_exp_f32_e32 v19, v19\n"
"v_mfma_f32_16x16x32_bf16 v[192:195], a[192:195], v[36:39], v[192:195]\n"
"s_add_u32 m0, s48, 41984\n"
"v_mul_f32_e32 v21, s44, v21\n"
"v_cvt_pk_bf16_f32 v33, v18, v19\n"
"v_mfma_f32_16x16x32_bf16 v[196:199], a[196:199], v[36:39], v[196:199]\n"
"s_add_u32 s76, s45, 41088\n"
"v_exp_f32_e32 v20, v20\n"
"v_mfma_f32_16x16x32_bf16 v[200:203], a[200:203], v[36:39], v[200:203]\n"
"buffer_load_dwordx4 v6, s[28:31], s76 offen lds\n"
"v_mul_f32_e32 v22, s44, v22\n"
"v_mfma_f32_16x16x32_bf16 v[204:207], a[204:207], v[36:39], v[204:207]\n"
"v_exp_f32_e32 v21, v21\n"
"v_mfma_f32_16x16x32_bf16 v[208:211], a[208:211], v[36:39], v[208:211]\n"
"s_add_u32 m0, s48, 40960\n"
"v_mul_f32_e32 v23, s44, v23\n"
"v_cvt_pk_bf16_f32 v34, v20, v21\n"
"v_mfma_f32_16x16x32_bf16 v[212:215], a[212:215], v[36:39], v[212:215]\n"
"s_add_u32 s76, s45, 40960\n"
"v_exp_f32_e32 v22, v22\n"
"v_mfma_f32_16x16x32_bf16 v[216:219], a[216:219], v[36:39], v[216:219]\n"
"buffer_load_dwordx4 v6, s[28:31], s76 offen lds\n"
"v_mfma_f32_16x16x32_bf16 v[220:223], a[220:223], v[36:39], v[220:223]\n"
"v_exp_f32_e32 v23, v23\n"
"v_mfma_f32_16x16x32_bf16 a[244:247], a[224:227], v[36:39], a[244:247]\n"
"s_waitcnt lgkmcnt(4)\n"
"v_cvt_pk_bf16_f32 v35, v22, v23\n"
"v_mfma_f32_16x16x32_bf16 v[16:19], a[96:99], a[0:3], 0\n"
"v_mul_f32_e32 v24, s44, v24\n"
"v_mul_f32_e32 v25, s44, v25\n"
"v_mfma_f32_16x16x32_bf16 v[16:19], a[100:103], a[4:7], v[16:19]\n"
"v_exp_f32_e32 v24, v24\n"
"v_mfma_f32_16x16x32_bf16 v[16:19], a[104:107], a[8:11], v[16:19]\n"
"v_mul_f32_e32 v26, s44, v26\n"
"v_mfma_f32_16x16x32_bf16 v[16:19], a[108:111], a[12:15], v[16:19]\n"
"s_waitcnt lgkmcnt(1)\n"
"v_exp_f32_e32 v25, v25\n"
"v_mfma_f32_16x16x32_bf16 v[20:23], a[112:115], a[0:3], 0\n"
"s_waitcnt lgkmcnt(0)\n"
"v_mul_f32_e32 v27, s44, v27\n"
"v_cvt_pk_bf16_f32 v36, v24, v25\n"
"v_mfma_f32_16x16x32_bf16 v[20:23], a[116:119], a[4:7], v[20:23]\n"
"s_waitcnt vmcnt(9)\n"
"s_barrier\n"
"v_exp_f32_e32 v26, v26\n"
"v_mfma_f32_16x16x32_bf16 v[20:23], a[120:123], a[8:11], v[20:23]\n"
"ds_read_b128 a[128:131], v1 offset:24576\n"
"ds_read_b128 a[140:143], v1 offset:26112\n"
"v_mul_f32_e32 v28, s44, v28\n"
"v_mfma_f32_16x16x32_bf16 v[20:23], a[124:127], a[12:15], v[20:23]\n"
"v_exp_f32_e32 v27, v27\n"
"v_mfma_f32_16x16x32_bf16 v[224:227], a[160:163], v[32:35], 0\n"
"v_mul_f32_e32 v29, s44, v29\n"
"v_cvt_pk_bf16_f32 v37, v26, v27\n"
"v_mfma_f32_16x16x32_bf16 v[228:231], a[164:167], v[32:35], 0\n"
"ds_read_b64_tr_b16 a[160:161], v3 offset:16384\n"
"v_exp_f32_e32 v28, v28\n"
"v_mfma_f32_16x16x32_bf16 v[232:235], a[168:171], v[32:35], 0\n"
"ds_read_b128 a[132:135], v1 offset:25088\n"
"ds_read_b64_tr_b16 a[162:163], v4 offset:16384\n"
"v_mul_f32_e32 v30, s44, v30\n"
"v_mfma_f32_16x16x32_bf16 v[236:239], a[172:175], v[32:35], 0\n"
"ds_read_b64_tr_b16 a[164:165], v11 offset:16384\n"
"v_exp_f32_e32 v29, v29\n"
"v_mfma_f32_16x16x32_bf16 v[240:243], a[176:179], v[32:35], 0\n"
"s_add_u32 m0, s48, 16384\n"
"ds_read_b64_tr_b16 a[166:167], v14 offset:16384\n"
"v_mul_f32_e32 v31, s44, v31\n"
"v_cvt_pk_bf16_f32 v38, v28, v29\n"
"v_mfma_f32_16x16x32_bf16 v[244:247], a[180:183], v[32:35], 0\n"
"s_add_u32 s47, s45, 49152\n"
"ds_read_b64_tr_b16 a[168:169], v3 offset:16896\n"
"v_exp_f32_e32 v30, v30\n"
"v_mfma_f32_16x16x32_bf16 v[248:251], a[184:187], v[32:35], 0\n"
"buffer_load_dwordx4 v6, s[24:27], s47 offen lds\n"
"ds_read_b64_tr_b16 a[170:171], v4 offset:16896\n"
"v_mfma_f32_16x16x32_bf16 v[252:255], a[188:191], v[32:35], 0\n"
"v_exp_f32_e32 v31, v31\n"
"v_mfma_f32_16x16x32_bf16 a[248:251], a[224:227], v[32:35], 0\n"
"ds_read_b128 a[136:139], v1 offset:25600\n"
"v_cvt_pk_bf16_f32 v39, v30, v31\n"
"v_mfma_f32_16x16x32_bf16 v[24:27], a[96:99], a[16:19], 0\n"
"ds_read_b64_tr_b16 a[172:173], v11 offset:16896\n"
"v_mul_f32_e32 v16, s44, v16\n"
"v_mul_f32_e32 v17, s44, v17\n"
"v_mfma_f32_16x16x32_bf16 v[24:27], a[100:103], a[20:23], v[24:27]\n"
"ds_read_b64_tr_b16 a[174:175], v14 offset:16896\n"
"v_exp_f32_e32 v16, v16\n"
"v_mfma_f32_16x16x32_bf16 v[24:27], a[104:107], a[24:27], v[24:27]\n"
"ds_read_b128 a[144:147], v2 offset:24576\n"
"ds_read_b64_tr_b16 a[176:177], v3 offset:17408\n"
"v_mul_f32_e32 v18, s44, v18\n"
"v_mfma_f32_16x16x32_bf16 v[24:27], a[108:111], a[28:31], v[24:27]\n"
"ds_read_b64_tr_b16 a[178:179], v4 offset:17408\n"
"v_exp_f32_e32 v17, v17\n"
"v_mfma_f32_16x16x32_bf16 v[28:31], a[112:115], a[16:19], 0\n"
"ds_read_b64_tr_b16 a[180:181], v11 offset:17408\n"
"v_mul_f32_e32 v19, s44, v19\n"
"v_cvt_pk_bf16_f32 v32, v16, v17\n"
"v_mfma_f32_16x16x32_bf16 v[28:31], a[116:119], a[20:23], v[28:31]\n"
"ds_read_b64_tr_b16 a[182:183], v14 offset:17408\n"
"v_exp_f32_e32 v18, v18\n"
"v_mfma_f32_16x16x32_bf16 v[28:31], a[120:123], a[24:27], v[28:31]\n"
"ds_read_b128 a[148:151], v2 offset:25088\n"
"ds_read_b64_tr_b16 a[184:185], v3 offset:17920\n"
"v_mul_f32_e32 v20, s44, v20\n"
"v_mfma_f32_16x16x32_bf16 v[28:31], a[124:127], a[28:31], v[28:31]\n"
"ds_read_b64_tr_b16 a[186:187], v4 offset:17920\n"
"v_exp_f32_e32 v19, v19\n"
"v_mfma_f32_16x16x32_bf16 v[224:227], a[192:195], v[36:39], v[224:227]\n"
"ds_read_b64_tr_b16 a[188:189], v11 offset:17920\n"
"v_mul_f32_e32 v21, s44, v21\n"
"v_cvt_pk_bf16_f32 v33, v18, v19\n"
"v_mfma_f32_16x16x32_bf16 v[228:231], a[196:199], v[36:39], v[228:231]\n"
"ds_read_b64_tr_b16 a[190:191], v14 offset:17920\n"
"v_exp_f32_e32 v20, v20\n"
"v_mfma_f32_16x16x32_bf16 v[232:235], a[200:203], v[36:39], v[232:235]\n"
"ds_read_b128 a[152:155], v2 offset:25600\n"
"v_mul_f32_e32 v22, s44, v22\n"
"v_mfma_f32_16x16x32_bf16 v[236:239], a[204:207], v[36:39], v[236:239]\n"
"v_exp_f32_e32 v21, v21\n"
"v_mfma_f32_16x16x32_bf16 v[240:243], a[208:211], v[36:39], v[240:243]\n"
"s_add_u32 m0, s48, 17408\n"
"v_mul_f32_e32 v23, s44, v23\n"
"v_cvt_pk_bf16_f32 v34, v20, v21\n"
"v_mfma_f32_16x16x32_bf16 v[244:247], a[212:215], v[36:39], v[244:247]\n"
"s_add_u32 s47, s45, 49280\n"
"v_exp_f32_e32 v22, v22\n"
"v_mfma_f32_16x16x32_bf16 v[248:251], a[216:219], v[36:39], v[248:251]\n"
"buffer_load_dwordx4 v6, s[24:27], s47 offen lds\n"
"v_mfma_f32_16x16x32_bf16 v[252:255], a[220:223], v[36:39], v[252:255]\n"
"v_exp_f32_e32 v23, v23\n"
"v_mfma_f32_16x16x32_bf16 a[248:251], a[224:227], v[36:39], a[248:251]\n"
"ds_read_b128 a[156:159], v2 offset:26112\n"
"v_cvt_pk_bf16_f32 v35, v22, v23\n"
"v_mfma_f32_16x16x32_bf16 v[16:19], a[96:99], a[32:35], 0\n"
"ds_read_b64_tr_b16 a[198:199], v14 offset:24576\n"
"v_mul_f32_e32 v24, s44, v24\n"
"v_mul_f32_e32 v25, s44, v25\n"
"v_mfma_f32_16x16x32_bf16 v[16:19], a[100:103], a[36:39], v[16:19]\n"
"v_exp_f32_e32 v24, v24\n"
"v_mfma_f32_16x16x32_bf16 v[16:19], a[104:107], a[40:43], v[16:19]\n"
"ds_read_b64_tr_b16 a[192:193], v3 offset:24576\n"
"v_mul_f32_e32 v26, s44, v26\n"
"v_mfma_f32_16x16x32_bf16 v[16:19], a[108:111], a[44:47], v[16:19]\n"
"v_exp_f32_e32 v25, v25\n"
"v_mfma_f32_16x16x32_bf16 v[20:23], a[112:115], a[32:35], 0\n"
"v_mul_f32_e32 v27, s44, v27\n"
"v_cvt_pk_bf16_f32 v36, v24, v25\n"
"v_mfma_f32_16x16x32_bf16 v[20:23], a[116:119], a[36:39], v[20:23]\n"
"s_waitcnt lgkmcnt(4)\n"
"s_barrier\n"
"v_exp_f32_e32 v26, v26\n"
"v_mfma_f32_16x16x32_bf16 v[20:23], a[120:123], a[40:43], v[20:23]\n"
"ds_read_b64_tr_b16 a[194:195], v4 offset:24576\n"
"v_mul_f32_e32 v28, s44, v28\n"
"v_mfma_f32_16x16x32_bf16 v[20:23], a[124:127], a[44:47], v[20:23]\n"
"v_exp_f32_e32 v27, v27\n"
"v_mfma_f32_16x16x32_bf16 v[64:67], a[160:163], v[32:35], v[64:67]\n"
"s_add_u32 m0, s49, 0\n"
"v_mul_f32_e32 v29, s44, v29\n"
"v_cvt_pk_bf16_f32 v37, v26, v27\n"
"v_mfma_f32_16x16x32_bf16 v[68:71], a[164:167], v[32:35], v[68:71]\n"
"s_add_u32 s61, s58, 0\n"
"v_exp_f32_e32 v28, v28\n"
"v_mfma_f32_16x16x32_bf16 v[72:75], a[168:171], v[32:35], v[72:75]\n"
"buffer_load_dwordx4 v7, s[36:39], s61 offen  nt lds\n"
"v_mul_f32_e32 v30, s44, v30\n"
"v_mfma_f32_16x16x32_bf16 v[76:79], a[172:175], v[32:35], v[76:79]\n"
"v_exp_f32_e32 v29, v29\n"
"v_mfma_f32_16x16x32_bf16 v[80:83], a[176:179], v[32:35], v[80:83]\n"
"s_add_u32 m0, s48, 49152\n"
"v_mul_f32_e32 v31, s44, v31\n"
"v_cvt_pk_bf16_f32 v38, v28, v29\n"
"v_mfma_f32_16x16x32_bf16 v[84:87], a[180:183], v[32:35], v[84:87]\n"
"s_add_u32 s76, s45, 49152\n"
"v_exp_f32_e32 v30, v30\n"
"v_mfma_f32_16x16x32_bf16 v[88:91], a[184:187], v[32:35], v[88:91]\n"
"buffer_load_dwordx4 v6, s[28:31], s76 offen lds\n"
"v_mfma_f32_16x16x32_bf16 v[92:95], a[188:191], v[32:35], v[92:95]\n"
"v_exp_f32_e32 v31, v31\n"
"v_mfma_f32_16x16x32_bf16 a[228:231], a[224:227], v[32:35], a[228:231]\n"
"ds_read_b64_tr_b16 a[196:197], v11 offset:24576\n"
"v_cvt_pk_bf16_f32 v39, v30, v31\n"
"v_mfma_f32_16x16x32_bf16 v[24:27], a[96:99], a[48:51], 0\n"
"ds_read_b64_tr_b16 a[206:207], v14 offset:25088\n"
"v_mul_f32_e32 v16, s44, v16\n"
"v_mul_f32_e32 v17, s44, v17\n"
"v_mfma_f32_16x16x32_bf16 v[24:27], a[100:103], a[52:55], v[24:27]\n"
"v_exp_f32_e32 v16, v16\n"
"v_mfma_f32_16x16x32_bf16 v[24:27], a[104:107], a[56:59], v[24:27]\n"
"ds_read_b64_tr_b16 a[200:201], v3 offset:25088\n"
"v_mul_f32_e32 v18, s44, v18\n"
"v_mfma_f32_16x16x32_bf16 v[24:27], a[108:111], a[60:63], v[24:27]\n"
"v_exp_f32_e32 v17, v17\n"
"v_mfma_f32_16x16x32_bf16 v[28:31], a[112:115], a[48:51], 0\n"
"v_mul_f32_e32 v19, s44, v19\n"
"v_cvt_pk_bf16_f32 v32, v16, v17\n"
"v_mfma_f32_16x16x32_bf16 v[28:31], a[116:119], a[52:55], v[28:31]\n"
"v_exp_f32_e32 v18, v18\n"
"v_mfma_f32_16x16x32_bf16 v[28:31], a[120:123], a[56:59], v[28:31]\n"
"ds_read_b64_tr_b16 a[202:203], v4 offset:25088\n"
"v_mul_f32_e32 v20, s44, v20\n"
"v_mfma_f32_16x16x32_bf16 v[28:31], a[124:127], a[60:63], v[28:31]\n"
"v_exp_f32_e32 v19, v19\n"
"v_mfma_f32_16x16x32_bf16 v[96:99], a[160:163], v[36:39], v[96:99]\n"
"s_add_u32 m0, s49, 1024\n"
"v_mul_f32_e32 v21, s44, v21\n"
"v_cvt_pk_bf16_f32 v33, v18, v19\n"
"v_mfma_f32_16x16x32_bf16 v[100:103], a[164:167], v[36:39], v[100:103]\n"
"s_add_u32 s61, s58, 128\n"
"v_exp_f32_e32 v20, v20\n"
"v_mfma_f32_16x16x32_bf16 v[104:107], a[168:171], v[36:39], v[104:107]\n"
"buffer_load_dwordx4 v7, s[36:39], s61 offen  nt lds\n"
"v_mul_f32_e32 v22, s44, v22\n"
"v_mfma_f32_16x16x32_bf16 v[108:111], a[172:175], v[36:39], v[108:111]\n"
"v_exp_f32_e32 v21, v21\n"
"v_mfma_f32_16x16x32_bf16 v[112:115], a[176:179], v[36:39], v[112:115]\n"
"s_add_u32 m0, s48, 50176\n"
"v_mul_f32_e32 v23, s44, v23\n"
"v_cvt_pk_bf16_f32 v34, v20, v21\n"
"v_mfma_f32_16x16x32_bf16 v[116:119], a[180:183], v[36:39], v[116:119]\n"
"s_add_u32 s76, s45, 49280\n"
"v_exp_f32_e32 v22, v22\n"
"v_mfma_f32_16x16x32_bf16 v[120:123], a[184:187], v[36:39], v[120:123]\n"
"buffer_load_dwordx4 v6, s[28:31], s76 offen lds\n"
"v_mfma_f32_16x16x32_bf16 v[124:127], a[188:191], v[36:39], v[124:127]\n"
"v_exp_f32_e32 v23, v23\n"
"v_mfma_f32_16x16x32_bf16 a[232:235], a[224:227], v[36:39], a[232:235]\n"
"ds_read_b64_tr_b16 a[204:205], v11 offset:25088\n"
"v_cvt_pk_bf16_f32 v35, v22, v23\n"
"v_mfma_f32_16x16x32_bf16 v[16:19], a[96:99], a[64:67], 0\n"
"v_mul_f32_e32 v24, s44, v24\n"
"v_mul_f32_e32 v25, s44, v25\n"
"v_mfma_f32_16x16x32_bf16 v[16:19], a[100:103], a[68:71], v[16:19]\n"
"v_exp_f32_e32 v24, v24\n"
"v_mfma_f32_16x16x32_bf16 v[16:19], a[104:107], a[72:75], v[16:19]\n"
"ds_read_b64_tr_b16 a[208:209], v3 offset:25600\n"
"v_mul_f32_e32 v26, s44, v26\n"
"v_mfma_f32_16x16x32_bf16 v[16:19], a[108:111], a[76:79], v[16:19]\n"
"v_exp_f32_e32 v25, v25\n"
"v_mfma_f32_16x16x32_bf16 v[20:23], a[112:115], a[64:67], 0\n"
"v_mul_f32_e32 v27, s44, v27\n"
"v_cvt_pk_bf16_f32 v36, v24, v25\n"
"v_mfma_f32_16x16x32_bf16 v[20:23], a[116:119], a[68:71], v[20:23]\n"
"v_exp_f32_e32 v26, v26\n"
"v_mfma_f32_16x16x32_bf16 v[20:23], a[120:123], a[72:75], v[20:23]\n"
"ds_read_b64_tr_b16 a[210:211], v4 offset:25600\n"
"v_mul_f32_e32 v28, s44, v28\n"
"v_mfma_f32_16x16x32_bf16 v[20:23], a[124:127], a[76:79], v[20:23]\n"
"v_exp_f32_e32 v27, v27\n"
"v_mfma_f32_16x16x32_bf16 v[128:131], a[160:163], v[32:35], v[128:131]\n"
"v_mul_f32_e32 v29, s44, v29\n"
"v_cvt_pk_bf16_f32 v37, v26, v27\n"
"v_mfma_f32_16x16x32_bf16 v[132:135], a[164:167], v[32:35], v[132:135]\n"
"v_exp_f32_e32 v28, v28\n"
"v_mfma_f32_16x16x32_bf16 v[136:139], a[168:171], v[32:35], v[136:139]\n"
"ds_read_b64_tr_b16 a[212:213], v11 offset:25600\n"
"v_mul_f32_e32 v30, s44, v30\n"
"v_mfma_f32_16x16x32_bf16 v[140:143], a[172:175], v[32:35], v[140:143]\n"
"v_exp_f32_e32 v29, v29\n"
"v_mfma_f32_16x16x32_bf16 v[144:147], a[176:179], v[32:35], v[144:147]\n"
"s_add_u32 m0, s49, 2048\n"
"v_mul_f32_e32 v31, s44, v31\n"
"v_cvt_pk_bf16_f32 v38, v28, v29\n"
"v_mfma_f32_16x16x32_bf16 v[148:151], a[180:183], v[32:35], v[148:151]\n"
"s_add_u32 s61, s58, 0\n"
"v_exp_f32_e32 v30, v30\n"
"v_mfma_f32_16x16x32_bf16 v[152:155], a[184:187], v[32:35], v[152:155]\n"
"buffer_load_dwordx4 v8, s[36:39], s61 offen  nt lds\n"
"v_mfma_f32_16x16x32_bf16 v[156:159], a[188:191], v[32:35], v[156:159]\n"
"v_exp_f32_e32 v31, v31\n"
"v_mfma_f32_16x16x32_bf16 a[236:239], a[224:227], v[32:35], a[236:239]\n"
"ds_read_b64_tr_b16 a[214:215], v14 offset:25600\n"
"v_cvt_pk_bf16_f32 v39, v30, v31\n"
"v_mfma_f32_16x16x32_bf16 v[24:27], a[96:99], a[80:83], 0\n"
"ds_read_b64_tr_b16 a[222:223], v14 offset:26112\n"
"v_mul_f32_e32 v16, s44, v16\n"
"v_mul_f32_e32 v17, s44, v17\n"
"v_mfma_f32_16x16x32_bf16 v[24:27], a[100:103], a[84:87], v[24:27]\n"
"v_exp_f32_e32 v16, v16\n"
"v_mfma_f32_16x16x32_bf16 v[24:27], a[104:107], a[88:91], v[24:27]\n"
"ds_read_b64_tr_b16 a[216:217], v3 offset:26112\n"
"v_mul_f32_e32 v18, s44, v18\n"
"v_mfma_f32_16x16x32_bf16 v[24:27], a[108:111], a[92:95], v[24:27]\n"
"v_exp_f32_e32 v17, v17\n"
"v_mfma_f32_16x16x32_bf16 v[28:31], a[112:115], a[80:83], 0\n"
"v_mul_f32_e32 v19, s44, v19\n"
"v_cvt_pk_bf16_f32 v32, v16, v17\n"
"v_mfma_f32_16x16x32_bf16 v[28:31], a[116:119], a[84:87], v[28:31]\n"
"v_exp_f32_e32 v18, v18\n"
"v_mfma_f32_16x16x32_bf16 v[28:31], a[120:123], a[88:91], v[28:31]\n"
"ds_read_b64_tr_b16 a[218:219], v4 offset:26112\n"
"v_mul_f32_e32 v20, s44, v20\n"
"v_mfma_f32_16x16x32_bf16 v[28:31], a[124:127], a[92:95], v[28:31]\n"
"v_exp_f32_e32 v19, v19\n"
"v_mfma_f32_16x16x32_bf16 v[160:163], a[160:163], v[36:39], v[160:163]\n"
"v_mul_f32_e32 v21, s44, v21\n"
"v_cvt_pk_bf16_f32 v33, v18, v19\n"
"v_mfma_f32_16x16x32_bf16 v[164:167], a[164:167], v[36:39], v[164:167]\n"
"v_exp_f32_e32 v20, v20\n"
"v_mfma_f32_16x16x32_bf16 v[168:171], a[168:171], v[36:39], v[168:171]\n"
"ds_read_b64_tr_b16 a[220:221], v11 offset:26112\n"
"v_mul_f32_e32 v22, s44, v22\n"
"v_mfma_f32_16x16x32_bf16 v[172:175], a[172:175], v[36:39], v[172:175]\n"
"v_exp_f32_e32 v21, v21\n"
"v_mfma_f32_16x16x32_bf16 v[176:179], a[176:179], v[36:39], v[176:179]\n"
"s_add_u32 m0, s49, 3072\n"
"v_mul_f32_e32 v23, s44, v23\n"
"v_cvt_pk_bf16_f32 v34, v20, v21\n"
"v_mfma_f32_16x16x32_bf16 v[180:183], a[180:183], v[36:39], v[180:183]\n"
"s_add_u32 s61, s58, 128\n"
"v_exp_f32_e32 v22, v22\n"
"v_mfma_f32_16x16x32_bf16 v[184:187], a[184:187], v[36:39], v[184:187]\n"
"buffer_load_dwordx4 v8, s[36:39], s61 offen  nt lds\n"
"v_mfma_f32_16x16x32_bf16 v[188:191], a[188:191], v[36:39], v[188:191]\n"
"v_exp_f32_e32 v23, v23\n"
"v_mfma_f32_16x16x32_bf16 a[240:243], a[224:227], v[36:39], a[240:243]\n"
"v_cvt_pk_bf16_f32 v35, v22, v23\n"
".p2align 6\n"
"L_L6%=:\n"
"v_mfma_f32_16x16x32_bf16 v[16:19], a[128:131], a[0:3], 0\n"
"v_mul_f32_e32 v24, s44, v24\n"
"v_mul_f32_e32 v25, s44, v25\n"
"v_mfma_f32_16x16x32_bf16 v[16:19], a[132:135], a[4:7], v[16:19]\n"
"v_exp_f32_e32 v24, v24\n"
"v_mfma_f32_16x16x32_bf16 v[16:19], a[136:139], a[8:11], v[16:19]\n"
"v_mul_f32_e32 v26, s44, v26\n"
"v_mfma_f32_16x16x32_bf16 v[16:19], a[140:143], a[12:15], v[16:19]\n"
"v_exp_f32_e32 v25, v25\n"
"v_mfma_f32_16x16x32_bf16 v[20:23], a[144:147], a[0:3], 0\n"
"v_mul_f32_e32 v27, s44, v27\n"
"v_cvt_pk_bf16_f32 v36, v24, v25\n"
"v_mfma_f32_16x16x32_bf16 v[20:23], a[148:151], a[4:7], v[20:23]\n"
"s_waitcnt vmcnt(12) lgkmcnt(0)\n"
"s_barrier\n"
"v_exp_f32_e32 v26, v26\n"
"v_mfma_f32_16x16x32_bf16 v[20:23], a[152:155], a[8:11], v[20:23]\n"
"ds_read_b128 a[96:99], v1 offset:0\n"
"ds_read_b128 a[108:111], v1 offset:1536\n"
"v_mul_f32_e32 v28, s44, v28\n"
"v_mfma_f32_16x16x32_bf16 v[20:23], a[156:159], a[12:15], v[20:23]\n"
"ds_read_b128 v[40:43], v58 offset:4096\n"
"v_exp_f32_e32 v27, v27\n"
"v_mfma_f32_16x16x32_bf16 v[192:195], a[160:163], v[32:35], v[192:195]\n"
"ds_read_b128 v[44:47], v59 offset:5120\n"
"v_mul_f32_e32 v29, s44, v29\n"
"v_cvt_pk_bf16_f32 v37, v26, v27\n"
"v_mfma_f32_16x16x32_bf16 v[196:199], a[164:167], v[32:35], v[196:199]\n"
"ds_read_b128 v[48:51], v58 offset:6144\n"
"v_exp_f32_e32 v28, v28\n"
"v_mfma_f32_16x16x32_bf16 v[200:203], a[168:171], v[32:35], v[200:203]\n"
"ds_read_b128 a[100:103], v1 offset:512\n"
"ds_read_b128 v[52:55], v59 offset:7168\n"
"v_mul_f32_e32 v30, s44, v30\n"
"v_mfma_f32_16x16x32_bf16 v[204:207], a[172:175], v[32:35], v[204:207]\n"
"v_exp_f32_e32 v29, v29\n"
"v_mfma_f32_16x16x32_bf16 v[208:211], a[176:179], v[32:35], v[208:211]\n"
"s_add_u32 m0, s48, 24576\n"
"v_mul_f32_e32 v31, s44, v31\n"
"v_cvt_pk_bf16_f32 v38, v28, v29\n"
"v_mfma_f32_16x16x32_bf16 v[212:215], a[180:183], v[32:35], v[212:215]\n"
"s_add_u32 s47, s45, 57344\n"
"v_exp_f32_e32 v30, v30\n"
"v_mfma_f32_16x16x32_bf16 v[216:219], a[184:187], v[32:35], v[216:219]\n"
"buffer_load_dwordx4 v6, s[24:27], s47 offen lds\n"
"v_mfma_f32_16x16x32_bf16 v[220:223], a[188:191], v[32:35], v[220:223]\n"
"v_exp_f32_e32 v31, v31\n"
"v_mfma_f32_16x16x32_bf16 a[244:247], a[224:227], v[32:35], a[244:247]\n"
"ds_read_b128 a[104:107], v1 offset:1024\n"
"v_cvt_pk_bf16_f32 v39, v30, v31\n"
"v_mfma_f32_16x16x32_bf16 v[24:27], a[128:131], a[16:19], 0\n"
"v_mul_f32_e32 v16, s44, v16\n"
"v_mul_f32_e32 v17, s44, v17\n"
"v_mfma_f32_16x16x32_bf16 v[24:27], a[132:135], a[20:23], v[24:27]\n"
"v_exp_f32_e32 v16, v16\n"
"s_add_u32 s77, s60, 4096\n"
"v_mfma_f32_16x16x32_bf16 v[24:27], a[136:139], a[24:27], v[24:27]\n"
"ds_read_b128 a[112:115], v2 offset:0\n"
"v_mul_f32_e32 v18, s44, v18\n"
"s_waitcnt lgkmcnt(6)\n"
"buffer_store_dwordx4 v[40:43], v9, s[40:43], s77 offen offset:0  sc1\n"
"v_mfma_f32_16x16x32_bf16 v[24:27], a[140:143], a[28:31], v[24:27]\n"
"v_exp_f32_e32 v17, v17\n"
"v_mfma_f32_16x16x32_bf16 v[28:31], a[144:147], a[16:19], 0\n"
"v_mul_f32_e32 v19, s44, v19\n"
"v_cvt_pk_bf16_f32 v32, v16, v17\n"
"v_mfma_f32_16x16x32_bf16 v[28:31], a[148:151], a[20:23], v[28:31]\n"
"v_exp_f32_e32 v18, v18\n"
"v_mfma_f32_16x16x32_bf16 v[28:31], a[152:155], a[24:27], v[28:31]\n"
"ds_read_b128 a[116:119], v2 offset:512\n"
"v_mul_f32_e32 v20, s44, v20\n"
"s_waitcnt lgkmcnt(6)\n"
"buffer_store_dwordx4 v[44:47], v9, s[40:43], s77 offen offset:1024  sc1\n"
"v_mfma_f32_16x16x32_bf16 v[28:31], a[156:159], a[28:31], v[28:31]\n"
"v_exp_f32_e32 v19, v19\n"
"v_mfma_f32_16x16x32_bf16 v[224:227], a[160:163], v[36:39], v[224:227]\n"
"v_mul_f32_e32 v21, s44, v21\n"
"v_cvt_pk_bf16_f32 v33, v18, v19\n"
"v_mfma_f32_16x16x32_bf16 v[228:231], a[164:167], v[36:39], v[228:231]\n"
"v_exp_f32_e32 v20, v20\n"
"v_mfma_f32_16x16x32_bf16 v[232:235], a[168:171], v[36:39], v[232:235]\n"
"ds_read_b128 a[120:123], v2 offset:1024\n"
"v_mul_f32_e32 v22, s44, v22\n"
"s_waitcnt lgkmcnt(6)\n"
"buffer_store_dwordx4 v[48:51], v9, s[40:43], s77 offen offset:2048  sc1\n"
"v_mfma_f32_16x16x32_bf16 v[236:239], a[172:175], v[36:39], v[236:239]\n"
"v_exp_f32_e32 v21, v21\n"
"v_mfma_f32_16x16x32_bf16 v[240:243], a[176:179], v[36:39], v[240:243]\n"
"s_add_u32 m0, s48, 25600\n"
"v_mul_f32_e32 v23, s44, v23\n"
"v_cvt_pk_bf16_f32 v34, v20, v21\n"
"v_mfma_f32_16x16x32_bf16 v[244:247], a[180:183], v[36:39], v[244:247]\n"
"s_add_u32 s47, s45, 57472\n"
"v_exp_f32_e32 v22, v22\n"
"v_mfma_f32_16x16x32_bf16 v[248:251], a[184:187], v[36:39], v[248:251]\n"
"buffer_load_dwordx4 v6, s[24:27], s47 offen lds\n"
"v_mfma_f32_16x16x32_bf16 v[252:255], a[188:191], v[36:39], v[252:255]\n"
"v_exp_f32_e32 v23, v23\n"
"v_mfma_f32_16x16x32_bf16 a[248:251], a[224:227], v[36:39], a[248:251]\n"
"ds_read_b128 a[124:127], v2 offset:1536\n"
"v_cvt_pk_bf16_f32 v35, v22, v23\n"
"s_waitcnt lgkmcnt(5)\n"
"buffer_store_dwordx4 v[52:55], v9, s[40:43], s77 offen offset:3072  sc1\n"
"v_mfma_f32_16x16x32_bf16 v[16:19], a[128:131], a[32:35], 0\n"
"v_mul_f32_e32 v24, s44, v24\n"
"v_mul_f32_e32 v25, s44, v25\n"
"v_mfma_f32_16x16x32_bf16 v[16:19], a[132:135], a[36:39], v[16:19]\n"
"v_exp_f32_e32 v24, v24\n"
"v_mfma_f32_16x16x32_bf16 v[16:19], a[136:139], a[40:43], v[16:19]\n"
"ds_read_b64_tr_b16 a[160:161], v3 offset:0\n"
"v_mul_f32_e32 v26, s44, v26\n"
"v_mfma_f32_16x16x32_bf16 v[16:19], a[140:143], a[44:47], v[16:19]\n"
"v_exp_f32_e32 v25, v25\n"
"v_mfma_f32_16x16x32_bf16 v[20:23], a[144:147], a[32:35], 0\n"
"v_mul_f32_e32 v27, s44, v27\n"
"v_cvt_pk_bf16_f32 v36, v24, v25\n"
"v_mfma_f32_16x16x32_bf16 v[20:23], a[148:151], a[36:39], v[20:23]\n"
"v_exp_f32_e32 v26, v26\n"
"v_mfma_f32_16x16x32_bf16 v[20:23], a[152:155], a[40:43], v[20:23]\n"
"ds_read_b64_tr_b16 a[162:163], v4 offset:0\n"
"v_mul_f32_e32 v28, s44, v28\n"
"v_mfma_f32_16x16x32_bf16 v[20:23], a[156:159], a[44:47], v[20:23]\n"
"v_exp_f32_e32 v27, v27\n"
"v_mfma_f32_16x16x32_bf16 v[64:67], a[192:195], v[32:35], v[64:67]\n"
"v_mul_f32_e32 v29, s44, v29\n"
"v_cvt_pk_bf16_f32 v37, v26, v27\n"
"v_mfma_f32_16x16x32_bf16 v[68:71], a[196:199], v[32:35], v[68:71]\n"
"v_exp_f32_e32 v28, v28\n"
"v_mfma_f32_16x16x32_bf16 v[72:75], a[200:203], v[32:35], v[72:75]\n"
"ds_read_b64_tr_b16 a[164:165], v11 offset:0\n"
"v_mul_f32_e32 v30, s44, v30\n"
"v_mfma_f32_16x16x32_bf16 v[76:79], a[204:207], v[32:35], v[76:79]\n"
"v_exp_f32_e32 v29, v29\n"
"v_mfma_f32_16x16x32_bf16 v[80:83], a[208:211], v[32:35], v[80:83]\n"
"s_add_u32 m0, s48, 57344\n"
"v_mul_f32_e32 v31, s44, v31\n"
"v_cvt_pk_bf16_f32 v38, v28, v29\n"
"v_mfma_f32_16x16x32_bf16 v[84:87], a[212:215], v[32:35], v[84:87]\n"
"s_add_u32 s76, s45, 57344\n"
"v_exp_f32_e32 v30, v30\n"
"v_mfma_f32_16x16x32_bf16 v[88:91], a[216:219], v[32:35], v[88:91]\n"
"buffer_load_dwordx4 v6, s[28:31], s76 offen lds\n"
"v_mfma_f32_16x16x32_bf16 v[92:95], a[220:223], v[32:35], v[92:95]\n"
"v_exp_f32_e32 v31, v31\n"
"v_mfma_f32_16x16x32_bf16 a[228:231], a[224:227], v[32:35], a[228:231]\n"
"ds_read_b64_tr_b16 a[166:167], v14 offset:0\n"
"v_cvt_pk_bf16_f32 v39, v30, v31\n"
"v_mfma_f32_16x16x32_bf16 v[24:27], a[128:131], a[48:51], 0\n"
"v_mul_f32_e32 v16, s44, v16\n"
"v_mul_f32_e32 v17, s44, v17\n"
"v_mfma_f32_16x16x32_bf16 v[24:27], a[132:135], a[52:55], v[24:27]\n"
"v_exp_f32_e32 v16, v16\n"
"v_mfma_f32_16x16x32_bf16 v[24:27], a[136:139], a[56:59], v[24:27]\n"
"ds_read_b64_tr_b16 a[168:169], v3 offset:512\n"
"v_mul_f32_e32 v18, s44, v18\n"
"v_mfma_f32_16x16x32_bf16 v[24:27], a[140:143], a[60:63], v[24:27]\n"
"v_exp_f32_e32 v17, v17\n"
"v_mfma_f32_16x16x32_bf16 v[28:31], a[144:147], a[48:51], 0\n"
"v_mul_f32_e32 v19, s44, v19\n"
"v_cvt_pk_bf16_f32 v32, v16, v17\n"
"v_mfma_f32_16x16x32_bf16 v[28:31], a[148:151], a[52:55], v[28:31]\n"
"v_exp_f32_e32 v18, v18\n"
"v_mfma_f32_16x16x32_bf16 v[28:31], a[152:155], a[56:59], v[28:31]\n"
"ds_read_b64_tr_b16 a[170:171], v4 offset:512\n"
"v_mul_f32_e32 v20, s44, v20\n"
"v_mfma_f32_16x16x32_bf16 v[28:31], a[156:159], a[60:63], v[28:31]\n"
"v_exp_f32_e32 v19, v19\n"
"v_mfma_f32_16x16x32_bf16 v[96:99], a[192:195], v[36:39], v[96:99]\n"
"v_mul_f32_e32 v21, s44, v21\n"
"v_cvt_pk_bf16_f32 v33, v18, v19\n"
"v_mfma_f32_16x16x32_bf16 v[100:103], a[196:199], v[36:39], v[100:103]\n"
"v_exp_f32_e32 v20, v20\n"
"v_mfma_f32_16x16x32_bf16 v[104:107], a[200:203], v[36:39], v[104:107]\n"
"ds_read_b64_tr_b16 a[172:173], v11 offset:512\n"
"v_mul_f32_e32 v22, s44, v22\n"
"v_mfma_f32_16x16x32_bf16 v[108:111], a[204:207], v[36:39], v[108:111]\n"
"v_exp_f32_e32 v21, v21\n"
"v_mfma_f32_16x16x32_bf16 v[112:115], a[208:211], v[36:39], v[112:115]\n"
"s_add_u32 m0, s48, 58368\n"
"v_mul_f32_e32 v23, s44, v23\n"
"v_cvt_pk_bf16_f32 v34, v20, v21\n"
"v_mfma_f32_16x16x32_bf16 v[116:119], a[212:215], v[36:39], v[116:119]\n"
"s_add_u32 s76, s45, 57472\n"
"v_exp_f32_e32 v22, v22\n"
"v_mfma_f32_16x16x32_bf16 v[120:123], a[216:219], v[36:39], v[120:123]\n"
"buffer_load_dwordx4 v6, s[28:31], s76 offen lds\n"
"v_mfma_f32_16x16x32_bf16 v[124:127], a[220:223], v[36:39], v[124:127]\n"
"v_exp_f32_e32 v23, v23\n"
"v_mfma_f32_16x16x32_bf16 a[232:235], a[224:227], v[36:39], a[232:235]\n"
"ds_read_b64_tr_b16 a[174:175], v14 offset:512\n"
"v_cvt_pk_bf16_f32 v35, v22, v23\n"
"v_mfma_f32_16x16x32_bf16 v[16:19], a[128:131], a[64:67], 0\n"
"v_mul_f32_e32 v24, s44, v24\n"
"v_mul_f32_e32 v25, s44, v25\n"
"v_mfma_f32_16x16x32_bf16 v[16:19], a[132:135], a[68:71], v[16:19]\n"
"v_exp_f32_e32 v24, v24\n"
"v_mfma_f32_16x16x32_bf16 v[16:19], a[136:139], a[72:75], v[16:19]\n"
"ds_read_b64_tr_b16 a[176:177], v3 offset:1024\n"
"v_mul_f32_e32 v26, s44, v26\n"
"v_mfma_f32_16x16x32_bf16 v[16:19], a[140:143], a[76:79], v[16:19]\n"
"v_exp_f32_e32 v25, v25\n"
"v_mfma_f32_16x16x32_bf16 v[20:23], a[144:147], a[64:67], 0\n"
"v_mul_f32_e32 v27, s44, v27\n"
"v_cvt_pk_bf16_f32 v36, v24, v25\n"
"v_mfma_f32_16x16x32_bf16 v[20:23], a[148:151], a[68:71], v[20:23]\n"
"v_exp_f32_e32 v26, v26\n"
"v_mfma_f32_16x16x32_bf16 v[20:23], a[152:155], a[72:75], v[20:23]\n"
"ds_read_b64_tr_b16 a[178:179], v4 offset:1024\n"
"v_mul_f32_e32 v28, s44, v28\n"
"v_mfma_f32_16x16x32_bf16 v[20:23], a[156:159], a[76:79], v[20:23]\n"
"v_exp_f32_e32 v27, v27\n"
"v_mfma_f32_16x16x32_bf16 v[128:131], a[192:195], v[32:35], v[128:131]\n"
"v_mul_f32_e32 v29, s44, v29\n"
"v_cvt_pk_bf16_f32 v37, v26, v27\n"
"v_mfma_f32_16x16x32_bf16 v[132:135], a[196:199], v[32:35], v[132:135]\n"
"v_exp_f32_e32 v28, v28\n"
"v_mfma_f32_16x16x32_bf16 v[136:139], a[200:203], v[32:35], v[136:139]\n"
"ds_read_b64_tr_b16 a[180:181], v11 offset:1024\n"
"v_mul_f32_e32 v30, s44, v30\n"
"v_mfma_f32_16x16x32_bf16 v[140:143], a[204:207], v[32:35], v[140:143]\n"
"v_exp_f32_e32 v29, v29\n"
"v_mfma_f32_16x16x32_bf16 v[144:147], a[208:211], v[32:35], v[144:147]\n"
"s_add_u32 m0, s49, 4096\n"
"v_mul_f32_e32 v31, s44, v31\n"
"v_cvt_pk_bf16_f32 v38, v28, v29\n"
"v_mfma_f32_16x16x32_bf16 v[148:151], a[212:215], v[32:35], v[148:151]\n"
"s_add_u32 s61, s58, 4096\n"
"v_exp_f32_e32 v30, v30\n"
"v_mfma_f32_16x16x32_bf16 v[152:155], a[216:219], v[32:35], v[152:155]\n"
"buffer_load_dwordx4 v7, s[36:39], s61 offen  nt lds\n"
"v_mfma_f32_16x16x32_bf16 v[156:159], a[220:223], v[32:35], v[156:159]\n"
"v_exp_f32_e32 v31, v31\n"
"v_mfma_f32_16x16x32_bf16 a[236:239], a[224:227], v[32:35], a[236:239]\n"
"ds_read_b64_tr_b16 a[182:183], v14 offset:1024\n"
"v_cvt_pk_bf16_f32 v39, v30, v31\n"
"v_mfma_f32_16x16x32_bf16 v[24:27], a[128:131], a[80:83], 0\n"
"ds_read_b64_tr_b16 a[190:191], v14 offset:1536\n"
"v_mul_f32_e32 v16, s44, v16\n"
"v_mul_f32_e32 v17, s44, v17\n"
"v_mfma_f32_16x16x32_bf16 v[24:27], a[132:135], a[84:87], v[24:27]\n"
"v_exp_f32_e32 v16, v16\n"
"v_mfma_f32_16x16x32_bf16 v[24:27], a[136:139], a[88:91], v[24:27]\n"
"ds_read_b64_tr_b16 a[184:185], v3 offset:1536\n"
"v_mul_f32_e32 v18, s44, v18\n"
"v_mfma_f32_16x16x32_bf16 v[24:27], a[140:143], a[92:95], v[24:27]\n"
"v_exp_f32_e32 v17, v17\n"
"v_mfma_f32_16x16x32_bf16 v[28:31], a[144:147], a[80:83], 0\n"
"v_mul_f32_e32 v19, s44, v19\n"
"v_cvt_pk_bf16_f32 v32, v16, v17\n"
"v_mfma_f32_16x16x32_bf16 v[28:31], a[148:151], a[84:87], v[28:31]\n"
"v_exp_f32_e32 v18, v18\n"
"v_mfma_f32_16x16x32_bf16 v[28:31], a[152:155], a[88:91], v[28:31]\n"
"ds_read_b64_tr_b16 a[186:187], v4 offset:1536\n"
"v_mul_f32_e32 v20, s44, v20\n"
"v_mfma_f32_16x16x32_bf16 v[28:31], a[156:159], a[92:95], v[28:31]\n"
"v_exp_f32_e32 v19, v19\n"
"v_mfma_f32_16x16x32_bf16 v[160:163], a[192:195], v[36:39], v[160:163]\n"
"v_mul_f32_e32 v21, s44, v21\n"
"v_cvt_pk_bf16_f32 v33, v18, v19\n"
"v_mfma_f32_16x16x32_bf16 v[164:167], a[196:199], v[36:39], v[164:167]\n"
"v_exp_f32_e32 v20, v20\n"
"v_mfma_f32_16x16x32_bf16 v[168:171], a[200:203], v[36:39], v[168:171]\n"
"ds_read_b64_tr_b16 a[188:189], v11 offset:1536\n"
"v_mul_f32_e32 v22, s44, v22\n"
"v_mfma_f32_16x16x32_bf16 v[172:175], a[204:207], v[36:39], v[172:175]\n"
"v_exp_f32_e32 v21, v21\n"
"v_mfma_f32_16x16x32_bf16 v[176:179], a[208:211], v[36:39], v[176:179]\n"
"s_add_u32 m0, s49, 5120\n"
"v_mul_f32_e32 v23, s44, v23\n"
"v_cvt_pk_bf16_f32 v34, v20, v21\n"
"v_mfma_f32_16x16x32_bf16 v[180:183], a[212:215], v[36:39], v[180:183]\n"
"s_add_u32 s61, s58, 4224\n"
"v_exp_f32_e32 v22, v22\n"
"v_mfma_f32_16x16x32_bf16 v[184:187], a[216:219], v[36:39], v[184:187]\n"
"buffer_load_dwordx4 v7, s[36:39], s61 offen  nt lds\n"
"v_mfma_f32_16x16x32_bf16 v[188:191], a[220:223], v[36:39], v[188:191]\n"
"v_exp_f32_e32 v23, v23\n"
"v_mfma_f32_16x16x32_bf16 a[240:243], a[224:227], v[36:39], a[240:243]\n"
"v_cvt_pk_bf16_f32 v35, v22, v23\n"
"v_mfma_f32_16x16x32_bf16 v[16:19], a[96:99], a[0:3], 0\n"
"v_mul_f32_e32 v24, s44, v24\n"
"v_mul_f32_e32 v25, s44, v25\n"
"v_mfma_f32_16x16x32_bf16 v[16:19], a[100:103], a[4:7], v[16:19]\n"
"s_waitcnt lgkmcnt(15)\n"
"v_exp_f32_e32 v24, v24\n"
"v_mfma_f32_16x16x32_bf16 v[16:19], a[104:107], a[8:11], v[16:19]\n"
"v_mul_f32_e32 v26, s44, v26\n"
"v_mfma_f32_16x16x32_bf16 v[16:19], a[108:111], a[12:15], v[16:19]\n"
"v_exp_f32_e32 v25, v25\n"
"v_mfma_f32_16x16x32_bf16 v[20:23], a[112:115], a[0:3], 0\n"
"v_mul_f32_e32 v27, s44, v27\n"
"v_cvt_pk_bf16_f32 v36, v24, v25\n"
"v_mfma_f32_16x16x32_bf16 v[20:23], a[116:119], a[4:7], v[20:23]\n"
"s_waitcnt vmcnt(18) lgkmcnt(0)\n"
"s_barrier\n"
"v_exp_f32_e32 v26, v26\n"
"v_mfma_f32_16x16x32_bf16 v[20:23], a[120:123], a[8:11], v[20:23]\n"
"ds_read_b128 a[128:131], v1 offset:8192\n"
"ds_read_b128 a[140:143], v1 offset:9728\n"
"v_mul_f32_e32 v28, s44, v28\n"
"v_mfma_f32_16x16x32_bf16 v[20:23], a[124:127], a[12:15], v[20:23]\n"
"v_exp_f32_e32 v27, v27\n"
"v_mfma_f32_16x16x32_bf16 v[192:195], a[192:195], v[32:35], v[192:195]\n"
"v_mul_f32_e32 v29, s44, v29\n"
"v_cvt_pk_bf16_f32 v37, v26, v27\n"
"v_mfma_f32_16x16x32_bf16 v[196:199], a[196:199], v[32:35], v[196:199]\n"
"v_exp_f32_e32 v28, v28\n"
"v_mfma_f32_16x16x32_bf16 v[200:203], a[200:203], v[32:35], v[200:203]\n"
"ds_read_b128 a[132:135], v1 offset:8704\n"
"v_mul_f32_e32 v30, s44, v30\n"
"v_mfma_f32_16x16x32_bf16 v[204:207], a[204:207], v[32:35], v[204:207]\n"
"v_exp_f32_e32 v29, v29\n"
"v_mfma_f32_16x16x32_bf16 v[208:211], a[208:211], v[32:35], v[208:211]\n"
"s_add_u32 m0, s48, 0\n"
"v_mul_f32_e32 v31, s44, v31\n"
"v_cvt_pk_bf16_f32 v38, v28, v29\n"
"v_mfma_f32_16x16x32_bf16 v[212:215], a[212:215], v[32:35], v[212:215]\n"
"s_add_u32 s47, s45, 65536\n"
"v_exp_f32_e32 v30, v30\n"
"v_mfma_f32_16x16x32_bf16 v[216:219], a[216:219], v[32:35], v[216:219]\n"
"buffer_load_dwordx4 v6, s[24:27], s47 offen lds\n"
"v_mfma_f32_16x16x32_bf16 v[220:223], a[220:223], v[32:35], v[220:223]\n"
"v_exp_f32_e32 v31, v31\n"
"v_mfma_f32_16x16x32_bf16 a[244:247], a[224:227], v[32:35], a[244:247]\n"
"ds_read_b128 a[136:139], v1 offset:9216\n"
"v_cvt_pk_bf16_f32 v39, v30, v31\n"
"v_mfma_f32_16x16x32_bf16 v[24:27], a[96:99], a[16:19], 0\n"
"v_mul_f32_e32 v16, s44, v16\n"
"v_mul_f32_e32 v17, s44, v17\n"
"v_mfma_f32_16x16x32_bf16 v[24:27], a[100:103], a[20:23], v[24:27]\n"
"v_exp_f32_e32 v16, v16\n"
"v_mfma_f32_16x16x32_bf16 v[24:27], a[104:107], a[24:27], v[24:27]\n"
"ds_read_b128 a[144:147], v2 offset:8192\n"
"v_mul_f32_e32 v18, s44, v18\n"
"v_mfma_f32_16x16x32_bf16 v[24:27], a[108:111], a[28:31], v[24:27]\n"
"v_exp_f32_e32 v17, v17\n"
"v_mfma_f32_16x16x32_bf16 v[28:31], a[112:115], a[16:19], 0\n"
"v_mul_f32_e32 v19, s44, v19\n"
"v_cvt_pk_bf16_f32 v32, v16, v17\n"
"v_mfma_f32_16x16x32_bf16 v[28:31], a[116:119], a[20:23], v[28:31]\n"
"v_exp_f32_e32 v18, v18\n"
"v_mfma_f32_16x16x32_bf16 v[28:31], a[120:123], a[24:27], v[28:31]\n"
"ds_read_b128 a[148:151], v2 offset:8704\n"
"v_mul_f32_e32 v20, s44, v20\n"
"v_mfma_f32_16x16x32_bf16 v[28:31], a[124:127], a[28:31], v[28:31]\n"
"v_exp_f32_e32 v19, v19\n"
"v_mfma_f32_16x16x32_bf16 v[224:227], a[192:195], v[36:39], v[224:227]\n"
"v_mul_f32_e32 v21, s44, v21\n"
"v_cvt_pk_bf16_f32 v33, v18, v19\n"
"v_mfma_f32_16x16x32_bf16 v[228:231], a[196:199], v[36:39], v[228:231]\n"
"v_exp_f32_e32 v20, v20\n"
"v_mfma_f32_16x16x32_bf16 v[232:235], a[200:203], v[36:39], v[232:235]\n"
"ds_read_b128 a[152:155], v2 offset:9216\n"
"v_mul_f32_e32 v22, s44, v22\n"
"v_mfma_f32_16x16x32_bf16 v[236:239], a[204:207], v[36:39], v[236:239]\n"
"v_exp_f32_e32 v21, v21\n"
"v_mfma_f32_16x16x32_bf16 v[240:243], a[208:211], v[36:39], v[240:243]\n"
"s_add_u32 m0, s48, 1024\n"
"v_mul_f32_e32 v23, s44, v23\n"
"v_cvt_pk_bf16_f32 v34, v20, v21\n"
"v_mfma_f32_16x16x32_bf16 v[244:247], a[212:215], v[36:39], v[244:247]\n"
"s_add_u32 s47, s45, 65664\n"
"v_exp_f32_e32 v22, v22\n"
"v_mfma_f32_16x16x32_bf16 v[248:251], a[216:219], v[36:39], v[248:251]\n"
"buffer_load_dwordx4 v6, s[24:27], s47 offen lds\n"
"v_mfma_f32_16x16x32_bf16 v[252:255], a[220:223], v[36:39], v[252:255]\n"
"v_exp_f32_e32 v23, v23\n"
"v_mfma_f32_16x16x32_bf16 a[248:251], a[224:227], v[36:39], a[248:251]\n"
"ds_read_b128 a[156:159], v2 offset:9728\n"
"v_cvt_pk_bf16_f32 v35, v22, v23\n"
"v_mfma_f32_16x16x32_bf16 v[16:19], a[96:99], a[32:35], 0\n"
"v_mul_f32_e32 v24, s44, v24\n"
"v_mul_f32_e32 v25, s44, v25\n"
"v_mfma_f32_16x16x32_bf16 v[16:19], a[100:103], a[36:39], v[16:19]\n"
"v_exp_f32_e32 v24, v24\n"
"v_mfma_f32_16x16x32_bf16 v[16:19], a[104:107], a[40:43], v[16:19]\n"
"ds_read_b64_tr_b16 a[192:193], v3 offset:8192\n"
"v_mul_f32_e32 v26, s44, v26\n"
"v_mfma_f32_16x16x32_bf16 v[16:19], a[108:111], a[44:47], v[16:19]\n"
"v_exp_f32_e32 v25, v25\n"
"v_mfma_f32_16x16x32_bf16 v[20:23], a[112:115], a[32:35], 0\n"
"v_mul_f32_e32 v27, s44, v27\n"
"v_cvt_pk_bf16_f32 v36, v24, v25\n"
"v_mfma_f32_16x16x32_bf16 v[20:23], a[116:119], a[36:39], v[20:23]\n"
"v_exp_f32_e32 v26, v26\n"
"v_mfma_f32_16x16x32_bf16 v[20:23], a[120:123], a[40:43], v[20:23]\n"
"ds_read_b64_tr_b16 a[194:195], v4 offset:8192\n"
"v_mul_f32_e32 v28, s44, v28\n"
"v_mfma_f32_16x16x32_bf16 v[20:23], a[124:127], a[44:47], v[20:23]\n"
"v_exp_f32_e32 v27, v27\n"
"v_mfma_f32_16x16x32_bf16 v[64:67], a[160:163], v[32:35], v[64:67]\n"
"v_mul_f32_e32 v29, s44, v29\n"
"v_cvt_pk_bf16_f32 v37, v26, v27\n"
"v_mfma_f32_16x16x32_bf16 v[68:71], a[164:167], v[32:35], v[68:71]\n"
"v_exp_f32_e32 v28, v28\n"
"v_mfma_f32_16x16x32_bf16 v[72:75], a[168:171], v[32:35], v[72:75]\n"
"ds_read_b64_tr_b16 a[196:197], v11 offset:8192\n"
"v_mul_f32_e32 v30, s44, v30\n"
"v_mfma_f32_16x16x32_bf16 v[76:79], a[172:175], v[32:35], v[76:79]\n"
"v_exp_f32_e32 v29, v29\n"
"v_mfma_f32_16x16x32_bf16 v[80:83], a[176:179], v[32:35], v[80:83]\n"
"s_add_u32 m0, s48, 32768\n"
"v_mul_f32_e32 v31, s44, v31\n"
"v_cvt_pk_bf16_f32 v38, v28, v29\n"
"v_mfma_f32_16x16x32_bf16 v[84:87], a[180:183], v[32:35], v[84:87]\n"
"s_add_u32 s76, s45, 65536\n"
"v_exp_f32_e32 v30, v30\n"
"v_mfma_f32_16x16x32_bf16 v[88:91], a[184:187], v[32:35], v[88:91]\n"
"buffer_load_dwordx4 v6, s[28:31], s76 offen lds\n"
"v_mfma_f32_16x16x32_bf16 v[92:95], a[188:191], v[32:35], v[92:95]\n"
"v_exp_f32_e32 v31, v31\n"
"v_mfma_f32_16x16x32_bf16 a[228:231], a[224:227], v[32:35], a[228:231]\n"
"ds_read_b64_tr_b16 a[198:199], v14 offset:8192\n"
"v_cvt_pk_bf16_f32 v39, v30, v31\n"
"v_mfma_f32_16x16x32_bf16 v[24:27], a[96:99], a[48:51], 0\n"
"v_mul_f32_e32 v16, s44, v16\n"
"v_mul_f32_e32 v17, s44, v17\n"
"v_mfma_f32_16x16x32_bf16 v[24:27], a[100:103], a[52:55], v[24:27]\n"
"v_exp_f32_e32 v16, v16\n"
"v_mfma_f32_16x16x32_bf16 v[24:27], a[104:107], a[56:59], v[24:27]\n"
"ds_read_b64_tr_b16 a[200:201], v3 offset:8704\n"
"v_mul_f32_e32 v18, s44, v18\n"
"v_mfma_f32_16x16x32_bf16 v[24:27], a[108:111], a[60:63], v[24:27]\n"
"v_exp_f32_e32 v17, v17\n"
"v_mfma_f32_16x16x32_bf16 v[28:31], a[112:115], a[48:51], 0\n"
"v_mul_f32_e32 v19, s44, v19\n"
"v_cvt_pk_bf16_f32 v32, v16, v17\n"
"v_mfma_f32_16x16x32_bf16 v[28:31], a[116:119], a[52:55], v[28:31]\n"
"v_exp_f32_e32 v18, v18\n"
"v_mfma_f32_16x16x32_bf16 v[28:31], a[120:123], a[56:59], v[28:31]\n"
"ds_read_b64_tr_b16 a[202:203], v4 offset:8704\n"
"v_mul_f32_e32 v20, s44, v20\n"
"v_mfma_f32_16x16x32_bf16 v[28:31], a[124:127], a[60:63], v[28:31]\n"
"v_exp_f32_e32 v19, v19\n"
"v_mfma_f32_16x16x32_bf16 v[96:99], a[160:163], v[36:39], v[96:99]\n"
"v_mul_f32_e32 v21, s44, v21\n"
"v_cvt_pk_bf16_f32 v33, v18, v19\n"
"v_mfma_f32_16x16x32_bf16 v[100:103], a[164:167], v[36:39], v[100:103]\n"
"v_exp_f32_e32 v20, v20\n"
"v_mfma_f32_16x16x32_bf16 v[104:107], a[168:171], v[36:39], v[104:107]\n"
"ds_read_b64_tr_b16 a[204:205], v11 offset:8704\n"
"v_mul_f32_e32 v22, s44, v22\n"
"v_mfma_f32_16x16x32_bf16 v[108:111], a[172:175], v[36:39], v[108:111]\n"
"v_exp_f32_e32 v21, v21\n"
"v_mfma_f32_16x16x32_bf16 v[112:115], a[176:179], v[36:39], v[112:115]\n"
"s_add_u32 m0, s48, 33792\n"
"v_mul_f32_e32 v23, s44, v23\n"
"v_cvt_pk_bf16_f32 v34, v20, v21\n"
"v_mfma_f32_16x16x32_bf16 v[116:119], a[180:183], v[36:39], v[116:119]\n"
"s_add_u32 s76, s45, 65664\n"
"v_exp_f32_e32 v22, v22\n"
"v_mfma_f32_16x16x32_bf16 v[120:123], a[184:187], v[36:39], v[120:123]\n"
"buffer_load_dwordx4 v6, s[28:31], s76 offen lds\n"
"v_mfma_f32_16x16x32_bf16 v[124:127], a[188:191], v[36:39], v[124:127]\n"
"v_exp_f32_e32 v23, v23\n"
"v_mfma_f32_16x16x32_bf16 a[232:235], a[224:227], v[36:39], a[232:235]\n"
"ds_read_b64_tr_b16 a[206:207], v14 offset:8704\n"
"v_cvt_pk_bf16_f32 v35, v22, v23\n"
"v_mfma_f32_16x16x32_bf16 v[16:19], a[96:99], a[64:67], 0\n"
"v_mul_f32_e32 v24, s44, v24\n"
"v_mul_f32_e32 v25, s44, v25\n"
"v_mfma_f32_16x16x32_bf16 v[16:19], a[100:103], a[68:71], v[16:19]\n"
"v_exp_f32_e32 v24, v24\n"
"v_mfma_f32_16x16x32_bf16 v[16:19], a[104:107], a[72:75], v[16:19]\n"
"ds_read_b64_tr_b16 a[208:209], v3 offset:9216\n"
"v_mul_f32_e32 v26, s44, v26\n"
"v_mfma_f32_16x16x32_bf16 v[16:19], a[108:111], a[76:79], v[16:19]\n"
"v_exp_f32_e32 v25, v25\n"
"v_mfma_f32_16x16x32_bf16 v[20:23], a[112:115], a[64:67], 0\n"
"v_mul_f32_e32 v27, s44, v27\n"
"v_cvt_pk_bf16_f32 v36, v24, v25\n"
"v_mfma_f32_16x16x32_bf16 v[20:23], a[116:119], a[68:71], v[20:23]\n"
"v_exp_f32_e32 v26, v26\n"
"v_mfma_f32_16x16x32_bf16 v[20:23], a[120:123], a[72:75], v[20:23]\n"
"ds_read_b64_tr_b16 a[210:211], v4 offset:9216\n"
"v_mul_f32_e32 v28, s44, v28\n"
"v_mfma_f32_16x16x32_bf16 v[20:23], a[124:127], a[76:79], v[20:23]\n"
"v_exp_f32_e32 v27, v27\n"
"v_mfma_f32_16x16x32_bf16 v[128:131], a[160:163], v[32:35], v[128:131]\n"
"v_mul_f32_e32 v29, s44, v29\n"
"v_cvt_pk_bf16_f32 v37, v26, v27\n"
"v_mfma_f32_16x16x32_bf16 v[132:135], a[164:167], v[32:35], v[132:135]\n"
"v_exp_f32_e32 v28, v28\n"
"v_mfma_f32_16x16x32_bf16 v[136:139], a[168:171], v[32:35], v[136:139]\n"
"ds_read_b64_tr_b16 a[212:213], v11 offset:9216\n"
"v_mul_f32_e32 v30, s44, v30\n"
"v_mfma_f32_16x16x32_bf16 v[140:143], a[172:175], v[32:35], v[140:143]\n"
"v_exp_f32_e32 v29, v29\n"
"v_mfma_f32_16x16x32_bf16 v[144:147], a[176:179], v[32:35], v[144:147]\n"
"s_add_u32 m0, s49, 6144\n"
"v_mul_f32_e32 v31, s44, v31\n"
"v_cvt_pk_bf16_f32 v38, v28, v29\n"
"v_mfma_f32_16x16x32_bf16 v[148:151], a[180:183], v[32:35], v[148:151]\n"
"s_add_u32 s61, s58, 4096\n"
"v_exp_f32_e32 v30, v30\n"
"v_mfma_f32_16x16x32_bf16 v[152:155], a[184:187], v[32:35], v[152:155]\n"
"buffer_load_dwordx4 v8, s[36:39], s61 offen  nt lds\n"
"v_mfma_f32_16x16x32_bf16 v[156:159], a[188:191], v[32:35], v[156:159]\n"
"v_exp_f32_e32 v31, v31\n"
"v_mfma_f32_16x16x32_bf16 a[236:239], a[224:227], v[32:35], a[236:239]\n"
"ds_read_b64_tr_b16 a[214:215], v14 offset:9216\n"
"v_cvt_pk_bf16_f32 v39, v30, v31\n"
"v_mfma_f32_16x16x32_bf16 v[24:27], a[96:99], a[80:83], 0\n"
"ds_read_b64_tr_b16 a[222:223], v14 offset:9728\n"
"v_mul_f32_e32 v16, s44, v16\n"
"v_mul_f32_e32 v17, s44, v17\n"
"v_mfma_f32_16x16x32_bf16 v[24:27], a[100:103], a[84:87], v[24:27]\n"
"v_exp_f32_e32 v16, v16\n"
"v_mfma_f32_16x16x32_bf16 v[24:27], a[104:107], a[88:91], v[24:27]\n"
"ds_read_b64_tr_b16 a[216:217], v3 offset:9728\n"
"v_mul_f32_e32 v18, s44, v18\n"
"v_mfma_f32_16x16x32_bf16 v[24:27], a[108:111], a[92:95], v[24:27]\n"
"v_exp_f32_e32 v17, v17\n"
"v_mfma_f32_16x16x32_bf16 v[28:31], a[112:115], a[80:83], 0\n"
"v_mul_f32_e32 v19, s44, v19\n"
"v_cvt_pk_bf16_f32 v32, v16, v17\n"
"v_mfma_f32_16x16x32_bf16 v[28:31], a[116:119], a[84:87], v[28:31]\n"
"v_exp_f32_e32 v18, v18\n"
"v_mfma_f32_16x16x32_bf16 v[28:31], a[120:123], a[88:91], v[28:31]\n"
"ds_read_b64_tr_b16 a[218:219], v4 offset:9728\n"
"v_mul_f32_e32 v20, s44, v20\n"
"v_mfma_f32_16x16x32_bf16 v[28:31], a[124:127], a[92:95], v[28:31]\n"
"v_exp_f32_e32 v19, v19\n"
"v_mfma_f32_16x16x32_bf16 v[160:163], a[160:163], v[36:39], v[160:163]\n"
"v_mul_f32_e32 v21, s44, v21\n"
"v_cvt_pk_bf16_f32 v33, v18, v19\n"
"v_mfma_f32_16x16x32_bf16 v[164:167], a[164:167], v[36:39], v[164:167]\n"
"v_exp_f32_e32 v20, v20\n"
"v_mfma_f32_16x16x32_bf16 v[168:171], a[168:171], v[36:39], v[168:171]\n"
"ds_read_b64_tr_b16 a[220:221], v11 offset:9728\n"
"v_mul_f32_e32 v22, s44, v22\n"
"v_mfma_f32_16x16x32_bf16 v[172:175], a[172:175], v[36:39], v[172:175]\n"
"v_exp_f32_e32 v21, v21\n"
"v_mfma_f32_16x16x32_bf16 v[176:179], a[176:179], v[36:39], v[176:179]\n"
"s_add_u32 m0, s49, 7168\n"
"v_mul_f32_e32 v23, s44, v23\n"
"v_cvt_pk_bf16_f32 v34, v20, v21\n"
"v_mfma_f32_16x16x32_bf16 v[180:183], a[180:183], v[36:39], v[180:183]\n"
"s_add_u32 s61, s58, 4224\n"
"v_exp_f32_e32 v22, v22\n"
"v_mfma_f32_16x16x32_bf16 v[184:187], a[184:187], v[36:39], v[184:187]\n"
"buffer_load_dwordx4 v8, s[36:39], s61 offen  nt lds\n"
"v_mfma_f32_16x16x32_bf16 v[188:191], a[188:191], v[36:39], v[188:191]\n"
"v_exp_f32_e32 v23, v23\n"
"v_mfma_f32_16x16x32_bf16 a[240:243], a[224:227], v[36:39], a[240:243]\n"
"s_waitcnt lgkmcnt(15)\n"
"v_cvt_pk_bf16_f32 v35, v22, v23\n"
"v_mfma_f32_16x16x32_bf16 v[16:19], a[128:131], a[0:3], 0\n"
"v_mul_f32_e32 v24, s44, v24\n"
"v_mul_f32_e32 v25, s44, v25\n"
"v_mfma_f32_16x16x32_bf16 v[16:19], a[132:135], a[4:7], v[16:19]\n"
"v_exp_f32_e32 v24, v24\n"
"v_mfma_f32_16x16x32_bf16 v[16:19], a[136:139], a[8:11], v[16:19]\n"
"v_mul_f32_e32 v26, s44, v26\n"
"v_mfma_f32_16x16x32_bf16 v[16:19], a[140:143], a[12:15], v[16:19]\n"
"v_exp_f32_e32 v25, v25\n"
"v_mfma_f32_16x16x32_bf16 v[20:23], a[144:147], a[0:3], 0\n"
"v_mul_f32_e32 v27, s44, v27\n"
"v_cvt_pk_bf16_f32 v36, v24, v25\n"
"v_mfma_f32_16x16x32_bf16 v[20:23], a[148:151], a[4:7], v[20:23]\n"
"s_waitcnt vmcnt(18) lgkmcnt(0)\n"
"s_barrier\n"
"v_exp_f32_e32 v26, v26\n"
"v_mfma_f32_16x16x32_bf16 v[20:23], a[152:155], a[8:11], v[20:23]\n"
"ds_read_b128 a[96:99], v1 offset:16384\n"
"ds_read_b128 a[108:111], v1 offset:17920\n"
"v_mul_f32_e32 v28, s44, v28\n"
"v_mfma_f32_16x16x32_bf16 v[20:23], a[156:159], a[12:15], v[20:23]\n"
"ds_read_b128 v[40:43], v58 offset:8192\n"
"v_exp_f32_e32 v27, v27\n"
"v_mfma_f32_16x16x32_bf16 v[192:195], a[160:163], v[32:35], v[192:195]\n"
"ds_read_b128 v[44:47], v59 offset:9216\n"
"v_mul_f32_e32 v29, s44, v29\n"
"v_cvt_pk_bf16_f32 v37, v26, v27\n"
"v_mfma_f32_16x16x32_bf16 v[196:199], a[164:167], v[32:35], v[196:199]\n"
"ds_read_b128 v[48:51], v58 offset:10240\n"
"v_exp_f32_e32 v28, v28\n"
"v_mfma_f32_16x16x32_bf16 v[200:203], a[168:171], v[32:35], v[200:203]\n"
"ds_read_b128 a[100:103], v1 offset:16896\n"
"ds_read_b128 v[52:55], v59 offset:11264\n"
"v_mul_f32_e32 v30, s44, v30\n"
"v_mfma_f32_16x16x32_bf16 v[204:207], a[172:175], v[32:35], v[204:207]\n"
"v_exp_f32_e32 v29, v29\n"
"v_mfma_f32_16x16x32_bf16 v[208:211], a[176:179], v[32:35], v[208:211]\n"
"s_add_u32 m0, s48, 8192\n"
"v_mul_f32_e32 v31, s44, v31\n"
"v_cvt_pk_bf16_f32 v38, v28, v29\n"
"v_mfma_f32_16x16x32_bf16 v[212:215], a[180:183], v[32:35], v[212:215]\n"
"s_add_u32 s47, s45, 73728\n"
"v_exp_f32_e32 v30, v30\n"
"v_mfma_f32_16x16x32_bf16 v[216:219], a[184:187], v[32:35], v[216:219]\n"
"buffer_load_dwordx4 v6, s[24:27], s47 offen lds\n"
"v_mfma_f32_16x16x32_bf16 v[220:223], a[188:191], v[32:35], v[220:223]\n"
"v_exp_f32_e32 v31, v31\n"
"v_mfma_f32_16x16x32_bf16 a[244:247], a[224:227], v[32:35], a[244:247]\n"
"ds_read_b128 a[104:107], v1 offset:17408\n"
"v_cvt_pk_bf16_f32 v39, v30, v31\n"
"v_mfma_f32_16x16x32_bf16 v[24:27], a[128:131], a[16:19], 0\n"
"v_mul_f32_e32 v16, s44, v16\n"
"v_mul_f32_e32 v17, s44, v17\n"
"v_mfma_f32_16x16x32_bf16 v[24:27], a[132:135], a[20:23], v[24:27]\n"
"v_exp_f32_e32 v16, v16\n"
"s_add_u32 s77, s60, 8192\n"
"v_mfma_f32_16x16x32_bf16 v[24:27], a[136:139], a[24:27], v[24:27]\n"
"ds_read_b128 a[112:115], v2 offset:16384\n"
"v_mul_f32_e32 v18, s44, v18\n"
"s_waitcnt lgkmcnt(6)\n"
"buffer_store_dwordx4 v[40:43], v9, s[40:43], s77 offen offset:0  sc1\n"
"v_mfma_f32_16x16x32_bf16 v[24:27], a[140:143], a[28:31], v[24:27]\n"
"v_exp_f32_e32 v17, v17\n"
"v_mfma_f32_16x16x32_bf16 v[28:31], a[144:147], a[16:19], 0\n"
"v_mul_f32_e32 v19, s44, v19\n"
"v_cvt_pk_bf16_f32 v32, v16, v17\n"
"v_mfma_f32_16x16x32_bf16 v[28:31], a[148:151], a[20:23], v[28:31]\n"
"v_exp_f32_e32 v18, v18\n"
"v_mfma_f32_16x16x32_bf16 v[28:31], a[152:155], a[24:27], v[28:31]\n"
"ds_read_b128 a[116:119], v2 offset:16896\n"
"v_mul_f32_e32 v20, s44, v20\n"
"s_waitcnt lgkmcnt(6)\n"
"buffer_store_dwordx4 v[44:47], v9, s[40:43], s77 offen offset:1024  sc1\n"
"v_mfma_f32_16x16x32_bf16 v[28:31], a[156:159], a[28:31], v[28:31]\n"
"v_exp_f32_e32 v19, v19\n"
"v_mfma_f32_16x16x32_bf16 v[224:227], a[160:163], v[36:39], v[224:227]\n"
"v_mul_f32_e32 v21, s44, v21\n"
"v_cvt_pk_bf16_f32 v33, v18, v19\n"
"v_mfma_f32_16x16x32_bf16 v[228:231], a[164:167], v[36:39], v[228:231]\n"
"v_exp_f32_e32 v20, v20\n"
"v_mfma_f32_16x16x32_bf16 v[232:235], a[168:171], v[36:39], v[232:235]\n"
"ds_read_b128 a[120:123], v2 offset:17408\n"
"v_mul_f32_e32 v22, s44, v22\n"
"s_waitcnt lgkmcnt(6)\n"
"buffer_store_dwordx4 v[48:51], v9, s[40:43], s77 offen offset:2048  sc1\n"
"v_mfma_f32_16x16x32_bf16 v[236:239], a[172:175], v[36:39], v[236:239]\n"
"v_exp_f32_e32 v21, v21\n"
"v_mfma_f32_16x16x32_bf16 v[240:243], a[176:179], v[36:39], v[240:243]\n"
"s_add_u32 m0, s48, 9216\n"
"v_mul_f32_e32 v23, s44, v23\n"
"v_cvt_pk_bf16_f32 v34, v20, v21\n"
"v_mfma_f32_16x16x32_bf16 v[244:247], a[180:183], v[36:39], v[244:247]\n"
"s_add_u32 s47, s45, 73856\n"
"v_exp_f32_e32 v22, v22\n"
"v_mfma_f32_16x16x32_bf16 v[248:251], a[184:187], v[36:39], v[248:251]\n"
"buffer_load_dwordx4 v6, s[24:27], s47 offen lds\n"
"v_mfma_f32_16x16x32_bf16 v[252:255], a[188:191], v[36:39], v[252:255]\n"
"v_exp_f32_e32 v23, v23\n"
"v_mfma_f32_16x16x32_bf16 a[248:251], a[224:227], v[36:39], a[248:251]\n"
"ds_read_b128 a[124:127], v2 offset:17920\n"
"v_cvt_pk_bf16_f32 v35, v22, v23\n"
"s_waitcnt lgkmcnt(5)\n"
"buffer_store_dwordx4 v[52:55], v9, s[40:43], s77 offen offset:3072  sc1\n"
"v_mfma_f32_16x16x32_bf16 v[16:19], a[128:131], a[32:35], 0\n"
"v_mul_f32_e32 v24, s44, v24\n"
"v_mul_f32_e32 v25, s44, v25\n"
"v_mfma_f32_16x16x32_bf16 v[16:19], a[132:135], a[36:39], v[16:19]\n"
"v_exp_f32_e32 v24, v24\n"
"v_mfma_f32_16x16x32_bf16 v[16:19], a[136:139], a[40:43], v[16:19]\n"
"ds_read_b64_tr_b16 a[160:161], v3 offset:16384\n"
"v_mul_f32_e32 v26, s44, v26\n"
"v_mfma_f32_16x16x32_bf16 v[16:19], a[140:143], a[44:47], v[16:19]\n"
"v_exp_f32_e32 v25, v25\n"
"v_mfma_f32_16x16x32_bf16 v[20:23], a[144:147], a[32:35], 0\n"
"v_mul_f32_e32 v27, s44, v27\n"
"v_cvt_pk_bf16_f32 v36, v24, v25\n"
"v_mfma_f32_16x16x32_bf16 v[20:23], a[148:151], a[36:39], v[20:23]\n"
"v_exp_f32_e32 v26, v26\n"
"v_mfma_f32_16x16x32_bf16 v[20:23], a[152:155], a[40:43], v[20:23]\n"
"ds_read_b64_tr_b16 a[162:163], v4 offset:16384\n"
"v_mul_f32_e32 v28, s44, v28\n"
"v_mfma_f32_16x16x32_bf16 v[20:23], a[156:159], a[44:47], v[20:23]\n"
"v_exp_f32_e32 v27, v27\n"
"v_mfma_f32_16x16x32_bf16 v[64:67], a[192:195], v[32:35], v[64:67]\n"
"v_mul_f32_e32 v29, s44, v29\n"
"v_cvt_pk_bf16_f32 v37, v26, v27\n"
"v_mfma_f32_16x16x32_bf16 v[68:71], a[196:199], v[32:35], v[68:71]\n"
"v_exp_f32_e32 v28, v28\n"
"v_mfma_f32_16x16x32_bf16 v[72:75], a[200:203], v[32:35], v[72:75]\n"
"ds_read_b64_tr_b16 a[164:165], v11 offset:16384\n"
"v_mul_f32_e32 v30, s44, v30\n"
"v_mfma_f32_16x16x32_bf16 v[76:79], a[204:207], v[32:35], v[76:79]\n"
"v_exp_f32_e32 v29, v29\n"
"v_mfma_f32_16x16x32_bf16 v[80:83], a[208:211], v[32:35], v[80:83]\n"
"s_add_u32 m0, s48, 40960\n"
"v_mul_f32_e32 v31, s44, v31\n"
"v_cvt_pk_bf16_f32 v38, v28, v29\n"
"v_mfma_f32_16x16x32_bf16 v[84:87], a[212:215], v[32:35], v[84:87]\n"
"s_add_u32 s76, s45, 73728\n"
"v_exp_f32_e32 v30, v30\n"
"v_mfma_f32_16x16x32_bf16 v[88:91], a[216:219], v[32:35], v[88:91]\n"
"buffer_load_dwordx4 v6, s[28:31], s76 offen lds\n"
"v_mfma_f32_16x16x32_bf16 v[92:95], a[220:223], v[32:35], v[92:95]\n"
"v_exp_f32_e32 v31, v31\n"
"v_mfma_f32_16x16x32_bf16 a[228:231], a[224:227], v[32:35], a[228:231]\n"
"ds_read_b64_tr_b16 a[166:167], v14 offset:16384\n"
"v_cvt_pk_bf16_f32 v39, v30, v31\n"
"v_mfma_f32_16x16x32_bf16 v[24:27], a[128:131], a[48:51], 0\n"
"v_mul_f32_e32 v16, s44, v16\n"
"v_mul_f32_e32 v17, s44, v17\n"
"v_mfma_f32_16x16x32_bf16 v[24:27], a[132:135], a[52:55], v[24:27]\n"
"v_exp_f32_e32 v16, v16\n"
"v_mfma_f32_16x16x32_bf16 v[24:27], a[136:139], a[56:59], v[24:27]\n"
"ds_read_b64_tr_b16 a[168:169], v3 offset:16896\n"
"v_mul_f32_e32 v18, s44, v18\n"
"v_mfma_f32_16x16x32_bf16 v[24:27], a[140:143], a[60:63], v[24:27]\n"
"v_exp_f32_e32 v17, v17\n"
"v_mfma_f32_16x16x32_bf16 v[28:31], a[144:147], a[48:51], 0\n"
"v_mul_f32_e32 v19, s44, v19\n"
"v_cvt_pk_bf16_f32 v32, v16, v17\n"
"v_mfma_f32_16x16x32_bf16 v[28:31], a[148:151], a[52:55], v[28:31]\n"
"v_exp_f32_e32 v18, v18\n"
"v_mfma_f32_16x16x32_bf16 v[28:31], a[152:155], a[56:59], v[28:31]\n"
"ds_read_b64_tr_b16 a[170:171], v4 offset:16896\n"
"v_mul_f32_e32 v20, s44, v20\n"
"v_mfma_f32_16x16x32_bf16 v[28:31], a[156:159], a[60:63], v[28:31]\n"
"v_exp_f32_e32 v19, v19\n"
"v_mfma_f32_16x16x32_bf16 v[96:99], a[192:195], v[36:39], v[96:99]\n"
"v_mul_f32_e32 v21, s44, v21\n"
"v_cvt_pk_bf16_f32 v33, v18, v19\n"
"v_mfma_f32_16x16x32_bf16 v[100:103], a[196:199], v[36:39], v[100:103]\n"
"v_exp_f32_e32 v20, v20\n"
"v_mfma_f32_16x16x32_bf16 v[104:107], a[200:203], v[36:39], v[104:107]\n"
"ds_read_b64_tr_b16 a[172:173], v11 offset:16896\n"
"v_mul_f32_e32 v22, s44, v22\n"
"v_mfma_f32_16x16x32_bf16 v[108:111], a[204:207], v[36:39], v[108:111]\n"
"v_exp_f32_e32 v21, v21\n"
"v_mfma_f32_16x16x32_bf16 v[112:115], a[208:211], v[36:39], v[112:115]\n"
"s_add_u32 m0, s48, 41984\n"
"v_mul_f32_e32 v23, s44, v23\n"
"v_cvt_pk_bf16_f32 v34, v20, v21\n"
"v_mfma_f32_16x16x32_bf16 v[116:119], a[212:215], v[36:39], v[116:119]\n"
"s_add_u32 s76, s45, 73856\n"
"v_exp_f32_e32 v22, v22\n"
"v_mfma_f32_16x16x32_bf16 v[120:123], a[216:219], v[36:39], v[120:123]\n"
"buffer_load_dwordx4 v6, s[28:31], s76 offen lds\n"
"v_mfma_f32_16x16x32_bf16 v[124:127], a[220:223], v[36:39], v[124:127]\n"
"v_exp_f32_e32 v23, v23\n"
"v_mfma_f32_16x16x32_bf16 a[232:235], a[224:227], v[36:39], a[232:235]\n"
"ds_read_b64_tr_b16 a[174:175], v14 offset:16896\n"
"v_cvt_pk_bf16_f32 v35, v22, v23\n"
"v_mfma_f32_16x16x32_bf16 v[16:19], a[128:131], a[64:67], 0\n"
"ds_read_b64_tr_b16 a[182:183], v14 offset:17408\n"
"v_mul_f32_e32 v24, s44, v24\n"
"v_mul_f32_e32 v25, s44, v25\n"
"v_mfma_f32_16x16x32_bf16 v[16:19], a[132:135], a[68:71], v[16:19]\n"
"v_exp_f32_e32 v24, v24\n"
"v_mfma_f32_16x16x32_bf16 v[16:19], a[136:139], a[72:75], v[16:19]\n"
"ds_read_b64_tr_b16 a[176:177], v3 offset:17408\n"
"v_mul_f32_e32 v26, s44, v26\n"
"v_mfma_f32_16x16x32_bf16 v[16:19], a[140:143], a[76:79], v[16:19]\n"
"v_exp_f32_e32 v25, v25\n"
"v_mfma_f32_16x16x32_bf16 v[20:23], a[144:147], a[64:67], 0\n"
"v_mul_f32_e32 v27, s44, v27\n"
"v_cvt_pk_bf16_f32 v36, v24, v25\n"
"v_mfma_f32_16x16x32_bf16 v[20:23], a[148:151], a[68:71], v[20:23]\n"
"v_exp_f32_e32 v26, v26\n"
"v_mfma_f32_16x16x32_bf16 v[20:23], a[152:155], a[72:75], v[20:23]\n"
"ds_read_b64_tr_b16 a[178:179], v4 offset:17408\n"
"v_mul_f32_e32 v28, s44, v28\n"
"v_mfma_f32_16x16x32_bf16 v[20:23], a[156:159], a[76:79], v[20:23]\n"
"v_exp_f32_e32 v27, v27\n"
"v_mfma_f32_16x16x32_bf16 v[128:131], a[192:195], v[32:35], v[128:131]\n"
"v_mul_f32_e32 v29, s44, v29\n"
"v_cvt_pk_bf16_f32 v37, v26, v27\n"
"v_mfma_f32_16x16x32_bf16 v[132:135], a[196:199], v[32:35], v[132:135]\n"
"v_exp_f32_e32 v28, v28\n"
"v_mfma_f32_16x16x32_bf16 v[136:139], a[200:203], v[32:35], v[136:139]\n"
"ds_read_b64_tr_b16 a[180:181], v11 offset:17408\n"
"v_mul_f32_e32 v30, s44, v30\n"
"v_mfma_f32_16x16x32_bf16 v[140:143], a[204:207], v[32:35], v[140:143]\n"
"v_exp_f32_e32 v29, v29\n"
"v_mfma_f32_16x16x32_bf16 v[144:147], a[208:211], v[32:35], v[144:147]\n"
"v_mul_f32_e32 v31, s44, v31\n"
"v_cvt_pk_bf16_f32 v38, v28, v29\n"
"v_mfma_f32_16x16x32_bf16 v[148:151], a[212:215], v[32:35], v[148:151]\n"
"s_mov_b32 s88, L_S6%=+0-L_code%=\n"
"v_exp_f32_e32 v30, v30\n"
"v_mfma_f32_16x16x32_bf16 v[152:155], a[216:219], v[32:35], v[152:155]\n"
"s_add_u32 m0, s49, 8192\n"
"buffer_load_dword v63, v62, s[84:87], s88 offen\n"
"v_mfma_f32_16x16x32_bf16 v[156:159], a[220:223], v[32:35], v[156:159]\n"
"s_add_u32 s61, s58, 8192\n"
"v_exp_f32_e32 v31, v31\n"
"v_mfma_f32_16x16x32_bf16 a[236:239], a[224:227], v[32:35], a[236:239]\n"
"buffer_load_dwordx4 v7, s[36:39], s61 offen  nt lds\n"
"v_cvt_pk_bf16_f32 v39, v30, v31\n"
"v_mfma_f32_16x16x32_bf16 v[24:27], a[128:131], a[80:83], 0\n"
"ds_read_b64_tr_b16 a[190:191], v14 offset:17920\n"
"v_mul_f32_e32 v16, s44, v16\n"
"v_mul_f32_e32 v17, s44, v17\n"
"v_mfma_f32_16x16x32_bf16 v[24:27], a[132:135], a[84:87], v[24:27]\n"
"v_exp_f32_e32 v16, v16\n"
"v_mfma_f32_16x16x32_bf16 v[24:27], a[136:139], a[88:91], v[24:27]\n"
"ds_read_b64_tr_b16 a[184:185], v3 offset:17920\n"
"v_mul_f32_e32 v18, s44, v18\n"
"v_mfma_f32_16x16x32_bf16 v[24:27], a[140:143], a[92:95], v[24:27]\n"
"v_exp_f32_e32 v17, v17\n"
"v_mfma_f32_16x16x32_bf16 v[28:31], a[144:147], a[80:83], 0\n"
"v_mul_f32_e32 v19, s44, v19\n"
"v_cvt_pk_bf16_f32 v32, v16, v17\n"
"v_mfma_f32_16x16x32_bf16 v[28:31], a[148:151], a[84:87], v[28:31]\n"
"v_exp_f32_e32 v18, v18\n"
"v_mfma_f32_16x16x32_bf16 v[28:31], a[152:155], a[88:91], v[28:31]\n"
"ds_read_b64_tr_b16 a[186:187], v4 offset:17920\n"
"v_mul_f32_e32 v20, s44, v20\n"
"v_mfma_f32_16x16x32_bf16 v[28:31], a[156:159], a[92:95], v[28:31]\n"
"v_exp_f32_e32 v19, v19\n"
"v_mfma_f32_16x16x32_bf16 v[160:163], a[192:195], v[36:39], v[160:163]\n"
"v_mul_f32_e32 v21, s44, v21\n"
"v_cvt_pk_bf16_f32 v33, v18, v19\n"
"v_mfma_f32_16x16x32_bf16 v[164:167], a[196:199], v[36:39], v[164:167]\n"
"v_exp_f32_e32 v20, v20\n"
"v_mfma_f32_16x16x32_bf16 v[168:171], a[200:203], v[36:39], v[168:171]\n"
"ds_read_b64_tr_b16 a[188:189], v11 offset:17920\n"
"v_mul_f32_e32 v22, s44, v22\n"
"v_mfma_f32_16x16x32_bf16 v[172:175], a[204:207], v[36:39], v[172:175]\n"
"v_exp_f32_e32 v21, v21\n"
"v_mfma_f32_16x16x32_bf16 v[176:179], a[208:211], v[36:39], v[176:179]\n"
"s_add_u32 m0, s49, 9216\n"
"v_mul_f32_e32 v23, s44, v23\n"
"v_cvt_pk_bf16_f32 v34, v20, v21\n"
"v_mfma_f32_16x16x32_bf16 v[180:183], a[212:215], v[36:39], v[180:183]\n"
"s_add_u32 s61, s58, 8320\n"
"v_exp_f32_e32 v22, v22\n"
"v_mfma_f32_16x16x32_bf16 v[184:187], a[216:219], v[36:39], v[184:187]\n"
"buffer_load_dwordx4 v7, s[36:39], s61 offen  nt lds\n"
"v_mfma_f32_16x16x32_bf16 v[188:191], a[220:223], v[36:39], v[188:191]\n"
"v_exp_f32_e32 v23, v23\n"
"v_mfma_f32_16x16x32_bf16 a[240:243], a[224:227], v[36:39], a[240:243]\n"
"v_cvt_pk_bf16_f32 v35, v22, v23\n"
"v_mfma_f32_16x16x32_bf16 v[16:19], a[96:99], a[0:3], 0\n"
"v_mul_f32_e32 v24, s44, v24\n"
"v_mul_f32_e32 v25, s44, v25\n"
"v_mfma_f32_16x16x32_bf16 v[16:19], a[100:103], a[4:7], v[16:19]\n"
"s_waitcnt lgkmcnt(15)\n"
"v_exp_f32_e32 v24, v24\n"
"v_mfma_f32_16x16x32_bf16 v[16:19], a[104:107], a[8:11], v[16:19]\n"
"v_mul_f32_e32 v26, s44, v26\n"
"v_mfma_f32_16x16x32_bf16 v[16:19], a[108:111], a[12:15], v[16:19]\n"
"v_exp_f32_e32 v25, v25\n"
"v_mfma_f32_16x16x32_bf16 v[20:23], a[112:115], a[0:3], 0\n"
"v_mul_f32_e32 v27, s44, v27\n"
"v_cvt_pk_bf16_f32 v36, v24, v25\n"
"v_mfma_f32_16x16x32_bf16 v[20:23], a[116:119], a[4:7], v[20:23]\n"
"s_waitcnt vmcnt(19) lgkmcnt(0)\n"
"s_barrier\n"
"v_exp_f32_e32 v26, v26\n"
"v_mfma_f32_16x16x32_bf16 v[20:23], a[120:123], a[8:11], v[20:23]\n"
"ds_read_b128 a[128:131], v1 offset:24576\n"
"ds_read_b128 a[140:143], v1 offset:26112\n"
"v_mul_f32_e32 v28, s44, v28\n"
"v_mfma_f32_16x16x32_bf16 v[20:23], a[124:127], a[12:15], v[20:23]\n"
"v_exp_f32_e32 v27, v27\n"
"v_mfma_f32_16x16x32_bf16 v[192:195], a[192:195], v[32:35], v[192:195]\n"
"v_mul_f32_e32 v29, s44, v29\n"
"v_cvt_pk_bf16_f32 v37, v26, v27\n"
"v_mfma_f32_16x16x32_bf16 v[196:199], a[196:199], v[32:35], v[196:199]\n"
"v_exp_f32_e32 v28, v28\n"
"v_mfma_f32_16x16x32_bf16 v[200:203], a[200:203], v[32:35], v[200:203]\n"
"ds_read_b128 a[132:135], v1 offset:25088\n"
"v_mul_f32_e32 v30, s44, v30\n"
"v_mfma_f32_16x16x32_bf16 v[204:207], a[204:207], v[32:35], v[204:207]\n"
"v_exp_f32_e32 v29, v29\n"
"v_mfma_f32_16x16x32_bf16 v[208:211], a[208:211], v[32:35], v[208:211]\n"
"s_add_u32 m0, s48, 16384\n"
"v_mul_f32_e32 v31, s44, v31\n"
"v_cvt_pk_bf16_f32 v38, v28, v29\n"
"v_mfma_f32_16x16x32_bf16 v[212:215], a[212:215], v[32:35], v[212:215]\n"
"s_add_u32 s47, s45, 81920\n"
"v_exp_f32_e32 v30, v30\n"
"v_mfma_f32_16x16x32_bf16 v[216:219], a[216:219], v[32:35], v[216:219]\n"
"buffer_load_dwordx4 v6, s[24:27], s47 offen lds\n"
"v_mfma_f32_16x16x32_bf16 v[220:223], a[220:223], v[32:35], v[220:223]\n"
"v_exp_f32_e32 v31, v31\n"
"v_mfma_f32_16x16x32_bf16 a[244:247], a[224:227], v[32:35], a[244:247]\n"
"ds_read_b128 a[136:139], v1 offset:25600\n"
"v_cvt_pk_bf16_f32 v39, v30, v31\n"
"v_mfma_f32_16x16x32_bf16 v[24:27], a[96:99], a[16:19], 0\n"
"v_mul_f32_e32 v16, s44, v16\n"
"v_mul_f32_e32 v17, s44, v17\n"
"v_mfma_f32_16x16x32_bf16 v[24:27], a[100:103], a[20:23], v[24:27]\n"
"v_exp_f32_e32 v16, v16\n"
"v_mfma_f32_16x16x32_bf16 v[24:27], a[104:107], a[24:27], v[24:27]\n"
"ds_read_b128 a[144:147], v2 offset:24576\n"
"v_mul_f32_e32 v18, s44, v18\n"
"v_mfma_f32_16x16x32_bf16 v[24:27], a[108:111], a[28:31], v[24:27]\n"
"v_exp_f32_e32 v17, v17\n"
"v_mfma_f32_16x16x32_bf16 v[28:31], a[112:115], a[16:19], 0\n"
"v_mul_f32_e32 v19, s44, v19\n"
"v_cvt_pk_bf16_f32 v32, v16, v17\n"
"v_mfma_f32_16x16x32_bf16 v[28:31], a[116:119], a[20:23], v[28:31]\n"
"v_exp_f32_e32 v18, v18\n"
"v_mfma_f32_16x16x32_bf16 v[28:31], a[120:123], a[24:27], v[28:31]\n"
"ds_read_b128 a[148:151], v2 offset:25088\n"
"v_mul_f32_e32 v20, s44, v20\n"
"v_mfma_f32_16x16x32_bf16 v[28:31], a[124:127], a[28:31], v[28:31]\n"
"v_exp_f32_e32 v19, v19\n"
"v_mfma_f32_16x16x32_bf16 v[224:227], a[192:195], v[36:39], v[224:227]\n"
"v_mul_f32_e32 v21, s44, v21\n"
"v_cvt_pk_bf16_f32 v33, v18, v19\n"
"v_mfma_f32_16x16x32_bf16 v[228:231], a[196:199], v[36:39], v[228:231]\n"
"v_exp_f32_e32 v20, v20\n"
"v_mfma_f32_16x16x32_bf16 v[232:235], a[200:203], v[36:39], v[232:235]\n"
"ds_read_b128 a[152:155], v2 offset:25600\n"
"v_mul_f32_e32 v22, s44, v22\n"
"v_mfma_f32_16x16x32_bf16 v[236:239], a[204:207], v[36:39], v[236:239]\n"
"v_exp_f32_e32 v21, v21\n"
"v_mfma_f32_16x16x32_bf16 v[240:243], a[208:211], v[36:39], v[240:243]\n"
"s_add_u32 m0, s48, 17408\n"
"v_mul_f32_e32 v23, s44, v23\n"
"v_cvt_pk_bf16_f32 v34, v20, v21\n"
"v_mfma_f32_16x16x32_bf16 v[244:247], a[212:215], v[36:39], v[244:247]\n"
"s_add_u32 s47, s45, 82048\n"
"v_exp_f32_e32 v22, v22\n"
"v_mfma_f32_16x16x32_bf16 v[248:251], a[216:219], v[36:39], v[248:251]\n"
"buffer_load_dwordx4 v6, s[24:27], s47 offen lds\n"
"v_mfma_f32_16x16x32_bf16 v[252:255], a[220:223], v[36:39], v[252:255]\n"
"v_exp_f32_e32 v23, v23\n"
"v_mfma_f32_16x16x32_bf16 a[248:251], a[224:227], v[36:39], a[248:251]\n"
"ds_read_b128 a[156:159], v2 offset:26112\n"
"v_cvt_pk_bf16_f32 v35, v22, v23\n"
"v_mfma_f32_16x16x32_bf16 v[16:19], a[96:99], a[32:35], 0\n"
"v_mul_f32_e32 v24, s44, v24\n"
"v_mul_f32_e32 v25, s44, v25\n"
"v_mfma_f32_16x16x32_bf16 v[16:19], a[100:103], a[36:39], v[16:19]\n"
"v_exp_f32_e32 v24, v24\n"
"v_mfma_f32_16x16x32_bf16 v[16:19], a[104:107], a[40:43], v[16:19]\n"
"ds_read_b64_tr_b16 a[192:193], v3 offset:24576\n"
"v_mul_f32_e32 v26, s44, v26\n"
"v_mfma_f32_16x16x32_bf16 v[16:19], a[108:111], a[44:47], v[16:19]\n"
"v_exp_f32_e32 v25, v25\n"
"v_mfma_f32_16x16x32_bf16 v[20:23], a[112:115], a[32:35], 0\n"
"v_mul_f32_e32 v27, s44, v27\n"
"v_cvt_pk_bf16_f32 v36, v24, v25\n"
"v_mfma_f32_16x16x32_bf16 v[20:23], a[116:119], a[36:39], v[20:23]\n"
"v_exp_f32_e32 v26, v26\n"
"v_mfma_f32_16x16x32_bf16 v[20:23], a[120:123], a[40:43], v[20:23]\n"
"ds_read_b64_tr_b16 a[194:195], v4 offset:24576\n"
"v_mul_f32_e32 v28, s44, v28\n"
"v_mfma_f32_16x16x32_bf16 v[20:23], a[124:127], a[44:47], v[20:23]\n"
"v_exp_f32_e32 v27, v27\n"
"v_mfma_f32_16x16x32_bf16 v[64:67], a[160:163], v[32:35], v[64:67]\n"
"v_mul_f32_e32 v29, s44, v29\n"
"v_cvt_pk_bf16_f32 v37, v26, v27\n"
"v_mfma_f32_16x16x32_bf16 v[68:71], a[164:167], v[32:35], v[68:71]\n"
"v_exp_f32_e32 v28, v28\n"
"v_mfma_f32_16x16x32_bf16 v[72:75], a[168:171], v[32:35], v[72:75]\n"
"ds_read_b64_tr_b16 a[196:197], v11 offset:24576\n"
"v_mul_f32_e32 v30, s44, v30\n"
"v_mfma_f32_16x16x32_bf16 v[76:79], a[172:175], v[32:35], v[76:79]\n"
"v_exp_f32_e32 v29, v29\n"
"v_mfma_f32_16x16x32_bf16 v[80:83], a[176:179], v[32:35], v[80:83]\n"
"s_add_u32 m0, s48, 49152\n"
"v_mul_f32_e32 v31, s44, v31\n"
"v_cvt_pk_bf16_f32 v38, v28, v29\n"
"v_mfma_f32_16x16x32_bf16 v[84:87], a[180:183], v[32:35], v[84:87]\n"
"s_add_u32 s76, s45, 81920\n"
"v_exp_f32_e32 v30, v30\n"
"v_mfma_f32_16x16x32_bf16 v[88:91], a[184:187], v[32:35], v[88:91]\n"
"buffer_load_dwordx4 v6, s[28:31], s76 offen lds\n"
"v_mfma_f32_16x16x32_bf16 v[92:95], a[188:191], v[32:35], v[92:95]\n"
"v_exp_f32_e32 v31, v31\n"
"v_mfma_f32_16x16x32_bf16 a[228:231], a[224:227], v[32:35], a[228:231]\n"
"ds_read_b64_tr_b16 a[198:199], v14 offset:24576\n"
"v_cvt_pk_bf16_f32 v39, v30, v31\n"
"v_mfma_f32_16x16x32_bf16 v[24:27], a[96:99], a[48:51], 0\n"
"v_mul_f32_e32 v16, s44, v16\n"
"v_mul_f32_e32 v17, s44, v17\n"
"v_mfma_f32_16x16x32_bf16 v[24:27], a[100:103], a[52:55], v[24:27]\n"
"v_exp_f32_e32 v16, v16\n"
"v_mfma_f32_16x16x32_bf16 v[24:27], a[104:107], a[56:59], v[24:27]\n"
"ds_read_b64_tr_b16 a[200:201], v3 offset:25088\n"
"v_mul_f32_e32 v18, s44, v18\n"
"v_mfma_f32_16x16x32_bf16 v[24:27], a[108:111], a[60:63], v[24:27]\n"
"v_exp_f32_e32 v17, v17\n"
"v_mfma_f32_16x16x32_bf16 v[28:31], a[112:115], a[48:51], 0\n"
"v_mul_f32_e32 v19, s44, v19\n"
"v_cvt_pk_bf16_f32 v32, v16, v17\n"
"v_mfma_f32_16x16x32_bf16 v[28:31], a[116:119], a[52:55], v[28:31]\n"
"v_exp_f32_e32 v18, v18\n"
"v_mfma_f32_16x16x32_bf16 v[28:31], a[120:123], a[56:59], v[28:31]\n"
"ds_read_b64_tr_b16 a[202:203], v4 offset:25088\n"
"v_mul_f32_e32 v20, s44, v20\n"
"v_mfma_f32_16x16x32_bf16 v[28:31], a[124:127], a[60:63], v[28:31]\n"
"v_exp_f32_e32 v19, v19\n"
"v_mfma_f32_16x16x32_bf16 v[96:99], a[160:163], v[36:39], v[96:99]\n"
"v_mul_f32_e32 v21, s44, v21\n"
"v_cvt_pk_bf16_f32 v33, v18, v19\n"
"v_mfma_f32_16x16x32_bf16 v[100:103], a[164:167], v[36:39], v[100:103]\n"
"v_exp_f32_e32 v20, v20\n"
"v_mfma_f32_16x16x32_bf16 v[104:107], a[168:171], v[36:39], v[104:107]\n"
"ds_read_b64_tr_b16 a[204:205], v11 offset:25088\n"
"v_mul_f32_e32 v22, s44, v22\n"
"v_mfma_f32_16x16x32_bf16 v[108:111], a[172:175], v[36:39], v[108:111]\n"
"v_exp_f32_e32 v21, v21\n"
"v_mfma_f32_16x16x32_bf16 v[112:115], a[176:179], v[36:39], v[112:115]\n"
"s_add_u32 m0, s48, 50176\n"
"v_mul_f32_e32 v23, s44, v23\n"
"v_cvt_pk_bf16_f32 v34, v20, v21\n"
"v_mfma_f32_16x16x32_bf16 v[116:119], a[180:183], v[36:39], v[116:119]\n"
"s_add_u32 s76, s45, 82048\n"
"v_exp_f32_e32 v22, v22\n"
"v_mfma_f32_16x16x32_bf16 v[120:123], a[184:187], v[36:39], v[120:123]\n"
"buffer_load_dwordx4 v6, s[28:31], s76 offen lds\n"
"v_mfma_f32_16x16x32_bf16 v[124:127], a[188:191], v[36:39], v[124:127]\n"
"v_exp_f32_e32 v23, v23\n"
"v_mfma_f32_16x16x32_bf16 a[232:235], a[224:227], v[36:39], a[232:235]\n"
"ds_read_b64_tr_b16 a[206:207], v14 offset:25088\n"
"v_cvt_pk_bf16_f32 v35, v22, v23\n"
"v_mfma_f32_16x16x32_bf16 v[16:19], a[96:99], a[64:67], 0\n"
"v_mul_f32_e32 v24, s44, v24\n"
"v_mul_f32_e32 v25, s44, v25\n"
"v_mfma_f32_16x16x32_bf16 v[16:19], a[100:103], a[68:71], v[16:19]\n"
"v_exp_f32_e32 v24, v24\n"
"v_mfma_f32_16x16x32_bf16 v[16:19], a[104:107], a[72:75], v[16:19]\n"
"ds_read_b64_tr_b16 a[208:209], v3 offset:25600\n"
"v_mul_f32_e32 v26, s44, v26\n"
"v_mfma_f32_16x16x32_bf16 v[16:19], a[108:111], a[76:79], v[16:19]\n"
"v_exp_f32_e32 v25, v25\n"
"v_mfma_f32_16x16x32_bf16 v[20:23], a[112:115], a[64:67], 0\n"
"v_mul_f32_e32 v27, s44, v27\n"
"v_cvt_pk_bf16_f32 v36, v24, v25\n"
"v_mfma_f32_16x16x32_bf16 v[20:23], a[116:119], a[68:71], v[20:23]\n"
"v_exp_f32_e32 v26, v26\n"
"v_mfma_f32_16x16x32_bf16 v[20:23], a[120:123], a[72:75], v[20:23]\n"
"ds_read_b64_tr_b16 a[210:211], v4 offset:25600\n"
"v_mul_f32_e32 v28, s44, v28\n"
"v_mfma_f32_16x16x32_bf16 v[20:23], a[124:127], a[76:79], v[20:23]\n"
"v_exp_f32_e32 v27, v27\n"
"v_mfma_f32_16x16x32_bf16 v[128:131], a[160:163], v[32:35], v[128:131]\n"
"v_mul_f32_e32 v29, s44, v29\n"
"v_cvt_pk_bf16_f32 v37, v26, v27\n"
"v_mfma_f32_16x16x32_bf16 v[132:135], a[164:167], v[32:35], v[132:135]\n"
"v_exp_f32_e32 v28, v28\n"
"v_mfma_f32_16x16x32_bf16 v[136:139], a[168:171], v[32:35], v[136:139]\n"
"ds_read_b64_tr_b16 a[212:213], v11 offset:25600\n"
"v_mul_f32_e32 v30, s44, v30\n"
"v_mfma_f32_16x16x32_bf16 v[140:143], a[172:175], v[32:35], v[140:143]\n"
"v_exp_f32_e32 v29, v29\n"
"v_mfma_f32_16x16x32_bf16 v[144:147], a[176:179], v[32:35], v[144:147]\n"
"s_add_u32 m0, s49, 10240\n"
"v_mul_f32_e32 v31, s44, v31\n"
"v_cvt_pk_bf16_f32 v38, v28, v29\n"
"v_mfma_f32_16x16x32_bf16 v[148:151], a[180:183], v[32:35], v[148:151]\n"
"s_add_u32 s61, s58, 8192\n"
"v_exp_f32_e32 v30, v30\n"
"v_mfma_f32_16x16x32_bf16 v[152:155], a[184:187], v[32:35], v[152:155]\n"
"buffer_load_dwordx4 v8, s[36:39], s61 offen  nt lds\n"
"v_mfma_f32_16x16x32_bf16 v[156:159], a[188:191], v[32:35], v[156:159]\n"
"v_exp_f32_e32 v31, v31\n"
"v_mfma_f32_16x16x32_bf16 a[236:239], a[224:227], v[32:35], a[236:239]\n"
"ds_read_b64_tr_b16 a[214:215], v14 offset:25600\n"
"v_cvt_pk_bf16_f32 v39, v30, v31\n"
"v_mfma_f32_16x16x32_bf16 v[24:27], a[96:99], a[80:83], 0\n"
"ds_read_b64_tr_b16 a[222:223], v14 offset:26112\n"
"v_mul_f32_e32 v16, s44, v16\n"
"v_mul_f32_e32 v17, s44, v17\n"
"v_mfma_f32_16x16x32_bf16 v[24:27], a[100:103], a[84:87], v[24:27]\n"
"v_exp_f32_e32 v16, v16\n"
"v_mfma_f32_16x16x32_bf16 v[24:27], a[104:107], a[88:91], v[24:27]\n"
"ds_read_b64_tr_b16 a[216:217], v3 offset:26112\n"
"v_mul_f32_e32 v18, s44, v18\n"
"v_mfma_f32_16x16x32_bf16 v[24:27], a[108:111], a[92:95], v[24:27]\n"
"v_exp_f32_e32 v17, v17\n"
"v_mfma_f32_16x16x32_bf16 v[28:31], a[112:115], a[80:83], 0\n"
"v_mul_f32_e32 v19, s44, v19\n"
"v_cvt_pk_bf16_f32 v32, v16, v17\n"
"v_mfma_f32_16x16x32_bf16 v[28:31], a[116:119], a[84:87], v[28:31]\n"
"v_exp_f32_e32 v18, v18\n"
"v_mfma_f32_16x16x32_bf16 v[28:31], a[120:123], a[88:91], v[28:31]\n"
"ds_read_b64_tr_b16 a[218:219], v4 offset:26112\n"
"v_mul_f32_e32 v20, s44, v20\n"
"v_mfma_f32_16x16x32_bf16 v[28:31], a[124:127], a[92:95], v[28:31]\n"
"v_exp_f32_e32 v19, v19\n"
"v_mfma_f32_16x16x32_bf16 v[160:163], a[160:163], v[36:39], v[160:163]\n"
"v_mul_f32_e32 v21, s44, v21\n"
"v_cvt_pk_bf16_f32 v33, v18, v19\n"
"v_mfma_f32_16x16x32_bf16 v[164:167], a[164:167], v[36:39], v[164:167]\n"
"v_exp_f32_e32 v20, v20\n"
"v_mfma_f32_16x16x32_bf16 v[168:171], a[168:171], v[36:39], v[168:171]\n"
"ds_read_b64_tr_b16 a[220:221], v11 offset:26112\n"
"v_mul_f32_e32 v22, s44, v22\n"
"v_mfma_f32_16x16x32_bf16 v[172:175], a[172:175], v[36:39], v[172:175]\n"
"v_exp_f32_e32 v21, v21\n"
"v_mfma_f32_16x16x32_bf16 v[176:179], a[176:179], v[36:39], v[176:179]\n"
"s_add_u32 m0, s49, 11264\n"
"v_mul_f32_e32 v23, s44, v23\n"
"v_cvt_pk_bf16_f32 v34, v20, v21\n"
"v_mfma_f32_16x16x32_bf16 v[180:183], a[180:183], v[36:39], v[180:183]\n"
"s_add_u32 s61, s58, 8320\n"
"v_exp_f32_e32 v22, v22\n"
"v_mfma_f32_16x16x32_bf16 v[184:187], a[184:187], v[36:39], v[184:187]\n"
"buffer_load_dwordx4 v8, s[36:39], s61 offen  nt lds\n"
"v_mfma_f32_16x16x32_bf16 v[188:191], a[188:191], v[36:39], v[188:191]\n"
"v_exp_f32_e32 v23, v23\n"
"v_mfma_f32_16x16x32_bf16 a[240:243], a[224:227], v[36:39], a[240:243]\n"
"s_waitcnt lgkmcnt(15)\n"
"v_cvt_pk_bf16_f32 v35, v22, v23\n"
"v_mfma_f32_16x16x32_bf16 v[16:19], a[128:131], a[0:3], 0\n"
"v_mul_f32_e32 v24, s44, v24\n"
"v_mul_f32_e32 v25, s44, v25\n"
"v_mfma_f32_16x16x32_bf16 v[16:19], a[132:135], a[4:7], v[16:19]\n"
"v_exp_f32_e32 v24, v24\n"
"v_mfma_f32_16x16x32_bf16 v[16:19], a[136:139], a[8:11], v[16:19]\n"
"v_mul_f32_e32 v26, s44, v26\n"
"v_mfma_f32_16x16x32_bf16 v[16:19], a[140:143], a[12:15], v[16:19]\n"
"v_exp_f32_e32 v25, v25\n"
"v_mfma_f32_16x16x32_bf16 v[20:23], a[144:147], a[0:3], 0\n"
"v_mul_f32_e32 v27, s44, v27\n"
"v_cvt_pk_bf16_f32 v36, v24, v25\n"
"v_mfma_f32_16x16x32_bf16 v[20:23], a[148:151], a[4:7], v[20:23]\n"
"s_waitcnt vmcnt(19) lgkmcnt(0)\n"
"s_barrier\n"
"v_exp_f32_e32 v26, v26\n"
"v_mfma_f32_16x16x32_bf16 v[20:23], a[152:155], a[8:11], v[20:23]\n"
"ds_read_b128 a[96:99], v1 offset:0\n"
"ds_read_b128 a[108:111], v1 offset:1536\n"
"v_mul_f32_e32 v28, s44, v28\n"
"v_mfma_f32_16x16x32_bf16 v[20:23], a[156:159], a[12:15], v[20:23]\n"
"ds_read_b128 v[40:43], v58 offset:12288\n"
"v_exp_f32_e32 v27, v27\n"
"v_mfma_f32_16x16x32_bf16 v[192:195], a[160:163], v[32:35], v[192:195]\n"
"ds_read_b128 v[44:47], v59 offset:13312\n"
"v_mul_f32_e32 v29, s44, v29\n"
"v_cvt_pk_bf16_f32 v37, v26, v27\n"
"v_mfma_f32_16x16x32_bf16 v[196:199], a[164:167], v[32:35], v[196:199]\n"
"ds_read_b128 v[48:51], v58 offset:14336\n"
"v_exp_f32_e32 v28, v28\n"
"v_mfma_f32_16x16x32_bf16 v[200:203], a[168:171], v[32:35], v[200:203]\n"
"ds_read_b128 a[100:103], v1 offset:512\n"
"ds_read_b128 v[52:55], v59 offset:15360\n"
"v_mul_f32_e32 v30, s44, v30\n"
"v_mfma_f32_16x16x32_bf16 v[204:207], a[172:175], v[32:35], v[204:207]\n"
"v_exp_f32_e32 v29, v29\n"
"v_mfma_f32_16x16x32_bf16 v[208:211], a[176:179], v[32:35], v[208:211]\n"
"s_add_u32 m0, s48, 24576\n"
"v_mul_f32_e32 v31, s44, v31\n"
"v_cvt_pk_bf16_f32 v38, v28, v29\n"
"v_mfma_f32_16x16x32_bf16 v[212:215], a[180:183], v[32:35], v[212:215]\n"
"s_add_u32 s47, s45, 90112\n"
"v_exp_f32_e32 v30, v30\n"
"v_mfma_f32_16x16x32_bf16 v[216:219], a[184:187], v[32:35], v[216:219]\n"
"buffer_load_dwordx4 v6, s[24:27], s47 offen lds\n"
"v_mfma_f32_16x16x32_bf16 v[220:223], a[188:191], v[32:35], v[220:223]\n"
"v_exp_f32_e32 v31, v31\n"
"v_mfma_f32_16x16x32_bf16 a[244:247], a[224:227], v[32:35], a[244:247]\n"
"ds_read_b128 a[104:107], v1 offset:1024\n"
"v_cvt_pk_bf16_f32 v39, v30, v31\n"
"v_mfma_f32_16x16x32_bf16 v[24:27], a[128:131], a[16:19], 0\n"
"v_mul_f32_e32 v16, s44, v16\n"
"v_mul_f32_e32 v17, s44, v17\n"
"v_mfma_f32_16x16x32_bf16 v[24:27], a[132:135], a[20:23], v[24:27]\n"
"v_exp_f32_e32 v16, v16\n"
"s_add_u32 s77, s60, 12288\n"
"v_mfma_f32_16x16x32_bf16 v[24:27], a[136:139], a[24:27], v[24:27]\n"
"ds_read_b128 a[112:115], v2 offset:0\n"
"v_mul_f32_e32 v18, s44, v18\n"
"s_waitcnt lgkmcnt(6)\n"
"buffer_store_dwordx4 v[40:43], v9, s[40:43], s77 offen offset:0  sc1\n"
"v_mfma_f32_16x16x32_bf16 v[24:27], a[140:143], a[28:31], v[24:27]\n"
"v_exp_f32_e32 v17, v17\n"
"v_mfma_f32_16x16x32_bf16 v[28:31], a[144:147], a[16:19], 0\n"
"v_mul_f32_e32 v19, s44, v19\n"
"v_cvt_pk_bf16_f32 v32, v16, v17\n"
"v_mfma_f32_16x16x32_bf16 v[28:31], a[148:151], a[20:23], v[28:31]\n"
"v_exp_f32_e32 v18, v18\n"
"v_mfma_f32_16x16x32_bf16 v[28:31], a[152:155], a[24:27], v[28:31]\n"
"ds_read_b128 a[116:119], v2 offset:512\n"
"v_mul_f32_e32 v20, s44, v20\n"
"s_waitcnt lgkmcnt(6)\n"
"buffer_store_dwordx4 v[44:47], v9, s[40:43], s77 offen offset:1024  sc1\n"
"v_mfma_f32_16x16x32_bf16 v[28:31], a[156:159], a[28:31], v[28:31]\n"
"v_exp_f32_e32 v19, v19\n"
"v_mfma_f32_16x16x32_bf16 v[224:227], a[160:163], v[36:39], v[224:227]\n"
"v_mul_f32_e32 v21, s44, v21\n"
"v_cvt_pk_bf16_f32 v33, v18, v19\n"
"v_mfma_f32_16x16x32_bf16 v[228:231], a[164:167], v[36:39], v[228:231]\n"
"v_exp_f32_e32 v20, v20\n"
"v_mfma_f32_16x16x32_bf16 v[232:235], a[168:171], v[36:39], v[232:235]\n"
"ds_read_b128 a[120:123], v2 offset:1024\n"
"v_mul_f32_e32 v22, s44, v22\n"
"s_waitcnt lgkmcnt(6)\n"
"buffer_store_dwordx4 v[48:51], v9, s[40:43], s77 offen offset:2048  sc1\n"
"v_mfma_f32_16x16x32_bf16 v[236:239], a[172:175], v[36:39], v[236:239]\n"
"v_exp_f32_e32 v21, v21\n"
"v_mfma_f32_16x16x32_bf16 v[240:243], a[176:179], v[36:39], v[240:243]\n"
"s_add_u32 m0, s48, 25600\n"
"v_mul_f32_e32 v23, s44, v23\n"
"v_cvt_pk_bf16_f32 v34, v20, v21\n"
"v_mfma_f32_16x16x32_bf16 v[244:247], a[180:183], v[36:39], v[244:247]\n"
"s_add_u32 s47, s45, 90240\n"
"v_exp_f32_e32 v22, v22\n"
"v_mfma_f32_16x16x32_bf16 v[248:251], a[184:187], v[36:39], v[248:251]\n"
"buffer_load_dwordx4 v6, s[24:27], s47 offen lds\n"
"v_mfma_f32_16x16x32_bf16 v[252:255], a[188:191], v[36:39], v[252:255]\n"
"v_exp_f32_e32 v23, v23\n"
"v_mfma_f32_16x16x32_bf16 a[248:251], a[224:227], v[36:39], a[248:251]\n"
"ds_read_b128 a[124:127], v2 offset:1536\n"
"v_cvt_pk_bf16_f32 v35, v22, v23\n"
"s_waitcnt lgkmcnt(5)\n"
"buffer_store_dwordx4 v[52:55], v9, s[40:43], s77 offen offset:3072  sc1\n"
"v_mfma_f32_16x16x32_bf16 v[16:19], a[128:131], a[32:35], 0\n"
"v_mul_f32_e32 v24, s44, v24\n"
"v_mul_f32_e32 v25, s44, v25\n"
"v_mfma_f32_16x16x32_bf16 v[16:19], a[132:135], a[36:39], v[16:19]\n"
"v_exp_f32_e32 v24, v24\n"
"v_mfma_f32_16x16x32_bf16 v[16:19], a[136:139], a[40:43], v[16:19]\n"
"ds_read_b64_tr_b16 a[160:161], v3 offset:0\n"
"v_mul_f32_e32 v26, s44, v26\n"
"v_mfma_f32_16x16x32_bf16 v[16:19], a[140:143], a[44:47], v[16:19]\n"
"v_exp_f32_e32 v25, v25\n"
"v_mfma_f32_16x16x32_bf16 v[20:23], a[144:147], a[32:35], 0\n"
"v_mul_f32_e32 v27, s44, v27\n"
"v_cvt_pk_bf16_f32 v36, v24, v25\n"
"v_mfma_f32_16x16x32_bf16 v[20:23], a[148:151], a[36:39], v[20:23]\n"
"v_exp_f32_e32 v26, v26\n"
"v_mfma_f32_16x16x32_bf16 v[20:23], a[152:155], a[40:43], v[20:23]\n"
"ds_read_b64_tr_b16 a[162:163], v4 offset:0\n"
"v_mul_f32_e32 v28, s44, v28\n"
"v_mfma_f32_16x16x32_bf16 v[20:23], a[156:159], a[44:47], v[20:23]\n"
"v_exp_f32_e32 v27, v27\n"
"v_mfma_f32_16x16x32_bf16 v[64:67], a[192:195], v[32:35], v[64:67]\n"
"v_mul_f32_e32 v29, s44, v29\n"
"v_cvt_pk_bf16_f32 v37, v26, v27\n"
"v_mfma_f32_16x16x32_bf16 v[68:71], a[196:199], v[32:35], v[68:71]\n"
"v_exp_f32_e32 v28, v28\n"
"v_mfma_f32_16x16x32_bf16 v[72:75], a[200:203], v[32:35], v[72:75]\n"
"ds_read_b64_tr_b16 a[164:165], v11 offset:0\n"
"v_mul_f32_e32 v30, s44, v30\n"
"v_mfma_f32_16x16x32_bf16 v[76:79], a[204:207], v[32:35], v[76:79]\n"
"v_exp_f32_e32 v29, v29\n"
"v_mfma_f32_16x16x32_bf16 v[80:83], a[208:211], v[32:35], v[80:83]\n"
"s_add_u32 m0, s48, 57344\n"
"v_mul_f32_e32 v31, s44, v31\n"
"v_cvt_pk_bf16_f32 v38, v28, v29\n"
"v_mfma_f32_16x16x32_bf16 v[84:87], a[212:215], v[32:35], v[84:87]\n"
"s_add_u32 s76, s45, 90112\n"
"v_exp_f32_e32 v30, v30\n"
"v_mfma_f32_16x16x32_bf16 v[88:91], a[216:219], v[32:35], v[88:91]\n"
"buffer_load_dwordx4 v6, s[28:31], s76 offen lds\n"
"v_mfma_f32_16x16x32_bf16 v[92:95], a[220:223], v[32:35], v[92:95]\n"
"v_exp_f32_e32 v31, v31\n"
"v_mfma_f32_16x16x32_bf16 a[228:231], a[224:227], v[32:35], a[228:231]\n"
"ds_read_b64_tr_b16 a[166:167], v14 offset:0\n"
"v_cvt_pk_bf16_f32 v39, v30, v31\n"
"v_mfma_f32_16x16x32_bf16 v[24:27], a[128:131], a[48:51], 0\n"
"v_mul_f32_e32 v16, s44, v16\n"
"v_mul_f32_e32 v17, s44, v17\n"
"v_mfma_f32_16x16x32_bf16 v[24:27], a[132:135], a[52:55], v[24:27]\n"
"v_exp_f32_e32 v16, v16\n"
"v_mfma_f32_16x16x32_bf16 v[24:27], a[136:139], a[56:59], v[24:27]\n"
"ds_read_b64_tr_b16 a[168:169], v3 offset:512\n"
"v_mul_f32_e32 v18, s44, v18\n"
"v_mfma_f32_16x16x32_bf16 v[24:27], a[140:143], a[60:63], v[24:27]\n"
"v_exp_f32_e32 v17, v17\n"
"v_mfma_f32_16x16x32_bf16 v[28:31], a[144:147], a[48:51], 0\n"
"v_mul_f32_e32 v19, s44, v19\n"
"v_cvt_pk_bf16_f32 v32, v16, v17\n"
"v_mfma_f32_16x16x32_bf16 v[28:31], a[148:151], a[52:55], v[28:31]\n"
"v_exp_f32_e32 v18, v18\n"
"v_mfma_f32_16x16x32_bf16 v[28:31], a[152:155], a[56:59], v[28:31]\n"
"ds_read_b64_tr_b16 a[170:171], v4 offset:512\n"
"v_mul_f32_e32 v20, s44, v20\n"
"v_mfma_f32_16x16x32_bf16 v[28:31], a[156:159], a[60:63], v[28:31]\n"
"v_exp_f32_e32 v19, v19\n"
"v_mfma_f32_16x16x32_bf16 v[96:99], a[192:195], v[36:39], v[96:99]\n"
"v_mul_f32_e32 v21, s44, v21\n"
"v_cvt_pk_bf16_f32 v33, v18, v19\n"
"v_mfma_f32_16x16x32_bf16 v[100:103], a[196:199], v[36:39], v[100:103]\n"
"v_exp_f32_e32 v20, v20\n"
"v_mfma_f32_16x16x32_bf16 v[104:107], a[200:203], v[36:39], v[104:107]\n"
"ds_read_b64_tr_b16 a[172:173], v11 offset:512\n"
"v_mul_f32_e32 v22, s44, v22\n"
"v_mfma_f32_16x16x32_bf16 v[108:111], a[204:207], v[36:39], v[108:111]\n"
"v_exp_f32_e32 v21, v21\n"
"v_mfma_f32_16x16x32_bf16 v[112:115], a[208:211], v[36:39], v[112:115]\n"
"s_add_u32 m0, s48, 58368\n"
"v_mul_f32_e32 v23, s44, v23\n"
"v_cvt_pk_bf16_f32 v34, v20, v21\n"
"v_mfma_f32_16x16x32_bf16 v[116:119], a[212:215], v[36:39], v[116:119]\n"
"s_add_u32 s76, s45, 90240\n"
"v_exp_f32_e32 v22, v22\n"
"v_mfma_f32_16x16x32_bf16 v[120:123], a[216:219], v[36:39], v[120:123]\n"
"buffer_load_dwordx4 v6, s[28:31], s76 offen lds\n"
"v_mfma_f32_16x16x32_bf16 v[124:127], a[220:223], v[36:39], v[124:127]\n"
"v_exp_f32_e32 v23, v23\n"
"v_mfma_f32_16x16x32_bf16 a[232:235], a[224:227], v[36:39], a[232:235]\n"
"ds_read_b64_tr_b16 a[174:175], v14 offset:512\n"
"v_cvt_pk_bf16_f32 v35, v22, v23\n"
"v_mfma_f32_16x16x32_bf16 v[16:19], a[128:131], a[64:67], 0\n"
"ds_read_b64_tr_b16 a[182:183], v14 offset:1024\n"
"v_mul_f32_e32 v24, s44, v24\n"
"v_mul_f32_e32 v25, s44, v25\n"
"v_mfma_f32_16x16x32_bf16 v[16:19], a[132:135], a[68:71], v[16:19]\n"
"v_exp_f32_e32 v24, v24\n"
"v_mfma_f32_16x16x32_bf16 v[16:19], a[136:139], a[72:75], v[16:19]\n"
"ds_read_b64_tr_b16 a[176:177], v3 offset:1024\n"
"v_mul_f32_e32 v26, s44, v26\n"
"v_mfma_f32_16x16x32_bf16 v[16:19], a[140:143], a[76:79], v[16:19]\n"
"v_exp_f32_e32 v25, v25\n"
"v_mfma_f32_16x16x32_bf16 v[20:23], a[144:147], a[64:67], 0\n"
"v_mul_f32_e32 v27, s44, v27\n"
"v_cvt_pk_bf16_f32 v36, v24, v25\n"
"v_mfma_f32_16x16x32_bf16 v[20:23], a[148:151], a[68:71], v[20:23]\n"
"v_exp_f32_e32 v26, v26\n"
"v_mfma_f32_16x16x32_bf16 v[20:23], a[152:155], a[72:75], v[20:23]\n"
"ds_read_b64_tr_b16 a[178:179], v4 offset:1024\n"
"v_mul_f32_e32 v28, s44, v28\n"
"v_mfma_f32_16x16x32_bf16 v[20:23], a[156:159], a[76:79], v[20:23]\n"
"v_exp_f32_e32 v27, v27\n"
"v_mfma_f32_16x16x32_bf16 v[128:131], a[192:195], v[32:35], v[128:131]\n"
"v_mul_f32_e32 v29, s44, v29\n"
"v_cvt_pk_bf16_f32 v37, v26, v27\n"
"v_mfma_f32_16x16x32_bf16 v[132:135], a[196:199], v[32:35], v[132:135]\n"
"v_exp_f32_e32 v28, v28\n"
"v_mfma_f32_16x16x32_bf16 v[136:139], a[200:203], v[32:35], v[136:139]\n"
"ds_read_b64_tr_b16 a[180:181], v11 offset:1024\n"
"v_mul_f32_e32 v30, s44, v30\n"
"v_mfma_f32_16x16x32_bf16 v[140:143], a[204:207], v[32:35], v[140:143]\n"
"v_exp_f32_e32 v29, v29\n"
"v_mfma_f32_16x16x32_bf16 v[144:147], a[208:211], v[32:35], v[144:147]\n"
"v_mul_f32_e32 v31, s44, v31\n"
"v_cvt_pk_bf16_f32 v38, v28, v29\n"
"v_mfma_f32_16x16x32_bf16 v[148:151], a[212:215], v[32:35], v[148:151]\n"
"s_mov_b32 s88, L_T66%=+0-L_code%=\n"
"v_exp_f32_e32 v30, v30\n"
"v_mfma_f32_16x16x32_bf16 v[152:155], a[216:219], v[32:35], v[152:155]\n"
"s_add_u32 m0, s49, 12288\n"
"buffer_load_dword v63, v62, s[84:87], s88 offen\n"
"v_mfma_f32_16x16x32_bf16 v[156:159], a[220:223], v[32:35], v[156:159]\n"
"s_add_u32 s61, s58, 12288\n"
"v_exp_f32_e32 v31, v31\n"
"v_mfma_f32_16x16x32_bf16 a[236:239], a[224:227], v[32:35], a[236:239]\n"
"buffer_load_dwordx4 v7, s[36:39], s61 offen  nt lds\n"
"v_cvt_pk_bf16_f32 v39, v30, v31\n"
"v_mfma_f32_16x16x32_bf16 v[24:27], a[128:131], a[80:83], 0\n"
"ds_read_b64_tr_b16 a[190:191], v14 offset:1536\n"
"v_mul_f32_e32 v16, s44, v16\n"
"v_mul_f32_e32 v17, s44, v17\n"
"v_mfma_f32_16x16x32_bf16 v[24:27], a[132:135], a[84:87], v[24:27]\n"
"v_exp_f32_e32 v16, v16\n"
"v_mfma_f32_16x16x32_bf16 v[24:27], a[136:139], a[88:91], v[24:27]\n"
"ds_read_b64_tr_b16 a[184:185], v3 offset:1536\n"
"v_mul_f32_e32 v18, s44, v18\n"
"v_mfma_f32_16x16x32_bf16 v[24:27], a[140:143], a[92:95], v[24:27]\n"
"v_exp_f32_e32 v17, v17\n"
"v_mfma_f32_16x16x32_bf16 v[28:31], a[144:147], a[80:83], 0\n"
"v_mul_f32_e32 v19, s44, v19\n"
"v_cvt_pk_bf16_f32 v32, v16, v17\n"
"v_mfma_f32_16x16x32_bf16 v[28:31], a[148:151], a[84:87], v[28:31]\n"
"v_exp_f32_e32 v18, v18\n"
"v_mfma_f32_16x16x32_bf16 v[28:31], a[152:155], a[88:91], v[28:31]\n"
"ds_read_b64_tr_b16 a[186:187], v4 offset:1536\n"
"v_mul_f32_e32 v20, s44, v20\n"
"v_mfma_f32_16x16x32_bf16 v[28:31], a[156:159], a[92:95], v[28:31]\n"
"v_exp_f32_e32 v19, v19\n"
"v_mfma_f32_16x16x32_bf16 v[160:163], a[192:195], v[36:39], v[160:163]\n"
"v_mul_f32_e32 v21, s44, v21\n"
"v_cvt_pk_bf16_f32 v33, v18, v19\n"
"v_mfma_f32_16x16x32_bf16 v[164:167], a[196:199], v[36:39], v[164:167]\n"
"v_exp_f32_e32 v20, v20\n"
"v_mfma_f32_16x16x32_bf16 v[168:171], a[200:203], v[36:39], v[168:171]\n"
"ds_read_b64_tr_b16 a[188:189], v11 offset:1536\n"
"v_mul_f32_e32 v22, s44, v22\n"
"v_mfma_f32_16x16x32_bf16 v[172:175], a[204:207], v[36:39], v[172:175]\n"
"v_exp_f32_e32 v21, v21\n"
"v_mfma_f32_16x16x32_bf16 v[176:179], a[208:211], v[36:39], v[176:179]\n"
"s_add_u32 m0, s49, 13312\n"
"v_mul_f32_e32 v23, s44, v23\n"
"v_cvt_pk_bf16_f32 v34, v20, v21\n"
"v_mfma_f32_16x16x32_bf16 v[180:183], a[212:215], v[36:39], v[180:183]\n"
"s_add_u32 s61, s58, 12416\n"
"v_exp_f32_e32 v22, v22\n"
"v_mfma_f32_16x16x32_bf16 v[184:187], a[216:219], v[36:39], v[184:187]\n"
"buffer_load_dwordx4 v7, s[36:39], s61 offen  nt lds\n"
"v_mfma_f32_16x16x32_bf16 v[188:191], a[220:223], v[36:39], v[188:191]\n"
"v_exp_f32_e32 v23, v23\n"
"v_mfma_f32_16x16x32_bf16 a[240:243], a[224:227], v[36:39], a[240:243]\n"
"v_cvt_pk_bf16_f32 v35, v22, v23\n"
"v_mfma_f32_16x16x32_bf16 v[16:19], a[96:99], a[0:3], 0\n"
"v_mul_f32_e32 v24, s44, v24\n"
"v_mul_f32_e32 v25, s44, v25\n"
"v_mfma_f32_16x16x32_bf16 v[16:19], a[100:103], a[4:7], v[16:19]\n"
"s_waitcnt lgkmcnt(15)\n"
"v_exp_f32_e32 v24, v24\n"
"v_mfma_f32_16x16x32_bf16 v[16:19], a[104:107], a[8:11], v[16:19]\n"
"v_mul_f32_e32 v26, s44, v26\n"
"v_mfma_f32_16x16x32_bf16 v[16:19], a[108:111], a[12:15], v[16:19]\n"
"v_exp_f32_e32 v25, v25\n"
"v_mfma_f32_16x16x32_bf16 v[20:23], a[112:115], a[0:3], 0\n"
"v_mul_f32_e32 v27, s44, v27\n"
"v_cvt_pk_bf16_f32 v36, v24, v25\n"
"v_mfma_f32_16x16x32_bf16 v[20:23], a[116:119], a[4:7], v[20:23]\n"
"s_waitcnt vmcnt(20) lgkmcnt(0)\n"
"s_barrier\n"
"v_exp_f32_e32 v26, v26\n"
"v_mfma_f32_16x16x32_bf16 v[20:23], a[120:123], a[8:11], v[20:23]\n"
"ds_read_b128 a[128:131], v1 offset:8192\n"
"ds_read_b128 a[140:143], v1 offset:9728\n"
"v_mul_f32_e32 v28, s44, v28\n"
"v_mfma_f32_16x16x32_bf16 v[20:23], a[124:127], a[12:15], v[20:23]\n"
"v_exp_f32_e32 v27, v27\n"
"v_mfma_f32_16x16x32_bf16 v[192:195], a[192:195], v[32:35], v[192:195]\n"
"v_mul_f32_e32 v29, s44, v29\n"
"v_cvt_pk_bf16_f32 v37, v26, v27\n"
"v_mfma_f32_16x16x32_bf16 v[196:199], a[196:199], v[32:35], v[196:199]\n"
"v_exp_f32_e32 v28, v28\n"
"v_mfma_f32_16x16x32_bf16 v[200:203], a[200:203], v[32:35], v[200:203]\n"
"ds_read_b128 a[132:135], v1 offset:8704\n"
"v_mul_f32_e32 v30, s44, v30\n"
"v_mfma_f32_16x16x32_bf16 v[204:207], a[204:207], v[32:35], v[204:207]\n"
"v_exp_f32_e32 v29, v29\n"
"v_mfma_f32_16x16x32_bf16 v[208:211], a[208:211], v[32:35], v[208:211]\n"
"s_add_u32 m0, s48, 0\n"
"v_mul_f32_e32 v31, s44, v31\n"
"v_cvt_pk_bf16_f32 v38, v28, v29\n"
"v_mfma_f32_16x16x32_bf16 v[212:215], a[212:215], v[32:35], v[212:215]\n"
"s_add_u32 s47, s45, 98304\n"
"v_exp_f32_e32 v30, v30\n"
"v_mfma_f32_16x16x32_bf16 v[216:219], a[216:219], v[32:35], v[216:219]\n"
"buffer_load_dwordx4 v6, s[24:27], s47 offen lds\n"
"v_mfma_f32_16x16x32_bf16 v[220:223], a[220:223], v[32:35], v[220:223]\n"
"v_exp_f32_e32 v31, v31\n"
"v_mfma_f32_16x16x32_bf16 a[244:247], a[224:227], v[32:35], a[244:247]\n"
"ds_read_b128 a[136:139], v1 offset:9216\n"
"v_cvt_pk_bf16_f32 v39, v30, v31\n"
"v_mfma_f32_16x16x32_bf16 v[24:27], a[96:99], a[16:19], 0\n"
"v_mul_f32_e32 v16, s44, v16\n"
"v_mul_f32_e32 v17, s44, v17\n"
"v_mfma_f32_16x16x32_bf16 v[24:27], a[100:103], a[20:23], v[24:27]\n"
"v_exp_f32_e32 v16, v16\n"
"v_mfma_f32_16x16x32_bf16 v[24:27], a[104:107], a[24:27], v[24:27]\n"
"ds_read_b128 a[144:147], v2 offset:8192\n"
"v_mul_f32_e32 v18, s44, v18\n"
"v_mfma_f32_16x16x32_bf16 v[24:27], a[108:111], a[28:31], v[24:27]\n"
"v_exp_f32_e32 v17, v17\n"
"v_mfma_f32_16x16x32_bf16 v[28:31], a[112:115], a[16:19], 0\n"
"v_mul_f32_e32 v19, s44, v19\n"
"v_cvt_pk_bf16_f32 v32, v16, v17\n"
"v_mfma_f32_16x16x32_bf16 v[28:31], a[116:119], a[20:23], v[28:31]\n"
"v_exp_f32_e32 v18, v18\n"
"v_mfma_f32_16x16x32_bf16 v[28:31], a[120:123], a[24:27], v[28:31]\n"
"ds_read_b128 a[148:151], v2 offset:8704\n"
"v_mul_f32_e32 v20, s44, v20\n"
"v_mfma_f32_16x16x32_bf16 v[28:31], a[124:127], a[28:31], v[28:31]\n"
"v_exp_f32_e32 v19, v19\n"
"v_mfma_f32_16x16x32_bf16 v[224:227], a[192:195], v[36:39], v[224:227]\n"
"v_mul_f32_e32 v21, s44, v21\n"
"v_cvt_pk_bf16_f32 v33, v18, v19\n"
"v_mfma_f32_16x16x32_bf16 v[228:231], a[196:199], v[36:39], v[228:231]\n"
"v_exp_f32_e32 v20, v20\n"
"v_mfma_f32_16x16x32_bf16 v[232:235], a[200:203], v[36:39], v[232:235]\n"
"ds_read_b128 a[152:155], v2 offset:9216\n"
"v_mul_f32_e32 v22, s44, v22\n"
"v_mfma_f32_16x16x32_bf16 v[236:239], a[204:207], v[36:39], v[236:239]\n"
"v_exp_f32_e32 v21, v21\n"
"v_mfma_f32_16x16x32_bf16 v[240:243], a[208:211], v[36:39], v[240:243]\n"
"s_add_u32 m0, s48, 1024\n"
"v_mul_f32_e32 v23, s44, v23\n"
"v_cvt_pk_bf16_f32 v34, v20, v21\n"
"v_mfma_f32_16x16x32_bf16 v[244:247], a[212:215], v[36:39], v[244:247]\n"
"s_add_u32 s47, s45, 98432\n"
"v_exp_f32_e32 v22, v22\n"
"v_mfma_f32_16x16x32_bf16 v[248:251], a[216:219], v[36:39], v[248:251]\n"
"buffer_load_dwordx4 v6, s[24:27], s47 offen lds\n"
"v_mfma_f32_16x16x32_bf16 v[252:255], a[220:223], v[36:39], v[252:255]\n"
"v_exp_f32_e32 v23, v23\n"
"v_mfma_f32_16x16x32_bf16 a[248:251], a[224:227], v[36:39], a[248:251]\n"
"ds_read_b128 a[156:159], v2 offset:9728\n"
"v_cvt_pk_bf16_f32 v35, v22, v23\n"
"v_mfma_f32_16x16x32_bf16 v[16:19], a[96:99], a[32:35], 0\n"
"v_mul_f32_e32 v24, s44, v24\n"
"v_mul_f32_e32 v25, s44, v25\n"
"v_mfma_f32_16x16x32_bf16 v[16:19], a[100:103], a[36:39], v[16:19]\n"
"v_exp_f32_e32 v24, v24\n"
"v_mfma_f32_16x16x32_bf16 v[16:19], a[104:107], a[40:43], v[16:19]\n"
"ds_read_b64_tr_b16 a[192:193], v3 offset:8192\n"
"v_mul_f32_e32 v26, s44, v26\n"
"v_mfma_f32_16x16x32_bf16 v[16:19], a[108:111], a[44:47], v[16:19]\n"
"v_exp_f32_e32 v25, v25\n"
"v_mfma_f32_16x16x32_bf16 v[20:23], a[112:115], a[32:35], 0\n"
"v_mul_f32_e32 v27, s44, v27\n"
"v_cvt_pk_bf16_f32 v36, v24, v25\n"
"v_mfma_f32_16x16x32_bf16 v[20:23], a[116:119], a[36:39], v[20:23]\n"
"v_exp_f32_e32 v26, v26\n"
"v_mfma_f32_16x16x32_bf16 v[20:23], a[120:123], a[40:43], v[20:23]\n"
"ds_read_b64_tr_b16 a[194:195], v4 offset:8192\n"
"v_mul_f32_e32 v28, s44, v28\n"
"v_mfma_f32_16x16x32_bf16 v[20:23], a[124:127], a[44:47], v[20:23]\n"
"v_exp_f32_e32 v27, v27\n"
"v_mfma_f32_16x16x32_bf16 v[64:67], a[160:163], v[32:35], v[64:67]\n"
"v_mul_f32_e32 v29, s44, v29\n"
"v_cvt_pk_bf16_f32 v37, v26, v27\n"
"v_mfma_f32_16x16x32_bf16 v[68:71], a[164:167], v[32:35], v[68:71]\n"
"v_exp_f32_e32 v28, v28\n"
"v_mfma_f32_16x16x32_bf16 v[72:75], a[168:171], v[32:35], v[72:75]\n"
"ds_read_b64_tr_b16 a[196:197], v11 offset:8192\n"
"v_mul_f32_e32 v30, s44, v30\n"
"v_mfma_f32_16x16x32_bf16 v[76:79], a[172:175], v[32:35], v[76:79]\n"
"v_exp_f32_e32 v29, v29\n"
"v_mfma_f32_16x16x32_bf16 v[80:83], a[176:179], v[32:35], v[80:83]\n"
"s_add_u32 m0, s48, 32768\n"
"v_mul_f32_e32 v31, s44, v31\n"
"v_cvt_pk_bf16_f32 v38, v28, v29\n"
"v_mfma_f32_16x16x32_bf16 v[84:87], a[180:183], v[32:35], v[84:87]\n"
"s_add_u32 s76, s45, 98304\n"
"v_exp_f32_e32 v30, v30\n"
"v_mfma_f32_16x16x32_bf16 v[88:91], a[184:187], v[32:35], v[88:91]\n"
"buffer_load_dwordx4 v6, s[28:31], s76 offen lds\n"
"v_mfma_f32_16x16x32_bf16 v[92:95], a[188:191], v[32:35], v[92:95]\n"
"v_exp_f32_e32 v31, v31\n"
"v_mfma_f32_16x16x32_bf16 a[228:231], a[224:227], v[32:35], a[228:231]\n"
"ds_read_b64_tr_b16 a[198:199], v14 offset:8192\n"
"v_cvt_pk_bf16_f32 v39, v30, v31\n"
"v_mfma_f32_16x16x32_bf16 v[24:27], a[96:99], a[48:51], 0\n"
"v_mul_f32_e32 v16, s44, v16\n"
"v_mul_f32_e32 v17, s44, v17\n"
"v_mfma_f32_16x16x32_bf16 v[24:27], a[100:103], a[52:55], v[24:27]\n"
"v_exp_f32_e32 v16, v16\n"
"v_mfma_f32_16x16x32_bf16 v[24:27], a[104:107], a[56:59], v[24:27]\n"
"ds_read_b64_tr_b16 a[200:201], v3 offset:8704\n"
"v_mul_f32_e32 v18, s44, v18\n"
"v_mfma_f32_16x16x32_bf16 v[24:27], a[108:111], a[60:63], v[24:27]\n"
"v_exp_f32_e32 v17, v17\n"
"v_mfma_f32_16x16x32_bf16 v[28:31], a[112:115], a[48:51], 0\n"
"v_mul_f32_e32 v19, s44, v19\n"
"v_cvt_pk_bf16_f32 v32, v16, v17\n"
"v_mfma_f32_16x16x32_bf16 v[28:31], a[116:119], a[52:55], v[28:31]\n"
"v_exp_f32_e32 v18, v18\n"
"v_mfma_f32_16x16x32_bf16 v[28:31], a[120:123], a[56:59], v[28:31]\n"
"ds_read_b64_tr_b16 a[202:203], v4 offset:8704\n"
"v_mul_f32_e32 v20, s44, v20\n"
"v_mfma_f32_16x16x32_bf16 v[28:31], a[124:127], a[60:63], v[28:31]\n"
"v_exp_f32_e32 v19, v19\n"
"v_mfma_f32_16x16x32_bf16 v[96:99], a[160:163], v[36:39], v[96:99]\n"
"v_mul_f32_e32 v21, s44, v21\n"
"v_cvt_pk_bf16_f32 v33, v18, v19\n"
"v_mfma_f32_16x16x32_bf16 v[100:103], a[164:167], v[36:39], v[100:103]\n"
"v_exp_f32_e32 v20, v20\n"
"v_mfma_f32_16x16x32_bf16 v[104:107], a[168:171], v[36:39], v[104:107]\n"
"ds_read_b64_tr_b16 a[204:205], v11 offset:8704\n"
"v_mul_f32_e32 v22, s44, v22\n"
"v_mfma_f32_16x16x32_bf16 v[108:111], a[172:175], v[36:39], v[108:111]\n"
"v_exp_f32_e32 v21, v21\n"
"v_mfma_f32_16x16x32_bf16 v[112:115], a[176:179], v[36:39], v[112:115]\n"
"s_add_u32 m0, s48, 33792\n"
"v_mul_f32_e32 v23, s44, v23\n"
"v_cvt_pk_bf16_f32 v34, v20, v21\n"
"v_mfma_f32_16x16x32_bf16 v[116:119], a[180:183], v[36:39], v[116:119]\n"
"s_add_u32 s76, s45, 98432\n"
"v_exp_f32_e32 v22, v22\n"
"v_mfma_f32_16x16x32_bf16 v[120:123], a[184:187], v[36:39], v[120:123]\n"
"buffer_load_dwordx4 v6, s[28:31], s76 offen lds\n"
"v_mfma_f32_16x16x32_bf16 v[124:127], a[188:191], v[36:39], v[124:127]\n"
"v_exp_f32_e32 v23, v23\n"
"v_mfma_f32_16x16x32_bf16 a[232:235], a[224:227], v[36:39], a[232:235]\n"
"ds_read_b64_tr_b16 a[206:207], v14 offset:8704\n"
"v_cvt_pk_bf16_f32 v35, v22, v23\n"
"v_mfma_f32_16x16x32_bf16 v[16:19], a[96:99], a[64:67], 0\n"
"v_mul_f32_e32 v24, s44, v24\n"
"v_mul_f32_e32 v25, s44, v25\n"
"v_mfma_f32_16x16x32_bf16 v[16:19], a[100:103], a[68:71], v[16:19]\n"
"v_exp_f32_e32 v24, v24\n"
"v_mfma_f32_16x16x32_bf16 v[16:19], a[104:107], a[72:75], v[16:19]\n"
"ds_read_b64_tr_b16 a[208:209], v3 offset:9216\n"
"v_mul_f32_e32 v26, s44, v26\n"
"v_mfma_f32_16x16x32_bf16 v[16:19], a[108:111], a[76:79], v[16:19]\n"
"v_exp_f32_e32 v25, v25\n"
"v_mfma_f32_16x16x32_bf16 v[20:23], a[112:115], a[64:67], 0\n"
"v_mul_f32_e32 v27, s44, v27\n"
"v_cvt_pk_bf16_f32 v36, v24, v25\n"
"v_mfma_f32_16x16x32_bf16 v[20:23], a[116:119], a[68:71], v[20:23]\n"
"v_exp_f32_e32 v26, v26\n"
"v_mfma_f32_16x16x32_bf16 v[20:23], a[120:123], a[72:75], v[20:23]\n"
"ds_read_b64_tr_b16 a[210:211], v4 offset:9216\n"
"v_mul_f32_e32 v28, s44, v28\n"
"v_mfma_f32_16x16x32_bf16 v[20:23], a[124:127], a[76:79], v[20:23]\n"
"v_exp_f32_e32 v27, v27\n"
"v_mfma_f32_16x16x32_bf16 v[128:131], a[160:163], v[32:35], v[128:131]\n"
"v_mul_f32_e32 v29, s44, v29\n"
"v_cvt_pk_bf16_f32 v37, v26, v27\n"
"v_mfma_f32_16x16x32_bf16 v[132:135], a[164:167], v[32:35], v[132:135]\n"
"v_exp_f32_e32 v28, v28\n"
"v_mfma_f32_16x16x32_bf16 v[136:139], a[168:171], v[32:35], v[136:139]\n"
"ds_read_b64_tr_b16 a[212:213], v11 offset:9216\n"
"v_mul_f32_e32 v30, s44, v30\n"
"v_mfma_f32_16x16x32_bf16 v[140:143], a[172:175], v[32:35], v[140:143]\n"
"v_exp_f32_e32 v29, v29\n"
"v_mfma_f32_16x16x32_bf16 v[144:147], a[176:179], v[32:35], v[144:147]\n"
"s_add_u32 m0, s49, 14336\n"
"v_mul_f32_e32 v31, s44, v31\n"
"v_cvt_pk_bf16_f32 v38, v28, v29\n"
"v_mfma_f32_16x16x32_bf16 v[148:151], a[180:183], v[32:35], v[148:151]\n"
"s_add_u32 s61, s58, 12288\n"
"v_exp_f32_e32 v30, v30\n"
"v_mfma_f32_16x16x32_bf16 v[152:155], a[184:187], v[32:35], v[152:155]\n"
"buffer_load_dwordx4 v8, s[36:39], s61 offen  nt lds\n"
"v_mfma_f32_16x16x32_bf16 v[156:159], a[188:191], v[32:35], v[156:159]\n"
"v_exp_f32_e32 v31, v31\n"
"v_mfma_f32_16x16x32_bf16 a[236:239], a[224:227], v[32:35], a[236:239]\n"
"ds_read_b64_tr_b16 a[214:215], v14 offset:9216\n"
"v_cvt_pk_bf16_f32 v39, v30, v31\n"
"v_mfma_f32_16x16x32_bf16 v[24:27], a[96:99], a[80:83], 0\n"
"ds_read_b64_tr_b16 a[222:223], v14 offset:9728\n"
"v_mul_f32_e32 v16, s44, v16\n"
"v_mul_f32_e32 v17, s44, v17\n"
"v_mfma_f32_16x16x32_bf16 v[24:27], a[100:103], a[84:87], v[24:27]\n"
"v_exp_f32_e32 v16, v16\n"
"v_mfma_f32_16x16x32_bf16 v[24:27], a[104:107], a[88:91], v[24:27]\n"
"ds_read_b64_tr_b16 a[216:217], v3 offset:9728\n"
"v_mul_f32_e32 v18, s44, v18\n"
"v_mfma_f32_16x16x32_bf16 v[24:27], a[108:111], a[92:95], v[24:27]\n"
"v_exp_f32_e32 v17, v17\n"
"v_mfma_f32_16x16x32_bf16 v[28:31], a[112:115], a[80:83], 0\n"
"v_mul_f32_e32 v19, s44, v19\n"
"v_cvt_pk_bf16_f32 v32, v16, v17\n"
"v_mfma_f32_16x16x32_bf16 v[28:31], a[116:119], a[84:87], v[28:31]\n"
"v_exp_f32_e32 v18, v18\n"
"v_mfma_f32_16x16x32_bf16 v[28:31], a[120:123], a[88:91], v[28:31]\n"
"ds_read_b64_tr_b16 a[218:219], v4 offset:9728\n"
"v_mul_f32_e32 v20, s44, v20\n"
"v_mfma_f32_16x16x32_bf16 v[28:31], a[124:127], a[92:95], v[28:31]\n"
"v_exp_f32_e32 v19, v19\n"
"v_mfma_f32_16x16x32_bf16 v[160:163], a[160:163], v[36:39], v[160:163]\n"
"v_mul_f32_e32 v21, s44, v21\n"
"v_cvt_pk_bf16_f32 v33, v18, v19\n"
"v_mfma_f32_16x16x32_bf16 v[164:167], a[164:167], v[36:39], v[164:167]\n"
"v_exp_f32_e32 v20, v20\n"
"v_mfma_f32_16x16x32_bf16 v[168:171], a[168:171], v[36:39], v[168:171]\n"
"ds_read_b64_tr_b16 a[220:221], v11 offset:9728\n"
"v_mul_f32_e32 v22, s44, v22\n"
"v_mfma_f32_16x16x32_bf16 v[172:175], a[172:175], v[36:39], v[172:175]\n"
"v_exp_f32_e32 v21, v21\n"
"v_mfma_f32_16x16x32_bf16 v[176:179], a[176:179], v[36:39], v[176:179]\n"
"s_add_u32 m0, s49, 15360\n"
"v_mul_f32_e32 v23, s44, v23\n"
"v_cvt_pk_bf16_f32 v34, v20, v21\n"
"v_mfma_f32_16x16x32_bf16 v[180:183], a[180:183], v[36:39], v[180:183]\n"
"s_add_u32 s61, s58, 12416\n"
"v_exp_f32_e32 v22, v22\n"
"v_mfma_f32_16x16x32_bf16 v[184:187], a[184:187], v[36:39], v[184:187]\n"
"buffer_load_dwordx4 v8, s[36:39], s61 offen  nt lds\n"
"v_mfma_f32_16x16x32_bf16 v[188:191], a[188:191], v[36:39], v[188:191]\n"
"v_exp_f32_e32 v23, v23\n"
"v_mfma_f32_16x16x32_bf16 a[240:243], a[224:227], v[36:39], a[240:243]\n"
"s_waitcnt lgkmcnt(15)\n"
"v_cvt_pk_bf16_f32 v35, v22, v23\n"
"v_mfma_f32_16x16x32_bf16 v[16:19], a[128:131], a[0:3], 0\n"
"v_mul_f32_e32 v24, s44, v24\n"
"v_mul_f32_e32 v25, s44, v25\n"
"v_mfma_f32_16x16x32_bf16 v[16:19], a[132:135], a[4:7], v[16:19]\n"
"v_exp_f32_e32 v24, v24\n"
"v_mfma_f32_16x16x32_bf16 v[16:19], a[136:139], a[8:11], v[16:19]\n"
"v_mul_f32_e32 v26, s44, v26\n"
"v_mfma_f32_16x16x32_bf16 v[16:19], a[140:143], a[12:15], v[16:19]\n"
"v_exp_f32_e32 v25, v25\n"
"v_mfma_f32_16x16x32_bf16 v[20:23], a[144:147], a[0:3], 0\n"
"v_mul_f32_e32 v27, s44, v27\n"
"v_cvt_pk_bf16_f32 v36, v24, v25\n"
"v_mfma_f32_16x16x32_bf16 v[20:23], a[148:151], a[4:7], v[20:23]\n"
"s_waitcnt vmcnt(19) lgkmcnt(0)\n"
"s_barrier\n"
"v_exp_f32_e32 v26, v26\n"
"v_mfma_f32_16x16x32_bf16 v[20:23], a[152:155], a[8:11], v[20:23]\n"
"ds_read_b128 a[96:99], v1 offset:16384\n"
"ds_read_b128 a[108:111], v1 offset:17920\n"
"v_mul_f32_e32 v28, s44, v28\n"
"v_mfma_f32_16x16x32_bf16 v[20:23], a[156:159], a[12:15], v[20:23]\n"
"ds_read_b128 v[40:43], v58 offset:16384\n"
"v_exp_f32_e32 v27, v27\n"
"v_mfma_f32_16x16x32_bf16 v[192:195], a[160:163], v[32:35], v[192:195]\n"
"ds_read_b128 v[44:47], v59 offset:17408\n"
"v_mul_f32_e32 v29, s44, v29\n"
"v_cvt_pk_bf16_f32 v37, v26, v27\n"
"v_mfma_f32_16x16x32_bf16 v[196:199], a[164:167], v[32:35], v[196:199]\n"
"ds_read_b128 v[48:51], v58 offset:18432\n"
"v_exp_f32_e32 v28, v28\n"
"v_mfma_f32_16x16x32_bf16 v[200:203], a[168:171], v[32:35], v[200:203]\n"
"ds_read_b128 a[100:103], v1 offset:16896\n"
"ds_read_b128 v[52:55], v59 offset:19456\n"
"v_mul_f32_e32 v30, s44, v30\n"
"v_mfma_f32_16x16x32_bf16 v[204:207], a[172:175], v[32:35], v[204:207]\n"
"v_exp_f32_e32 v29, v29\n"
"v_mfma_f32_16x16x32_bf16 v[208:211], a[176:179], v[32:35], v[208:211]\n"
"s_add_u32 m0, s48, 8192\n"
"v_mul_f32_e32 v31, s44, v31\n"
"v_cvt_pk_bf16_f32 v38, v28, v29\n"
"v_mfma_f32_16x16x32_bf16 v[212:215], a[180:183], v[32:35], v[212:215]\n"
"s_add_u32 s47, s45, 106496\n"
"v_exp_f32_e32 v30, v30\n"
"v_mfma_f32_16x16x32_bf16 v[216:219], a[184:187], v[32:35], v[216:219]\n"
"buffer_load_dwordx4 v6, s[24:27], s47 offen lds\n"
"v_mfma_f32_16x16x32_bf16 v[220:223], a[188:191], v[32:35], v[220:223]\n"
"v_exp_f32_e32 v31, v31\n"
"v_mfma_f32_16x16x32_bf16 a[244:247], a[224:227], v[32:35], a[244:247]\n"
"ds_read_b128 a[104:107], v1 offset:17408\n"
"v_cvt_pk_bf16_f32 v39, v30, v31\n"
"v_mfma_f32_16x16x32_bf16 v[24:27], a[128:131], a[16:19], 0\n"
"v_mul_f32_e32 v16, s44, v16\n"
"v_mul_f32_e32 v17, s44, v17\n"
"v_mfma_f32_16x16x32_bf16 v[24:27], a[132:135], a[20:23], v[24:27]\n"
"v_exp_f32_e32 v16, v16\n"
"s_add_u32 s77, s60, 16384\n"
"v_mfma_f32_16x16x32_bf16 v[24:27], a[136:139], a[24:27], v[24:27]\n"
"ds_read_b128 a[112:115], v2 offset:16384\n"
"v_mul_f32_e32 v18, s44, v18\n"
"s_waitcnt lgkmcnt(6)\n"
"buffer_store_dwordx4 v[40:43], v9, s[40:43], s77 offen offset:0  sc1\n"
"v_mfma_f32_16x16x32_bf16 v[24:27], a[140:143], a[28:31], v[24:27]\n"
"v_exp_f32_e32 v17, v17\n"
"v_mfma_f32_16x16x32_bf16 v[28:31], a[144:147], a[16:19], 0\n"
"v_mul_f32_e32 v19, s44, v19\n"
"v_cvt_pk_bf16_f32 v32, v16, v17\n"
"v_mfma_f32_16x16x32_bf16 v[28:31], a[148:151], a[20:23], v[28:31]\n"
"v_exp_f32_e32 v18, v18\n"
"v_mfma_f32_16x16x32_bf16 v[28:31], a[152:155], a[24:27], v[28:31]\n"
"ds_read_b128 a[116:119], v2 offset:16896\n"
"v_mul_f32_e32 v20, s44, v20\n"
"s_waitcnt lgkmcnt(6)\n"
"buffer_store_dwordx4 v[44:47], v9, s[40:43], s77 offen offset:1024  sc1\n"
"v_mfma_f32_16x16x32_bf16 v[28:31], a[156:159], a[28:31], v[28:31]\n"
"v_exp_f32_e32 v19, v19\n"
"v_mfma_f32_16x16x32_bf16 v[224:227], a[160:163], v[36:39], v[224:227]\n"
"v_mul_f32_e32 v21, s44, v21\n"
"v_cvt_pk_bf16_f32 v33, v18, v19\n"
"v_mfma_f32_16x16x32_bf16 v[228:231], a[164:167], v[36:39], v[228:231]\n"
"v_exp_f32_e32 v20, v20\n"
"v_mfma_f32_16x16x32_bf16 v[232:235], a[168:171], v[36:39], v[232:235]\n"
"ds_read_b128 a[120:123], v2 offset:17408\n"
"v_mul_f32_e32 v22, s44, v22\n"
"s_waitcnt lgkmcnt(6)\n"
"buffer_store_dwordx4 v[48:51], v9, s[40:43], s77 offen offset:2048  sc1\n"
"v_mfma_f32_16x16x32_bf16 v[236:239], a[172:175], v[36:39], v[236:239]\n"
"v_exp_f32_e32 v21, v21\n"
"v_mfma_f32_16x16x32_bf16 v[240:243], a[176:179], v[36:39], v[240:243]\n"
"s_add_u32 m0, s48, 9216\n"
"v_mul_f32_e32 v23, s44, v23\n"
"v_cvt_pk_bf16_f32 v34, v20, v21\n"
"v_mfma_f32_16x16x32_bf16 v[244:247], a[180:183], v[36:39], v[244:247]\n"
"s_add_u32 s47, s45, 106624\n"
"v_exp_f32_e32 v22, v22\n"
"v_mfma_f32_16x16x32_bf16 v[248:251], a[184:187], v[36:39], v[248:251]\n"
"buffer_load_dwordx4 v6, s[24:27], s47 offen lds\n"
"v_mfma_f32_16x16x32_bf16 v[252:255], a[188:191], v[36:39], v[252:255]\n"
"v_exp_f32_e32 v23, v23\n"
"v_mfma_f32_16x16x32_bf16 a[248:251], a[224:227], v[36:39], a[248:251]\n"
"ds_read_b128 a[124:127], v2 offset:17920\n"
"v_cvt_pk_bf16_f32 v35, v22, v23\n"
"s_waitcnt lgkmcnt(5)\n"
"buffer_store_dwordx4 v[52:55], v9, s[40:43], s77 offen offset:3072  sc1\n"
"v_mfma_f32_16x16x32_bf16 v[16:19], a[128:131], a[32:35], 0\n"
"v_mul_f32_e32 v24, s44, v24\n"
"v_mul_f32_e32 v25, s44, v25\n"
"v_mfma_f32_16x16x32_bf16 v[16:19], a[132:135], a[36:39], v[16:19]\n"
"v_exp_f32_e32 v24, v24\n"
"v_mfma_f32_16x16x32_bf16 v[16:19], a[136:139], a[40:43], v[16:19]\n"
"ds_read_b64_tr_b16 a[160:161], v3 offset:16384\n"
"v_mul_f32_e32 v26, s44, v26\n"
"v_mfma_f32_16x16x32_bf16 v[16:19], a[140:143], a[44:47], v[16:19]\n"
"v_exp_f32_e32 v25, v25\n"
"v_mfma_f32_16x16x32_bf16 v[20:23], a[144:147], a[32:35], 0\n"
"v_mul_f32_e32 v27, s44, v27\n"
"v_cvt_pk_bf16_f32 v36, v24, v25\n"
"v_mfma_f32_16x16x32_bf16 v[20:23], a[148:151], a[36:39], v[20:23]\n"
"v_exp_f32_e32 v26, v26\n"
"v_mfma_f32_16x16x32_bf16 v[20:23], a[152:155], a[40:43], v[20:23]\n"
"ds_read_b64_tr_b16 a[162:163], v4 offset:16384\n"
"v_mul_f32_e32 v28, s44, v28\n"
"v_mfma_f32_16x16x32_bf16 v[20:23], a[156:159], a[44:47], v[20:23]\n"
"v_exp_f32_e32 v27, v27\n"
"v_mfma_f32_16x16x32_bf16 v[64:67], a[192:195], v[32:35], v[64:67]\n"
"v_mul_f32_e32 v29, s44, v29\n"
"v_cvt_pk_bf16_f32 v37, v26, v27\n"
"v_mfma_f32_16x16x32_bf16 v[68:71], a[196:199], v[32:35], v[68:71]\n"
"v_exp_f32_e32 v28, v28\n"
"v_mfma_f32_16x16x32_bf16 v[72:75], a[200:203], v[32:35], v[72:75]\n"
"ds_read_b64_tr_b16 a[164:165], v11 offset:16384\n"
"v_mul_f32_e32 v30, s44, v30\n"
"v_mfma_f32_16x16x32_bf16 v[76:79], a[204:207], v[32:35], v[76:79]\n"
"v_exp_f32_e32 v29, v29\n"
"v_mfma_f32_16x16x32_bf16 v[80:83], a[208:211], v[32:35], v[80:83]\n"
"s_add_u32 m0, s48, 40960\n"
"v_mul_f32_e32 v31, s44, v31\n"
"v_cvt_pk_bf16_f32 v38, v28, v29\n"
"v_mfma_f32_16x16x32_bf16 v[84:87], a[212:215], v[32:35], v[84:87]\n"
"s_add_u32 s76, s45, 106496\n"
"v_exp_f32_e32 v30, v30\n"
"v_mfma_f32_16x16x32_bf16 v[88:91], a[216:219], v[32:35], v[88:91]\n"
"buffer_load_dwordx4 v6, s[28:31], s76 offen lds\n"
"v_mfma_f32_16x16x32_bf16 v[92:95], a[220:223], v[32:35], v[92:95]\n"
"v_exp_f32_e32 v31, v31\n"
"v_mfma_f32_16x16x32_bf16 a[228:231], a[224:227], v[32:35], a[228:231]\n"
"ds_read_b64_tr_b16 a[166:167], v14 offset:16384\n"
"v_cvt_pk_bf16_f32 v39, v30, v31\n"
"v_mfma_f32_16x16x32_bf16 v[24:27], a[128:131], a[48:51], 0\n"
"v_mul_f32_e32 v16, s44, v16\n"
"v_mul_f32_e32 v17, s44, v17\n"
"v_mfma_f32_16x16x32_bf16 v[24:27], a[132:135], a[52:55], v[24:27]\n"
"v_exp_f32_e32 v16, v16\n"
"v_mfma_f32_16x16x32_bf16 v[24:27], a[136:139], a[56:59], v[24:27]\n"
"ds_read_b64_tr_b16 a[168:169], v3 offset:16896\n"
"v_mul_f32_e32 v18, s44, v18\n"
"v_mfma_f32_16x16x32_bf16 v[24:27], a[140:143], a[60:63], v[24:27]\n"
"v_exp_f32_e32 v17, v17\n"
"v_mfma_f32_16x16x32_bf16 v[28:31], a[144:147], a[48:51], 0\n"
"v_mul_f32_e32 v19, s44, v19\n"
"v_cvt_pk_bf16_f32 v32, v16, v17\n"
"v_mfma_f32_16x16x32_bf16 v[28:31], a[148:151], a[52:55], v[28:31]\n"
"v_exp_f32_e32 v18, v18\n"
"v_mfma_f32_16x16x32_bf16 v[28:31], a[152:155], a[56:59], v[28:31]\n"
"ds_read_b64_tr_b16 a[170:171], v4 offset:16896\n"
"v_mul_f32_e32 v20, s44, v20\n"
"v_mfma_f32_16x16x32_bf16 v[28:31], a[156:159], a[60:63], v[28:31]\n"
"v_exp_f32_e32 v19, v19\n"
"v_mfma_f32_16x16x32_bf16 v[96:99], a[192:195], v[36:39], v[96:99]\n"
"v_mul_f32_e32 v21, s44, v21\n"
"v_cvt_pk_bf16_f32 v33, v18, v19\n"
"v_mfma_f32_16x16x32_bf16 v[100:103], a[196:199], v[36:39], v[100:103]\n"
"v_exp_f32_e32 v20, v20\n"
"v_mfma_f32_16x16x32_bf16 v[104:107], a[200:203], v[36:39], v[104:107]\n"
"ds_read_b64_tr_b16 a[172:173], v11 offset:16896\n"
"v_mul_f32_e32 v22, s44, v22\n"
"v_mfma_f32_16x16x32_bf16 v[108:111], a[204:207], v[36:39], v[108:111]\n"
"v_exp_f32_e32 v21, v21\n"
"v_mfma_f32_16x16x32_bf16 v[112:115], a[208:211], v[36:39], v[112:115]\n"
"s_add_u32 m0, s48, 41984\n"
"v_mul_f32_e32 v23, s44, v23\n"
"v_cvt_pk_bf16_f32 v34, v20, v21\n"
"v_mfma_f32_16x16x32_bf16 v[116:119], a[212:215], v[36:39], v[116:119]\n"
"s_add_u32 s76, s45, 106624\n"
"v_exp_f32_e32 v22, v22\n"
"v_mfma_f32_16x16x32_bf16 v[120:123], a[216:219], v[36:39], v[120:123]\n"
"buffer_load_dwordx4 v6, s[28:31], s76 offen lds\n"
"v_mfma_f32_16x16x32_bf16 v[124:127], a[220:223], v[36:39], v[124:127]\n"
"v_exp_f32_e32 v23, v23\n"
"v_mfma_f32_16x16x32_bf16 a[232:235], a[224:227], v[36:39], a[232:235]\n"
"ds_read_b64_tr_b16 a[174:175], v14 offset:16896\n"
"v_cvt_pk_bf16_f32 v35, v22, v23\n"
"v_mfma_f32_16x16x32_bf16 v[16:19], a[128:131], a[64:67], 0\n"
"ds_read_b64_tr_b16 a[182:183], v14 offset:17408\n"
"v_mul_f32_e32 v24, s44, v24\n"
"v_mul_f32_e32 v25, s44, v25\n"
"v_mfma_f32_16x16x32_bf16 v[16:19], a[132:135], a[68:71], v[16:19]\n"
"v_exp_f32_e32 v24, v24\n"
"v_mfma_f32_16x16x32_bf16 v[16:19], a[136:139], a[72:75], v[16:19]\n"
"ds_read_b64_tr_b16 a[176:177], v3 offset:17408\n"
"v_mul_f32_e32 v26, s44, v26\n"
"v_mfma_f32_16x16x32_bf16 v[16:19], a[140:143], a[76:79], v[16:19]\n"
"v_exp_f32_e32 v25, v25\n"
"v_mfma_f32_16x16x32_bf16 v[20:23], a[144:147], a[64:67], 0\n"
"v_mul_f32_e32 v27, s44, v27\n"
"v_cvt_pk_bf16_f32 v36, v24, v25\n"
"v_mfma_f32_16x16x32_bf16 v[20:23], a[148:151], a[68:71], v[20:23]\n"
"v_exp_f32_e32 v26, v26\n"
"v_mfma_f32_16x16x32_bf16 v[20:23], a[152:155], a[72:75], v[20:23]\n"
"ds_read_b64_tr_b16 a[178:179], v4 offset:17408\n"
"v_mul_f32_e32 v28, s44, v28\n"
"v_mfma_f32_16x16x32_bf16 v[20:23], a[156:159], a[76:79], v[20:23]\n"
"v_exp_f32_e32 v27, v27\n"
"v_mfma_f32_16x16x32_bf16 v[128:131], a[192:195], v[32:35], v[128:131]\n"
"v_mul_f32_e32 v29, s44, v29\n"
"v_cvt_pk_bf16_f32 v37, v26, v27\n"
"v_mfma_f32_16x16x32_bf16 v[132:135], a[196:199], v[32:35], v[132:135]\n"
"v_exp_f32_e32 v28, v28\n"
"v_mfma_f32_16x16x32_bf16 v[136:139], a[200:203], v[32:35], v[136:139]\n"
"ds_read_b64_tr_b16 a[180:181], v11 offset:17408\n"
"v_mul_f32_e32 v30, s44, v30\n"
"v_mfma_f32_16x16x32_bf16 v[140:143], a[204:207], v[32:35], v[140:143]\n"
"v_exp_f32_e32 v29, v29\n"
"v_mfma_f32_16x16x32_bf16 v[144:147], a[208:211], v[32:35], v[144:147]\n"
"v_mul_f32_e32 v31, s44, v31\n"
"v_cvt_pk_bf16_f32 v38, v28, v29\n"
"v_mfma_f32_16x16x32_bf16 v[148:151], a[212:215], v[32:35], v[148:151]\n"
"s_mov_b32 s88, L_T65%=+0-L_code%=\n"
"v_exp_f32_e32 v30, v30\n"
"v_mfma_f32_16x16x32_bf16 v[152:155], a[216:219], v[32:35], v[152:155]\n"
"s_add_u32 m0, s49, 16384\n"
"buffer_load_dword v63, v62, s[84:87], s88 offen\n"
"v_mfma_f32_16x16x32_bf16 v[156:159], a[220:223], v[32:35], v[156:159]\n"
"s_add_u32 s61, s58, 16384\n"
"v_exp_f32_e32 v31, v31\n"
"v_mfma_f32_16x16x32_bf16 a[236:239], a[224:227], v[32:35], a[236:239]\n"
"buffer_load_dwordx4 v7, s[36:39], s61 offen  nt lds\n"
"v_cvt_pk_bf16_f32 v39, v30, v31\n"
"v_mfma_f32_16x16x32_bf16 v[24:27], a[128:131], a[80:83], 0\n"
"ds_read_b64_tr_b16 a[190:191], v14 offset:17920\n"
"v_mul_f32_e32 v16, s44, v16\n"
"v_mul_f32_e32 v17, s44, v17\n"
"v_mfma_f32_16x16x32_bf16 v[24:27], a[132:135], a[84:87], v[24:27]\n"
"v_exp_f32_e32 v16, v16\n"
"v_mfma_f32_16x16x32_bf16 v[24:27], a[136:139], a[88:91], v[24:27]\n"
"ds_read_b64_tr_b16 a[184:185], v3 offset:17920\n"
"v_mul_f32_e32 v18, s44, v18\n"
"v_mfma_f32_16x16x32_bf16 v[24:27], a[140:143], a[92:95], v[24:27]\n"
"v_exp_f32_e32 v17, v17\n"
"v_mfma_f32_16x16x32_bf16 v[28:31], a[144:147], a[80:83], 0\n"
"v_mul_f32_e32 v19, s44, v19\n"
"v_cvt_pk_bf16_f32 v32, v16, v17\n"
"v_mfma_f32_16x16x32_bf16 v[28:31], a[148:151], a[84:87], v[28:31]\n"
"v_exp_f32_e32 v18, v18\n"
"v_mfma_f32_16x16x32_bf16 v[28:31], a[152:155], a[88:91], v[28:31]\n"
"ds_read_b64_tr_b16 a[186:187], v4 offset:17920\n"
"v_mul_f32_e32 v20, s44, v20\n"
"v_mfma_f32_16x16x32_bf16 v[28:31], a[156:159], a[92:95], v[28:31]\n"
"v_exp_f32_e32 v19, v19\n"
"v_mfma_f32_16x16x32_bf16 v[160:163], a[192:195], v[36:39], v[160:163]\n"
"v_mul_f32_e32 v21, s44, v21\n"
"v_cvt_pk_bf16_f32 v33, v18, v19\n"
"v_mfma_f32_16x16x32_bf16 v[164:167], a[196:199], v[36:39], v[164:167]\n"
"v_exp_f32_e32 v20, v20\n"
"v_mfma_f32_16x16x32_bf16 v[168:171], a[200:203], v[36:39], v[168:171]\n"
"ds_read_b64_tr_b16 a[188:189], v11 offset:17920\n"
"v_mul_f32_e32 v22, s44, v22\n"
"v_mfma_f32_16x16x32_bf16 v[172:175], a[204:207], v[36:39], v[172:175]\n"
"v_exp_f32_e32 v21, v21\n"
"v_mfma_f32_16x16x32_bf16 v[176:179], a[208:211], v[36:39], v[176:179]\n"
"s_add_u32 m0, s49, 17408\n"
"v_mul_f32_e32 v23, s44, v23\n"
"v_cvt_pk_bf16_f32 v34, v20, v21\n"
"v_mfma_f32_16x16x32_bf16 v[180:183], a[212:215], v[36:39], v[180:183]\n"
"s_add_u32 s61, s58, 16512\n"
"v_exp_f32_e32 v22, v22\n"
"v_mfma_f32_16x16x32_bf16 v[184:187], a[216:219], v[36:39], v[184:187]\n"
"buffer_load_dwordx4 v7, s[36:39], s61 offen  nt lds\n"
"v_mfma_f32_16x16x32_bf16 v[188:191], a[220:223], v[36:39], v[188:191]\n"
"v_exp_f32_e32 v23, v23\n"
"v_mfma_f32_16x16x32_bf16 a[240:243], a[224:227], v[36:39], a[240:243]\n"
"v_cvt_pk_bf16_f32 v35, v22, v23\n"
"v_mfma_f32_16x16x32_bf16 v[16:19], a[96:99], a[0:3], 0\n"
"v_mul_f32_e32 v24, s44, v24\n"
"v_mul_f32_e32 v25, s44, v25\n"
"v_mfma_f32_16x16x32_bf16 v[16:19], a[100:103], a[4:7], v[16:19]\n"
"s_waitcnt lgkmcnt(15)\n"
"v_exp_f32_e32 v24, v24\n"
"v_mfma_f32_16x16x32_bf16 v[16:19], a[104:107], a[8:11], v[16:19]\n"
"v_mul_f32_e32 v26, s44, v26\n"
"v_mfma_f32_16x16x32_bf16 v[16:19], a[108:111], a[12:15], v[16:19]\n"
"v_exp_f32_e32 v25, v25\n"
"v_mfma_f32_16x16x32_bf16 v[20:23], a[112:115], a[0:3], 0\n"
"v_mul_f32_e32 v27, s44, v27\n"
"v_cvt_pk_bf16_f32 v36, v24, v25\n"
"v_mfma_f32_16x16x32_bf16 v[20:23], a[116:119], a[4:7], v[20:23]\n"
"s_waitcnt vmcnt(20) lgkmcnt(0)\n"
"s_barrier\n"
"v_exp_f32_e32 v26, v26\n"
"v_mfma_f32_16x16x32_bf16 v[20:23], a[120:123], a[8:11], v[20:23]\n"
"ds_read_b128 a[128:131], v1 offset:24576\n"
"ds_read_b128 a[140:143], v1 offset:26112\n"
"v_mul_f32_e32 v28, s44, v28\n"
"v_mfma_f32_16x16x32_bf16 v[20:23], a[124:127], a[12:15], v[20:23]\n"
"v_exp_f32_e32 v27, v27\n"
"v_mfma_f32_16x16x32_bf16 v[192:195], a[192:195], v[32:35], v[192:195]\n"
"v_mul_f32_e32 v29, s44, v29\n"
"v_cvt_pk_bf16_f32 v37, v26, v27\n"
"v_mfma_f32_16x16x32_bf16 v[196:199], a[196:199], v[32:35], v[196:199]\n"
"v_exp_f32_e32 v28, v28\n"
"v_mfma_f32_16x16x32_bf16 v[200:203], a[200:203], v[32:35], v[200:203]\n"
"ds_read_b128 a[132:135], v1 offset:25088\n"
"v_mul_f32_e32 v30, s44, v30\n"
"v_mfma_f32_16x16x32_bf16 v[204:207], a[204:207], v[32:35], v[204:207]\n"
"v_exp_f32_e32 v29, v29\n"
"v_mfma_f32_16x16x32_bf16 v[208:211], a[208:211], v[32:35], v[208:211]\n"
"s_add_u32 m0, s48, 16384\n"
"v_mul_f32_e32 v31, s44, v31\n"
"v_cvt_pk_bf16_f32 v38, v28, v29\n"
"v_mfma_f32_16x16x32_bf16 v[212:215], a[212:215], v[32:35], v[212:215]\n"
"s_add_u32 s47, s45, 114688\n"
"v_exp_f32_e32 v30, v30\n"
"v_mfma_f32_16x16x32_bf16 v[216:219], a[216:219], v[32:35], v[216:219]\n"
"buffer_load_dwordx4 v6, s[24:27], s47 offen lds\n"
"v_mfma_f32_16x16x32_bf16 v[220:223], a[220:223], v[32:35], v[220:223]\n"
"v_exp_f32_e32 v31, v31\n"
"v_mfma_f32_16x16x32_bf16 a[244:247], a[224:227], v[32:35], a[244:247]\n"
"ds_read_b128 a[136:139], v1 offset:25600\n"
"v_cvt_pk_bf16_f32 v39, v30, v31\n"
"v_mfma_f32_16x16x32_bf16 v[24:27], a[96:99], a[16:19], 0\n"
"v_mul_f32_e32 v16, s44, v16\n"
"v_mul_f32_e32 v17, s44, v17\n"
"v_mfma_f32_16x16x32_bf16 v[24:27], a[100:103], a[20:23], v[24:27]\n"
"v_exp_f32_e32 v16, v16\n"
"v_mfma_f32_16x16x32_bf16 v[24:27], a[104:107], a[24:27], v[24:27]\n"
"ds_read_b128 a[144:147], v2 offset:24576\n"
"v_mul_f32_e32 v18, s44, v18\n"
"v_mfma_f32_16x16x32_bf16 v[24:27], a[108:111], a[28:31], v[24:27]\n"
"v_exp_f32_e32 v17, v17\n"
"v_mfma_f32_16x16x32_bf16 v[28:31], a[112:115], a[16:19], 0\n"
"v_mul_f32_e32 v19, s44, v19\n"
"v_cvt_pk_bf16_f32 v32, v16, v17\n"
"v_mfma_f32_16x16x32_bf16 v[28:31], a[116:119], a[20:23], v[28:31]\n"
"v_exp_f32_e32 v18, v18\n"
"v_mfma_f32_16x16x32_bf16 v[28:31], a[120:123], a[24:27], v[28:31]\n"
"ds_read_b128 a[148:151], v2 offset:25088\n"
"v_mul_f32_e32 v20, s44, v20\n"
"v_mfma_f32_16x16x32_bf16 v[28:31], a[124:127], a[28:31], v[28:31]\n"
"v_exp_f32_e32 v19, v19\n"
"v_mfma_f32_16x16x32_bf16 v[224:227], a[192:195], v[36:39], v[224:227]\n"
"v_mul_f32_e32 v21, s44, v21\n"
"v_cvt_pk_bf16_f32 v33, v18, v19\n"
"v_mfma_f32_16x16x32_bf16 v[228:231], a[196:199], v[36:39], v[228:231]\n"
"v_exp_f32_e32 v20, v20\n"
"v_mfma_f32_16x16x32_bf16 v[232:235], a[200:203], v[36:39], v[232:235]\n"
"ds_read_b128 a[152:155], v2 offset:25600\n"
"v_mul_f32_e32 v22, s44, v22\n"
"v_mfma_f32_16x16x32_bf16 v[236:239], a[204:207], v[36:39], v[236:239]\n"
"v_exp_f32_e32 v21, v21\n"
"v_mfma_f32_16x16x32_bf16 v[240:243], a[208:211], v[36:39], v[240:243]\n"
"s_add_u32 m0, s48, 17408\n"
"v_mul_f32_e32 v23, s44, v23\n"
"v_cvt_pk_bf16_f32 v34, v20, v21\n"
"v_mfma_f32_16x16x32_bf16 v[244:247], a[212:215], v[36:39], v[244:247]\n"
"s_add_u32 s47, s45, 114816\n"
"v_exp_f32_e32 v22, v22\n"
"v_mfma_f32_16x16x32_bf16 v[248:251], a[216:219], v[36:39], v[248:251]\n"
"buffer_load_dwordx4 v6, s[24:27], s47 offen lds\n"
"v_mfma_f32_16x16x32_bf16 v[252:255], a[220:223], v[36:39], v[252:255]\n"
"v_exp_f32_e32 v23, v23\n"
"v_mfma_f32_16x16x32_bf16 a[248:251], a[224:227], v[36:39], a[248:251]\n"
"ds_read_b128 a[156:159], v2 offset:26112\n"
"v_cvt_pk_bf16_f32 v35, v22, v23\n"
"v_mfma_f32_16x16x32_bf16 v[16:19], a[96:99], a[32:35], 0\n"
"v_mul_f32_e32 v24, s44, v24\n"
"v_mul_f32_e32 v25, s44, v25\n"
"v_mfma_f32_16x16x32_bf16 v[16:19], a[100:103], a[36:39], v[16:19]\n"
"v_exp_f32_e32 v24, v24\n"
"v_mfma_f32_16x16x32_bf16 v[16:19], a[104:107], a[40:43], v[16:19]\n"
"ds_read_b64_tr_b16 a[192:193], v3 offset:24576\n"
"v_mul_f32_e32 v26, s44, v26\n"
"v_mfma_f32_16x16x32_bf16 v[16:19], a[108:111], a[44:47], v[16:19]\n"
"v_exp_f32_e32 v25, v25\n"
"v_mfma_f32_16x16x32_bf16 v[20:23], a[112:115], a[32:35], 0\n"
"v_mul_f32_e32 v27, s44, v27\n"
"v_cvt_pk_bf16_f32 v36, v24, v25\n"
"v_mfma_f32_16x16x32_bf16 v[20:23], a[116:119], a[36:39], v[20:23]\n"
"v_exp_f32_e32 v26, v26\n"
"v_mfma_f32_16x16x32_bf16 v[20:23], a[120:123], a[40:43], v[20:23]\n"
"ds_read_b64_tr_b16 a[194:195], v4 offset:24576\n"
"v_mul_f32_e32 v28, s44, v28\n"
"v_mfma_f32_16x16x32_bf16 v[20:23], a[124:127], a[44:47], v[20:23]\n"
"v_exp_f32_e32 v27, v27\n"
"v_mfma_f32_16x16x32_bf16 v[64:67], a[160:163], v[32:35], v[64:67]\n"
"v_mul_f32_e32 v29, s44, v29\n"
"v_cvt_pk_bf16_f32 v37, v26, v27\n"
"v_mfma_f32_16x16x32_bf16 v[68:71], a[164:167], v[32:35], v[68:71]\n"
"v_exp_f32_e32 v28, v28\n"
"v_mfma_f32_16x16x32_bf16 v[72:75], a[168:171], v[32:35], v[72:75]\n"
"ds_read_b64_tr_b16 a[196:197], v11 offset:24576\n"
"v_mul_f32_e32 v30, s44, v30\n"
"v_mfma_f32_16x16x32_bf16 v[76:79], a[172:175], v[32:35], v[76:79]\n"
"v_exp_f32_e32 v29, v29\n"
"v_mfma_f32_16x16x32_bf16 v[80:83], a[176:179], v[32:35], v[80:83]\n"
"s_add_u32 m0, s48, 49152\n"
"v_mul_f32_e32 v31, s44, v31\n"
"v_cvt_pk_bf16_f32 v38, v28, v29\n"
"v_mfma_f32_16x16x32_bf16 v[84:87], a[180:183], v[32:35], v[84:87]\n"
"s_add_u32 s76, s45, 114688\n"
"v_exp_f32_e32 v30, v30\n"
"v_mfma_f32_16x16x32_bf16 v[88:91], a[184:187], v[32:35], v[88:91]\n"
"buffer_load_dwordx4 v6, s[28:31], s76 offen lds\n"
"v_mfma_f32_16x16x32_bf16 v[92:95], a[188:191], v[32:35], v[92:95]\n"
"v_exp_f32_e32 v31, v31\n"
"v_mfma_f32_16x16x32_bf16 a[228:231], a[224:227], v[32:35], a[228:231]\n"
"ds_read_b64_tr_b16 a[198:199], v14 offset:24576\n"
"v_cvt_pk_bf16_f32 v39, v30, v31\n"
"v_mfma_f32_16x16x32_bf16 v[24:27], a[96:99], a[48:51], 0\n"
"v_mul_f32_e32 v16, s44, v16\n"
"v_mul_f32_e32 v17, s44, v17\n"
"v_mfma_f32_16x16x32_bf16 v[24:27], a[100:103], a[52:55], v[24:27]\n"
"v_exp_f32_e32 v16, v16\n"
"v_mfma_f32_16x16x32_bf16 v[24:27], a[104:107], a[56:59], v[24:27]\n"
"ds_read_b64_tr_b16 a[200:201], v3 offset:25088\n"
"v_mul_f32_e32 v18, s44, v18\n"
"v_mfma_f32_16x16x32_bf16 v[24:27], a[108:111], a[60:63], v[24:27]\n"
"v_exp_f32_e32 v17, v17\n"
"v_mfma_f32_16x16x32_bf16 v[28:31], a[112:115], a[48:51], 0\n"
"v_mul_f32_e32 v19, s44, v19\n"
"v_cvt_pk_bf16_f32 v32, v16, v17\n"
"v_mfma_f32_16x16x32_bf16 v[28:31], a[116:119], a[52:55], v[28:31]\n"
"v_exp_f32_e32 v18, v18\n"
"v_mfma_f32_16x16x32_bf16 v[28:31], a[120:123], a[56:59], v[28:31]\n"
"ds_read_b64_tr_b16 a[202:203], v4 offset:25088\n"
"v_mul_f32_e32 v20, s44, v20\n"
"v_mfma_f32_16x16x32_bf16 v[28:31], a[124:127], a[60:63], v[28:31]\n"
"v_exp_f32_e32 v19, v19\n"
"v_mfma_f32_16x16x32_bf16 v[96:99], a[160:163], v[36:39], v[96:99]\n"
"v_mul_f32_e32 v21, s44, v21\n"
"v_cvt_pk_bf16_f32 v33, v18, v19\n"
"v_mfma_f32_16x16x32_bf16 v[100:103], a[164:167], v[36:39], v[100:103]\n"
"v_exp_f32_e32 v20, v20\n"
"v_mfma_f32_16x16x32_bf16 v[104:107], a[168:171], v[36:39], v[104:107]\n"
"ds_read_b64_tr_b16 a[204:205], v11 offset:25088\n"
"v_mul_f32_e32 v22, s44, v22\n"
"v_mfma_f32_16x16x32_bf16 v[108:111], a[172:175], v[36:39], v[108:111]\n"
"v_exp_f32_e32 v21, v21\n"
"v_mfma_f32_16x16x32_bf16 v[112:115], a[176:179], v[36:39], v[112:115]\n"
"s_add_u32 m0, s48, 50176\n"
"v_mul_f32_e32 v23, s44, v23\n"
"v_cvt_pk_bf16_f32 v34, v20, v21\n"
"v_mfma_f32_16x16x32_bf16 v[116:119], a[180:183], v[36:39], v[116:119]\n"
"s_add_u32 s76, s45, 114816\n"
"v_exp_f32_e32 v22, v22\n"
"v_mfma_f32_16x16x32_bf16 v[120:123], a[184:187], v[36:39], v[120:123]\n"
"buffer_load_dwordx4 v6, s[28:31], s76 offen lds\n"
"v_mfma_f32_16x16x32_bf16 v[124:127], a[188:191], v[36:39], v[124:127]\n"
"v_exp_f32_e32 v23, v23\n"
"v_mfma_f32_16x16x32_bf16 a[232:235], a[224:227], v[36:39], a[232:235]\n"
"ds_read_b64_tr_b16 a[206:207], v14 offset:25088\n"
"v_cvt_pk_bf16_f32 v35, v22, v23\n"
"v_mfma_f32_16x16x32_bf16 v[16:19], a[96:99], a[64:67], 0\n"
"v_mul_f32_e32 v24, s44, v24\n"
"v_mul_f32_e32 v25, s44, v25\n"
"v_mfma_f32_16x16x32_bf16 v[16:19], a[100:103], a[68:71], v[16:19]\n"
"v_exp_f32_e32 v24, v24\n"
"v_mfma_f32_16x16x32_bf16 v[16:19], a[104:107], a[72:75], v[16:19]\n"
"ds_read_b64_tr_b16 a[208:209], v3 offset:25600\n"
"v_mul_f32_e32 v26, s44, v26\n"
"v_mfma_f32_16x16x32_bf16 v[16:19], a[108:111], a[76:79], v[16:19]\n"
"v_exp_f32_e32 v25, v25\n"
"v_mfma_f32_16x16x32_bf16 v[20:23], a[112:115], a[64:67], 0\n"
"v_mul_f32_e32 v27, s44, v27\n"
"v_cvt_pk_bf16_f32 v36, v24, v25\n"
"v_mfma_f32_16x16x32_bf16 v[20:23], a[116:119], a[68:71], v[20:23]\n"
"v_exp_f32_e32 v26, v26\n"
"v_mfma_f32_16x16x32_bf16 v[20:23], a[120:123], a[72:75], v[20:23]\n"
"ds_read_b64_tr_b16 a[210:211], v4 offset:25600\n"
"v_mul_f32_e32 v28, s44, v28\n"
"v_mfma_f32_16x16x32_bf16 v[20:23], a[124:127], a[76:79], v[20:23]\n"
"v_exp_f32_e32 v27, v27\n"
"v_mfma_f32_16x16x32_bf16 v[128:131], a[160:163], v[32:35], v[128:131]\n"
"v_mul_f32_e32 v29, s44, v29\n"
"v_cvt_pk_bf16_f32 v37, v26, v27\n"
"v_mfma_f32_16x16x32_bf16 v[132:135], a[164:167], v[32:35], v[132:135]\n"
"v_exp_f32_e32 v28, v28\n"
"v_mfma_f32_16x16x32_bf16 v[136:139], a[168:171], v[32:35], v[136:139]\n"
"ds_read_b64_tr_b16 a[212:213], v11 offset:25600\n"
"v_mul_f32_e32 v30, s44, v30\n"
"v_mfma_f32_16x16x32_bf16 v[140:143], a[172:175], v[32:35], v[140:143]\n"
"v_exp_f32_e32 v29, v29\n"
"v_mfma_f32_16x16x32_bf16 v[144:147], a[176:179], v[32:35], v[144:147]\n"
"s_add_u32 m0, s49, 18432\n"
"v_mul_f32_e32 v31, s44, v31\n"
"v_cvt_pk_bf16_f32 v38, v28, v29\n"
"v_mfma_f32_16x16x32_bf16 v[148:151], a[180:183], v[32:35], v[148:151]\n"
"s_add_u32 s61, s58, 16384\n"
"v_exp_f32_e32 v30, v30\n"
"v_mfma_f32_16x16x32_bf16 v[152:155], a[184:187], v[32:35], v[152:155]\n"
"buffer_load_dwordx4 v8, s[36:39], s61 offen  nt lds\n"
"v_mfma_f32_16x16x32_bf16 v[156:159], a[188:191], v[32:35], v[156:159]\n"
"v_exp_f32_e32 v31, v31\n"
"v_mfma_f32_16x16x32_bf16 a[236:239], a[224:227], v[32:35], a[236:239]\n"
"ds_read_b64_tr_b16 a[214:215], v14 offset:25600\n"
"v_cvt_pk_bf16_f32 v39, v30, v31\n"
"v_mfma_f32_16x16x32_bf16 v[24:27], a[96:99], a[80:83], 0\n"
"ds_read_b64_tr_b16 a[222:223], v14 offset:26112\n"
"v_mul_f32_e32 v16, s44, v16\n"
"v_mul_f32_e32 v17, s44, v17\n"
"v_mfma_f32_16x16x32_bf16 v[24:27], a[100:103], a[84:87], v[24:27]\n"
"v_exp_f32_e32 v16, v16\n"
"v_mfma_f32_16x16x32_bf16 v[24:27], a[104:107], a[88:91], v[24:27]\n"
"ds_read_b64_tr_b16 a[216:217], v3 offset:26112\n"
"v_mul_f32_e32 v18, s44, v18\n"
"v_mfma_f32_16x16x32_bf16 v[24:27], a[108:111], a[92:95], v[24:27]\n"
"v_exp_f32_e32 v17, v17\n"
"v_mfma_f32_16x16x32_bf16 v[28:31], a[112:115], a[80:83], 0\n"
"v_mul_f32_e32 v19, s44, v19\n"
"v_cvt_pk_bf16_f32 v32, v16, v17\n"
"v_mfma_f32_16x16x32_bf16 v[28:31], a[116:119], a[84:87], v[28:31]\n"
"v_exp_f32_e32 v18, v18\n"
"v_mfma_f32_16x16x32_bf16 v[28:31], a[120:123], a[88:91], v[28:31]\n"
"ds_read_b64_tr_b16 a[218:219], v4 offset:26112\n"
"v_mul_f32_e32 v20, s44, v20\n"
"v_mfma_f32_16x16x32_bf16 v[28:31], a[124:127], a[92:95], v[28:31]\n"
"v_exp_f32_e32 v19, v19\n"
"v_mfma_f32_16x16x32_bf16 v[160:163], a[160:163], v[36:39], v[160:163]\n"
"v_mul_f32_e32 v21, s44, v21\n"
"v_cvt_pk_bf16_f32 v33, v18, v19\n"
"v_mfma_f32_16x16x32_bf16 v[164:167], a[164:167], v[36:39], v[164:167]\n"
"v_exp_f32_e32 v20, v20\n"
"v_mfma_f32_16x16x32_bf16 v[168:171], a[168:171], v[36:39], v[168:171]\n"
"ds_read_b64_tr_b16 a[220:221], v11 offset:26112\n"
"v_mul_f32_e32 v22, s44, v22\n"
"v_mfma_f32_16x16x32_bf16 v[172:175], a[172:175], v[36:39], v[172:175]\n"
"v_exp_f32_e32 v21, v21\n"
"v_mfma_f32_16x16x32_bf16 v[176:179], a[176:179], v[36:39], v[176:179]\n"
"s_add_u32 m0, s49, 19456\n"
"v_mul_f32_e32 v23, s44, v23\n"
"v_cvt_pk_bf16_f32 v34, v20, v21\n"
"v_mfma_f32_16x16x32_bf16 v[180:183], a[180:183], v[36:39], v[180:183]\n"
"s_add_u32 s61, s58, 16512\n"
"v_exp_f32_e32 v22, v22\n"
"v_mfma_f32_16x16x32_bf16 v[184:187], a[184:187], v[36:39], v[184:187]\n"
"buffer_load_dwordx4 v8, s[36:39], s61 offen  nt lds\n"
"v_mfma_f32_16x16x32_bf16 v[188:191], a[188:191], v[36:39], v[188:191]\n"
"v_exp_f32_e32 v23, v23\n"
"v_mfma_f32_16x16x32_bf16 a[240:243], a[224:227], v[36:39], a[240:243]\n"
"s_waitcnt lgkmcnt(15)\n"
"v_cvt_pk_bf16_f32 v35, v22, v23\n"
"v_mfma_f32_16x16x32_bf16 v[16:19], a[128:131], a[0:3], 0\n"
"v_mul_f32_e32 v24, s44, v24\n"
"v_mul_f32_e32 v25, s44, v25\n"
"v_mfma_f32_16x16x32_bf16 v[16:19], a[132:135], a[4:7], v[16:19]\n"
"v_exp_f32_e32 v24, v24\n"
"v_mfma_f32_16x16x32_bf16 v[16:19], a[136:139], a[8:11], v[16:19]\n"
"v_mul_f32_e32 v26, s44, v26\n"
"v_mfma_f32_16x16x32_bf16 v[16:19], a[140:143], a[12:15], v[16:19]\n"
"v_exp_f32_e32 v25, v25\n"
"v_mfma_f32_16x16x32_bf16 v[20:23], a[144:147], a[0:3], 0\n"
"v_mul_f32_e32 v27, s44, v27\n"
"v_cvt_pk_bf16_f32 v36, v24, v25\n"
"v_mfma_f32_16x16x32_bf16 v[20:23], a[148:151], a[4:7], v[20:23]\n"
"s_waitcnt vmcnt(19) lgkmcnt(0)\n"
"s_barrier\n"
"v_exp_f32_e32 v26, v26\n"
"v_mfma_f32_16x16x32_bf16 v[20:23], a[152:155], a[8:11], v[20:23]\n"
"ds_read_b128 a[96:99], v1 offset:0\n"
"ds_read_b128 a[108:111], v1 offset:1536\n"
"v_mul_f32_e32 v28, s44, v28\n"
"v_mfma_f32_16x16x32_bf16 v[20:23], a[156:159], a[12:15], v[20:23]\n"
"ds_read_b128 v[40:43], v58 offset:20480\n"
"v_exp_f32_e32 v27, v27\n"
"v_mfma_f32_16x16x32_bf16 v[192:195], a[160:163], v[32:35], v[192:195]\n"
"ds_read_b128 v[44:47], v59 offset:21504\n"
"v_mul_f32_e32 v29, s44, v29\n"
"v_cvt_pk_bf16_f32 v37, v26, v27\n"
"v_mfma_f32_16x16x32_bf16 v[196:199], a[164:167], v[32:35], v[196:199]\n"
"ds_read_b128 v[48:51], v58 offset:22528\n"
"v_exp_f32_e32 v28, v28\n"
"v_mfma_f32_16x16x32_bf16 v[200:203], a[168:171], v[32:35], v[200:203]\n"
"ds_read_b128 a[100:103], v1 offset:512\n"
"ds_read_b128 v[52:55], v59 offset:23552\n"
"v_mul_f32_e32 v30, s44, v30\n"
"v_mfma_f32_16x16x32_bf16 v[204:207], a[172:175], v[32:35], v[204:207]\n"
"v_exp_f32_e32 v29, v29\n"
"v_mfma_f32_16x16x32_bf16 v[208:211], a[176:179], v[32:35], v[208:211]\n"
"s_add_u32 m0, s48, 24576\n"
"v_mul_f32_e32 v31, s44, v31\n"
"v_cvt_pk_bf16_f32 v38, v28, v29\n"
"v_mfma_f32_16x16x32_bf16 v[212:215], a[180:183], v[32:35], v[212:215]\n"
"s_add_u32 s47, s45, 122880\n"
"v_exp_f32_e32 v30, v30\n"
"v_mfma_f32_16x16x32_bf16 v[216:219], a[184:187], v[32:35], v[216:219]\n"
"buffer_load_dwordx4 v6, s[24:27], s47 offen lds\n"
"v_mfma_f32_16x16x32_bf16 v[220:223], a[188:191], v[32:35], v[220:223]\n"
"v_exp_f32_e32 v31, v31\n"
"v_mfma_f32_16x16x32_bf16 a[244:247], a[224:227], v[32:35], a[244:247]\n"
"ds_read_b128 a[104:107], v1 offset:1024\n"
"v_cvt_pk_bf16_f32 v39, v30, v31\n"
"v_mfma_f32_16x16x32_bf16 v[24:27], a[128:131], a[16:19], 0\n"
"v_mul_f32_e32 v16, s44, v16\n"
"v_mul_f32_e32 v17, s44, v17\n"
"v_mfma_f32_16x16x32_bf16 v[24:27], a[132:135], a[20:23], v[24:27]\n"
"v_exp_f32_e32 v16, v16\n"
"s_add_u32 s77, s62, 20480\n"
"v_mfma_f32_16x16x32_bf16 v[24:27], a[136:139], a[24:27], v[24:27]\n"
"ds_read_b128 a[112:115], v2 offset:0\n"
"v_mul_f32_e32 v18, s44, v18\n"
"s_waitcnt lgkmcnt(6)\n"
"buffer_store_dwordx4 v[40:43], v9, s[40:43], s77 offen offset:0  sc1\n"
"v_mfma_f32_16x16x32_bf16 v[24:27], a[140:143], a[28:31], v[24:27]\n"
"v_exp_f32_e32 v17, v17\n"
"v_mfma_f32_16x16x32_bf16 v[28:31], a[144:147], a[16:19], 0\n"
"v_mul_f32_e32 v19, s44, v19\n"
"v_cvt_pk_bf16_f32 v32, v16, v17\n"
"v_mfma_f32_16x16x32_bf16 v[28:31], a[148:151], a[20:23], v[28:31]\n"
"v_exp_f32_e32 v18, v18\n"
"v_mfma_f32_16x16x32_bf16 v[28:31], a[152:155], a[24:27], v[28:31]\n"
"ds_read_b128 a[116:119], v2 offset:512\n"
"v_mul_f32_e32 v20, s44, v20\n"
"s_waitcnt lgkmcnt(6)\n"
"buffer_store_dwordx4 v[44:47], v9, s[40:43], s77 offen offset:1024  sc1\n"
"v_mfma_f32_16x16x32_bf16 v[28:31], a[156:159], a[28:31], v[28:31]\n"
"v_exp_f32_e32 v19, v19\n"
"v_mfma_f32_16x16x32_bf16 v[224:227], a[160:163], v[36:39], v[224:227]\n"
"v_mul_f32_e32 v21, s44, v21\n"
"v_cvt_pk_bf16_f32 v33, v18, v19\n"
"v_mfma_f32_16x16x32_bf16 v[228:231], a[164:167], v[36:39], v[228:231]\n"
"v_exp_f32_e32 v20, v20\n"
"v_mfma_f32_16x16x32_bf16 v[232:235], a[168:171], v[36:39], v[232:235]\n"
"ds_read_b128 a[120:123], v2 offset:1024\n"
"v_mul_f32_e32 v22, s44, v22\n"
"s_waitcnt lgkmcnt(6)\n"
"buffer_store_dwordx4 v[48:51], v9, s[40:43], s77 offen offset:2048  sc1\n"
"v_mfma_f32_16x16x32_bf16 v[236:239], a[172:175], v[36:39], v[236:239]\n"
"v_exp_f32_e32 v21, v21\n"
"v_mfma_f32_16x16x32_bf16 v[240:243], a[176:179], v[36:39], v[240:243]\n"
"s_add_u32 m0, s48, 25600\n"
"v_mul_f32_e32 v23, s44, v23\n"
"v_cvt_pk_bf16_f32 v34, v20, v21\n"
"v_mfma_f32_16x16x32_bf16 v[244:247], a[180:183], v[36:39], v[244:247]\n"
"s_add_u32 s47, s45, 123008\n"
"v_exp_f32_e32 v22, v22\n"
"v_mfma_f32_16x16x32_bf16 v[248:251], a[184:187], v[36:39], v[248:251]\n"
"buffer_load_dwordx4 v6, s[24:27], s47 offen lds\n"
"v_mfma_f32_16x16x32_bf16 v[252:255], a[188:191], v[36:39], v[252:255]\n"
"v_exp_f32_e32 v23, v23\n"
"v_mfma_f32_16x16x32_bf16 a[248:251], a[224:227], v[36:39], a[248:251]\n"
"ds_read_b128 a[124:127], v2 offset:1536\n"
"v_cvt_pk_bf16_f32 v35, v22, v23\n"
"s_waitcnt lgkmcnt(5)\n"
"buffer_store_dwordx4 v[52:55], v9, s[40:43], s77 offen offset:3072  sc1\n"
"v_mfma_f32_16x16x32_bf16 v[16:19], a[128:131], a[32:35], 0\n"
"v_mul_f32_e32 v24, s44, v24\n"
"v_mul_f32_e32 v25, s44, v25\n"
"v_mfma_f32_16x16x32_bf16 v[16:19], a[132:135], a[36:39], v[16:19]\n"
"v_exp_f32_e32 v24, v24\n"
"v_mfma_f32_16x16x32_bf16 v[16:19], a[136:139], a[40:43], v[16:19]\n"
"ds_read_b64_tr_b16 a[160:161], v3 offset:0\n"
"v_mul_f32_e32 v26, s44, v26\n"
"v_mfma_f32_16x16x32_bf16 v[16:19], a[140:143], a[44:47], v[16:19]\n"
"v_exp_f32_e32 v25, v25\n"
"v_mfma_f32_16x16x32_bf16 v[20:23], a[144:147], a[32:35], 0\n"
"v_mul_f32_e32 v27, s44, v27\n"
"v_cvt_pk_bf16_f32 v36, v24, v25\n"
"v_mfma_f32_16x16x32_bf16 v[20:23], a[148:151], a[36:39], v[20:23]\n"
"v_exp_f32_e32 v26, v26\n"
"v_mfma_f32_16x16x32_bf16 v[20:23], a[152:155], a[40:43], v[20:23]\n"
"ds_read_b64_tr_b16 a[162:163], v4 offset:0\n"
"v_mul_f32_e32 v28, s44, v28\n"
"v_mfma_f32_16x16x32_bf16 v[20:23], a[156:159], a[44:47], v[20:23]\n"
"v_exp_f32_e32 v27, v27\n"
"v_mfma_f32_16x16x32_bf16 v[64:67], a[192:195], v[32:35], v[64:67]\n"
"v_mul_f32_e32 v29, s44, v29\n"
"v_cvt_pk_bf16_f32 v37, v26, v27\n"
"v_mfma_f32_16x16x32_bf16 v[68:71], a[196:199], v[32:35], v[68:71]\n"
"v_exp_f32_e32 v28, v28\n"
"v_mfma_f32_16x16x32_bf16 v[72:75], a[200:203], v[32:35], v[72:75]\n"
"ds_read_b64_tr_b16 a[164:165], v11 offset:0\n"
"v_mul_f32_e32 v30, s44, v30\n"
"v_mfma_f32_16x16x32_bf16 v[76:79], a[204:207], v[32:35], v[76:79]\n"
"v_exp_f32_e32 v29, v29\n"
"v_mfma_f32_16x16x32_bf16 v[80:83], a[208:211], v[32:35], v[80:83]\n"
"s_add_u32 m0, s48, 57344\n"
"v_mul_f32_e32 v31, s44, v31\n"
"v_cvt_pk_bf16_f32 v38, v28, v29\n"
"v_mfma_f32_16x16x32_bf16 v[84:87], a[212:215], v[32:35], v[84:87]\n"
"s_add_u32 s76, s45, 122880\n"
"v_exp_f32_e32 v30, v30\n"
"v_mfma_f32_16x16x32_bf16 v[88:91], a[216:219], v[32:35], v[88:91]\n"
"buffer_load_dwordx4 v6, s[28:31], s76 offen lds\n"
"v_mfma_f32_16x16x32_bf16 v[92:95], a[220:223], v[32:35], v[92:95]\n"
"v_exp_f32_e32 v31, v31\n"
"v_mfma_f32_16x16x32_bf16 a[228:231], a[224:227], v[32:35], a[228:231]\n"
"ds_read_b64_tr_b16 a[166:167], v14 offset:0\n"
"v_cvt_pk_bf16_f32 v39, v30, v31\n"
"v_mfma_f32_16x16x32_bf16 v[24:27], a[128:131], a[48:51], 0\n"
"v_mul_f32_e32 v16, s44, v16\n"
"v_mul_f32_e32 v17, s44, v17\n"
"v_mfma_f32_16x16x32_bf16 v[24:27], a[132:135], a[52:55], v[24:27]\n"
"v_exp_f32_e32 v16, v16\n"
"v_mfma_f32_16x16x32_bf16 v[24:27], a[136:139], a[56:59], v[24:27]\n"
"ds_read_b64_tr_b16 a[168:169], v3 offset:512\n"
"v_mul_f32_e32 v18, s44, v18\n"
"v_mfma_f32_16x16x32_bf16 v[24:27], a[140:143], a[60:63], v[24:27]\n"
"v_exp_f32_e32 v17, v17\n"
"v_mfma_f32_16x16x32_bf16 v[28:31], a[144:147], a[48:51], 0\n"
"v_mul_f32_e32 v19, s44, v19\n"
"v_cvt_pk_bf16_f32 v32, v16, v17\n"
"v_mfma_f32_16x16x32_bf16 v[28:31], a[148:151], a[52:55], v[28:31]\n"
"v_exp_f32_e32 v18, v18\n"
"v_mfma_f32_16x16x32_bf16 v[28:31], a[152:155], a[56:59], v[28:31]\n"
"ds_read_b64_tr_b16 a[170:171], v4 offset:512\n"
"v_mul_f32_e32 v20, s44, v20\n"
"v_mfma_f32_16x16x32_bf16 v[28:31], a[156:159], a[60:63], v[28:31]\n"
"v_exp_f32_e32 v19, v19\n"
"v_mfma_f32_16x16x32_bf16 v[96:99], a[192:195], v[36:39], v[96:99]\n"
"v_mul_f32_e32 v21, s44, v21\n"
"v_cvt_pk_bf16_f32 v33, v18, v19\n"
"v_mfma_f32_16x16x32_bf16 v[100:103], a[196:199], v[36:39], v[100:103]\n"
"v_exp_f32_e32 v20, v20\n"
"v_mfma_f32_16x16x32_bf16 v[104:107], a[200:203], v[36:39], v[104:107]\n"
"ds_read_b64_tr_b16 a[172:173], v11 offset:512\n"
"v_mul_f32_e32 v22, s44, v22\n"
"v_mfma_f32_16x16x32_bf16 v[108:111], a[204:207], v[36:39], v[108:111]\n"
"v_exp_f32_e32 v21, v21\n"
"v_mfma_f32_16x16x32_bf16 v[112:115], a[208:211], v[36:39], v[112:115]\n"
"s_add_u32 m0, s48, 58368\n"
"v_mul_f32_e32 v23, s44, v23\n"
"v_cvt_pk_bf16_f32 v34, v20, v21\n"
"v_mfma_f32_16x16x32_bf16 v[116:119], a[212:215], v[36:39], v[116:119]\n"
"s_add_u32 s76, s45, 123008\n"
"v_exp_f32_e32 v22, v22\n"
"v_mfma_f32_16x16x32_bf16 v[120:123], a[216:219], v[36:39], v[120:123]\n"
"buffer_load_dwordx4 v6, s[28:31], s76 offen lds\n"
"v_mfma_f32_16x16x32_bf16 v[124:127], a[220:223], v[36:39], v[124:127]\n"
"v_exp_f32_e32 v23, v23\n"
"v_mfma_f32_16x16x32_bf16 a[232:235], a[224:227], v[36:39], a[232:235]\n"
"ds_read_b64_tr_b16 a[174:175], v14 offset:512\n"
"v_cvt_pk_bf16_f32 v35, v22, v23\n"
"v_mfma_f32_16x16x32_bf16 v[16:19], a[128:131], a[64:67], 0\n"
"ds_read_b64_tr_b16 a[182:183], v14 offset:1024\n"
"v_mul_f32_e32 v24, s44, v24\n"
"v_mul_f32_e32 v25, s44, v25\n"
"v_mfma_f32_16x16x32_bf16 v[16:19], a[132:135], a[68:71], v[16:19]\n"
"v_exp_f32_e32 v24, v24\n"
"v_mfma_f32_16x16x32_bf16 v[16:19], a[136:139], a[72:75], v[16:19]\n"
"ds_read_b64_tr_b16 a[176:177], v3 offset:1024\n"
"v_mul_f32_e32 v26, s44, v26\n"
"v_mfma_f32_16x16x32_bf16 v[16:19], a[140:143], a[76:79], v[16:19]\n"
"v_exp_f32_e32 v25, v25\n"
"v_mfma_f32_16x16x32_bf16 v[20:23], a[144:147], a[64:67], 0\n"
"v_mul_f32_e32 v27, s44, v27\n"
"v_cvt_pk_bf16_f32 v36, v24, v25\n"
"v_mfma_f32_16x16x32_bf16 v[20:23], a[148:151], a[68:71], v[20:23]\n"
"v_exp_f32_e32 v26, v26\n"
"v_mfma_f32_16x16x32_bf16 v[20:23], a[152:155], a[72:75], v[20:23]\n"
"ds_read_b64_tr_b16 a[178:179], v4 offset:1024\n"
"v_mul_f32_e32 v28, s44, v28\n"
"v_mfma_f32_16x16x32_bf16 v[20:23], a[156:159], a[76:79], v[20:23]\n"
"v_exp_f32_e32 v27, v27\n"
"v_mfma_f32_16x16x32_bf16 v[128:131], a[192:195], v[32:35], v[128:131]\n"
"v_mul_f32_e32 v29, s44, v29\n"
"v_cvt_pk_bf16_f32 v37, v26, v27\n"
"v_mfma_f32_16x16x32_bf16 v[132:135], a[196:199], v[32:35], v[132:135]\n"
"v_exp_f32_e32 v28, v28\n"
"v_mfma_f32_16x16x32_bf16 v[136:139], a[200:203], v[32:35], v[136:139]\n"
"ds_read_b64_tr_b16 a[180:181], v11 offset:1024\n"
"v_mul_f32_e32 v30, s44, v30\n"
"v_mfma_f32_16x16x32_bf16 v[140:143], a[204:207], v[32:35], v[140:143]\n"
"v_exp_f32_e32 v29, v29\n"
"v_mfma_f32_16x16x32_bf16 v[144:147], a[208:211], v[32:35], v[144:147]\n"
"v_mul_f32_e32 v31, s44, v31\n"
"v_cvt_pk_bf16_f32 v38, v28, v29\n"
"v_mfma_f32_16x16x32_bf16 v[148:151], a[212:215], v[32:35], v[148:151]\n"
"s_mov_b32 s88, L_E6%=+0-L_code%=\n"
"v_exp_f32_e32 v30, v30\n"
"v_mfma_f32_16x16x32_bf16 v[152:155], a[216:219], v[32:35], v[152:155]\n"
"s_add_u32 m0, s49, 20480\n"
"buffer_load_dword v63, v62, s[84:87], s88 offen\n"
"v_mfma_f32_16x16x32_bf16 v[156:159], a[220:223], v[32:35], v[156:159]\n"
"s_add_u32 s61, s59, 20480\n"
"v_exp_f32_e32 v31, v31\n"
"v_mfma_f32_16x16x32_bf16 a[236:239], a[224:227], v[32:35], a[236:239]\n"
"buffer_load_dwordx4 v7, s[36:39], s61 offen  nt lds\n"
"v_cvt_pk_bf16_f32 v39, v30, v31\n"
"v_mfma_f32_16x16x32_bf16 v[24:27], a[128:131], a[80:83], 0\n"
"ds_read_b64_tr_b16 a[190:191], v14 offset:1536\n"
"v_mul_f32_e32 v16, s44, v16\n"
"v_mul_f32_e32 v17, s44, v17\n"
"v_mfma_f32_16x16x32_bf16 v[24:27], a[132:135], a[84:87], v[24:27]\n"
"v_exp_f32_e32 v16, v16\n"
"v_mfma_f32_16x16x32_bf16 v[24:27], a[136:139], a[88:91], v[24:27]\n"
"ds_read_b64_tr_b16 a[184:185], v3 offset:1536\n"
"v_mul_f32_e32 v18, s44, v18\n"
"v_mfma_f32_16x16x32_bf16 v[24:27], a[140:143], a[92:95], v[24:27]\n"
"v_exp_f32_e32 v17, v17\n"
"v_mfma_f32_16x16x32_bf16 v[28:31], a[144:147], a[80:83], 0\n"
"v_mul_f32_e32 v19, s44, v19\n"
"v_cvt_pk_bf16_f32 v32, v16, v17\n"
"v_mfma_f32_16x16x32_bf16 v[28:31], a[148:151], a[84:87], v[28:31]\n"
"v_exp_f32_e32 v18, v18\n"
"v_mfma_f32_16x16x32_bf16 v[28:31], a[152:155], a[88:91], v[28:31]\n"
"ds_read_b64_tr_b16 a[186:187], v4 offset:1536\n"
"v_mul_f32_e32 v20, s44, v20\n"
"v_mfma_f32_16x16x32_bf16 v[28:31], a[156:159], a[92:95], v[28:31]\n"
"v_exp_f32_e32 v19, v19\n"
"v_mfma_f32_16x16x32_bf16 v[160:163], a[192:195], v[36:39], v[160:163]\n"
"v_mul_f32_e32 v21, s44, v21\n"
"v_cvt_pk_bf16_f32 v33, v18, v19\n"
"v_mfma_f32_16x16x32_bf16 v[164:167], a[196:199], v[36:39], v[164:167]\n"
"v_exp_f32_e32 v20, v20\n"
"v_mfma_f32_16x16x32_bf16 v[168:171], a[200:203], v[36:39], v[168:171]\n"
"ds_read_b64_tr_b16 a[188:189], v11 offset:1536\n"
"v_mul_f32_e32 v22, s44, v22\n"
"v_mfma_f32_16x16x32_bf16 v[172:175], a[204:207], v[36:39], v[172:175]\n"
"v_exp_f32_e32 v21, v21\n"
"v_mfma_f32_16x16x32_bf16 v[176:179], a[208:211], v[36:39], v[176:179]\n"
"s_add_u32 m0, s49, 21504\n"
"v_mul_f32_e32 v23, s44, v23\n"
"v_cvt_pk_bf16_f32 v34, v20, v21\n"
"v_mfma_f32_16x16x32_bf16 v[180:183], a[212:215], v[36:39], v[180:183]\n"
"s_add_u32 s61, s59, 20608\n"
"v_exp_f32_e32 v22, v22\n"
"v_mfma_f32_16x16x32_bf16 v[184:187], a[216:219], v[36:39], v[184:187]\n"
"buffer_load_dwordx4 v7, s[36:39], s61 offen  nt lds\n"
"v_mfma_f32_16x16x32_bf16 v[188:191], a[220:223], v[36:39], v[188:191]\n"
"v_exp_f32_e32 v23, v23\n"
"v_mfma_f32_16x16x32_bf16 a[240:243], a[224:227], v[36:39], a[240:243]\n"
"v_cvt_pk_bf16_f32 v35, v22, v23\n"
"s_mul_i32 s70, s50, 3014656\n"
"s_cmp_gt_u32 s63, s50\n"
"s_cselect_b32 s71, 0, 16384\n"
"s_add_u32 s70, s70, s71\n"
"s_add_u32 s40, s68, s70\n"
"s_addc_u32 s41, s69, 0\n"
"s_cmp_eq_u32 s63, s50\n"
"s_cselect_b32 s71, 5, 6\n"
"s_mul_i32 s42, s71, 16384\n"
"s_mul_i32 s60, s64, s71\n"
"s_mul_i32 s60, s60, 4096\n"
"s_cselect_b32 s62, 1073741824, s60\n"
"v_mfma_f32_16x16x32_bf16 v[16:19], a[96:99], a[0:3], 0\n"
"v_mul_f32_e32 v24, s44, v24\n"
"v_mul_f32_e32 v25, s44, v25\n"
"v_mfma_f32_16x16x32_bf16 v[16:19], a[100:103], a[4:7], v[16:19]\n"
"s_waitcnt lgkmcnt(15)\n"
"v_exp_f32_e32 v24, v24\n"
"v_mfma_f32_16x16x32_bf16 v[16:19], a[104:107], a[8:11], v[16:19]\n"
"v_mul_f32_e32 v26, s44, v26\n"
"v_mfma_f32_16x16x32_bf16 v[16:19], a[108:111], a[12:15], v[16:19]\n"
"v_exp_f32_e32 v25, v25\n"
"v_mfma_f32_16x16x32_bf16 v[20:23], a[112:115], a[0:3], 0\n"
"v_mul_f32_e32 v27, s44, v27\n"
"v_cvt_pk_bf16_f32 v36, v24, v25\n"
"v_mfma_f32_16x16x32_bf16 v[20:23], a[116:119], a[4:7], v[20:23]\n"
"s_waitcnt vmcnt(20) lgkmcnt(0)\n"
"s_barrier\n"
"v_exp_f32_e32 v26, v26\n"
"v_mfma_f32_16x16x32_bf16 v[20:23], a[120:123], a[8:11], v[20:23]\n"
"ds_read_b128 a[128:131], v1 offset:8192\n"
"ds_read_b128 a[140:143], v1 offset:9728\n"
"v_mul_f32_e32 v28, s44, v28\n"
"v_mfma_f32_16x16x32_bf16 v[20:23], a[124:127], a[12:15], v[20:23]\n"
"v_exp_f32_e32 v27, v27\n"
"v_mfma_f32_16x16x32_bf16 v[192:195], a[192:195], v[32:35], v[192:195]\n"
"v_mul_f32_e32 v29, s44, v29\n"
"v_cvt_pk_bf16_f32 v37, v26, v27\n"
"v_mfma_f32_16x16x32_bf16 v[196:199], a[196:199], v[32:35], v[196:199]\n"
"v_exp_f32_e32 v28, v28\n"
"v_mfma_f32_16x16x32_bf16 v[200:203], a[200:203], v[32:35], v[200:203]\n"
"ds_read_b128 a[132:135], v1 offset:8704\n"
"v_mul_f32_e32 v30, s44, v30\n"
"v_mfma_f32_16x16x32_bf16 v[204:207], a[204:207], v[32:35], v[204:207]\n"
"v_exp_f32_e32 v29, v29\n"
"v_mfma_f32_16x16x32_bf16 v[208:211], a[208:211], v[32:35], v[208:211]\n"
"s_add_u32 m0, s48, 0\n"
"v_mul_f32_e32 v31, s44, v31\n"
"v_cvt_pk_bf16_f32 v38, v28, v29\n"
"v_mfma_f32_16x16x32_bf16 v[212:215], a[212:215], v[32:35], v[212:215]\n"
"s_add_u32 s47, s46, 0\n"
"v_exp_f32_e32 v30, v30\n"
"v_mfma_f32_16x16x32_bf16 v[216:219], a[216:219], v[32:35], v[216:219]\n"
"buffer_load_dwordx4 v6, s[24:27], s47 offen lds\n"
"v_mfma_f32_16x16x32_bf16 v[220:223], a[220:223], v[32:35], v[220:223]\n"
"v_exp_f32_e32 v31, v31\n"
"v_mfma_f32_16x16x32_bf16 a[244:247], a[224:227], v[32:35], a[244:247]\n"
"ds_read_b128 a[136:139], v1 offset:9216\n"
"v_cvt_pk_bf16_f32 v39, v30, v31\n"
"v_mfma_f32_16x16x32_bf16 v[24:27], a[96:99], a[16:19], 0\n"
"v_mul_f32_e32 v16, s44, v16\n"
"v_mul_f32_e32 v17, s44, v17\n"
"v_mfma_f32_16x16x32_bf16 v[24:27], a[100:103], a[20:23], v[24:27]\n"
"v_exp_f32_e32 v16, v16\n"
"v_mfma_f32_16x16x32_bf16 v[24:27], a[104:107], a[24:27], v[24:27]\n"
"ds_read_b128 a[144:147], v2 offset:8192\n"
"v_mul_f32_e32 v18, s44, v18\n"
"v_mfma_f32_16x16x32_bf16 v[24:27], a[108:111], a[28:31], v[24:27]\n"
"v_exp_f32_e32 v17, v17\n"
"v_mfma_f32_16x16x32_bf16 v[28:31], a[112:115], a[16:19], 0\n"
"v_mul_f32_e32 v19, s44, v19\n"
"v_cvt_pk_bf16_f32 v32, v16, v17\n"
"v_mfma_f32_16x16x32_bf16 v[28:31], a[116:119], a[20:23], v[28:31]\n"
"v_exp_f32_e32 v18, v18\n"
"v_mfma_f32_16x16x32_bf16 v[28:31], a[120:123], a[24:27], v[28:31]\n"
"ds_read_b128 a[148:151], v2 offset:8704\n"
"v_mul_f32_e32 v20, s44, v20\n"
"v_mfma_f32_16x16x32_bf16 v[28:31], a[124:127], a[28:31], v[28:31]\n"
"v_exp_f32_e32 v19, v19\n"
"v_mfma_f32_16x16x32_bf16 v[224:227], a[192:195], v[36:39], v[224:227]\n"
"v_mul_f32_e32 v21, s44, v21\n"
"v_cvt_pk_bf16_f32 v33, v18, v19\n"
"v_mfma_f32_16x16x32_bf16 v[228:231], a[196:199], v[36:39], v[228:231]\n"
"v_exp_f32_e32 v20, v20\n"
"v_mfma_f32_16x16x32_bf16 v[232:235], a[200:203], v[36:39], v[232:235]\n"
"ds_read_b128 a[152:155], v2 offset:9216\n"
"v_mul_f32_e32 v22, s44, v22\n"
"v_mfma_f32_16x16x32_bf16 v[236:239], a[204:207], v[36:39], v[236:239]\n"
"v_exp_f32_e32 v21, v21\n"
"v_mfma_f32_16x16x32_bf16 v[240:243], a[208:211], v[36:39], v[240:243]\n"
"s_add_u32 m0, s48, 1024\n"
"v_mul_f32_e32 v23, s44, v23\n"
"v_cvt_pk_bf16_f32 v34, v20, v21\n"
"v_mfma_f32_16x16x32_bf16 v[244:247], a[212:215], v[36:39], v[244:247]\n"
"s_add_u32 s47, s46, 128\n"
"v_exp_f32_e32 v22, v22\n"
"v_mfma_f32_16x16x32_bf16 v[248:251], a[216:219], v[36:39], v[248:251]\n"
"buffer_load_dwordx4 v6, s[24:27], s47 offen lds\n"
"v_mfma_f32_16x16x32_bf16 v[252:255], a[220:223], v[36:39], v[252:255]\n"
"v_exp_f32_e32 v23, v23\n"
"v_mfma_f32_16x16x32_bf16 a[248:251], a[224:227], v[36:39], a[248:251]\n"
"ds_read_b128 a[156:159], v2 offset:9728\n"
"v_cvt_pk_bf16_f32 v35, v22, v23\n"
"v_mfma_f32_16x16x32_bf16 v[16:19], a[96:99], a[32:35], 0\n"
"v_mul_f32_e32 v24, s44, v24\n"
"v_mul_f32_e32 v25, s44, v25\n"
"v_mfma_f32_16x16x32_bf16 v[16:19], a[100:103], a[36:39], v[16:19]\n"
"v_exp_f32_e32 v24, v24\n"
"v_mfma_f32_16x16x32_bf16 v[16:19], a[104:107], a[40:43], v[16:19]\n"
"ds_read_b64_tr_b16 a[192:193], v3 offset:8192\n"
"v_mul_f32_e32 v26, s44, v26\n"
"v_mfma_f32_16x16x32_bf16 v[16:19], a[108:111], a[44:47], v[16:19]\n"
"v_exp_f32_e32 v25, v25\n"
"v_mfma_f32_16x16x32_bf16 v[20:23], a[112:115], a[32:35], 0\n"
"v_mul_f32_e32 v27, s44, v27\n"
"v_cvt_pk_bf16_f32 v36, v24, v25\n"
"v_mfma_f32_16x16x32_bf16 v[20:23], a[116:119], a[36:39], v[20:23]\n"
"v_exp_f32_e32 v26, v26\n"
"v_mfma_f32_16x16x32_bf16 v[20:23], a[120:123], a[40:43], v[20:23]\n"
"ds_read_b64_tr_b16 a[194:195], v4 offset:8192\n"
"v_mul_f32_e32 v28, s44, v28\n"
"v_mfma_f32_16x16x32_bf16 v[20:23], a[124:127], a[44:47], v[20:23]\n"
"v_exp_f32_e32 v27, v27\n"
"v_mfma_f32_16x16x32_bf16 v[64:67], a[160:163], v[32:35], v[64:67]\n"
"v_mul_f32_e32 v29, s44, v29\n"
"v_cvt_pk_bf16_f32 v37, v26, v27\n"
"v_mfma_f32_16x16x32_bf16 v[68:71], a[164:167], v[32:35], v[68:71]\n"
"v_exp_f32_e32 v28, v28\n"
"v_mfma_f32_16x16x32_bf16 v[72:75], a[168:171], v[32:35], v[72:75]\n"
"ds_read_b64_tr_b16 a[196:197], v11 offset:8192\n"
"v_mul_f32_e32 v30, s44, v30\n"
"v_mfma_f32_16x16x32_bf16 v[76:79], a[172:175], v[32:35], v[76:79]\n"
"v_exp_f32_e32 v29, v29\n"
"v_mfma_f32_16x16x32_bf16 v[80:83], a[176:179], v[32:35], v[80:83]\n"
"s_add_u32 m0, s48, 32768\n"
"v_mul_f32_e32 v31, s44, v31\n"
"v_cvt_pk_bf16_f32 v38, v28, v29\n"
"v_mfma_f32_16x16x32_bf16 v[84:87], a[180:183], v[32:35], v[84:87]\n"
"s_add_u32 s76, s46, 0\n"
"v_exp_f32_e32 v30, v30\n"
"v_mfma_f32_16x16x32_bf16 v[88:91], a[184:187], v[32:35], v[88:91]\n"
"buffer_load_dwordx4 v6, s[28:31], s76 offen lds\n"
"v_mfma_f32_16x16x32_bf16 v[92:95], a[188:191], v[32:35], v[92:95]\n"
"v_exp_f32_e32 v31, v31\n"
"v_mfma_f32_16x16x32_bf16 a[228:231], a[224:227], v[32:35], a[228:231]\n"
"ds_read_b64_tr_b16 a[198:199], v14 offset:8192\n"
"v_cvt_pk_bf16_f32 v39, v30, v31\n"
"v_mfma_f32_16x16x32_bf16 v[24:27], a[96:99], a[48:51], 0\n"
"v_mul_f32_e32 v16, s44, v16\n"
"v_mul_f32_e32 v17, s44, v17\n"
"v_mfma_f32_16x16x32_bf16 v[24:27], a[100:103], a[52:55], v[24:27]\n"
"v_exp_f32_e32 v16, v16\n"
"v_mfma_f32_16x16x32_bf16 v[24:27], a[104:107], a[56:59], v[24:27]\n"
"ds_read_b64_tr_b16 a[200:201], v3 offset:8704\n"
"v_mul_f32_e32 v18, s44, v18\n"
"v_mfma_f32_16x16x32_bf16 v[24:27], a[108:111], a[60:63], v[24:27]\n"
"v_exp_f32_e32 v17, v17\n"
"v_mfma_f32_16x16x32_bf16 v[28:31], a[112:115], a[48:51], 0\n"
"v_mul_f32_e32 v19, s44, v19\n"
"v_cvt_pk_bf16_f32 v32, v16, v17\n"
"v_mfma_f32_16x16x32_bf16 v[28:31], a[116:119], a[52:55], v[28:31]\n"
"v_exp_f32_e32 v18, v18\n"
"v_mfma_f32_16x16x32_bf16 v[28:31], a[120:123], a[56:59], v[28:31]\n"
"ds_read_b64_tr_b16 a[202:203], v4 offset:8704\n"
"v_mul_f32_e32 v20, s44, v20\n"
"v_mfma_f32_16x16x32_bf16 v[28:31], a[124:127], a[60:63], v[28:31]\n"
"v_exp_f32_e32 v19, v19\n"
"v_mfma_f32_16x16x32_bf16 v[96:99], a[160:163], v[36:39], v[96:99]\n"
"v_mul_f32_e32 v21, s44, v21\n"
"v_cvt_pk_bf16_f32 v33, v18, v19\n"
"v_mfma_f32_16x16x32_bf16 v[100:103], a[164:167], v[36:39], v[100:103]\n"
"v_exp_f32_e32 v20, v20\n"
"v_mfma_f32_16x16x32_bf16 v[104:107], a[168:171], v[36:39], v[104:107]\n"
"ds_read_b64_tr_b16 a[204:205], v11 offset:8704\n"
"v_mul_f32_e32 v22, s44, v22\n"
"v_mfma_f32_16x16x32_bf16 v[108:111], a[172:175], v[36:39], v[108:111]\n"
"v_exp_f32_e32 v21, v21\n"
"v_mfma_f32_16x16x32_bf16 v[112:115], a[176:179], v[36:39], v[112:115]\n"
"s_add_u32 m0, s48, 33792\n"
"v_mul_f32_e32 v23, s44, v23\n"
"v_cvt_pk_bf16_f32 v34, v20, v21\n"
"v_mfma_f32_16x16x32_bf16 v[116:119], a[180:183], v[36:39], v[116:119]\n"
"s_add_u32 s76, s46, 128\n"
"v_exp_f32_e32 v22, v22\n"
"v_mfma_f32_16x16x32_bf16 v[120:123], a[184:187], v[36:39], v[120:123]\n"
"buffer_load_dwordx4 v6, s[28:31], s76 offen lds\n"
"v_mfma_f32_16x16x32_bf16 v[124:127], a[188:191], v[36:39], v[124:127]\n"
"v_exp_f32_e32 v23, v23\n"
"v_mfma_f32_16x16x32_bf16 a[232:235], a[224:227], v[36:39], a[232:235]\n"
"ds_read_b64_tr_b16 a[206:207], v14 offset:8704\n"
"v_cvt_pk_bf16_f32 v35, v22, v23\n"
"v_mfma_f32_16x16x32_bf16 v[16:19], a[96:99], a[64:67], 0\n"
"v_mul_f32_e32 v24, s44, v24\n"
"v_mul_f32_e32 v25, s44, v25\n"
"v_mfma_f32_16x16x32_bf16 v[16:19], a[100:103], a[68:71], v[16:19]\n"
"v_exp_f32_e32 v24, v24\n"
"v_mfma_f32_16x16x32_bf16 v[16:19], a[104:107], a[72:75], v[16:19]\n"
"ds_read_b64_tr_b16 a[208:209], v3 offset:9216\n"
"v_mul_f32_e32 v26, s44, v26\n"
"v_mfma_f32_16x16x32_bf16 v[16:19], a[108:111], a[76:79], v[16:19]\n"
"v_exp_f32_e32 v25, v25\n"
"v_mfma_f32_16x16x32_bf16 v[20:23], a[112:115], a[64:67], 0\n"
"v_mul_f32_e32 v27, s44, v27\n"
"v_cvt_pk_bf16_f32 v36, v24, v25\n"
"v_mfma_f32_16x16x32_bf16 v[20:23], a[116:119], a[68:71], v[20:23]\n"
"v_exp_f32_e32 v26, v26\n"
"v_mfma_f32_16x16x32_bf16 v[20:23], a[120:123], a[72:75], v[20:23]\n"
"ds_read_b64_tr_b16 a[210:211], v4 offset:9216\n"
"v_mul_f32_e32 v28, s44, v28\n"
"v_mfma_f32_16x16x32_bf16 v[20:23], a[124:127], a[76:79], v[20:23]\n"
"v_exp_f32_e32 v27, v27\n"
"v_mfma_f32_16x16x32_bf16 v[128:131], a[160:163], v[32:35], v[128:131]\n"
"v_mul_f32_e32 v29, s44, v29\n"
"v_cvt_pk_bf16_f32 v37, v26, v27\n"
"v_mfma_f32_16x16x32_bf16 v[132:135], a[164:167], v[32:35], v[132:135]\n"
"v_exp_f32_e32 v28, v28\n"
"v_mfma_f32_16x16x32_bf16 v[136:139], a[168:171], v[32:35], v[136:139]\n"
"ds_read_b64_tr_b16 a[212:213], v11 offset:9216\n"
"v_mul_f32_e32 v30, s44, v30\n"
"v_mfma_f32_16x16x32_bf16 v[140:143], a[172:175], v[32:35], v[140:143]\n"
"v_exp_f32_e32 v29, v29\n"
"v_mfma_f32_16x16x32_bf16 v[144:147], a[176:179], v[32:35], v[144:147]\n"
"s_add_u32 m0, s49, 22528\n"
"v_mul_f32_e32 v31, s44, v31\n"
"v_cvt_pk_bf16_f32 v38, v28, v29\n"
"v_mfma_f32_16x16x32_bf16 v[148:151], a[180:183], v[32:35], v[148:151]\n"
"s_add_u32 s61, s59, 20480\n"
"v_exp_f32_e32 v30, v30\n"
"v_mfma_f32_16x16x32_bf16 v[152:155], a[184:187], v[32:35], v[152:155]\n"
"buffer_load_dwordx4 v8, s[36:39], s61 offen  nt lds\n"
"v_mfma_f32_16x16x32_bf16 v[156:159], a[188:191], v[32:35], v[156:159]\n"
"v_exp_f32_e32 v31, v31\n"
"v_mfma_f32_16x16x32_bf16 a[236:239], a[224:227], v[32:35], a[236:239]\n"
"ds_read_b64_tr_b16 a[214:215], v14 offset:9216\n"
"v_cvt_pk_bf16_f32 v39, v30, v31\n"
"v_mfma_f32_16x16x32_bf16 v[24:27], a[96:99], a[80:83], 0\n"
"ds_read_b64_tr_b16 a[222:223], v14 offset:9728\n"
"v_mul_f32_e32 v16, s44, v16\n"
"v_mul_f32_e32 v17, s44, v17\n"
"v_mfma_f32_16x16x32_bf16 v[24:27], a[100:103], a[84:87], v[24:27]\n"
"v_exp_f32_e32 v16, v16\n"
"v_mfma_f32_16x16x32_bf16 v[24:27], a[104:107], a[88:91], v[24:27]\n"
"ds_read_b64_tr_b16 a[216:217], v3 offset:9728\n"
"v_mul_f32_e32 v18, s44, v18\n"
"v_mfma_f32_16x16x32_bf16 v[24:27], a[108:111], a[92:95], v[24:27]\n"
"v_exp_f32_e32 v17, v17\n"
"v_mfma_f32_16x16x32_bf16 v[28:31], a[112:115], a[80:83], 0\n"
"v_mul_f32_e32 v19, s44, v19\n"
"v_cvt_pk_bf16_f32 v32, v16, v17\n"
"v_mfma_f32_16x16x32_bf16 v[28:31], a[116:119], a[84:87], v[28:31]\n"
"v_exp_f32_e32 v18, v18\n"
"v_mfma_f32_16x16x32_bf16 v[28:31], a[120:123], a[88:91], v[28:31]\n"
"ds_read_b64_tr_b16 a[218:219], v4 offset:9728\n"
"v_mul_f32_e32 v20, s44, v20\n"
"v_mfma_f32_16x16x32_bf16 v[28:31], a[124:127], a[92:95], v[28:31]\n"
"v_exp_f32_e32 v19, v19\n"
"v_mfma_f32_16x16x32_bf16 v[160:163], a[160:163], v[36:39], v[160:163]\n"
"v_mul_f32_e32 v21, s44, v21\n"
"v_cvt_pk_bf16_f32 v33, v18, v19\n"
"v_mfma_f32_16x16x32_bf16 v[164:167], a[164:167], v[36:39], v[164:167]\n"
"v_exp_f32_e32 v20, v20\n"
"v_mfma_f32_16x16x32_bf16 v[168:171], a[168:171], v[36:39], v[168:171]\n"
"ds_read_b64_tr_b16 a[220:221], v11 offset:9728\n"
"v_mul_f32_e32 v22, s44, v22\n"
"v_mfma_f32_16x16x32_bf16 v[172:175], a[172:175], v[36:39], v[172:175]\n"
"v_exp_f32_e32 v21, v21\n"
"v_mfma_f32_16x16x32_bf16 v[176:179], a[176:179], v[36:39], v[176:179]\n"
"s_add_u32 m0, s49, 23552\n"
"v_mul_f32_e32 v23, s44, v23\n"
"v_cvt_pk_bf16_f32 v34, v20, v21\n"
"v_mfma_f32_16x16x32_bf16 v[180:183], a[180:183], v[36:39], v[180:183]\n"
"s_add_u32 s61, s59, 20608\n"
"v_exp_f32_e32 v22, v22\n"
"v_mfma_f32_16x16x32_bf16 v[184:187], a[184:187], v[36:39], v[184:187]\n"
"buffer_load_dwordx4 v8, s[36:39], s61 offen  nt lds\n"
"v_mfma_f32_16x16x32_bf16 v[188:191], a[188:191], v[36:39], v[188:191]\n"
"v_exp_f32_e32 v23, v23\n"
"v_mfma_f32_16x16x32_bf16 a[240:243], a[224:227], v[36:39], a[240:243]\n"
"s_waitcnt lgkmcnt(15)\n"
"v_cvt_pk_bf16_f32 v35, v22, v23\n"
"v_mfma_f32_16x16x32_bf16 v[16:19], a[128:131], a[0:3], 0\n"
"v_mul_f32_e32 v24, s44, v24\n"
"v_mul_f32_e32 v25, s44, v25\n"
"v_mfma_f32_16x16x32_bf16 v[16:19], a[132:135], a[4:7], v[16:19]\n"
"v_exp_f32_e32 v24, v24\n"
"v_mfma_f32_16x16x32_bf16 v[16:19], a[136:139], a[8:11], v[16:19]\n"
"v_mul_f32_e32 v26, s44, v26\n"
"v_mfma_f32_16x16x32_bf16 v[16:19], a[140:143], a[12:15], v[16:19]\n"
"v_exp_f32_e32 v25, v25\n"
"v_mfma_f32_16x16x32_bf16 v[20:23], a[144:147], a[0:3], 0\n"
"v_mul_f32_e32 v27, s44, v27\n"
"v_cvt_pk_bf16_f32 v36, v24, v25\n"
"v_mfma_f32_16x16x32_bf16 v[20:23], a[148:151], a[4:7], v[20:23]\n"
"s_waitcnt vmcnt(19) lgkmcnt(0)\n"
"s_barrier\n"
"v_exp_f32_e32 v26, v26\n"
"v_mfma_f32_16x16x32_bf16 v[20:23], a[152:155], a[8:11], v[20:23]\n"
"ds_read_b128 a[96:99], v1 offset:16384\n"
"ds_read_b128 a[108:111], v1 offset:17920\n"
"v_mul_f32_e32 v28, s44, v28\n"
"v_mfma_f32_16x16x32_bf16 v[20:23], a[156:159], a[12:15], v[20:23]\n"
"v_exp_f32_e32 v27, v27\n"
"v_mfma_f32_16x16x32_bf16 v[192:195], a[160:163], v[32:35], v[192:195]\n"
"v_mul_f32_e32 v29, s44, v29\n"
"v_cvt_pk_bf16_f32 v37, v26, v27\n"
"v_mfma_f32_16x16x32_bf16 v[196:199], a[164:167], v[32:35], v[196:199]\n"
"v_exp_f32_e32 v28, v28\n"
"v_mfma_f32_16x16x32_bf16 v[200:203], a[168:171], v[32:35], v[200:203]\n"
"ds_read_b128 a[100:103], v1 offset:16896\n"
"v_mul_f32_e32 v30, s44, v30\n"
"v_mfma_f32_16x16x32_bf16 v[204:207], a[172:175], v[32:35], v[204:207]\n"
"v_exp_f32_e32 v29, v29\n"
"v_mfma_f32_16x16x32_bf16 v[208:211], a[176:179], v[32:35], v[208:211]\n"
"s_add_u32 m0, s48, 8192\n"
"v_mul_f32_e32 v31, s44, v31\n"
"v_cvt_pk_bf16_f32 v38, v28, v29\n"
"v_mfma_f32_16x16x32_bf16 v[212:215], a[180:183], v[32:35], v[212:215]\n"
"s_add_u32 s47, s46, 8192\n"
"v_exp_f32_e32 v30, v30\n"
"v_mfma_f32_16x16x32_bf16 v[216:219], a[184:187], v[32:35], v[216:219]\n"
"buffer_load_dwordx4 v6, s[24:27], s47 offen lds\n"
"v_mfma_f32_16x16x32_bf16 v[220:223], a[188:191], v[32:35], v[220:223]\n"
"v_exp_f32_e32 v31, v31\n"
"v_mfma_f32_16x16x32_bf16 a[244:247], a[224:227], v[32:35], a[244:247]\n"
"ds_read_b128 a[104:107], v1 offset:17408\n"
"v_cvt_pk_bf16_f32 v39, v30, v31\n"
"v_mfma_f32_16x16x32_bf16 v[24:27], a[128:131], a[16:19], 0\n"
"v_mul_f32_e32 v16, s44, v16\n"
"v_mul_f32_e32 v17, s44, v17\n"
"v_mfma_f32_16x16x32_bf16 v[24:27], a[132:135], a[20:23], v[24:27]\n"
"v_exp_f32_e32 v16, v16\n"
"v_mfma_f32_16x16x32_bf16 v[24:27], a[136:139], a[24:27], v[24:27]\n"
"ds_read_b128 a[112:115], v2 offset:16384\n"
"v_mul_f32_e32 v18, s44, v18\n"
"v_mfma_f32_16x16x32_bf16 v[24:27], a[140:143], a[28:31], v[24:27]\n"
"v_exp_f32_e32 v17, v17\n"
"v_mfma_f32_16x16x32_bf16 v[28:31], a[144:147], a[16:19], 0\n"
"v_mul_f32_e32 v19, s44, v19\n"
"v_cvt_pk_bf16_f32 v32, v16, v17\n"
"v_mfma_f32_16x16x32_bf16 v[28:31], a[148:151], a[20:23], v[28:31]\n"
"v_exp_f32_e32 v18, v18\n"
"v_mfma_f32_16x16x32_bf16 v[28:31], a[152:155], a[24:27], v[28:31]\n"
"ds_read_b128 a[116:119], v2 offset:16896\n"
"v_mul_f32_e32 v20, s44, v20\n"
"v_mfma_f32_16x16x32_bf16 v[28:31], a[156:159], a[28:31], v[28:31]\n"
"v_exp_f32_e32 v19, v19\n"
"v_mfma_f32_16x16x32_bf16 v[224:227], a[160:163], v[36:39], v[224:227]\n"
"v_mul_f32_e32 v21, s44, v21\n"
"v_cvt_pk_bf16_f32 v33, v18, v19\n"
"v_mfma_f32_16x16x32_bf16 v[228:231], a[164:167], v[36:39], v[228:231]\n"
"v_exp_f32_e32 v20, v20\n"
"v_mfma_f32_16x16x32_bf16 v[232:235], a[168:171], v[36:39], v[232:235]\n"
"ds_read_b128 a[120:123], v2 offset:17408\n"
"v_mul_f32_e32 v22, s44, v22\n"
"v_mfma_f32_16x16x32_bf16 v[236:239], a[172:175], v[36:39], v[236:239]\n"
"v_exp_f32_e32 v21, v21\n"
"v_mfma_f32_16x16x32_bf16 v[240:243], a[176:179], v[36:39], v[240:243]\n"
"s_add_u32 m0, s48, 9216\n"
"v_mul_f32_e32 v23, s44, v23\n"
"v_cvt_pk_bf16_f32 v34, v20, v21\n"
"v_mfma_f32_16x16x32_bf16 v[244:247], a[180:183], v[36:39], v[244:247]\n"
"s_add_u32 s47, s46, 8320\n"
"v_exp_f32_e32 v22, v22\n"
"v_mfma_f32_16x16x32_bf16 v[248:251], a[184:187], v[36:39], v[248:251]\n"
"buffer_load_dwordx4 v6, s[24:27], s47 offen lds\n"
"v_mfma_f32_16x16x32_bf16 v[252:255], a[188:191], v[36:39], v[252:255]\n"
"v_exp_f32_e32 v23, v23\n"
"v_mfma_f32_16x16x32_bf16 a[248:251], a[224:227], v[36:39], a[248:251]\n"
"ds_read_b128 a[124:127], v2 offset:17920\n"
"v_cvt_pk_bf16_f32 v35, v22, v23\n"
"v_mfma_f32_16x16x32_bf16 v[16:19], a[128:131], a[32:35], 0\n"
"v_mul_f32_e32 v24, s44, v24\n"
"v_mul_f32_e32 v25, s44, v25\n"
"v_mfma_f32_16x16x32_bf16 v[16:19], a[132:135], a[36:39], v[16:19]\n"
"v_exp_f32_e32 v24, v24\n"
"v_mfma_f32_16x16x32_bf16 v[16:19], a[136:139], a[40:43], v[16:19]\n"
"ds_read_b64_tr_b16 a[160:161], v3 offset:16384\n"
"v_mul_f32_e32 v26, s44, v26\n"
"v_mfma_f32_16x16x32_bf16 v[16:19], a[140:143], a[44:47], v[16:19]\n"
"v_exp_f32_e32 v25, v25\n"
"v_mfma_f32_16x16x32_bf16 v[20:23], a[144:147], a[32:35], 0\n"
"v_mul_f32_e32 v27, s44, v27\n"
"v_cvt_pk_bf16_f32 v36, v24, v25\n"
"v_mfma_f32_16x16x32_bf16 v[20:23], a[148:151], a[36:39], v[20:23]\n"
"v_exp_f32_e32 v26, v26\n"
"v_mfma_f32_16x16x32_bf16 v[20:23], a[152:155], a[40:43], v[20:23]\n"
"ds_read_b64_tr_b16 a[162:163], v4 offset:16384\n"
"v_mul_f32_e32 v28, s44, v28\n"
"v_mfma_f32_16x16x32_bf16 v[20:23], a[156:159], a[44:47], v[20:23]\n"
"v_exp_f32_e32 v27, v27\n"
"v_mfma_f32_16x16x32_bf16 v[64:67], a[192:195], v[32:35], v[64:67]\n"
"v_mul_f32_e32 v29, s44, v29\n"
"v_cvt_pk_bf16_f32 v37, v26, v27\n"
"v_mfma_f32_16x16x32_bf16 v[68:71], a[196:199], v[32:35], v[68:71]\n"
"v_exp_f32_e32 v28, v28\n"
"v_mfma_f32_16x16x32_bf16 v[72:75], a[200:203], v[32:35], v[72:75]\n"
"ds_read_b64_tr_b16 a[164:165], v11 offset:16384\n"
"v_mul_f32_e32 v30, s44, v30\n"
"v_mfma_f32_16x16x32_bf16 v[76:79], a[204:207], v[32:35], v[76:79]\n"
"v_exp_f32_e32 v29, v29\n"
"v_mfma_f32_16x16x32_bf16 v[80:83], a[208:211], v[32:35], v[80:83]\n"
"s_add_u32 m0, s48, 40960\n"
"v_mul_f32_e32 v31, s44, v31\n"
"v_cvt_pk_bf16_f32 v38, v28, v29\n"
"v_mfma_f32_16x16x32_bf16 v[84:87], a[212:215], v[32:35], v[84:87]\n"
"s_add_u32 s76, s46, 8192\n"
"v_exp_f32_e32 v30, v30\n"
"v_mfma_f32_16x16x32_bf16 v[88:91], a[216:219], v[32:35], v[88:91]\n"
"buffer_load_dwordx4 v6, s[28:31], s76 offen lds\n"
"v_mfma_f32_16x16x32_bf16 v[92:95], a[220:223], v[32:35], v[92:95]\n"
"v_exp_f32_e32 v31, v31\n"
"v_mfma_f32_16x16x32_bf16 a[228:231], a[224:227], v[32:35], a[228:231]\n"
"ds_read_b64_tr_b16 a[166:167], v14 offset:16384\n"
"v_cvt_pk_bf16_f32 v39, v30, v31\n"
"v_mfma_f32_16x16x32_bf16 v[24:27], a[128:131], a[48:51], 0\n"
"v_mul_f32_e32 v16, s44, v16\n"
"v_mul_f32_e32 v17, s44, v17\n"
"v_mfma_f32_16x16x32_bf16 v[24:27], a[132:135], a[52:55], v[24:27]\n"
"v_exp_f32_e32 v16, v16\n"
"v_mfma_f32_16x16x32_bf16 v[24:27], a[136:139], a[56:59], v[24:27]\n"
"ds_read_b64_tr_b16 a[168:169], v3 offset:16896\n"
"v_mul_f32_e32 v18, s44, v18\n"
"v_mfma_f32_16x16x32_bf16 v[24:27], a[140:143], a[60:63], v[24:27]\n"
"v_exp_f32_e32 v17, v17\n"
"v_mfma_f32_16x16x32_bf16 v[28:31], a[144:147], a[48:51], 0\n"
"v_mul_f32_e32 v19, s44, v19\n"
"v_cvt_pk_bf16_f32 v32, v16, v17\n"
"v_mfma_f32_16x16x32_bf16 v[28:31], a[148:151], a[52:55], v[28:31]\n"
"v_exp_f32_e32 v18, v18\n"
"v_mfma_f32_16x16x32_bf16 v[28:31], a[152:155], a[56:59], v[28:31]\n"
"ds_read_b64_tr_b16 a[170:171], v4 offset:16896\n"
"v_mul_f32_e32 v20, s44, v20\n"
"v_mfma_f32_16x16x32_bf16 v[28:31], a[156:159], a[60:63], v[28:31]\n"
"v_exp_f32_e32 v19, v19\n"
"v_mfma_f32_16x16x32_bf16 v[96:99], a[192:195], v[36:39], v[96:99]\n"
"v_mul_f32_e32 v21, s44, v21\n"
"v_cvt_pk_bf16_f32 v33, v18, v19\n"
"v_mfma_f32_16x16x32_bf16 v[100:103], a[196:199], v[36:39], v[100:103]\n"
"v_exp_f32_e32 v20, v20\n"
"v_mfma_f32_16x16x32_bf16 v[104:107], a[200:203], v[36:39], v[104:107]\n"
"ds_read_b64_tr_b16 a[172:173], v11 offset:16896\n"
"v_mul_f32_e32 v22, s44, v22\n"
"v_mfma_f32_16x16x32_bf16 v[108:111], a[204:207], v[36:39], v[108:111]\n"
"v_exp_f32_e32 v21, v21\n"
"v_mfma_f32_16x16x32_bf16 v[112:115], a[208:211], v[36:39], v[112:115]\n"
"s_add_u32 m0, s48, 41984\n"
"v_mul_f32_e32 v23, s44, v23\n"
"v_cvt_pk_bf16_f32 v34, v20, v21\n"
"v_mfma_f32_16x16x32_bf16 v[116:119], a[212:215], v[36:39], v[116:119]\n"
"s_add_u32 s76, s46, 8320\n"
"v_exp_f32_e32 v22, v22\n"
"v_mfma_f32_16x16x32_bf16 v[120:123], a[216:219], v[36:39], v[120:123]\n"
"buffer_load_dwordx4 v6, s[28:31], s76 offen lds\n"
"v_mfma_f32_16x16x32_bf16 v[124:127], a[220:223], v[36:39], v[124:127]\n"
"v_exp_f32_e32 v23, v23\n"
"v_mfma_f32_16x16x32_bf16 a[232:235], a[224:227], v[36:39], a[232:235]\n"
"ds_read_b64_tr_b16 a[174:175], v14 offset:16896\n"
"v_cvt_pk_bf16_f32 v35, v22, v23\n"
"v_mfma_f32_16x16x32_bf16 v[16:19], a[128:131], a[64:67], 0\n"
"v_mul_f32_e32 v24, s44, v24\n"
"v_mul_f32_e32 v25, s44, v25\n"
"v_mfma_f32_16x16x32_bf16 v[16:19], a[132:135], a[68:71], v[16:19]\n"
"v_exp_f32_e32 v24, v24\n"
"v_mfma_f32_16x16x32_bf16 v[16:19], a[136:139], a[72:75], v[16:19]\n"
"ds_read_b64_tr_b16 a[178:179], v4 offset:17408\n"
"v_mul_f32_e32 v26, s44, v26\n"
"v_mfma_f32_16x16x32_bf16 v[16:19], a[140:143], a[76:79], v[16:19]\n"
"v_exp_f32_e32 v25, v25\n"
"v_mfma_f32_16x16x32_bf16 v[20:23], a[144:147], a[64:67], 0\n"
"v_mul_f32_e32 v27, s44, v27\n"
"v_cvt_pk_bf16_f32 v36, v24, v25\n"
"v_mfma_f32_16x16x32_bf16 v[20:23], a[148:151], a[68:71], v[20:23]\n"
"v_exp_f32_e32 v26, v26\n"
"v_mfma_f32_16x16x32_bf16 v[20:23], a[152:155], a[72:75], v[20:23]\n"
"ds_read_b64_tr_b16 a[180:181], v11 offset:17408\n"
"v_mul_f32_e32 v28, s44, v28\n"
"v_mfma_f32_16x16x32_bf16 v[20:23], a[156:159], a[76:79], v[20:23]\n"
"v_exp_f32_e32 v27, v27\n"
"v_mfma_f32_16x16x32_bf16 v[128:131], a[192:195], v[32:35], v[128:131]\n"
"v_mul_f32_e32 v29, s44, v29\n"
"v_cvt_pk_bf16_f32 v37, v26, v27\n"
"v_mfma_f32_16x16x32_bf16 v[132:135], a[196:199], v[32:35], v[132:135]\n"
"v_exp_f32_e32 v28, v28\n"
"v_mfma_f32_16x16x32_bf16 v[136:139], a[200:203], v[32:35], v[136:139]\n"
"ds_read_b64_tr_b16 a[182:183], v14 offset:17408\n"
"v_mul_f32_e32 v30, s44, v30\n"
"v_mfma_f32_16x16x32_bf16 v[140:143], a[204:207], v[32:35], v[140:143]\n"
"v_exp_f32_e32 v29, v29\n"
"v_mfma_f32_16x16x32_bf16 v[144:147], a[208:211], v[32:35], v[144:147]\n"
"v_mul_f32_e32 v31, s44, v31\n"
"v_cvt_pk_bf16_f32 v38, v28, v29\n"
"v_mfma_f32_16x16x32_bf16 v[148:151], a[212:215], v[32:35], v[148:151]\n"
"v_exp_f32_e32 v30, v30\n"
"v_mfma_f32_16x16x32_bf16 v[152:155], a[216:219], v[32:35], v[152:155]\n"
"ds_read_b64_tr_b16 a[176:177], v3 offset:17408\n"
"v_mfma_f32_16x16x32_bf16 v[156:159], a[220:223], v[32:35], v[156:159]\n"
"v_exp_f32_e32 v31, v31\n"
"v_mfma_f32_16x16x32_bf16 a[236:239], a[224:227], v[32:35], a[236:239]\n"
"v_cvt_pk_bf16_f32 v39, v30, v31\n"
"v_mfma_f32_16x16x32_bf16 v[24:27], a[128:131], a[80:83], 0\n"
"ds_read_b64_tr_b16 a[190:191], v14 offset:17920\n"
"v_mul_f32_e32 v16, s44, v16\n"
"v_mul_f32_e32 v17, s44, v17\n"
"v_mfma_f32_16x16x32_bf16 v[24:27], a[132:135], a[84:87], v[24:27]\n"
"v_exp_f32_e32 v16, v16\n"
"v_mfma_f32_16x16x32_bf16 v[24:27], a[136:139], a[88:91], v[24:27]\n"
"ds_read_b64_tr_b16 a[184:185], v3 offset:17920\n"
"v_mul_f32_e32 v18, s44, v18\n"
"v_mfma_f32_16x16x32_bf16 v[24:27], a[140:143], a[92:95], v[24:27]\n"
"v_exp_f32_e32 v17, v17\n"
"v_mfma_f32_16x16x32_bf16 v[28:31], a[144:147], a[80:83], 0\n"
"v_mul_f32_e32 v19, s44, v19\n"
"v_cvt_pk_bf16_f32 v32, v16, v17\n"
"v_mfma_f32_16x16x32_bf16 v[28:31], a[148:151], a[84:87], v[28:31]\n"
"v_exp_f32_e32 v18, v18\n"
"v_mfma_f32_16x16x32_bf16 v[28:31], a[152:155], a[88:91], v[28:31]\n"
"ds_read_b64_tr_b16 a[186:187], v4 offset:17920\n"
"v_mul_f32_e32 v20, s44, v20\n"
"v_mfma_f32_16x16x32_bf16 v[28:31], a[156:159], a[92:95], v[28:31]\n"
"v_exp_f32_e32 v19, v19\n"
"v_mfma_f32_16x16x32_bf16 v[160:163], a[192:195], v[36:39], v[160:163]\n"
"v_mul_f32_e32 v21, s44, v21\n"
"v_cvt_pk_bf16_f32 v33, v18, v19\n"
"v_mfma_f32_16x16x32_bf16 v[164:167], a[196:199], v[36:39], v[164:167]\n"
"v_exp_f32_e32 v20, v20\n"
"v_mfma_f32_16x16x32_bf16 v[168:171], a[200:203], v[36:39], v[168:171]\n"
"ds_read_b64_tr_b16 a[188:189], v11 offset:17920\n"
"v_mul_f32_e32 v22, s44, v22\n"
"v_mfma_f32_16x16x32_bf16 v[172:175], a[204:207], v[36:39], v[172:175]\n"
"v_exp_f32_e32 v21, v21\n"
"v_mfma_f32_16x16x32_bf16 v[176:179], a[208:211], v[36:39], v[176:179]\n"
"v_mul_f32_e32 v23, s44, v23\n"
"v_cvt_pk_bf16_f32 v34, v20, v21\n"
"v_mfma_f32_16x16x32_bf16 v[180:183], a[212:215], v[36:39], v[180:183]\n"
"v_exp_f32_e32 v22, v22\n"
"v_mfma_f32_16x16x32_bf16 v[184:187], a[216:219], v[36:39], v[184:187]\n"
"v_mfma_f32_16x16x32_bf16 v[188:191], a[220:223], v[36:39], v[188:191]\n"
"v_exp_f32_e32 v23, v23\n"
"v_mfma_f32_16x16x32_bf16 a[240:243], a[224:227], v[36:39], a[240:243]\n"
"v_cvt_pk_bf16_f32 v35, v22, v23\n"
"s_waitcnt lgkmcnt(15)\n"
"s_cmp_eq_u32 s50, 3\n"
"s_cbranch_scc1 L_E6%=\n"
"L_S6%=:\n"
"v_mfma_f32_16x16x32_bf16 v[16:19], a[96:99], a[0:3], 0\n"
"v_mul_f32_e32 v24, s44, v24\n"
"v_mul_f32_e32 v25, s44, v25\n"
"v_mfma_f32_16x16x32_bf16 v[16:19], a[100:103], a[4:7], v[16:19]\n"
"v_exp_f32_e32 v24, v24\n"
"v_mfma_f32_16x16x32_bf16 v[16:19], a[104:107], a[8:11], v[16:19]\n"
"v_mul_f32_e32 v26, s44, v26\n"
"v_mfma_f32_16x16x32_bf16 v[16:19], a[108:111], a[12:15], v[16:19]\n"
"v_exp_f32_e32 v25, v25\n"
"v_mfma_f32_16x16x32_bf16 v[20:23], a[112:115], a[0:3], 0\n"
"v_mul_f32_e32 v27, s44, v27\n"
"v_cvt_pk_bf16_f32 v36, v24, v25\n"
"v_mfma_f32_16x16x32_bf16 v[20:23], a[116:119], a[4:7], v[20:23]\n"
"s_waitcnt vmcnt(13) lgkmcnt(0)\n"
"s_barrier\n"
"v_exp_f32_e32 v26, v26\n"
"v_mfma_f32_16x16x32_bf16 v[20:23], a[120:123], a[8:11], v[20:23]\n"
"ds_read_b128 a[128:131], v1 offset:24576\n"
"ds_read_b128 a[140:143], v1 offset:26112\n"
"v_mul_f32_e32 v28, s44, v28\n"
"v_mfma_f32_16x16x32_bf16 v[20:23], a[124:127], a[12:15], v[20:23]\n"
"v_exp_f32_e32 v27, v27\n"
"v_mfma_f32_16x16x32_bf16 v[192:195], a[192:195], v[32:35], v[192:195]\n"
"v_mul_f32_e32 v29, s44, v29\n"
"v_cvt_pk_bf16_f32 v37, v26, v27\n"
"v_mfma_f32_16x16x32_bf16 v[196:199], a[196:199], v[32:35], v[196:199]\n"
"v_exp_f32_e32 v28, v28\n"
"v_mfma_f32_16x16x32_bf16 v[200:203], a[200:203], v[32:35], v[200:203]\n"
"ds_read_b128 a[132:135], v1 offset:25088\n"
"v_mul_f32_e32 v30, s44, v30\n"
"v_mfma_f32_16x16x32_bf16 v[204:207], a[204:207], v[32:35], v[204:207]\n"
"v_exp_f32_e32 v29, v29\n"
"v_mfma_f32_16x16x32_bf16 v[208:211], a[208:211], v[32:35], v[208:211]\n"
"s_add_u32 m0, s48, 16384\n"
"v_mul_f32_e32 v31, s44, v31\n"
"v_cvt_pk_bf16_f32 v38, v28, v29\n"
"v_mfma_f32_16x16x32_bf16 v[212:215], a[212:215], v[32:35], v[212:215]\n"
"s_add_u32 s47, s46, 16384\n"
"v_exp_f32_e32 v30, v30\n"
"v_mfma_f32_16x16x32_bf16 v[216:219], a[216:219], v[32:35], v[216:219]\n"
"buffer_load_dwordx4 v6, s[24:27], s47 offen lds\n"
"v_mfma_f32_16x16x32_bf16 v[220:223], a[220:223], v[32:35], v[220:223]\n"
"v_exp_f32_e32 v31, v31\n"
"v_mfma_f32_16x16x32_bf16 a[244:247], a[224:227], v[32:35], a[244:247]\n"
"ds_read_b128 a[136:139], v1 offset:25600\n"
"v_cvt_pk_bf16_f32 v39, v30, v31\n"
"v_mfma_f32_16x16x32_bf16 v[24:27], a[96:99], a[16:19], 0\n"
"v_mul_f32_e32 v16, s44, v16\n"
"v_mul_f32_e32 v17, s44, v17\n"
"v_mfma_f32_16x16x32_bf16 v[24:27], a[100:103], a[20:23], v[24:27]\n"
"v_exp_f32_e32 v16, v16\n"
"v_mfma_f32_16x16x32_bf16 v[24:27], a[104:107], a[24:27], v[24:27]\n"
"ds_read_b128 a[144:147], v2 offset:24576\n"
"v_mul_f32_e32 v18, s44, v18\n"
"v_mfma_f32_16x16x32_bf16 v[24:27], a[108:111], a[28:31], v[24:27]\n"
"v_exp_f32_e32 v17, v17\n"
"v_mfma_f32_16x16x32_bf16 v[28:31], a[112:115], a[16:19], 0\n"
"v_mul_f32_e32 v19, s44, v19\n"
"v_cvt_pk_bf16_f32 v32, v16, v17\n"
"v_mfma_f32_16x16x32_bf16 v[28:31], a[116:119], a[20:23], v[28:31]\n"
"v_exp_f32_e32 v18, v18\n"
"v_mfma_f32_16x16x32_bf16 v[28:31], a[120:123], a[24:27], v[28:31]\n"
"ds_read_b128 a[148:151], v2 offset:25088\n"
"v_mul_f32_e32 v20, s44, v20\n"
"v_mfma_f32_16x16x32_bf16 v[28:31], a[124:127], a[28:31], v[28:31]\n"
"v_exp_f32_e32 v19, v19\n"
"v_mfma_f32_16x16x32_bf16 v[224:227], a[192:195], v[36:39], v[224:227]\n"
"v_mul_f32_e32 v21, s44, v21\n"
"v_cvt_pk_bf16_f32 v33, v18, v19\n"
"v_mfma_f32_16x16x32_bf16 v[228:231], a[196:199], v[36:39], v[228:231]\n"
"v_exp_f32_e32 v20, v20\n"
"v_mfma_f32_16x16x32_bf16 v[232:235], a[200:203], v[36:39], v[232:235]\n"
"ds_read_b128 a[152:155], v2 offset:25600\n"
"v_mul_f32_e32 v22, s44, v22\n"
"v_mfma_f32_16x16x32_bf16 v[236:239], a[204:207], v[36:39], v[236:239]\n"
"v_exp_f32_e32 v21, v21\n"
"v_mfma_f32_16x16x32_bf16 v[240:243], a[208:211], v[36:39], v[240:243]\n"
"s_add_u32 m0, s48, 17408\n"
"v_mul_f32_e32 v23, s44, v23\n"
"v_cvt_pk_bf16_f32 v34, v20, v21\n"
"v_mfma_f32_16x16x32_bf16 v[244:247], a[212:215], v[36:39], v[244:247]\n"
"s_add_u32 s47, s46, 16512\n"
"v_exp_f32_e32 v22, v22\n"
"v_mfma_f32_16x16x32_bf16 v[248:251], a[216:219], v[36:39], v[248:251]\n"
"buffer_load_dwordx4 v6, s[24:27], s47 offen lds\n"
"v_mfma_f32_16x16x32_bf16 v[252:255], a[220:223], v[36:39], v[252:255]\n"
"v_exp_f32_e32 v23, v23\n"
"v_mfma_f32_16x16x32_bf16 a[248:251], a[224:227], v[36:39], a[248:251]\n"
"ds_read_b128 a[156:159], v2 offset:26112\n"
"v_cvt_pk_bf16_f32 v35, v22, v23\n"
"v_mfma_f32_16x16x32_bf16 v[16:19], a[96:99], a[32:35], 0\n"
"v_mul_f32_e32 v24, s44, v24\n"
"v_mul_f32_e32 v25, s44, v25\n"
"v_mfma_f32_16x16x32_bf16 v[16:19], a[100:103], a[36:39], v[16:19]\n"
"v_exp_f32_e32 v24, v24\n"
"v_mfma_f32_16x16x32_bf16 v[16:19], a[104:107], a[40:43], v[16:19]\n"
"ds_read_b64_tr_b16 a[192:193], v3 offset:24576\n"
"v_mul_f32_e32 v26, s44, v26\n"
"v_mfma_f32_16x16x32_bf16 v[16:19], a[108:111], a[44:47], v[16:19]\n"
"v_exp_f32_e32 v25, v25\n"
"v_mfma_f32_16x16x32_bf16 v[20:23], a[112:115], a[32:35], 0\n"
"v_mul_f32_e32 v27, s44, v27\n"
"v_cvt_pk_bf16_f32 v36, v24, v25\n"
"v_mfma_f32_16x16x32_bf16 v[20:23], a[116:119], a[36:39], v[20:23]\n"
"v_exp_f32_e32 v26, v26\n"
"v_mfma_f32_16x16x32_bf16 v[20:23], a[120:123], a[40:43], v[20:23]\n"
"ds_read_b64_tr_b16 a[194:195], v4 offset:24576\n"
"v_mul_f32_e32 v28, s44, v28\n"
"v_mfma_f32_16x16x32_bf16 v[20:23], a[124:127], a[44:47], v[20:23]\n"
"v_exp_f32_e32 v27, v27\n"
"v_mfma_f32_16x16x32_bf16 v[64:67], a[160:163], v[32:35], v[64:67]\n"
"v_mul_f32_e32 v29, s44, v29\n"
"v_cvt_pk_bf16_f32 v37, v26, v27\n"
"v_mfma_f32_16x16x32_bf16 v[68:71], a[164:167], v[32:35], v[68:71]\n"
"v_exp_f32_e32 v28, v28\n"
"v_mfma_f32_16x16x32_bf16 v[72:75], a[168:171], v[32:35], v[72:75]\n"
"ds_read_b64_tr_b16 a[196:197], v11 offset:24576\n"
"v_mul_f32_e32 v30, s44, v30\n"
"v_mfma_f32_16x16x32_bf16 v[76:79], a[172:175], v[32:35], v[76:79]\n"
"v_exp_f32_e32 v29, v29\n"
"v_mfma_f32_16x16x32_bf16 v[80:83], a[176:179], v[32:35], v[80:83]\n"
"s_add_u32 m0, s48, 49152\n"
"v_mul_f32_e32 v31, s44, v31\n"
"v_cvt_pk_bf16_f32 v38, v28, v29\n"
"v_mfma_f32_16x16x32_bf16 v[84:87], a[180:183], v[32:35], v[84:87]\n"
"s_add_u32 s76, s46, 16384\n"
"v_exp_f32_e32 v30, v30\n"
"v_mfma_f32_16x16x32_bf16 v[88:91], a[184:187], v[32:35], v[88:91]\n"
"buffer_load_dwordx4 v6, s[28:31], s76 offen lds\n"
"v_mfma_f32_16x16x32_bf16 v[92:95], a[188:191], v[32:35], v[92:95]\n"
"v_exp_f32_e32 v31, v31\n"
"v_mfma_f32_16x16x32_bf16 a[228:231], a[224:227], v[32:35], a[228:231]\n"
"ds_read_b64_tr_b16 a[198:199], v14 offset:24576\n"
"v_cvt_pk_bf16_f32 v39, v30, v31\n"
"v_mfma_f32_16x16x32_bf16 v[24:27], a[96:99], a[48:51], 0\n"
"v_mul_f32_e32 v16, s44, v16\n"
"v_mul_f32_e32 v17, s44, v17\n"
"v_mfma_f32_16x16x32_bf16 v[24:27], a[100:103], a[52:55], v[24:27]\n"
"v_exp_f32_e32 v16, v16\n"
"v_mfma_f32_16x16x32_bf16 v[24:27], a[104:107], a[56:59], v[24:27]\n"
"ds_read_b64_tr_b16 a[200:201], v3 offset:25088\n"
"v_mul_f32_e32 v18, s44, v18\n"
"v_mfma_f32_16x16x32_bf16 v[24:27], a[108:111], a[60:63], v[24:27]\n"
"v_exp_f32_e32 v17, v17\n"
"v_mfma_f32_16x16x32_bf16 v[28:31], a[112:115], a[48:51], 0\n"
"v_mul_f32_e32 v19, s44, v19\n"
"v_cvt_pk_bf16_f32 v32, v16, v17\n"
"v_mfma_f32_16x16x32_bf16 v[28:31], a[116:119], a[52:55], v[28:31]\n"
"v_exp_f32_e32 v18, v18\n"
"v_mfma_f32_16x16x32_bf16 v[28:31], a[120:123], a[56:59], v[28:31]\n"
"ds_read_b64_tr_b16 a[202:203], v4 offset:25088\n"
"v_mul_f32_e32 v20, s44, v20\n"
"v_mfma_f32_16x16x32_bf16 v[28:31], a[124:127], a[60:63], v[28:31]\n"
"v_exp_f32_e32 v19, v19\n"
"v_mfma_f32_16x16x32_bf16 v[96:99], a[160:163], v[36:39], v[96:99]\n"
"v_mul_f32_e32 v21, s44, v21\n"
"v_cvt_pk_bf16_f32 v33, v18, v19\n"
"v_mfma_f32_16x16x32_bf16 v[100:103], a[164:167], v[36:39], v[100:103]\n"
"v_exp_f32_e32 v20, v20\n"
"v_mfma_f32_16x16x32_bf16 v[104:107], a[168:171], v[36:39], v[104:107]\n"
"ds_read_b64_tr_b16 a[204:205], v11 offset:25088\n"
"v_mul_f32_e32 v22, s44, v22\n"
"v_mfma_f32_16x16x32_bf16 v[108:111], a[172:175], v[36:39], v[108:111]\n"
"v_exp_f32_e32 v21, v21\n"
"v_mfma_f32_16x16x32_bf16 v[112:115], a[176:179], v[36:39], v[112:115]\n"
"s_add_u32 m0, s48, 50176\n"
"v_mul_f32_e32 v23, s44, v23\n"
"v_cvt_pk_bf16_f32 v34, v20, v21\n"
"v_mfma_f32_16x16x32_bf16 v[116:119], a[180:183], v[36:39], v[116:119]\n"
"s_add_u32 s76, s46, 16512\n"
"v_exp_f32_e32 v22, v22\n"
"v_mfma_f32_16x16x32_bf16 v[120:123], a[184:187], v[36:39], v[120:123]\n"
"buffer_load_dwordx4 v6, s[28:31], s76 offen lds\n"
"v_mfma_f32_16x16x32_bf16 v[124:127], a[188:191], v[36:39], v[124:127]\n"
"v_exp_f32_e32 v23, v23\n"
"v_mfma_f32_16x16x32_bf16 a[232:235], a[224:227], v[36:39], a[232:235]\n"
"ds_read_b64_tr_b16 a[206:207], v14 offset:25088\n"
"v_cvt_pk_bf16_f32 v35, v22, v23\n"
"v_mfma_f32_16x16x32_bf16 v[16:19], a[96:99], a[64:67], 0\n"
"v_mul_f32_e32 v24, s44, v24\n"
"v_mul_f32_e32 v25, s44, v25\n"
"v_mfma_f32_16x16x32_bf16 v[16:19], a[100:103], a[68:71], v[16:19]\n"
"v_exp_f32_e32 v24, v24\n"
"v_mfma_f32_16x16x32_bf16 v[16:19], a[104:107], a[72:75], v[16:19]\n"
"ds_read_b64_tr_b16 a[208:209], v3 offset:25600\n"
"v_mul_f32_e32 v26, s44, v26\n"
"v_mfma_f32_16x16x32_bf16 v[16:19], a[108:111], a[76:79], v[16:19]\n"
"v_exp_f32_e32 v25, v25\n"
"v_mfma_f32_16x16x32_bf16 v[20:23], a[112:115], a[64:67], 0\n"
"v_mul_f32_e32 v27, s44, v27\n"
"v_cvt_pk_bf16_f32 v36, v24, v25\n"
"v_mfma_f32_16x16x32_bf16 v[20:23], a[116:119], a[68:71], v[20:23]\n"
"v_exp_f32_e32 v26, v26\n"
"v_mfma_f32_16x16x32_bf16 v[20:23], a[120:123], a[72:75], v[20:23]\n"
"ds_read_b64_tr_b16 a[210:211], v4 offset:25600\n"
"v_mul_f32_e32 v28, s44, v28\n"
"v_mfma_f32_16x16x32_bf16 v[20:23], a[124:127], a[76:79], v[20:23]\n"
"v_exp_f32_e32 v27, v27\n"
"v_mfma_f32_16x16x32_bf16 v[128:131], a[160:163], v[32:35], v[128:131]\n"
"v_mul_f32_e32 v29, s44, v29\n"
"v_cvt_pk_bf16_f32 v37, v26, v27\n"
"v_mfma_f32_16x16x32_bf16 v[132:135], a[164:167], v[32:35], v[132:135]\n"
"v_exp_f32_e32 v28, v28\n"
"v_mfma_f32_16x16x32_bf16 v[136:139], a[168:171], v[32:35], v[136:139]\n"
"ds_read_b64_tr_b16 a[212:213], v11 offset:25600\n"
"v_mul_f32_e32 v30, s44, v30\n"
"v_mfma_f32_16x16x32_bf16 v[140:143], a[172:175], v[32:35], v[140:143]\n"
"v_exp_f32_e32 v29, v29\n"
"v_mfma_f32_16x16x32_bf16 v[144:147], a[176:179], v[32:35], v[144:147]\n"
"v_mul_f32_e32 v31, s44, v31\n"
"v_cvt_pk_bf16_f32 v38, v28, v29\n"
"v_mfma_f32_16x16x32_bf16 v[148:151], a[180:183], v[32:35], v[148:151]\n"
"s_mov_b32 s88, L_L5%=+0-L_code%=\n"
"v_exp_f32_e32 v30, v30\n"
"v_mfma_f32_16x16x32_bf16 v[152:155], a[184:187], v[32:35], v[152:155]\n"
"buffer_load_dword v63, v62, s[84:87], s88 offen\n"
"v_mfma_f32_16x16x32_bf16 v[156:159], a[188:191], v[32:35], v[156:159]\n"
"v_exp_f32_e32 v31, v31\n"
"v_mfma_f32_16x16x32_bf16 a[236:239], a[224:227], v[32:35], a[236:239]\n"
"ds_read_b64_tr_b16 a[214:215], v14 offset:25600\n"
"v_cvt_pk_bf16_f32 v39, v30, v31\n"
"v_mfma_f32_16x16x32_bf16 v[24:27], a[96:99], a[80:83], 0\n"
"ds_read_b64_tr_b16 a[222:223], v14 offset:26112\n"
"v_mul_f32_e32 v16, s44, v16\n"
"v_mul_f32_e32 v17, s44, v17\n"
"v_mfma_f32_16x16x32_bf16 v[24:27], a[100:103], a[84:87], v[24:27]\n"
"v_exp_f32_e32 v16, v16\n"
"v_mfma_f32_16x16x32_bf16 v[24:27], a[104:107], a[88:91], v[24:27]\n"
"ds_read_b64_tr_b16 a[216:217], v3 offset:26112\n"
"v_mul_f32_e32 v18, s44, v18\n"
"v_mfma_f32_16x16x32_bf16 v[24:27], a[108:111], a[92:95], v[24:27]\n"
"v_exp_f32_e32 v17, v17\n"
"v_mfma_f32_16x16x32_bf16 v[28:31], a[112:115], a[80:83], 0\n"
"v_mul_f32_e32 v19, s44, v19\n"
"v_cvt_pk_bf16_f32 v32, v16, v17\n"
"v_mfma_f32_16x16x32_bf16 v[28:31], a[116:119], a[84:87], v[28:31]\n"
"v_exp_f32_e32 v18, v18\n"
"v_mfma_f32_16x16x32_bf16 v[28:31], a[120:123], a[88:91], v[28:31]\n"
"ds_read_b64_tr_b16 a[218:219], v4 offset:26112\n"
"v_mul_f32_e32 v20, s44, v20\n"
"v_mfma_f32_16x16x32_bf16 v[28:31], a[124:127], a[92:95], v[28:31]\n"
"v_exp_f32_e32 v19, v19\n"
"v_mfma_f32_16x16x32_bf16 v[160:163], a[160:163], v[36:39], v[160:163]\n"
"v_mul_f32_e32 v21, s44, v21\n"
"v_cvt_pk_bf16_f32 v33, v18, v19\n"
"v_mfma_f32_16x16x32_bf16 v[164:167], a[164:167], v[36:39], v[164:167]\n"
"v_exp_f32_e32 v20, v20\n"
"v_mfma_f32_16x16x32_bf16 v[168:171], a[168:171], v[36:39], v[168:171]\n"
"ds_read_b64_tr_b16 a[220:221], v11 offset:26112\n"
"v_mul_f32_e32 v22, s44, v22\n"
"v_mfma_f32_16x16x32_bf16 v[172:175], a[172:175], v[36:39], v[172:175]\n"
"v_exp_f32_e32 v21, v21\n"
"v_mfma_f32_16x16x32_bf16 v[176:179], a[176:179], v[36:39], v[176:179]\n"
"v_mul_f32_e32 v23, s44, v23\n"
"v_cvt_pk_bf16_f32 v34, v20, v21\n"
"v_mfma_f32_16x16x32_bf16 v[180:183], a[180:183], v[36:39], v[180:183]\n"
"v_exp_f32_e32 v22, v22\n"
"v_mfma_f32_16x16x32_bf16 v[184:187], a[184:187], v[36:39], v[184:187]\n"
"v_mfma_f32_16x16x32_bf16 v[188:191], a[188:191], v[36:39], v[188:191]\n"
"v_exp_f32_e32 v23, v23\n"
"v_mfma_f32_16x16x32_bf16 a[240:243], a[224:227], v[36:39], a[240:243]\n"
"s_waitcnt lgkmcnt(15)\n"
"v_cvt_pk_bf16_f32 v35, v22, v23\n"
"v_mfma_f32_16x16x32_bf16 v[16:19], a[128:131], a[0:3], 0\n"
"v_mul_f32_e32 v24, s44, v24\n"
"v_mul_f32_e32 v25, s44, v25\n"
"v_mfma_f32_16x16x32_bf16 v[16:19], a[132:135], a[4:7], v[16:19]\n"
"v_exp_f32_e32 v24, v24\n"
"v_mfma_f32_16x16x32_bf16 v[16:19], a[136:139], a[8:11], v[16:19]\n"
"v_mul_f32_e32 v26, s44, v26\n"
"v_mfma_f32_16x16x32_bf16 v[16:19], a[140:143], a[12:15], v[16:19]\n"
"v_exp_f32_e32 v25, v25\n"
"v_mfma_f32_16x16x32_bf16 v[20:23], a[144:147], a[0:3], 0\n"
"v_mul_f32_e32 v27, s44, v27\n"
"v_cvt_pk_bf16_f32 v36, v24, v25\n"
"v_mfma_f32_16x16x32_bf16 v[20:23], a[148:151], a[4:7], v[20:23]\n"
"s_waitcnt vmcnt(11) lgkmcnt(0)\n"
"s_barrier\n"
"v_exp_f32_e32 v26, v26\n"
"v_mfma_f32_16x16x32_bf16 v[20:23], a[152:155], a[8:11], v[20:23]\n"
"ds_read_b128 a[96:99], v1 offset:0\n"
"ds_read_b128 a[108:111], v1 offset:1536\n"
"v_mul_f32_e32 v28, s44, v28\n"
"v_mfma_f32_16x16x32_bf16 v[20:23], a[156:159], a[12:15], v[20:23]\n"
"v_exp_f32_e32 v27, v27\n"
"v_mfma_f32_16x16x32_bf16 v[192:195], a[160:163], v[32:35], v[192:195]\n"
"v_mul_f32_e32 v29, s44, v29\n"
"v_cvt_pk_bf16_f32 v37, v26, v27\n"
"v_mfma_f32_16x16x32_bf16 v[196:199], a[164:167], v[32:35], v[196:199]\n"
"ds_read_b128 a[0:3], v5 offset:0\n"
"v_exp_f32_e32 v28, v28\n"
"v_mfma_f32_16x16x32_bf16 v[200:203], a[168:171], v[32:35], v[200:203]\n"
"ds_read_b128 a[100:103], v1 offset:512\n"
"v_mul_f32_e32 v30, s44, v30\n"
"v_mfma_f32_16x16x32_bf16 v[204:207], a[172:175], v[32:35], v[204:207]\n"
"ds_read_b128 a[4:7], v5 offset:512\n"
"v_exp_f32_e32 v29, v29\n"
"v_mfma_f32_16x16x32_bf16 v[208:211], a[176:179], v[32:35], v[208:211]\n"
"s_add_u32 m0, s48, 24576\n"
"v_mul_f32_e32 v31, s44, v31\n"
"v_cvt_pk_bf16_f32 v38, v28, v29\n"
"v_mfma_f32_16x16x32_bf16 v[212:215], a[180:183], v[32:35], v[212:215]\n"
"s_add_u32 s47, s46, 24576\n"
"ds_read_b128 a[8:11], v5 offset:1024\n"
"v_exp_f32_e32 v30, v30\n"
"v_mfma_f32_16x16x32_bf16 v[216:219], a[184:187], v[32:35], v[216:219]\n"
"buffer_load_dwordx4 v6, s[24:27], s47 offen lds\n"
"v_mfma_f32_16x16x32_bf16 v[220:223], a[188:191], v[32:35], v[220:223]\n"
"ds_read_b128 a[12:15], v5 offset:1536\n"
"v_exp_f32_e32 v31, v31\n"
"v_mfma_f32_16x16x32_bf16 a[244:247], a[224:227], v[32:35], a[244:247]\n"
"ds_read_b128 a[104:107], v1 offset:1024\n"
"v_cvt_pk_bf16_f32 v39, v30, v31\n"
"v_mfma_f32_16x16x32_bf16 v[24:27], a[128:131], a[16:19], 0\n"
"v_mul_f32_e32 v16, s44, v16\n"
"v_mul_f32_e32 v17, s44, v17\n"
"v_mfma_f32_16x16x32_bf16 v[24:27], a[132:135], a[20:23], v[24:27]\n"
"v_exp_f32_e32 v16, v16\n"
"v_mfma_f32_16x16x32_bf16 v[24:27], a[136:139], a[24:27], v[24:27]\n"
"ds_read_b128 a[112:115], v2 offset:0\n"
"v_mul_f32_e32 v18, s44, v18\n"
"v_mfma_f32_16x16x32_bf16 v[24:27], a[140:143], a[28:31], v[24:27]\n"
"v_exp_f32_e32 v17, v17\n"
"v_mfma_f32_16x16x32_bf16 v[28:31], a[144:147], a[16:19], 0\n"
"v_mul_f32_e32 v19, s44, v19\n"
"v_cvt_pk_bf16_f32 v32, v16, v17\n"
"v_mfma_f32_16x16x32_bf16 v[28:31], a[148:151], a[20:23], v[28:31]\n"
"v_exp_f32_e32 v18, v18\n"
"v_mfma_f32_16x16x32_bf16 v[28:31], a[152:155], a[24:27], v[28:31]\n"
"ds_read_b128 a[116:119], v2 offset:512\n"
"v_mul_f32_e32 v20, s44, v20\n"
"v_mfma_f32_16x16x32_bf16 v[28:31], a[156:159], a[28:31], v[28:31]\n"
"v_exp_f32_e32 v19, v19\n"
"v_mfma_f32_16x16x32_bf16 v[224:227], a[160:163], v[36:39], v[224:227]\n"
"v_mul_f32_e32 v21, s44, v21\n"
"v_cvt_pk_bf16_f32 v33, v18, v19\n"
"v_mfma_f32_16x16x32_bf16 v[228:231], a[164:167], v[36:39], v[228:231]\n"
"ds_read_b128 a[16:19], v5 offset:4096\n"
"v_exp_f32_e32 v20, v20\n"
"v_mfma_f32_16x16x32_bf16 v[232:235], a[168:171], v[36:39], v[232:235]\n"
"ds_read_b128 a[120:123], v2 offset:1024\n"
"v_mul_f32_e32 v22, s44, v22\n"
"v_mfma_f32_16x16x32_bf16 v[236:239], a[172:175], v[36:39], v[236:239]\n"
"ds_read_b128 a[20:23], v5 offset:4608\n"
"v_exp_f32_e32 v21, v21\n"
"v_mfma_f32_16x16x32_bf16 v[240:243], a[176:179], v[36:39], v[240:243]\n"
"s_add_u32 m0, s48, 25600\n"
"v_mul_f32_e32 v23, s44, v23\n"
"v_cvt_pk_bf16_f32 v34, v20, v21\n"
"v_mfma_f32_16x16x32_bf16 v[244:247], a[180:183], v[36:39], v[244:247]\n"
"s_add_u32 s47, s46, 24704\n"
"ds_read_b128 a[24:27], v5 offset:5120\n"
"v_exp_f32_e32 v22, v22\n"
"v_mfma_f32_16x16x32_bf16 v[248:251], a[184:187], v[36:39], v[248:251]\n"
"buffer_load_dwordx4 v6, s[24:27], s47 offen lds\n"
"v_mfma_f32_16x16x32_bf16 v[252:255], a[188:191], v[36:39], v[252:255]\n"
"ds_read_b128 a[28:31], v5 offset:5632\n"
"v_exp_f32_e32 v23, v23\n"
"v_mfma_f32_16x16x32_bf16 a[248:251], a[224:227], v[36:39], a[248:251]\n"
"ds_read_b128 a[124:127], v2 offset:1536\n"
"v_cvt_pk_bf16_f32 v35, v22, v23\n"
"v_mfma_f32_16x16x32_bf16 v[16:19], a[128:131], a[32:35], 0\n"
"v_mul_f32_e32 v24, s44, v24\n"
"v_mul_f32_e32 v25, s44, v25\n"
"v_mfma_f32_16x16x32_bf16 v[16:19], a[132:135], a[36:39], v[16:19]\n"
"v_exp_f32_e32 v24, v24\n"
"v_mfma_f32_16x16x32_bf16 v[16:19], a[136:139], a[40:43], v[16:19]\n"
"ds_read_b64_tr_b16 a[160:161], v3 offset:0\n"
"v_mul_f32_e32 v26, s44, v26\n"
"v_mfma_f32_16x16x32_bf16 v[16:19], a[140:143], a[44:47], v[16:19]\n"
"v_exp_f32_e32 v25, v25\n"
"v_mfma_f32_16x16x32_bf16 v[20:23], a[144:147], a[32:35], 0\n"
"v_mul_f32_e32 v27, s44, v27\n"
"v_cvt_pk_bf16_f32 v36, v24, v25\n"
"v_mfma_f32_16x16x32_bf16 v[20:23], a[148:151], a[36:39], v[20:23]\n"
"v_exp_f32_e32 v26, v26\n"
"v_mfma_f32_16x16x32_bf16 v[20:23], a[152:155], a[40:43], v[20:23]\n"
"ds_read_b64_tr_b16 a[162:163], v4 offset:0\n"
"v_mul_f32_e32 v28, s44, v28\n"
"v_mfma_f32_16x16x32_bf16 v[20:23], a[156:159], a[44:47], v[20:23]\n"
"v_exp_f32_e32 v27, v27\n"
"v_mfma_f32_16x16x32_bf16 v[64:67], a[192:195], v[32:35], v[64:67]\n"
"v_mul_f32_e32 v29, s44, v29\n"
"v_cvt_pk_bf16_f32 v37, v26, v27\n"
"v_mfma_f32_16x16x32_bf16 v[68:71], a[196:199], v[32:35], v[68:71]\n"
"ds_read_b128 a[32:35], v5 offset:8192\n"
"v_exp_f32_e32 v28, v28\n"
"v_mfma_f32_16x16x32_bf16 v[72:75], a[200:203], v[32:35], v[72:75]\n"
"ds_read_b64_tr_b16 a[164:165], v11 offset:0\n"
"v_mul_f32_e32 v30, s44, v30\n"
"v_mfma_f32_16x16x32_bf16 v[76:79], a[204:207], v[32:35], v[76:79]\n"
"ds_read_b128 a[36:39], v5 offset:8704\n"
"v_exp_f32_e32 v29, v29\n"
"v_mfma_f32_16x16x32_bf16 v[80:83], a[208:211], v[32:35], v[80:83]\n"
"s_add_u32 m0, s48, 57344\n"
"v_mul_f32_e32 v31, s44, v31\n"
"v_cvt_pk_bf16_f32 v38, v28, v29\n"
"v_mfma_f32_16x16x32_bf16 v[84:87], a[212:215], v[32:35], v[84:87]\n"
"s_add_u32 s76, s46, 24576\n"
"ds_read_b128 a[40:43], v5 offset:9216\n"
"v_exp_f32_e32 v30, v30\n"
"v_mfma_f32_16x16x32_bf16 v[88:91], a[216:219], v[32:35], v[88:91]\n"
"buffer_load_dwordx4 v6, s[28:31], s76 offen lds\n"
"v_mfma_f32_16x16x32_bf16 v[92:95], a[220:223], v[32:35], v[92:95]\n"
"ds_read_b128 a[44:47], v5 offset:9728\n"
"v_exp_f32_e32 v31, v31\n"
"v_mfma_f32_16x16x32_bf16 a[228:231], a[224:227], v[32:35], a[228:231]\n"
"ds_read_b64_tr_b16 a[166:167], v14 offset:0\n"
"v_cvt_pk_bf16_f32 v39, v30, v31\n"
"v_mfma_f32_16x16x32_bf16 v[24:27], a[128:131], a[48:51], 0\n"
"v_mul_f32_e32 v16, s44, v16\n"
"v_mul_f32_e32 v17, s44, v17\n"
"v_mfma_f32_16x16x32_bf16 v[24:27], a[132:135], a[52:55], v[24:27]\n"
"v_exp_f32_e32 v16, v16\n"
"v_mfma_f32_16x16x32_bf16 v[24:27], a[136:139], a[56:59], v[24:27]\n"
"ds_read_b64_tr_b16 a[168:169], v3 offset:512\n"
"v_mul_f32_e32 v18, s44, v18\n"
"v_mfma_f32_16x16x32_bf16 v[24:27], a[140:143], a[60:63], v[24:27]\n"
"v_exp_f32_e32 v17, v17\n"
"v_mfma_f32_16x16x32_bf16 v[28:31], a[144:147], a[48:51], 0\n"
"v_mul_f32_e32 v19, s44, v19\n"
"v_cvt_pk_bf16_f32 v32, v16, v17\n"
"v_accvgpr_read_b32 v10, a228\n"
"v_subrev_u32_e32 v13, s51, v10\n"
"v_rcp_f32_e32 v12, v10\n"
"v_cmp_le_u32_e32 vcc, s52, v13\n"
"v_pk_mul_f32 v[64:65], v[64:65], v[12:13] op_sel_hi:[1,0]\n"
"v_pk_mul_f32 v[66:67], v[66:67], v[12:13] op_sel_hi:[1,0]\n"
"v_pk_mul_f32 v[68:69], v[68:69], v[12:13] op_sel_hi:[1,0]\n"
"v_pk_mul_f32 v[70:71], v[70:71], v[12:13] op_sel_hi:[1,0]\n"
"v_pk_mul_f32 v[72:73], v[72:73], v[12:13] op_sel_hi:[1,0]\n"
"v_pk_mul_f32 v[74:75], v[74:75], v[12:13] op_sel_hi:[1,0]\n"
"v_pk_mul_f32 v[76:77], v[76:77], v[12:13] op_sel_hi:[1,0]\n"
"v_pk_mul_f32 v[78:79], v[78:79], v[12:13] op_sel_hi:[1,0]\n"
"v_pk_mul_f32 v[80:81], v[80:81], v[12:13] op_sel_hi:[1,0]\n"
"v_pk_mul_f32 v[82:83], v[82:83], v[12:13] op_sel_hi:[1,0]\n"
"v_pk_mul_f32 v[84:85], v[84:85], v[12:13] op_sel_hi:[1,0]\n"
"v_pk_mul_f32 v[86:87], v[86:87], v[12:13] op_sel_hi:[1,0]\n"
"v_pk_mul_f32 v[88:89], v[88:89], v[12:13] op_sel_hi:[1,0]\n"
"v_pk_mul_f32 v[90:91], v[90:91], v[12:13] op_sel_hi:[1,0]\n"
"v_pk_mul_f32 v[92:93], v[92:93], v[12:13] op_sel_hi:[1,0]\n"
"v_pk_mul_f32 v[94:95], v[94:95], v[12:13] op_sel_hi:[1,0]\n"
"s_or_b64 s[54:55], s[54:55], vcc\n"
"v_mfma_f32_16x16x32_bf16 v[28:31], a[148:151], a[52:55], v[28:31]\n"
"v_exp_f32_e32 v18, v18\n"
"v_cvt_pk_bf16_f32 v40, v64, v65\n"
"v_cvt_pk_bf16_f32 v41, v66, v67\n"
"v_cvt_pk_bf16_f32 v42, v68, v69\n"
"v_cvt_pk_bf16_f32 v43, v70, v71\n"
"s_waitcnt lgkmcnt(15)\n"
"ds_write_b64 v56, v[40:41] offset:0\n"
"ds_write_b64 v57, v[42:43] offset:0\n"
"v_mfma_f32_16x16x32_bf16 v[28:31], a[152:155], a[56:59], v[28:31]\n"
"ds_read_b64_tr_b16 a[170:171], v4 offset:512\n"
"v_mul_f32_e32 v20, s44, v20\n"
"v_mfma_f32_16x16x32_bf16 v[28:31], a[156:159], a[60:63], v[28:31]\n"
"v_exp_f32_e32 v19, v19\n"
"v_cvt_pk_bf16_f32 v44, v72, v73\n"
"v_cvt_pk_bf16_f32 v45, v74, v75\n"
"v_cvt_pk_bf16_f32 v46, v76, v77\n"
"v_cvt_pk_bf16_f32 v47, v78, v79\n"
"ds_write_b64 v15, v[44:45] offset:0\n"
"ds_write_b64 v60, v[46:47] offset:0\n"
"v_mfma_f32_16x16x32_bf16 v[96:99], a[192:195], v[36:39], v[96:99]\n"
"v_mul_f32_e32 v21, s44, v21\n"
"v_cvt_pk_bf16_f32 v33, v18, v19\n"
"v_mfma_f32_16x16x32_bf16 v[100:103], a[196:199], v[36:39], v[100:103]\n"
"ds_read_b128 a[48:51], v5 offset:12288\n"
"v_exp_f32_e32 v20, v20\n"
"v_cvt_pk_bf16_f32 v48, v80, v81\n"
"v_cvt_pk_bf16_f32 v49, v82, v83\n"
"v_cvt_pk_bf16_f32 v50, v84, v85\n"
"v_cvt_pk_bf16_f32 v51, v86, v87\n"
"ds_write_b64 v56, v[48:49] offset:128\n"
"ds_write_b64 v57, v[50:51] offset:128\n"
"v_mfma_f32_16x16x32_bf16 v[104:107], a[200:203], v[36:39], v[104:107]\n"
"ds_read_b64_tr_b16 a[172:173], v11 offset:512\n"
"v_mul_f32_e32 v22, s44, v22\n"
"v_mfma_f32_16x16x32_bf16 v[108:111], a[204:207], v[36:39], v[108:111]\n"
"ds_read_b128 a[52:55], v5 offset:12800\n"
"v_exp_f32_e32 v21, v21\n"
"v_cvt_pk_bf16_f32 v52, v88, v89\n"
"v_cvt_pk_bf16_f32 v53, v90, v91\n"
"v_cvt_pk_bf16_f32 v54, v92, v93\n"
"v_cvt_pk_bf16_f32 v55, v94, v95\n"
"ds_write_b64 v15, v[52:53] offset:128\n"
"ds_write_b64 v60, v[54:55] offset:128\n"
"v_mfma_f32_16x16x32_bf16 v[112:115], a[208:211], v[36:39], v[112:115]\n"
"s_add_u32 m0, s48, 58368\n"
"v_mul_f32_e32 v23, s44, v23\n"
"v_cvt_pk_bf16_f32 v34, v20, v21\n"
"v_mfma_f32_16x16x32_bf16 v[116:119], a[212:215], v[36:39], v[116:119]\n"
"s_add_u32 s76, s46, 24704\n"
"ds_read_b128 a[56:59], v5 offset:13312\n"
"v_exp_f32_e32 v22, v22\n"
"v_mfma_f32_16x16x32_bf16 v[120:123], a[216:219], v[36:39], v[120:123]\n"
"buffer_load_dwordx4 v6, s[28:31], s76 offen lds\n"
"v_mfma_f32_16x16x32_bf16 v[124:127], a[220:223], v[36:39], v[124:127]\n"
"ds_read_b128 a[60:63], v5 offset:13824\n"
"v_exp_f32_e32 v23, v23\n"
"v_mfma_f32_16x16x32_bf16 a[232:235], a[224:227], v[36:39], a[232:235]\n"
"ds_read_b64_tr_b16 a[174:175], v14 offset:512\n"
"v_cvt_pk_bf16_f32 v35, v22, v23\n"
"v_mfma_f32_16x16x32_bf16 v[16:19], a[128:131], a[64:67], 0\n"
"v_mul_f32_e32 v24, s44, v24\n"
"v_mul_f32_e32 v25, s44, v25\n"
"v_mfma_f32_16x16x32_bf16 v[16:19], a[132:135], a[68:71], v[16:19]\n"
"v_exp_f32_e32 v24, v24\n"
"v_mfma_f32_16x16x32_bf16 v[16:19], a[136:139], a[72:75], v[16:19]\n"
"ds_read_b64_tr_b16 a[178:179], v4 offset:1024\n"
"v_mul_f32_e32 v26, s44, v26\n"
"v_mfma_f32_16x16x32_bf16 v[16:19], a[140:143], a[76:79], v[16:19]\n"
"v_exp_f32_e32 v25, v25\n"
"v_mfma_f32_16x16x32_bf16 v[20:23], a[144:147], a[64:67], 0\n"
"v_mul_f32_e32 v27, s44, v27\n"
"v_cvt_pk_bf16_f32 v36, v24, v25\n"
"v_accvgpr_read_b32 v10, a232\n"
"v_subrev_u32_e32 v13, s51, v10\n"
"v_rcp_f32_e32 v12, v10\n"
"v_cmp_le_u32_e32 vcc, s52, v13\n"
"v_pk_mul_f32 v[96:97], v[96:97], v[12:13] op_sel_hi:[1,0]\n"
"v_pk_mul_f32 v[98:99], v[98:99], v[12:13] op_sel_hi:[1,0]\n"
"v_pk_mul_f32 v[100:101], v[100:101], v[12:13] op_sel_hi:[1,0]\n"
"v_pk_mul_f32 v[102:103], v[102:103], v[12:13] op_sel_hi:[1,0]\n"
"v_pk_mul_f32 v[104:105], v[104:105], v[12:13] op_sel_hi:[1,0]\n"
"v_pk_mul_f32 v[106:107], v[106:107], v[12:13] op_sel_hi:[1,0]\n"
"v_pk_mul_f32 v[108:109], v[108:109], v[12:13] op_sel_hi:[1,0]\n"
"v_pk_mul_f32 v[110:111], v[110:111], v[12:13] op_sel_hi:[1,0]\n"
"v_pk_mul_f32 v[112:113], v[112:113], v[12:13] op_sel_hi:[1,0]\n"
"v_pk_mul_f32 v[114:115], v[114:115], v[12:13] op_sel_hi:[1,0]\n"
"v_pk_mul_f32 v[116:117], v[116:117], v[12:13] op_sel_hi:[1,0]\n"
"v_pk_mul_f32 v[118:119], v[118:119], v[12:13] op_sel_hi:[1,0]\n"
"v_pk_mul_f32 v[120:121], v[120:121], v[12:13] op_sel_hi:[1,0]\n"
"v_pk_mul_f32 v[122:123], v[122:123], v[12:13] op_sel_hi:[1,0]\n"
"v_pk_mul_f32 v[124:125], v[124:125], v[12:13] op_sel_hi:[1,0]\n"
"v_pk_mul_f32 v[126:127], v[126:127], v[12:13] op_sel_hi:[1,0]\n"
"s_or_b64 s[54:55], s[54:55], vcc\n"
"v_mfma_f32_16x16x32_bf16 v[20:23], a[148:151], a[68:71], v[20:23]\n"
"v_exp_f32_e32 v26, v26\n"
"v_cvt_pk_bf16_f32 v40, v96, v97\n"
"v_cvt_pk_bf16_f32 v41, v98, v99\n"
"v_cvt_pk_bf16_f32 v42, v100, v101\n"
"v_cvt_pk_bf16_f32 v43, v102, v103\n"
"s_waitcnt lgkmcnt(15)\n"
"ds_write_b64 v56, v[40:41] offset:4096\n"
"ds_write_b64 v57, v[42:43] offset:4096\n"
"v_mfma_f32_16x16x32_bf16 v[20:23], a[152:155], a[72:75], v[20:23]\n"
"ds_read_b64_tr_b16 a[180:181], v11 offset:1024\n"
"v_mul_f32_e32 v28, s44, v28\n"
"v_mfma_f32_16x16x32_bf16 v[20:23], a[156:159], a[76:79], v[20:23]\n"
"v_exp_f32_e32 v27, v27\n"
"v_cvt_pk_bf16_f32 v44, v104, v105\n"
"v_cvt_pk_bf16_f32 v45, v106, v107\n"
"v_cvt_pk_bf16_f32 v46, v108, v109\n"
"v_cvt_pk_bf16_f32 v47, v110, v111\n"
"ds_write_b64 v15, v[44:45] offset:4096\n"
"ds_write_b64 v60, v[46:47] offset:4096\n"
"v_mfma_f32_16x16x32_bf16 v[128:131], a[192:195], v[32:35], v[128:131]\n"
"v_mul_f32_e32 v29, s44, v29\n"
"v_cvt_pk_bf16_f32 v37, v26, v27\n"
"v_mfma_f32_16x16x32_bf16 v[132:135], a[196:199], v[32:35], v[132:135]\n"
"v_exp_f32_e32 v28, v28\n"
"v_cvt_pk_bf16_f32 v48, v112, v113\n"
"v_cvt_pk_bf16_f32 v49, v114, v115\n"
"v_cvt_pk_bf16_f32 v50, v116, v117\n"
"v_cvt_pk_bf16_f32 v51, v118, v119\n"
"ds_write_b64 v56, v[48:49] offset:4224\n"
"ds_write_b64 v57, v[50:51] offset:4224\n"
"v_mfma_f32_16x16x32_bf16 v[136:139], a[200:203], v[32:35], v[136:139]\n"
"ds_read_b64_tr_b16 a[182:183], v14 offset:1024\n"
"v_mul_f32_e32 v30, s44, v30\n"
"v_mfma_f32_16x16x32_bf16 v[140:143], a[204:207], v[32:35], v[140:143]\n"
"v_exp_f32_e32 v29, v29\n"
"v_cvt_pk_bf16_f32 v52, v120, v121\n"
"v_cvt_pk_bf16_f32 v53, v122, v123\n"
"v_cvt_pk_bf16_f32 v54, v124, v125\n"
"v_cvt_pk_bf16_f32 v55, v126, v127\n"
"ds_write_b64 v15, v[52:53] offset:4224\n"
"ds_write_b64 v60, v[54:55] offset:4224\n"
"v_mfma_f32_16x16x32_bf16 v[144:147], a[208:211], v[32:35], v[144:147]\n"
"v_mul_f32_e32 v31, s44, v31\n"
"v_cvt_pk_bf16_f32 v38, v28, v29\n"
"v_mfma_f32_16x16x32_bf16 v[148:151], a[212:215], v[32:35], v[148:151]\n"
"ds_read_b128 a[72:75], v5 offset:17408\n"
"v_exp_f32_e32 v30, v30\n"
"v_mfma_f32_16x16x32_bf16 v[152:155], a[216:219], v[32:35], v[152:155]\n"
"ds_read_b64_tr_b16 a[176:177], v3 offset:1024\n"
"ds_read_b128 a[64:67], v5 offset:16384\n"
"v_mfma_f32_16x16x32_bf16 v[156:159], a[220:223], v[32:35], v[156:159]\n"
"ds_read_b128 a[76:79], v5 offset:17920\n"
"v_exp_f32_e32 v31, v31\n"
"v_mfma_f32_16x16x32_bf16 a[236:239], a[224:227], v[32:35], a[236:239]\n"
"ds_read_b128 a[68:71], v5 offset:16896\n"
"v_cvt_pk_bf16_f32 v39, v30, v31\n"
"v_mfma_f32_16x16x32_bf16 v[24:27], a[128:131], a[80:83], 0\n"
"ds_read_b64_tr_b16 a[190:191], v14 offset:1536\n"
"v_mul_f32_e32 v16, s44, v16\n"
"v_mul_f32_e32 v17, s44, v17\n"
"v_mfma_f32_16x16x32_bf16 v[24:27], a[132:135], a[84:87], v[24:27]\n"
"v_exp_f32_e32 v16, v16\n"
"v_mfma_f32_16x16x32_bf16 v[24:27], a[136:139], a[88:91], v[24:27]\n"
"ds_read_b64_tr_b16 a[184:185], v3 offset:1536\n"
"v_mul_f32_e32 v18, s44, v18\n"
"v_mfma_f32_16x16x32_bf16 v[24:27], a[140:143], a[92:95], v[24:27]\n"
"v_exp_f32_e32 v17, v17\n"
"v_mfma_f32_16x16x32_bf16 v[28:31], a[144:147], a[80:83], 0\n"
"v_mul_f32_e32 v19, s44, v19\n"
"v_cvt_pk_bf16_f32 v32, v16, v17\n"
"v_accvgpr_read_b32 v10, a236\n"
"v_subrev_u32_e32 v13, s51, v10\n"
"v_rcp_f32_e32 v12, v10\n"
"v_cmp_le_u32_e32 vcc, s52, v13\n"
"v_pk_mul_f32 v[128:129], v[128:129], v[12:13] op_sel_hi:[1,0]\n"
"v_pk_mul_f32 v[130:131], v[130:131], v[12:13] op_sel_hi:[1,0]\n"
"v_pk_mul_f32 v[132:133], v[132:133], v[12:13] op_sel_hi:[1,0]\n"
"v_pk_mul_f32 v[134:135], v[134:135], v[12:13] op_sel_hi:[1,0]\n"
"v_pk_mul_f32 v[136:137], v[136:137], v[12:13] op_sel_hi:[1,0]\n"
"v_pk_mul_f32 v[138:139], v[138:139], v[12:13] op_sel_hi:[1,0]\n"
"v_pk_mul_f32 v[140:141], v[140:141], v[12:13] op_sel_hi:[1,0]\n"
"v_pk_mul_f32 v[142:143], v[142:143], v[12:13] op_sel_hi:[1,0]\n"
"v_pk_mul_f32 v[144:145], v[144:145], v[12:13] op_sel_hi:[1,0]\n"
"v_pk_mul_f32 v[146:147], v[146:147], v[12:13] op_sel_hi:[1,0]\n"
"v_pk_mul_f32 v[148:149], v[148:149], v[12:13] op_sel_hi:[1,0]\n"
"v_pk_mul_f32 v[150:151], v[150:151], v[12:13] op_sel_hi:[1,0]\n"
"v_pk_mul_f32 v[152:153], v[152:153], v[12:13] op_sel_hi:[1,0]\n"
"v_pk_mul_f32 v[154:155], v[154:155], v[12:13] op_sel_hi:[1,0]\n"
"v_pk_mul_f32 v[156:157], v[156:157], v[12:13] op_sel_hi:[1,0]\n"
"v_pk_mul_f32 v[158:159], v[158:159], v[12:13] op_sel_hi:[1,0]\n"
"s_or_b64 s[54:55], s[54:55], vcc\n"
"v_mfma_f32_16x16x32_bf16 v[28:31], a[148:151], a[84:87], v[28:31]\n"
"v_exp_f32_e32 v18, v18\n"
"v_cvt_pk_bf16_f32 v40, v128, v129\n"
"v_cvt_pk_bf16_f32 v41, v130, v131\n"
"v_cvt_pk_bf16_f32 v42, v132, v133\n"
"v_cvt_pk_bf16_f32 v43, v134, v135\n"
"ds_write_b64 v56, v[40:41] offset:8192\n"
"ds_write_b64 v57, v[42:43] offset:8192\n"
"v_mfma_f32_16x16x32_bf16 v[28:31], a[152:155], a[88:91], v[28:31]\n"
"ds_read_b64_tr_b16 a[186:187], v4 offset:1536\n"
"v_mul_f32_e32 v20, s44, v20\n"
"v_mfma_f32_16x16x32_bf16 v[28:31], a[156:159], a[92:95], v[28:31]\n"
"v_exp_f32_e32 v19, v19\n"
"v_cvt_pk_bf16_f32 v44, v136, v137\n"
"v_cvt_pk_bf16_f32 v45, v138, v139\n"
"v_cvt_pk_bf16_f32 v46, v140, v141\n"
"v_cvt_pk_bf16_f32 v47, v142, v143\n"
"ds_write_b64 v15, v[44:45] offset:8192\n"
"ds_write_b64 v60, v[46:47] offset:8192\n"
"v_mfma_f32_16x16x32_bf16 v[160:163], a[192:195], v[36:39], v[160:163]\n"
"v_mul_f32_e32 v21, s44, v21\n"
"v_cvt_pk_bf16_f32 v33, v18, v19\n"
"v_mfma_f32_16x16x32_bf16 v[164:167], a[196:199], v[36:39], v[164:167]\n"
"v_exp_f32_e32 v20, v20\n"
"v_cvt_pk_bf16_f32 v48, v144, v145\n"
"v_cvt_pk_bf16_f32 v49, v146, v147\n"
"v_cvt_pk_bf16_f32 v50, v148, v149\n"
"v_cvt_pk_bf16_f32 v51, v150, v151\n"
"ds_write_b64 v56, v[48:49] offset:8320\n"
"ds_write_b64 v57, v[50:51] offset:8320\n"
"v_mfma_f32_16x16x32_bf16 v[168:171], a[200:203], v[36:39], v[168:171]\n"
"ds_read_b64_tr_b16 a[188:189], v11 offset:1536\n"
"v_mul_f32_e32 v22, s44, v22\n"
"v_mfma_f32_16x16x32_bf16 v[172:175], a[204:207], v[36:39], v[172:175]\n"
"v_exp_f32_e32 v21, v21\n"
"v_cvt_pk_bf16_f32 v52, v152, v153\n"
"v_cvt_pk_bf16_f32 v53, v154, v155\n"
"v_cvt_pk_bf16_f32 v54, v156, v157\n"
"v_cvt_pk_bf16_f32 v55, v158, v159\n"
"ds_write_b64 v15, v[52:53] offset:8320\n"
"ds_write_b64 v60, v[54:55] offset:8320\n"
"v_mfma_f32_16x16x32_bf16 v[176:179], a[208:211], v[36:39], v[176:179]\n"
"v_mul_f32_e32 v23, s44, v23\n"
"v_cvt_pk_bf16_f32 v34, v20, v21\n"
"v_mfma_f32_16x16x32_bf16 v[180:183], a[212:215], v[36:39], v[180:183]\n"
"v_exp_f32_e32 v22, v22\n"
"v_mfma_f32_16x16x32_bf16 v[184:187], a[216:219], v[36:39], v[184:187]\n"
"s_waitcnt vmcnt(13)\n"
"ds_read_b128 a[80:83], v5 offset:20480\n"
"ds_read_b128 a[84:87], v5 offset:20992\n"
"v_mfma_f32_16x16x32_bf16 v[188:191], a[220:223], v[36:39], v[188:191]\n"
"ds_read_b128 a[92:95], v5 offset:22016\n"
"v_exp_f32_e32 v23, v23\n"
"v_mfma_f32_16x16x32_bf16 a[240:243], a[224:227], v[36:39], a[240:243]\n"
"ds_read_b128 a[88:91], v5 offset:21504\n"
"v_cvt_pk_bf16_f32 v35, v22, v23\n"
"s_mov_b32 s45, s46\n"
"s_add_u32 s46, s46, 1048576\n"
"s_add_u32 s50, s50, 1\n"
"s_cmp_ge_u32 s50, 3\n"
"s_cselect_b32 s46, 1073741824, s46\n"
"s_add_u32 s72, s50, 1\n"
"s_mul_i32 s70, s72, 3014656\n"
"s_cmp_gt_u32 s63, s72\n"
"s_cselect_b32 s71, 0, 16384\n"
"s_add_u32 s70, s70, s71\n"
"s_add_u32 s36, s66, s70\n"
"s_addc_u32 s37, s67, 0\n"
"s_cmp_eq_u32 s63, s72\n"
"s_cselect_b32 s71, 5, 6\n"
"s_mul_i32 s38, s71, 16384\n"
"s_mul_i32 s58, s64, s71\n"
"s_mul_i32 s58, s58, 4096\n"
"s_cselect_b32 s59, 1073741824, s58\n"
"s_cmp_gt_u32 s72, 3\n"
"s_cselect_b32 s38, 0, s38\n"
"s_cmp_eq_u32 s50, s63\n"
"s_cbranch_scc1 L_T65%=\n"
"L_T66%=:\n"
"v_mfma_f32_16x16x32_bf16 v[16:19], a[96:99], a[0:3], 0\n"
"v_mul_f32_e32 v24, s44, v24\n"
"v_mul_f32_e32 v25, s44, v25\n"
"v_mfma_f32_16x16x32_bf16 v[16:19], a[100:103], a[4:7], v[16:19]\n"
"v_exp_f32_e32 v24, v24\n"
"v_mfma_f32_16x16x32_bf16 v[16:19], a[104:107], a[8:11], v[16:19]\n"
"v_mul_f32_e32 v26, s44, v26\n"
"v_mfma_f32_16x16x32_bf16 v[16:19], a[108:111], a[12:15], v[16:19]\n"
"v_exp_f32_e32 v25, v25\n"
"v_mfma_f32_16x16x32_bf16 v[20:23], a[112:115], a[0:3], 0\n"
"v_mul_f32_e32 v27, s44, v27\n"
"v_cvt_pk_bf16_f32 v36, v24, v25\n"
"v_accvgpr_read_b32 v10, a240\n"
"v_subrev_u32_e32 v13, s51, v10\n"
"v_rcp_f32_e32 v12, v10\n"
"v_cmp_le_u32_e32 vcc, s52, v13\n"
"v_pk_mul_f32 v[160:161], v[160:161], v[12:13] op_sel_hi:[1,0]\n"
"v_pk_mul_f32 v[162:163], v[162:163], v[12:13] op_sel_hi:[1,0]\n"
"v_pk_mul_f32 v[164:165], v[164:165], v[12:13] op_sel_hi:[1,0]\n"
"v_pk_mul_f32 v[166:167], v[166:167], v[12:13] op_sel_hi:[1,0]\n"
"v_pk_mul_f32 v[168:169], v[168:169], v[12:13] op_sel_hi:[1,0]\n"
"v_pk_mul_f32 v[170:171], v[170:171], v[12:13] op_sel_hi:[1,0]\n"
"v_pk_mul_f32 v[172:173], v[172:173], v[12:13] op_sel_hi:[1,0]\n"
"v_pk_mul_f32 v[174:175], v[174:175], v[12:13] op_sel_hi:[1,0]\n"
"v_pk_mul_f32 v[176:177], v[176:177], v[12:13] op_sel_hi:[1,0]\n"
"v_pk_mul_f32 v[178:179], v[178:179], v[12:13] op_sel_hi:[1,0]\n"
"v_pk_mul_f32 v[180:181], v[180:181], v[12:13] op_sel_hi:[1,0]\n"
"v_pk_mul_f32 v[182:183], v[182:183], v[12:13] op_sel_hi:[1,0]\n"
"v_pk_mul_f32 v[184:185], v[184:185], v[12:13] op_sel_hi:[1,0]\n"
"v_pk_mul_f32 v[186:187], v[186:187], v[12:13] op_sel_hi:[1,0]\n"
"v_pk_mul_f32 v[188:189], v[188:189], v[12:13] op_sel_hi:[1,0]\n"
"v_pk_mul_f32 v[190:191], v[190:191], v[12:13] op_sel_hi:[1,0]\n"
"s_or_b64 s[54:55], s[54:55], vcc\n"
"v_mfma_f32_16x16x32_bf16 v[20:23], a[116:119], a[4:7], v[20:23]\n"
"s_waitcnt vmcnt(9) lgkmcnt(6)\n"
"s_barrier\n"
"v_exp_f32_e32 v26, v26\n"
"v_cvt_pk_bf16_f32 v40, v160, v161\n"
"v_cvt_pk_bf16_f32 v41, v162, v163\n"
"v_cvt_pk_bf16_f32 v42, v164, v165\n"
"v_cvt_pk_bf16_f32 v43, v166, v167\n"
"ds_write_b64 v56, v[40:41] offset:12288\n"
"ds_write_b64 v57, v[42:43] offset:12288\n"
"v_mfma_f32_16x16x32_bf16 v[20:23], a[120:123], a[8:11], v[20:23]\n"
"ds_read_b128 a[128:131], v1 offset:8192\n"
"ds_read_b128 a[140:143], v1 offset:9728\n"
"v_mul_f32_e32 v28, s44, v28\n"
"v_mfma_f32_16x16x32_bf16 v[20:23], a[124:127], a[12:15], v[20:23]\n"
"v_exp_f32_e32 v27, v27\n"
"v_cvt_pk_bf16_f32 v44, v168, v169\n"
"v_cvt_pk_bf16_f32 v45, v170, v171\n"
"v_cvt_pk_bf16_f32 v46, v172, v173\n"
"v_cvt_pk_bf16_f32 v47, v174, v175\n"
"ds_write_b64 v15, v[44:45] offset:12288\n"
"ds_write_b64 v60, v[46:47] offset:12288\n"
"v_mfma_f32_16x16x32_bf16 v[192:195], a[192:195], v[32:35], v[192:195]\n"
"v_mul_f32_e32 v29, s44, v29\n"
"v_cvt_pk_bf16_f32 v37, v26, v27\n"
"v_mfma_f32_16x16x32_bf16 v[196:199], a[196:199], v[32:35], v[196:199]\n"
"v_exp_f32_e32 v28, v28\n"
"v_cvt_pk_bf16_f32 v48, v176, v177\n"
"v_cvt_pk_bf16_f32 v49, v178, v179\n"
"v_cvt_pk_bf16_f32 v50, v180, v181\n"
"v_cvt_pk_bf16_f32 v51, v182, v183\n"
"ds_write_b64 v56, v[48:49] offset:12416\n"
"ds_write_b64 v57, v[50:51] offset:12416\n"
"v_mfma_f32_16x16x32_bf16 v[200:203], a[200:203], v[32:35], v[200:203]\n"
"ds_read_b128 a[132:135], v1 offset:8704\n"
"v_mul_f32_e32 v30, s44, v30\n"
"v_mfma_f32_16x16x32_bf16 v[204:207], a[204:207], v[32:35], v[204:207]\n"
"v_exp_f32_e32 v29, v29\n"
"v_cvt_pk_bf16_f32 v52, v184, v185\n"
"v_cvt_pk_bf16_f32 v53, v186, v187\n"
"v_cvt_pk_bf16_f32 v54, v188, v189\n"
"v_cvt_pk_bf16_f32 v55, v190, v191\n"
"ds_write_b64 v15, v[52:53] offset:12416\n"
"ds_write_b64 v60, v[54:55] offset:12416\n"
"v_mfma_f32_16x16x32_bf16 v[208:211], a[208:211], v[32:35], v[208:211]\n"
"s_add_u32 m0, s48, 0\n"
"v_mul_f32_e32 v31, s44, v31\n"
"v_cvt_pk_bf16_f32 v38, v28, v29\n"
"v_mfma_f32_16x16x32_bf16 v[212:215], a[212:215], v[32:35], v[212:215]\n"
"s_add_u32 s47, s45, 32768\n"
"v_exp_f32_e32 v30, v30\n"
"v_mfma_f32_16x16x32_bf16 v[216:219], a[216:219], v[32:35], v[216:219]\n"
"buffer_load_dwordx4 v6, s[24:27], s47 offen lds\n"
"v_mfma_f32_16x16x32_bf16 v[220:223], a[220:223], v[32:35], v[220:223]\n"
"v_exp_f32_e32 v31, v31\n"
"v_mfma_f32_16x16x32_bf16 a[244:247], a[224:227], v[32:35], a[244:247]\n"
"ds_read_b128 a[136:139], v1 offset:9216\n"
"v_cvt_pk_bf16_f32 v39, v30, v31\n"
"v_mfma_f32_16x16x32_bf16 v[24:27], a[96:99], a[16:19], 0\n"
"v_mul_f32_e32 v16, s44, v16\n"
"v_mul_f32_e32 v17, s44, v17\n"
"v_mfma_f32_16x16x32_bf16 v[24:27], a[100:103], a[20:23], v[24:27]\n"
"v_exp_f32_e32 v16, v16\n"
"v_mfma_f32_16x16x32_bf16 v[24:27], a[104:107], a[24:27], v[24:27]\n"
"ds_read_b128 a[144:147], v2 offset:8192\n"
"v_mul_f32_e32 v18, s44, v18\n"
"v_mfma_f32_16x16x32_bf16 v[24:27], a[108:111], a[28:31], v[24:27]\n"
"v_exp_f32_e32 v17, v17\n"
"v_mfma_f32_16x16x32_bf16 v[28:31], a[112:115], a[16:19], 0\n"
"v_mul_f32_e32 v19, s44, v19\n"
"v_cvt_pk_bf16_f32 v32, v16, v17\n"
"v_accvgpr_read_b32 v10, a244\n"
"v_subrev_u32_e32 v13, s51, v10\n"
"v_rcp_f32_e32 v12, v10\n"
"v_cmp_le_u32_e32 vcc, s52, v13\n"
"v_pk_mul_f32 v[192:193], v[192:193], v[12:13] op_sel_hi:[1,0]\n"
"v_pk_mul_f32 v[194:195], v[194:195], v[12:13] op_sel_hi:[1,0]\n"
"v_pk_mul_f32 v[196:197], v[196:197], v[12:13] op_sel_hi:[1,0]\n"
"v_pk_mul_f32 v[198:199], v[198:199], v[12:13] op_sel_hi:[1,0]\n"
"v_pk_mul_f32 v[200:201], v[200:201], v[12:13] op_sel_hi:[1,0]\n"
"v_pk_mul_f32 v[202:203], v[202:203], v[12:13] op_sel_hi:[1,0]\n"
"v_pk_mul_f32 v[204:205], v[204:205], v[12:13] op_sel_hi:[1,0]\n"
"v_pk_mul_f32 v[206:207], v[206:207], v[12:13] op_sel_hi:[1,0]\n"
"v_pk_mul_f32 v[208:209], v[208:209], v[12:13] op_sel_hi:[1,0]\n"
"v_pk_mul_f32 v[210:211], v[210:211], v[12:13] op_sel_hi:[1,0]\n"
"v_pk_mul_f32 v[212:213], v[212:213], v[12:13] op_sel_hi:[1,0]\n"
"v_pk_mul_f32 v[214:215], v[214:215], v[12:13] op_sel_hi:[1,0]\n"
"v_pk_mul_f32 v[216:217], v[216:217], v[12:13] op_sel_hi:[1,0]\n"
"v_pk_mul_f32 v[218:219], v[218:219], v[12:13] op_sel_hi:[1,0]\n"
"v_pk_mul_f32 v[220:221], v[220:221], v[12:13] op_sel_hi:[1,0]\n"
"v_pk_mul_f32 v[222:223], v[222:223], v[12:13] op_sel_hi:[1,0]\n"
"s_or_b64 s[54:55], s[54:55], vcc\n"
"v_mfma_f32_16x16x32_bf16 v[28:31], a[116:119], a[20:23], v[28:31]\n"
"v_exp_f32_e32 v18, v18\n"
"v_cvt_pk_bf16_f32 v40, v192, v193\n"
"v_cvt_pk_bf16_f32 v41, v194, v195\n"
"v_cvt_pk_bf16_f32 v42, v196, v197\n"
"v_cvt_pk_bf16_f32 v43, v198, v199\n"
"ds_write_b64 v56, v[40:41] offset:16384\n"
"ds_write_b64 v57, v[42:43] offset:16384\n"
"v_mfma_f32_16x16x32_bf16 v[28:31], a[120:123], a[24:27], v[28:31]\n"
"ds_read_b128 a[148:151], v2 offset:8704\n"
"v_mul_f32_e32 v20, s44, v20\n"
"v_mfma_f32_16x16x32_bf16 v[28:31], a[124:127], a[28:31], v[28:31]\n"
"v_exp_f32_e32 v19, v19\n"
"v_cvt_pk_bf16_f32 v44, v200, v201\n"
"v_cvt_pk_bf16_f32 v45, v202, v203\n"
"v_cvt_pk_bf16_f32 v46, v204, v205\n"
"v_cvt_pk_bf16_f32 v47, v206, v207\n"
"ds_write_b64 v15, v[44:45] offset:16384\n"
"ds_write_b64 v60, v[46:47] offset:16384\n"
"v_mfma_f32_16x16x32_bf16 v[224:227], a[192:195], v[36:39], v[224:227]\n"
"v_mul_f32_e32 v21, s44, v21\n"
"v_cvt_pk_bf16_f32 v33, v18, v19\n"
"v_mfma_f32_16x16x32_bf16 v[228:231], a[196:199], v[36:39], v[228:231]\n"
"v_exp_f32_e32 v20, v20\n"
"v_cvt_pk_bf16_f32 v48, v208, v209\n"
"v_cvt_pk_bf16_f32 v49, v210, v211\n"
"v_cvt_pk_bf16_f32 v50, v212, v213\n"
"v_cvt_pk_bf16_f32 v51, v214, v215\n"
"ds_write_b64 v56, v[48:49] offset:16512\n"
"ds_write_b64 v57, v[50:51] offset:16512\n"
"v_mfma_f32_16x16x32_bf16 v[232:235], a[200:203], v[36:39], v[232:235]\n"
"ds_read_b128 a[152:155], v2 offset:9216\n"
"v_mul_f32_e32 v22, s44, v22\n"
"v_mfma_f32_16x16x32_bf16 v[236:239], a[204:207], v[36:39], v[236:239]\n"
"v_exp_f32_e32 v21, v21\n"
"v_cvt_pk_bf16_f32 v52, v216, v217\n"
"v_cvt_pk_bf16_f32 v53, v218, v219\n"
"v_cvt_pk_bf16_f32 v54, v220, v221\n"
"v_cvt_pk_bf16_f32 v55, v222, v223\n"
"ds_write_b64 v15, v[52:53] offset:16512\n"
"ds_write_b64 v60, v[54:55] offset:16512\n"
"v_mfma_f32_16x16x32_bf16 v[240:243], a[208:211], v[36:39], v[240:243]\n"
"s_add_u32 m0, s48, 1024\n"
"v_mul_f32_e32 v23, s44, v23\n"
"v_cvt_pk_bf16_f32 v34, v20, v21\n"
"v_mfma_f32_16x16x32_bf16 v[244:247], a[212:215], v[36:39], v[244:247]\n"
"s_add_u32 s47, s45, 32896\n"
"v_exp_f32_e32 v22, v22\n"
"v_mfma_f32_16x16x32_bf16 v[248:251], a[216:219], v[36:39], v[248:251]\n"
"buffer_load_dwordx4 v6, s[24:27], s47 offen lds\n"
"v_mfma_f32_16x16x32_bf16 v[252:255], a[220:223], v[36:39], v[252:255]\n"
"v_exp_f32_e32 v23, v23\n"
"v_mfma_f32_16x16x32_bf16 a[248:251], a[224:227], v[36:39], a[248:251]\n"
"ds_read_b128 a[156:159], v2 offset:9728\n"
"v_cvt_pk_bf16_f32 v35, v22, v23\n"
"v_mfma_f32_16x16x32_bf16 v[16:19], a[96:99], a[32:35], 0\n"
"v_mul_f32_e32 v24, s44, v24\n"
"v_mul_f32_e32 v25, s44, v25\n"
"v_mfma_f32_16x16x32_bf16 v[16:19], a[100:103], a[36:39], v[16:19]\n"
"v_exp_f32_e32 v24, v24\n"
"v_mfma_f32_16x16x32_bf16 v[16:19], a[104:107], a[40:43], v[16:19]\n"
"ds_read_b64_tr_b16 a[192:193], v3 offset:8192\n"
"v_mul_f32_e32 v26, s44, v26\n"
"v_mfma_f32_16x16x32_bf16 v[16:19], a[108:111], a[44:47], v[16:19]\n"
"v_exp_f32_e32 v25, v25\n"
"v_mfma_f32_16x16x32_bf16 v[20:23], a[112:115], a[32:35], 0\n"
"v_mul_f32_e32 v27, s44, v27\n"
"v_cvt_pk_bf16_f32 v36, v24, v25\n"
"v_accvgpr_read_b32 v10, a248\n"
"v_subrev_u32_e32 v13, s51, v10\n"
"v_rcp_f32_e32 v12, v10\n"
"v_cmp_le_u32_e32 vcc, s52, v13\n"
"v_pk_mul_f32 v[224:225], v[224:225], v[12:13] op_sel_hi:[1,0]\n"
"v_pk_mul_f32 v[226:227], v[226:227], v[12:13] op_sel_hi:[1,0]\n"
"v_pk_mul_f32 v[228:229], v[228:229], v[12:13] op_sel_hi:[1,0]\n"
"v_pk_mul_f32 v[230:231], v[230:231], v[12:13] op_sel_hi:[1,0]\n"
"v_pk_mul_f32 v[232:233], v[232:233], v[12:13] op_sel_hi:[1,0]\n"
"v_pk_mul_f32 v[234:235], v[234:235], v[12:13] op_sel_hi:[1,0]\n"
"v_pk_mul_f32 v[236:237], v[236:237], v[12:13] op_sel_hi:[1,0]\n"
"v_pk_mul_f32 v[238:239], v[238:239], v[12:13] op_sel_hi:[1,0]\n"
"v_pk_mul_f32 v[240:241], v[240:241], v[12:13] op_sel_hi:[1,0]\n"
"v_pk_mul_f32 v[242:243], v[242:243], v[12:13] op_sel_hi:[1,0]\n"
"v_pk_mul_f32 v[244:245], v[244:245], v[12:13] op_sel_hi:[1,0]\n"
"v_pk_mul_f32 v[246:247], v[246:247], v[12:13] op_sel_hi:[1,0]\n"
"v_pk_mul_f32 v[248:249], v[248:249], v[12:13] op_sel_hi:[1,0]\n"
"v_pk_mul_f32 v[250:251], v[250:251], v[12:13] op_sel_hi:[1,0]\n"
"v_pk_mul_f32 v[252:253], v[252:253], v[12:13] op_sel_hi:[1,0]\n"
"v_pk_mul_f32 v[254:255], v[254:255], v[12:13] op_sel_hi:[1,0]\n"
"s_or_b64 s[54:55], s[54:55], vcc\n"
"v_mfma_f32_16x16x32_bf16 v[20:23], a[116:119], a[36:39], v[20:23]\n"
"v_exp_f32_e32 v26, v26\n"
"v_cvt_pk_bf16_f32 v40, v224, v225\n"
"v_cvt_pk_bf16_f32 v41, v226, v227\n"
"v_cvt_pk_bf16_f32 v42, v228, v229\n"
"v_cvt_pk_bf16_f32 v43, v230, v231\n"
"s_waitcnt lgkmcnt(15)\n"
"ds_write_b64 v56, v[40:41] offset:20480\n"
"ds_write_b64 v57, v[42:43] offset:20480\n"
"v_mfma_f32_16x16x32_bf16 v[20:23], a[120:123], a[40:43], v[20:23]\n"
"ds_read_b64_tr_b16 a[194:195], v4 offset:8192\n"
"v_mul_f32_e32 v28, s44, v28\n"
"v_mfma_f32_16x16x32_bf16 v[20:23], a[124:127], a[44:47], v[20:23]\n"
"v_exp_f32_e32 v27, v27\n"
"v_cvt_pk_bf16_f32 v44, v232, v233\n"
"v_cvt_pk_bf16_f32 v45, v234, v235\n"
"v_cvt_pk_bf16_f32 v46, v236, v237\n"
"v_cvt_pk_bf16_f32 v47, v238, v239\n"
"ds_write_b64 v15, v[44:45] offset:20480\n"
"ds_write_b64 v60, v[46:47] offset:20480\n"
"v_mfma_f32_16x16x32_bf16 v[64:67], a[160:163], v[32:35], 0\n"
"v_mul_f32_e32 v29, s44, v29\n"
"v_cvt_pk_bf16_f32 v37, v26, v27\n"
"v_mfma_f32_16x16x32_bf16 v[68:71], a[164:167], v[32:35], 0\n"
"v_exp_f32_e32 v28, v28\n"
"v_cvt_pk_bf16_f32 v48, v240, v241\n"
"v_cvt_pk_bf16_f32 v49, v242, v243\n"
"v_cvt_pk_bf16_f32 v50, v244, v245\n"
"v_cvt_pk_bf16_f32 v51, v246, v247\n"
"ds_write_b64 v56, v[48:49] offset:20608\n"
"ds_write_b64 v57, v[50:51] offset:20608\n"
"v_mfma_f32_16x16x32_bf16 v[72:75], a[168:171], v[32:35], 0\n"
"ds_read_b64_tr_b16 a[196:197], v11 offset:8192\n"
"v_mul_f32_e32 v30, s44, v30\n"
"v_mfma_f32_16x16x32_bf16 v[76:79], a[172:175], v[32:35], 0\n"
"v_exp_f32_e32 v29, v29\n"
"v_cvt_pk_bf16_f32 v52, v248, v249\n"
"v_cvt_pk_bf16_f32 v53, v250, v251\n"
"v_cvt_pk_bf16_f32 v54, v252, v253\n"
"v_cvt_pk_bf16_f32 v55, v254, v255\n"
"ds_write_b64 v15, v[52:53] offset:20608\n"
"ds_write_b64 v60, v[54:55] offset:20608\n"
"v_mfma_f32_16x16x32_bf16 v[80:83], a[176:179], v[32:35], 0\n"
"s_add_u32 m0, s48, 32768\n"
"v_mul_f32_e32 v31, s44, v31\n"
"v_cvt_pk_bf16_f32 v38, v28, v29\n"
"v_mfma_f32_16x16x32_bf16 v[84:87], a[180:183], v[32:35], 0\n"
"s_add_u32 s76, s45, 32768\n"
"v_exp_f32_e32 v30, v30\n"
"v_mfma_f32_16x16x32_bf16 v[88:91], a[184:187], v[32:35], 0\n"
"buffer_load_dwordx4 v6, s[28:31], s76 offen lds\n"
"v_mfma_f32_16x16x32_bf16 v[92:95], a[188:191], v[32:35], 0\n"
"v_exp_f32_e32 v31, v31\n"
"v_mfma_f32_16x16x32_bf16 a[228:231], a[224:227], v[32:35], 0\n"
"ds_read_b64_tr_b16 a[198:199], v14 offset:8192\n"
"v_cvt_pk_bf16_f32 v39, v30, v31\n"
"s_cmp_lg_u64 s[54:55], 0\n"
"s_cselect_b32 s57, s56, 0\n"
"s_or_b32 s53, s53, s57\n"
"s_lshl_b32 s56, s56, 1\n"
"s_mov_b64 s[54:55], 0\n"
"v_mfma_f32_16x16x32_bf16 v[24:27], a[96:99], a[48:51], 0\n"
"v_mul_f32_e32 v16, s44, v16\n"
"v_mul_f32_e32 v17, s44, v17\n"
"v_mfma_f32_16x16x32_bf16 v[24:27], a[100:103], a[52:55], v[24:27]\n"
"v_exp_f32_e32 v16, v16\n"
"v_mfma_f32_16x16x32_bf16 v[24:27], a[104:107], a[56:59], v[24:27]\n"
"ds_read_b64_tr_b16 a[200:201], v3 offset:8704\n"
"v_mul_f32_e32 v18, s44, v18\n"
"v_mfma_f32_16x16x32_bf16 v[24:27], a[108:111], a[60:63], v[24:27]\n"
"v_exp_f32_e32 v17, v17\n"
"v_mfma_f32_16x16x32_bf16 v[28:31], a[112:115], a[48:51], 0\n"
"v_mul_f32_e32 v19, s44, v19\n"
"v_cvt_pk_bf16_f32 v32, v16, v17\n"
"v_mfma_f32_16x16x32_bf16 v[28:31], a[116:119], a[52:55], v[28:31]\n"
"v_exp_f32_e32 v18, v18\n"
"v_mfma_f32_16x16x32_bf16 v[28:31], a[120:123], a[56:59], v[28:31]\n"
"ds_read_b64_tr_b16 a[202:203], v4 offset:8704\n"
"v_mul_f32_e32 v20, s44, v20\n"
"v_mfma_f32_16x16x32_bf16 v[28:31], a[124:127], a[60:63], v[28:31]\n"
"v_exp_f32_e32 v19, v19\n"
"v_mfma_f32_16x16x32_bf16 v[96:99], a[160:163], v[36:39], 0\n"
"v_mul_f32_e32 v21, s44, v21\n"
"v_cvt_pk_bf16_f32 v33, v18, v19\n"
"v_mfma_f32_16x16x32_bf16 v[100:103], a[164:167], v[36:39], 0\n"
"v_exp_f32_e32 v20, v20\n"
"v_mfma_f32_16x16x32_bf16 v[104:107], a[168:171], v[36:39], 0\n"
"ds_read_b64_tr_b16 a[204:205], v11 offset:8704\n"
"v_mul_f32_e32 v22, s44, v22\n"
"v_mfma_f32_16x16x32_bf16 v[108:111], a[172:175], v[36:39], 0\n"
"v_exp_f32_e32 v21, v21\n"
"v_mfma_f32_16x16x32_bf16 v[112:115], a[176:179], v[36:39], 0\n"
"s_add_u32 m0, s48, 33792\n"
"v_mul_f32_e32 v23, s44, v23\n"
"v_cvt_pk_bf16_f32 v34, v20, v21\n"
"v_mfma_f32_16x16x32_bf16 v[116:119], a[180:183], v[36:39], 0\n"
"s_add_u32 s76, s45, 32896\n"
"v_exp_f32_e32 v22, v22\n"
"v_mfma_f32_16x16x32_bf16 v[120:123], a[184:187], v[36:39], 0\n"
"buffer_load_dwordx4 v6, s[28:31], s76 offen lds\n"
"v_mfma_f32_16x16x32_bf16 v[124:127], a[188:191], v[36:39], 0\n"
"v_exp_f32_e32 v23, v23\n"
"v_mfma_f32_16x16x32_bf16 a[232:235], a[224:227], v[36:39], 0\n"
"ds_read_b64_tr_b16 a[206:207], v14 offset:8704\n"
"v_cvt_pk_bf16_f32 v35, v22, v23\n"
"v_mfma_f32_16x16x32_bf16 v[16:19], a[96:99], a[64:67], 0\n"
"v_mul_f32_e32 v24, s44, v24\n"
"v_mul_f32_e32 v25, s44, v25\n"
"v_mfma_f32_16x16x32_bf16 v[16:19], a[100:103], a[68:71], v[16:19]\n"
"v_exp_f32_e32 v24, v24\n"
"v_mfma_f32_16x16x32_bf16 v[16:19], a[104:107], a[72:75], v[16:19]\n"
"ds_read_b64_tr_b16 a[210:211], v4 offset:9216\n"
"v_mul_f32_e32 v26, s44, v26\n"
"v_mfma_f32_16x16x32_bf16 v[16:19], a[108:111], a[76:79], v[16:19]\n"
"v_exp_f32_e32 v25, v25\n"
"v_mfma_f32_16x16x32_bf16 v[20:23], a[112:115], a[64:67], 0\n"
"v_mul_f32_e32 v27, s44, v27\n"
"v_cvt_pk_bf16_f32 v36, v24, v25\n"
"v_mfma_f32_16x16x32_bf16 v[20:23], a[116:119], a[68:71], v[20:23]\n"
"v_exp_f32_e32 v26, v26\n"
"v_mfma_f32_16x16x32_bf16 v[20:23], a[120:123], a[72:75], v[20:23]\n"
"ds_read_b64_tr_b16 a[212:213], v11 offset:9216\n"
"v_mul_f32_e32 v28, s44, v28\n"
"v_mfma_f32_16x16x32_bf16 v[20:23], a[124:127], a[76:79], v[20:23]\n"
"v_exp_f32_e32 v27, v27\n"
"v_mfma_f32_16x16x32_bf16 v[128:131], a[160:163], v[32:35], 0\n"
"v_mul_f32_e32 v29, s44, v29\n"
"v_cvt_pk_bf16_f32 v37, v26, v27\n"
"v_mfma_f32_16x16x32_bf16 v[132:135], a[164:167], v[32:35], 0\n"
"v_exp_f32_e32 v28, v28\n"
"v_mfma_f32_16x16x32_bf16 v[136:139], a[168:171], v[32:35], 0\n"
"ds_read_b64_tr_b16 a[214:215], v14 offset:9216\n"
"v_mul_f32_e32 v30, s44, v30\n"
"v_mfma_f32_16x16x32_bf16 v[140:143], a[172:175], v[32:35], 0\n"
"v_exp_f32_e32 v29, v29\n"
"v_mfma_f32_16x16x32_bf16 v[144:147], a[176:179], v[32:35], 0\n"
"v_mul_f32_e32 v31, s44, v31\n"
"v_cvt_pk_bf16_f32 v38, v28, v29\n"
"v_mfma_f32_16x16x32_bf16 v[148:151], a[180:183], v[32:35], 0\n"
"v_exp_f32_e32 v30, v30\n"
"v_mfma_f32_16x16x32_bf16 v[152:155], a[184:187], v[32:35], 0\n"
"ds_read_b64_tr_b16 a[208:209], v3 offset:9216\n"
"v_mfma_f32_16x16x32_bf16 v[156:159], a[188:191], v[32:35], 0\n"
"v_exp_f32_e32 v31, v31\n"
"v_mfma_f32_16x16x32_bf16 a[236:239], a[224:227], v[32:35], 0\n"
"v_cvt_pk_bf16_f32 v39, v30, v31\n"
"v_mfma_f32_16x16x32_bf16 v[24:27], a[96:99], a[80:83], 0\n"
"ds_read_b64_tr_b16 a[222:223], v14 offset:9728\n"
"v_mul_f32_e32 v16, s44, v16\n"
"v_mul_f32_e32 v17, s44, v17\n"
"v_mfma_f32_16x16x32_bf16 v[24:27], a[100:103], a[84:87], v[24:27]\n"
"v_exp_f32_e32 v16, v16\n"
"v_mfma_f32_16x16x32_bf16 v[24:27], a[104:107], a[88:91], v[24:27]\n"
"ds_read_b64_tr_b16 a[216:217], v3 offset:9728\n"
"v_mul_f32_e32 v18, s44, v18\n"
"v_mfma_f32_16x16x32_bf16 v[24:27], a[108:111], a[92:95], v[24:27]\n"
"v_exp_f32_e32 v17, v17\n"
"v_mfma_f32_16x16x32_bf16 v[28:31], a[112:115], a[80:83], 0\n"
"v_mul_f32_e32 v19, s44, v19\n"
"v_cvt_pk_bf16_f32 v32, v16, v17\n"
"v_mfma_f32_16x16x32_bf16 v[28:31], a[116:119], a[84:87], v[28:31]\n"
"v_exp_f32_e32 v18, v18\n"
"v_mfma_f32_16x16x32_bf16 v[28:31], a[120:123], a[88:91], v[28:31]\n"
"ds_read_b64_tr_b16 a[218:219], v4 offset:9728\n"
"v_mul_f32_e32 v20, s44, v20\n"
"v_mfma_f32_16x16x32_bf16 v[28:31], a[124:127], a[92:95], v[28:31]\n"
"v_exp_f32_e32 v19, v19\n"
"v_mfma_f32_16x16x32_bf16 v[160:163], a[160:163], v[36:39], 0\n"
"v_mul_f32_e32 v21, s44, v21\n"
"v_cvt_pk_bf16_f32 v33, v18, v19\n"
"v_mfma_f32_16x16x32_bf16 v[164:167], a[164:167], v[36:39], 0\n"
"v_exp_f32_e32 v20, v20\n"
"v_mfma_f32_16x16x32_bf16 v[168:171], a[168:171], v[36:39], 0\n"
"ds_read_b64_tr_b16 a[220:221], v11 offset:9728\n"
"v_mul_f32_e32 v22, s44, v22\n"
"v_mfma_f32_16x16x32_bf16 v[172:175], a[172:175], v[36:39], 0\n"
"v_exp_f32_e32 v21, v21\n"
"v_mfma_f32_16x16x32_bf16 v[176:179], a[176:179], v[36:39], 0\n"
"v_mul_f32_e32 v23, s44, v23\n"
"v_cvt_pk_bf16_f32 v34, v20, v21\n"
"v_mfma_f32_16x16x32_bf16 v[180:183], a[180:183], v[36:39], 0\n"
"v_exp_f32_e32 v22, v22\n"
"v_mfma_f32_16x16x32_bf16 v[184:187], a[184:187], v[36:39], 0\n"
"v_mfma_f32_16x16x32_bf16 v[188:191], a[188:191], v[36:39], 0\n"
"v_exp_f32_e32 v23, v23\n"
"v_mfma_f32_16x16x32_bf16 a[240:243], a[224:227], v[36:39], 0\n"
"v_cvt_pk_bf16_f32 v35, v22, v23\n"
"v_mfma_f32_16x16x32_bf16 v[16:19], a[128:131], a[0:3], 0\n"
"v_mul_f32_e32 v24, s44, v24\n"
"v_mul_f32_e32 v25, s44, v25\n"
"v_mfma_f32_16x16x32_bf16 v[16:19], a[132:135], a[4:7], v[16:19]\n"
"s_waitcnt lgkmcnt(15)\n"
"v_exp_f32_e32 v24, v24\n"
"v_mfma_f32_16x16x32_bf16 v[16:19], a[136:139], a[8:11], v[16:19]\n"
"v_mul_f32_e32 v26, s44, v26\n"
"v_mfma_f32_16x16x32_bf16 v[16:19], a[140:143], a[12:15], v[16:19]\n"
"v_exp_f32_e32 v25, v25\n"
"v_mfma_f32_16x16x32_bf16 v[20:23], a[144:147], a[0:3], 0\n"
"v_mul_f32_e32 v27, s44, v27\n"
"v_cvt_pk_bf16_f32 v36, v24, v25\n"
"v_mfma_f32_16x16x32_bf16 v[20:23], a[148:151], a[4:7], v[20:23]\n"
"s_waitcnt vmcnt(9) lgkmcnt(0)\n"
"s_barrier\n"
"v_exp_f32_e32 v26, v26\n"
"v_mfma_f32_16x16x32_bf16 v[20:23], a[152:155], a[8:11], v[20:23]\n"
"ds_read_b128 a[96:99], v1 offset:16384\n"
"ds_read_b128 a[108:111], v1 offset:17920\n"
"v_mul_f32_e32 v28, s44, v28\n"
"v_mfma_f32_16x16x32_bf16 v[20:23], a[156:159], a[12:15], v[20:23]\n"
"ds_read_b128 v[40:43], v58 offset:0\n"
"v_exp_f32_e32 v27, v27\n"
"v_mfma_f32_16x16x32_bf16 v[192:195], a[160:163], v[32:35], 0\n"
"ds_read_b128 v[44:47], v59 offset:1024\n"
"v_mul_f32_e32 v29, s44, v29\n"
"v_cvt_pk_bf16_f32 v37, v26, v27\n"
"v_mfma_f32_16x16x32_bf16 v[196:199], a[164:167], v[32:35], 0\n"
"ds_read_b128 v[48:51], v58 offset:2048\n"
"v_exp_f32_e32 v28, v28\n"
"v_mfma_f32_16x16x32_bf16 v[200:203], a[168:171], v[32:35], 0\n"
"ds_read_b128 a[100:103], v1 offset:16896\n"
"ds_read_b128 v[52:55], v59 offset:3072\n"
"v_mul_f32_e32 v30, s44, v30\n"
"v_mfma_f32_16x16x32_bf16 v[204:207], a[172:175], v[32:35], 0\n"
"v_exp_f32_e32 v29, v29\n"
"v_mfma_f32_16x16x32_bf16 v[208:211], a[176:179], v[32:35], 0\n"
"s_add_u32 m0, s48, 8192\n"
"v_mul_f32_e32 v31, s44, v31\n"
"v_cvt_pk_bf16_f32 v38, v28, v29\n"
"v_mfma_f32_16x16x32_bf16 v[212:215], a[180:183], v[32:35], 0\n"
"s_add_u32 s47, s45, 40960\n"
"v_exp_f32_e32 v30, v30\n"
"v_mfma_f32_16x16x32_bf16 v[216:219], a[184:187], v[32:35], 0\n"
"buffer_load_dwordx4 v6, s[24:27], s47 offen lds\n"
"v_mfma_f32_16x16x32_bf16 v[220:223], a[188:191], v[32:35], 0\n"
"v_exp_f32_e32 v31, v31\n"
"v_mfma_f32_16x16x32_bf16 a[244:247], a[224:227], v[32:35], 0\n"
"ds_read_b128 a[104:107], v1 offset:17408\n"
"v_cvt_pk_bf16_f32 v39, v30, v31\n"
"v_mfma_f32_16x16x32_bf16 v[24:27], a[128:131], a[16:19], 0\n"
"v_mul_f32_e32 v16, s44, v16\n"
"v_mul_f32_e32 v17, s44, v17\n"
"v_mfma_f32_16x16x32_bf16 v[24:27], a[132:135], a[20:23], v[24:27]\n"
"v_exp_f32_e32 v16, v16\n"
"s_add_u32 s77, s60, 0\n"
"v_mfma_f32_16x16x32_bf16 v[24:27], a[136:139], a[24:27], v[24:27]\n"
"ds_read_b128 a[112:115], v2 offset:16384\n"
"v_mul_f32_e32 v18, s44, v18\n"
"s_waitcnt lgkmcnt(6)\n"
"buffer_store_dwordx4 v[40:43], v9, s[40:43], s77 offen offset:0  sc1\n"
"v_mfma_f32_16x16x32_bf16 v[24:27], a[140:143], a[28:31], v[24:27]\n"
"v_exp_f32_e32 v17, v17\n"
"v_mfma_f32_16x16x32_bf16 v[28:31], a[144:147], a[16:19], 0\n"
"v_mul_f32_e32 v19, s44, v19\n"
"v_cvt_pk_bf16_f32 v32, v16, v17\n"
"v_mfma_f32_16x16x32_bf16 v[28:31], a[148:151], a[20:23], v[28:31]\n"
"v_exp_f32_e32 v18, v18\n"
"v_mfma_f32_16x16x32_bf16 v[28:31], a[152:155], a[24:27], v[28:31]\n"
"ds_read_b128 a[116:119], v2 offset:16896\n"
"v_mul_f32_e32 v20, s44, v20\n"
"s_waitcnt lgkmcnt(6)\n"
"buffer_store_dwordx4 v[44:47], v9, s[40:43], s77 offen offset:1024  sc1\n"
"v_mfma_f32_16x16x32_bf16 v[28:31], a[156:159], a[28:31], v[28:31]\n"
"v_exp_f32_e32 v19, v19\n"
"v_mfma_f32_16x16x32_bf16 v[224:227], a[160:163], v[36:39], 0\n"
"v_mul_f32_e32 v21, s44, v21\n"
"v_cvt_pk_bf16_f32 v33, v18, v19\n"
"v_mfma_f32_16x16x32_bf16 v[228:231], a[164:167], v[36:39], 0\n"
"v_exp_f32_e32 v20, v20\n"
"v_mfma_f32_16x16x32_bf16 v[232:235], a[168:171], v[36:39], 0\n"
"ds_read_b128 a[120:123], v2 offset:17408\n"
"v_mul_f32_e32 v22, s44, v22\n"
"s_waitcnt lgkmcnt(6)\n"
"buffer_store_dwordx4 v[48:51], v9, s[40:43], s77 offen offset:2048  sc1\n"
"v_mfma_f32_16x16x32_bf16 v[236:239], a[172:175], v[36:39], 0\n"
"v_exp_f32_e32 v21, v21\n"
"v_mfma_f32_16x16x32_bf16 v[240:243], a[176:179], v[36:39], 0\n"
"s_add_u32 m0, s48, 9216\n"
"v_mul_f32_e32 v23, s44, v23\n"
"v_cvt_pk_bf16_f32 v34, v20, v21\n"
"v_mfma_f32_16x16x32_bf16 v[244:247], a[180:183], v[36:39], 0\n"
"s_add_u32 s47, s45, 41088\n"
"v_exp_f32_e32 v22, v22\n"
"v_mfma_f32_16x16x32_bf16 v[248:251], a[184:187], v[36:39], 0\n"
"buffer_load_dwordx4 v6, s[24:27], s47 offen lds\n"
"v_mfma_f32_16x16x32_bf16 v[252:255], a[188:191], v[36:39], 0\n"
"v_exp_f32_e32 v23, v23\n"
"v_mfma_f32_16x16x32_bf16 a[248:251], a[224:227], v[36:39], 0\n"
"ds_read_b128 a[124:127], v2 offset:17920\n"
"v_cvt_pk_bf16_f32 v35, v22, v23\n"
"s_waitcnt lgkmcnt(5)\n"
"buffer_store_dwordx4 v[52:55], v9, s[40:43], s77 offen offset:3072  sc1\n"
"v_mfma_f32_16x16x32_bf16 v[16:19], a[128:131], a[32:35], 0\n"
"v_mul_f32_e32 v24, s44, v24\n"
"v_mul_f32_e32 v25, s44, v25\n"
"v_mfma_f32_16x16x32_bf16 v[16:19], a[132:135], a[36:39], v[16:19]\n"
"v_exp_f32_e32 v24, v24\n"
"v_mfma_f32_16x16x32_bf16 v[16:19], a[136:139], a[40:43], v[16:19]\n"
"ds_read_b64_tr_b16 a[160:161], v3 offset:16384\n"
"v_mul_f32_e32 v26, s44, v26\n"
"v_mfma_f32_16x16x32_bf16 v[16:19], a[140:143], a[44:47], v[16:19]\n"
"v_exp_f32_e32 v25, v25\n"
"v_mfma_f32_16x16x32_bf16 v[20:23], a[144:147], a[32:35], 0\n"
"v_mul_f32_e32 v27, s44, v27\n"
"v_cvt_pk_bf16_f32 v36, v24, v25\n"
"v_mfma_f32_16x16x32_bf16 v[20:23], a[148:151], a[36:39], v[20:23]\n"
"v_exp_f32_e32 v26, v26\n"
"v_mfma_f32_16x16x32_bf16 v[20:23], a[152:155], a[40:43], v[20:23]\n"
"ds_read_b64_tr_b16 a[162:163], v4 offset:16384\n"
"v_mul_f32_e32 v28, s44, v28\n"
"v_mfma_f32_16x16x32_bf16 v[20:23], a[156:159], a[44:47], v[20:23]\n"
"v_exp_f32_e32 v27, v27\n"
"v_mfma_f32_16x16x32_bf16 v[64:67], a[192:195], v[32:35], v[64:67]\n"
"v_mul_f32_e32 v29, s44, v29\n"
"v_cvt_pk_bf16_f32 v37, v26, v27\n"
"v_mfma_f32_16x16x32_bf16 v[68:71], a[196:199], v[32:35], v[68:71]\n"
"v_exp_f32_e32 v28, v28\n"
"v_mfma_f32_16x16x32_bf16 v[72:75], a[200:203], v[32:35], v[72:75]\n"
"ds_read_b64_tr_b16 a[164:165], v11 offset:16384\n"
"v_mul_f32_e32 v30, s44, v30\n"
"v_mfma_f32_16x16x32_bf16 v[76:79], a[204:207], v[32:35], v[76:79]\n"
"v_exp_f32_e32 v29, v29\n"
"v_mfma_f32_16x16x32_bf16 v[80:83], a[208:211], v[32:35], v[80:83]\n"
"s_add_u32 m0, s48, 40960\n"
"v_mul_f32_e32 v31, s44, v31\n"
"v_cvt_pk_bf16_f32 v38, v28, v29\n"
"v_mfma_f32_16x16x32_bf16 v[84:87], a[212:215], v[32:35], v[84:87]\n"
"s_add_u32 s76, s45, 40960\n"
"v_exp_f32_e32 v30, v30\n"
"v_mfma_f32_16x16x32_bf16 v[88:91], a[216:219], v[32:35], v[88:91]\n"
"buffer_load_dwordx4 v6, s[28:31], s76 offen lds\n"
"v_mfma_f32_16x16x32_bf16 v[92:95], a[220:223], v[32:35], v[92:95]\n"
"v_exp_f32_e32 v31, v31\n"
"v_mfma_f32_16x16x32_bf16 a[228:231], a[224:227], v[32:35], a[228:231]\n"
"ds_read_b64_tr_b16 a[166:167], v14 offset:16384\n"
"v_cvt_pk_bf16_f32 v39, v30, v31\n"
"v_mfma_f32_16x16x32_bf16 v[24:27], a[128:131], a[48:51], 0\n"
"v_mul_f32_e32 v16, s44, v16\n"
"v_mul_f32_e32 v17, s44, v17\n"
"v_mfma_f32_16x16x32_bf16 v[24:27], a[132:135], a[52:55], v[24:27]\n"
"v_exp_f32_e32 v16, v16\n"
"v_mfma_f32_16x16x32_bf16 v[24:27], a[136:139], a[56:59], v[24:27]\n"
"ds_read_b64_tr_b16 a[168:169], v3 offset:16896\n"
"v_mul_f32_e32 v18, s44, v18\n"
"v_mfma_f32_16x16x32_bf16 v[24:27], a[140:143], a[60:63], v[24:27]\n"
"v_exp_f32_e32 v17, v17\n"
"v_mfma_f32_16x16x32_bf16 v[28:31], a[144:147], a[48:51], 0\n"
"v_mul_f32_e32 v19, s44, v19\n"
"v_cvt_pk_bf16_f32 v32, v16, v17\n"
"v_mfma_f32_16x16x32_bf16 v[28:31], a[148:151], a[52:55], v[28:31]\n"
"v_exp_f32_e32 v18, v18\n"
"v_mfma_f32_16x16x32_bf16 v[28:31], a[152:155], a[56:59], v[28:31]\n"
"ds_read_b64_tr_b16 a[170:171], v4 offset:16896\n"
"v_mul_f32_e32 v20, s44, v20\n"
"v_mfma_f32_16x16x32_bf16 v[28:31], a[156:159], a[60:63], v[28:31]\n"
"v_exp_f32_e32 v19, v19\n"
"v_mfma_f32_16x16x32_bf16 v[96:99], a[192:195], v[36:39], v[96:99]\n"
"v_mul_f32_e32 v21, s44, v21\n"
"v_cvt_pk_bf16_f32 v33, v18, v19\n"
"v_mfma_f32_16x16x32_bf16 v[100:103], a[196:199], v[36:39], v[100:103]\n"
"v_exp_f32_e32 v20, v20\n"
"v_mfma_f32_16x16x32_bf16 v[104:107], a[200:203], v[36:39], v[104:107]\n"
"ds_read_b64_tr_b16 a[172:173], v11 offset:16896\n"
"v_mul_f32_e32 v22, s44, v22\n"
"v_mfma_f32_16x16x32_bf16 v[108:111], a[204:207], v[36:39], v[108:111]\n"
"v_exp_f32_e32 v21, v21\n"
"v_mfma_f32_16x16x32_bf16 v[112:115], a[208:211], v[36:39], v[112:115]\n"
"s_add_u32 m0, s48, 41984\n"
"v_mul_f32_e32 v23, s44, v23\n"
"v_cvt_pk_bf16_f32 v34, v20, v21\n"
"v_mfma_f32_16x16x32_bf16 v[116:119], a[212:215], v[36:39], v[116:119]\n"
"s_add_u32 s76, s45, 41088\n"
"v_exp_f32_e32 v22, v22\n"
"v_mfma_f32_16x16x32_bf16 v[120:123], a[216:219], v[36:39], v[120:123]\n"
"buffer_load_dwordx4 v6, s[28:31], s76 offen lds\n"
"v_mfma_f32_16x16x32_bf16 v[124:127], a[220:223], v[36:39], v[124:127]\n"
"v_exp_f32_e32 v23, v23\n"
"v_mfma_f32_16x16x32_bf16 a[232:235], a[224:227], v[36:39], a[232:235]\n"
"ds_read_b64_tr_b16 a[174:175], v14 offset:16896\n"
"v_cvt_pk_bf16_f32 v35, v22, v23\n"
"v_mfma_f32_16x16x32_bf16 v[16:19], a[128:131], a[64:67], 0\n"
"v_mul_f32_e32 v24, s44, v24\n"
"v_mul_f32_e32 v25, s44, v25\n"
"v_mfma_f32_16x16x32_bf16 v[16:19], a[132:135], a[68:71], v[16:19]\n"
"v_exp_f32_e32 v24, v24\n"
"v_mfma_f32_16x16x32_bf16 v[16:19], a[136:139], a[72:75], v[16:19]\n"
"ds_read_b64_tr_b16 a[176:177], v3 offset:17408\n"
"v_mul_f32_e32 v26, s44, v26\n"
"v_mfma_f32_16x16x32_bf16 v[16:19], a[140:143], a[76:79], v[16:19]\n"
"v_exp_f32_e32 v25, v25\n"
"v_mfma_f32_16x16x32_bf16 v[20:23], a[144:147], a[64:67], 0\n"
"v_mul_f32_e32 v27, s44, v27\n"
"v_cvt_pk_bf16_f32 v36, v24, v25\n"
"v_mfma_f32_16x16x32_bf16 v[20:23], a[148:151], a[68:71], v[20:23]\n"
"v_exp_f32_e32 v26, v26\n"
"v_mfma_f32_16x16x32_bf16 v[20:23], a[152:155], a[72:75], v[20:23]\n"
"ds_read_b64_tr_b16 a[178:179], v4 offset:17408\n"
"v_mul_f32_e32 v28, s44, v28\n"
"v_mfma_f32_16x16x32_bf16 v[20:23], a[156:159], a[76:79], v[20:23]\n"
"v_exp_f32_e32 v27, v27\n"
"v_mfma_f32_16x16x32_bf16 v[128:131], a[192:195], v[32:35], v[128:131]\n"
"v_mul_f32_e32 v29, s44, v29\n"
"v_cvt_pk_bf16_f32 v37, v26, v27\n"
"v_mfma_f32_16x16x32_bf16 v[132:135], a[196:199], v[32:35], v[132:135]\n"
"v_exp_f32_e32 v28, v28\n"
"v_mfma_f32_16x16x32_bf16 v[136:139], a[200:203], v[32:35], v[136:139]\n"
"ds_read_b64_tr_b16 a[180:181], v11 offset:17408\n"
"v_mul_f32_e32 v30, s44, v30\n"
"v_mfma_f32_16x16x32_bf16 v[140:143], a[204:207], v[32:35], v[140:143]\n"
"v_exp_f32_e32 v29, v29\n"
"v_mfma_f32_16x16x32_bf16 v[144:147], a[208:211], v[32:35], v[144:147]\n"
"s_add_u32 m0, s49, 0\n"
"v_mul_f32_e32 v31, s44, v31\n"
"v_cvt_pk_bf16_f32 v38, v28, v29\n"
"v_mfma_f32_16x16x32_bf16 v[148:151], a[212:215], v[32:35], v[148:151]\n"
"s_add_u32 s61, s58, 0\n"
"v_exp_f32_e32 v30, v30\n"
"v_mfma_f32_16x16x32_bf16 v[152:155], a[216:219], v[32:35], v[152:155]\n"
"buffer_load_dwordx4 v7, s[36:39], s61 offen  nt lds\n"
"v_mfma_f32_16x16x32_bf16 v[156:159], a[220:223], v[32:35], v[156:159]\n"
"v_exp_f32_e32 v31, v31\n"
"v_mfma_f32_16x16x32_bf16 a[236:239], a[224:227], v[32:35], a[236:239]\n"
"ds_read_b64_tr_b16 a[182:183], v14 offset:17408\n"
"v_cvt_pk_bf16_f32 v39, v30, v31\n"
"v_mfma_f32_16x16x32_bf16 v[24:27], a[128:131], a[80:83], 0\n"
"ds_read_b64_tr_b16 a[190:191], v14 offset:17920\n"
"v_mul_f32_e32 v16, s44, v16\n"
"v_mul_f32_e32 v17, s44, v17\n"
"v_mfma_f32_16x16x32_bf16 v[24:27], a[132:135], a[84:87], v[24:27]\n"
"v_exp_f32_e32 v16, v16\n"
"v_mfma_f32_16x16x32_bf16 v[24:27], a[136:139], a[88:91], v[24:27]\n"
"ds_read_b64_tr_b16 a[184:185], v3 offset:17920\n"
"v_mul_f32_e32 v18, s44, v18\n"
"v_mfma_f32_16x16x32_bf16 v[24:27], a[140:143], a[92:95], v[24:27]\n"
"v_exp_f32_e32 v17, v17\n"
"v_mfma_f32_16x16x32_bf16 v[28:31], a[144:147], a[80:83], 0\n"
"v_mul_f32_e32 v19, s44, v19\n"
"v_cvt_pk_bf16_f32 v32, v16, v17\n"
"v_mfma_f32_16x16x32_bf16 v[28:31], a[148:151], a[84:87], v[28:31]\n"
"v_exp_f32_e32 v18, v18\n"
"v_mfma_f32_16x16x32_bf16 v[28:31], a[152:155], a[88:91], v[28:31]\n"
"ds_read_b64_tr_b16 a[186:187], v4 offset:17920\n"
"v_mul_f32_e32 v20, s44, v20\n"
"v_mfma_f32_16x16x32_bf16 v[28:31], a[156:159], a[92:95], v[28:31]\n"
"v_exp_f32_e32 v19, v19\n"
"v_mfma_f32_16x16x32_bf16 v[160:163], a[192:195], v[36:39], v[160:163]\n"
"v_mul_f32_e32 v21, s44, v21\n"
"v_cvt_pk_bf16_f32 v33, v18, v19\n"
"v_mfma_f32_16x16x32_bf16 v[164:167], a[196:199], v[36:39], v[164:167]\n"
"v_exp_f32_e32 v20, v20\n"
"v_mfma_f32_16x16x32_bf16 v[168:171], a[200:203], v[36:39], v[168:171]\n"
"ds_read_b64_tr_b16 a[188:189], v11 offset:17920\n"
"v_mul_f32_e32 v22, s44, v22\n"
"v_mfma_f32_16x16x32_bf16 v[172:175], a[204:207], v[36:39], v[172:175]\n"
"v_exp_f32_e32 v21, v21\n"
"v_mfma_f32_16x16x32_bf16 v[176:179], a[208:211], v[36:39], v[176:179]\n"
"s_add_u32 m0, s49, 1024\n"
"v_mul_f32_e32 v23, s44, v23\n"
"v_cvt_pk_bf16_f32 v34, v20, v21\n"
"v_mfma_f32_16x16x32_bf16 v[180:183], a[212:215], v[36:39], v[180:183]\n"
"s_add_u32 s61, s58, 128\n"
"v_exp_f32_e32 v22, v22\n"
"v_mfma_f32_16x16x32_bf16 v[184:187], a[216:219], v[36:39], v[184:187]\n"
"buffer_load_dwordx4 v7, s[36:39], s61 offen  nt lds\n"
"v_mfma_f32_16x16x32_bf16 v[188:191], a[220:223], v[36:39], v[188:191]\n"
"v_exp_f32_e32 v23, v23\n"
"v_mfma_f32_16x16x32_bf16 a[240:243], a[224:227], v[36:39], a[240:243]\n"
"v_cvt_pk_bf16_f32 v35, v22, v23\n"
"v_mfma_f32_16x16x32_bf16 v[16:19], a[96:99], a[0:3], 0\n"
"v_mul_f32_e32 v24, s44, v24\n"
"v_mul_f32_e32 v25, s44, v25\n"
"v_mfma_f32_16x16x32_bf16 v[16:19], a[100:103], a[4:7], v[16:19]\n"
"s_waitcnt lgkmcnt(15)\n"
"v_exp_f32_e32 v24, v24\n"
"v_mfma_f32_16x16x32_bf16 v[16:19], a[104:107], a[8:11], v[16:19]\n"
"v_mul_f32_e32 v26, s44, v26\n"
"v_mfma_f32_16x16x32_bf16 v[16:19], a[108:111], a[12:15], v[16:19]\n"
"v_exp_f32_e32 v25, v25\n"
"v_mfma_f32_16x16x32_bf16 v[20:23], a[112:115], a[0:3], 0\n"
"v_mul_f32_e32 v27, s44, v27\n"
"v_cvt_pk_bf16_f32 v36, v24, v25\n"
"v_mfma_f32_16x16x32_bf16 v[20:23], a[116:119], a[4:7], v[20:23]\n"
"s_waitcnt vmcnt(14) lgkmcnt(0)\n"
"s_barrier\n"
"v_exp_f32_e32 v26, v26\n"
"v_mfma_f32_16x16x32_bf16 v[20:23], a[120:123], a[8:11], v[20:23]\n"
"ds_read_b128 a[128:131], v1 offset:24576\n"
"ds_read_b128 a[140:143], v1 offset:26112\n"
"v_mul_f32_e32 v28, s44, v28\n"
"v_mfma_f32_16x16x32_bf16 v[20:23], a[124:127], a[12:15], v[20:23]\n"
"v_exp_f32_e32 v27, v27\n"
"v_mfma_f32_16x16x32_bf16 v[192:195], a[192:195], v[32:35], v[192:195]\n"
"v_mul_f32_e32 v29, s44, v29\n"
"v_cvt_pk_bf16_f32 v37, v26, v27\n"
"v_mfma_f32_16x16x32_bf16 v[196:199], a[196:199], v[32:35], v[196:199]\n"
"v_exp_f32_e32 v28, v28\n"
"v_mfma_f32_16x16x32_bf16 v[200:203], a[200:203], v[32:35], v[200:203]\n"
"ds_read_b128 a[132:135], v1 offset:25088\n"
"v_mul_f32_e32 v30, s44, v30\n"
"v_mfma_f32_16x16x32_bf16 v[204:207], a[204:207], v[32:35], v[204:207]\n"
"v_exp_f32_e32 v29, v29\n"
"v_mfma_f32_16x16x32_bf16 v[208:211], a[208:211], v[32:35], v[208:211]\n"
"s_add_u32 m0, s48, 16384\n"
"v_mul_f32_e32 v31, s44, v31\n"
"v_cvt_pk_bf16_f32 v38, v28, v29\n"
"v_mfma_f32_16x16x32_bf16 v[212:215], a[212:215], v[32:35], v[212:215]\n"
"s_add_u32 s47, s45, 49152\n"
"v_exp_f32_e32 v30, v30\n"
"v_mfma_f32_16x16x32_bf16 v[216:219], a[216:219], v[32:35], v[216:219]\n"
"buffer_load_dwordx4 v6, s[24:27], s47 offen lds\n"
"v_mfma_f32_16x16x32_bf16 v[220:223], a[220:223], v[32:35], v[220:223]\n"
"v_exp_f32_e32 v31, v31\n"
"v_mfma_f32_16x16x32_bf16 a[244:247], a[224:227], v[32:35], a[244:247]\n"
"ds_read_b128 a[136:139], v1 offset:25600\n"
"v_cvt_pk_bf16_f32 v39, v30, v31\n"
"v_mfma_f32_16x16x32_bf16 v[24:27], a[96:99], a[16:19], 0\n"
"v_mul_f32_e32 v16, s44, v16\n"
"v_mul_f32_e32 v17, s44, v17\n"
"v_mfma_f32_16x16x32_bf16 v[24:27], a[100:103], a[20:23], v[24:27]\n"
"v_exp_f32_e32 v16, v16\n"
"v_mfma_f32_16x16x32_bf16 v[24:27], a[104:107], a[24:27], v[24:27]\n"
"ds_read_b128 a[144:147], v2 offset:24576\n"
"v_mul_f32_e32 v18, s44, v18\n"
"v_mfma_f32_16x16x32_bf16 v[24:27], a[108:111], a[28:31], v[24:27]\n"
"v_exp_f32_e32 v17, v17\n"
"v_mfma_f32_16x16x32_bf16 v[28:31], a[112:115], a[16:19], 0\n"
"v_mul_f32_e32 v19, s44, v19\n"
"v_cvt_pk_bf16_f32 v32, v16, v17\n"
"v_mfma_f32_16x16x32_bf16 v[28:31], a[116:119], a[20:23], v[28:31]\n"
"v_exp_f32_e32 v18, v18\n"
"v_mfma_f32_16x16x32_bf16 v[28:31], a[120:123], a[24:27], v[28:31]\n"
"ds_read_b128 a[148:151], v2 offset:25088\n"
"v_mul_f32_e32 v20, s44, v20\n"
"v_mfma_f32_16x16x32_bf16 v[28:31], a[124:127], a[28:31], v[28:31]\n"
"v_exp_f32_e32 v19, v19\n"
"v_mfma_f32_16x16x32_bf16 v[224:227], a[192:195], v[36:39], v[224:227]\n"
"v_mul_f32_e32 v21, s44, v21\n"
"v_cvt_pk_bf16_f32 v33, v18, v19\n"
"v_mfma_f32_16x16x32_bf16 v[228:231], a[196:199], v[36:39], v[228:231]\n"
"v_exp_f32_e32 v20, v20\n"
"v_mfma_f32_16x16x32_bf16 v[232:235], a[200:203], v[36:39], v[232:235]\n"
"ds_read_b128 a[152:155], v2 offset:25600\n"
"v_mul_f32_e32 v22, s44, v22\n"
"v_mfma_f32_16x16x32_bf16 v[236:239], a[204:207], v[36:39], v[236:239]\n"
"v_exp_f32_e32 v21, v21\n"
"v_mfma_f32_16x16x32_bf16 v[240:243], a[208:211], v[36:39], v[240:243]\n"
"s_add_u32 m0, s48, 17408\n"
"v_mul_f32_e32 v23, s44, v23\n"
"v_cvt_pk_bf16_f32 v34, v20, v21\n"
"v_mfma_f32_16x16x32_bf16 v[244:247], a[212:215], v[36:39], v[244:247]\n"
"s_add_u32 s47, s45, 49280\n"
"v_exp_f32_e32 v22, v22\n"
"v_mfma_f32_16x16x32_bf16 v[248:251], a[216:219], v[36:39], v[248:251]\n"
"buffer_load_dwordx4 v6, s[24:27], s47 offen lds\n"
"v_mfma_f32_16x16x32_bf16 v[252:255], a[220:223], v[36:39], v[252:255]\n"
"v_exp_f32_e32 v23, v23\n"
"v_mfma_f32_16x16x32_bf16 a[248:251], a[224:227], v[36:39], a[248:251]\n"
"ds_read_b128 a[156:159], v2 offset:26112\n"
"v_cvt_pk_bf16_f32 v35, v22, v23\n"
"v_mfma_f32_16x16x32_bf16 v[16:19], a[96:99], a[32:35], 0\n"
"v_mul_f32_e32 v24, s44, v24\n"
"v_mul_f32_e32 v25, s44, v25\n"
"v_mfma_f32_16x16x32_bf16 v[16:19], a[100:103], a[36:39], v[16:19]\n"
"v_exp_f32_e32 v24, v24\n"
"v_mfma_f32_16x16x32_bf16 v[16:19], a[104:107], a[40:43], v[16:19]\n"
"ds_read_b64_tr_b16 a[192:193], v3 offset:24576\n"
"v_mul_f32_e32 v26, s44, v26\n"
"v_mfma_f32_16x16x32_bf16 v[16:19], a[108:111], a[44:47], v[16:19]\n"
"v_exp_f32_e32 v25, v25\n"
"v_mfma_f32_16x16x32_bf16 v[20:23], a[112:115], a[32:35], 0\n"
"v_mul_f32_e32 v27, s44, v27\n"
"v_cvt_pk_bf16_f32 v36, v24, v25\n"
"v_mfma_f32_16x16x32_bf16 v[20:23], a[116:119], a[36:39], v[20:23]\n"
"v_exp_f32_e32 v26, v26\n"
"v_mfma_f32_16x16x32_bf16 v[20:23], a[120:123], a[40:43], v[20:23]\n"
"ds_read_b64_tr_b16 a[194:195], v4 offset:24576\n"
"v_mul_f32_e32 v28, s44, v28\n"
"v_mfma_f32_16x16x32_bf16 v[20:23], a[124:127], a[44:47], v[20:23]\n"
"v_exp_f32_e32 v27, v27\n"
"v_mfma_f32_16x16x32_bf16 v[64:67], a[160:163], v[32:35], v[64:67]\n"
"v_mul_f32_e32 v29, s44, v29\n"
"v_cvt_pk_bf16_f32 v37, v26, v27\n"
"v_mfma_f32_16x16x32_bf16 v[68:71], a[164:167], v[32:35], v[68:71]\n"
"v_exp_f32_e32 v28, v28\n"
"v_mfma_f32_16x16x32_bf16 v[72:75], a[168:171], v[32:35], v[72:75]\n"
"ds_read_b64_tr_b16 a[196:197], v11 offset:24576\n"
"v_mul_f32_e32 v30, s44, v30\n"
"v_mfma_f32_16x16x32_bf16 v[76:79], a[172:175], v[32:35], v[76:79]\n"
"v_exp_f32_e32 v29, v29\n"
"v_mfma_f32_16x16x32_bf16 v[80:83], a[176:179], v[32:35], v[80:83]\n"
"s_add_u32 m0, s48, 49152\n"
"v_mul_f32_e32 v31, s44, v31\n"
"v_cvt_pk_bf16_f32 v38, v28, v29\n"
"v_mfma_f32_16x16x32_bf16 v[84:87], a[180:183], v[32:35], v[84:87]\n"
"s_add_u32 s76, s45, 49152\n"
"v_exp_f32_e32 v30, v30\n"
"v_mfma_f32_16x16x32_bf16 v[88:91], a[184:187], v[32:35], v[88:91]\n"
"buffer_load_dwordx4 v6, s[28:31], s76 offen lds\n"
"v_mfma_f32_16x16x32_bf16 v[92:95], a[188:191], v[32:35], v[92:95]\n"
"v_exp_f32_e32 v31, v31\n"
"v_mfma_f32_16x16x32_bf16 a[228:231], a[224:227], v[32:35], a[228:231]\n"
"ds_read_b64_tr_b16 a[198:199], v14 offset:24576\n"
"v_cvt_pk_bf16_f32 v39, v30, v31\n"
"v_mfma_f32_16x16x32_bf16 v[24:27], a[96:99], a[48:51], 0\n"
"v_mul_f32_e32 v16, s44, v16\n"
"v_mul_f32_e32 v17, s44, v17\n"
"v_mfma_f32_16x16x32_bf16 v[24:27], a[100:103], a[52:55], v[24:27]\n"
"v_exp_f32_e32 v16, v16\n"
"v_mfma_f32_16x16x32_bf16 v[24:27], a[104:107], a[56:59], v[24:27]\n"
"ds_read_b64_tr_b16 a[200:201], v3 offset:25088\n"
"v_mul_f32_e32 v18, s44, v18\n"
"v_mfma_f32_16x16x32_bf16 v[24:27], a[108:111], a[60:63], v[24:27]\n"
"v_exp_f32_e32 v17, v17\n"
"v_mfma_f32_16x16x32_bf16 v[28:31], a[112:115], a[48:51], 0\n"
"v_mul_f32_e32 v19, s44, v19\n"
"v_cvt_pk_bf16_f32 v32, v16, v17\n"
"v_mfma_f32_16x16x32_bf16 v[28:31], a[116:119], a[52:55], v[28:31]\n"
"v_exp_f32_e32 v18, v18\n"
"v_mfma_f32_16x16x32_bf16 v[28:31], a[120:123], a[56:59], v[28:31]\n"
"ds_read_b64_tr_b16 a[202:203], v4 offset:25088\n"
"v_mul_f32_e32 v20, s44, v20\n"
"v_mfma_f32_16x16x32_bf16 v[28:31], a[124:127], a[60:63], v[28:31]\n"
"v_exp_f32_e32 v19, v19\n"
"v_mfma_f32_16x16x32_bf16 v[96:99], a[160:163], v[36:39], v[96:99]\n"
"v_mul_f32_e32 v21, s44, v21\n"
"v_cvt_pk_bf16_f32 v33, v18, v19\n"
"v_mfma_f32_16x16x32_bf16 v[100:103], a[164:167], v[36:39], v[100:103]\n"
"v_exp_f32_e32 v20, v20\n"
"v_mfma_f32_16x16x32_bf16 v[104:107], a[168:171], v[36:39], v[104:107]\n"
"ds_read_b64_tr_b16 a[204:205], v11 offset:25088\n"
"v_mul_f32_e32 v22, s44, v22\n"
"v_mfma_f32_16x16x32_bf16 v[108:111], a[172:175], v[36:39], v[108:111]\n"
"v_exp_f32_e32 v21, v21\n"
"v_mfma_f32_16x16x32_bf16 v[112:115], a[176:179], v[36:39], v[112:115]\n"
"s_add_u32 m0, s48, 50176\n"
"v_mul_f32_e32 v23, s44, v23\n"
"v_cvt_pk_bf16_f32 v34, v20, v21\n"
"v_mfma_f32_16x16x32_bf16 v[116:119], a[180:183], v[36:39], v[116:119]\n"
"s_add_u32 s76, s45, 49280\n"
"v_exp_f32_e32 v22, v22\n"
"v_mfma_f32_16x16x32_bf16 v[120:123], a[184:187], v[36:39], v[120:123]\n"
"buffer_load_dwordx4 v6, s[28:31], s76 offen lds\n"
"v_mfma_f32_16x16x32_bf16 v[124:127], a[188:191], v[36:39], v[124:127]\n"
"v_exp_f32_e32 v23, v23\n"
"v_mfma_f32_16x16x32_bf16 a[232:235], a[224:227], v[36:39], a[232:235]\n"
"ds_read_b64_tr_b16 a[206:207], v14 offset:25088\n"
"v_cvt_pk_bf16_f32 v35, v22, v23\n"
"v_mfma_f32_16x16x32_bf16 v[16:19], a[96:99], a[64:67], 0\n"
"v_mul_f32_e32 v24, s44, v24\n"
"v_mul_f32_e32 v25, s44, v25\n"
"v_mfma_f32_16x16x32_bf16 v[16:19], a[100:103], a[68:71], v[16:19]\n"
"v_exp_f32_e32 v24, v24\n"
"v_mfma_f32_16x16x32_bf16 v[16:19], a[104:107], a[72:75], v[16:19]\n"
"ds_read_b64_tr_b16 a[208:209], v3 offset:25600\n"
"v_mul_f32_e32 v26, s44, v26\n"
"v_mfma_f32_16x16x32_bf16 v[16:19], a[108:111], a[76:79], v[16:19]\n"
"v_exp_f32_e32 v25, v25\n"
"v_mfma_f32_16x16x32_bf16 v[20:23], a[112:115], a[64:67], 0\n"
"v_mul_f32_e32 v27, s44, v27\n"
"v_cvt_pk_bf16_f32 v36, v24, v25\n"
"v_mfma_f32_16x16x32_bf16 v[20:23], a[116:119], a[68:71], v[20:23]\n"
"v_exp_f32_e32 v26, v26\n"
"v_mfma_f32_16x16x32_bf16 v[20:23], a[120:123], a[72:75], v[20:23]\n"
"ds_read_b64_tr_b16 a[210:211], v4 offset:25600\n"
"v_mul_f32_e32 v28, s44, v28\n"
"v_mfma_f32_16x16x32_bf16 v[20:23], a[124:127], a[76:79], v[20:23]\n"
"v_exp_f32_e32 v27, v27\n"
"v_mfma_f32_16x16x32_bf16 v[128:131], a[160:163], v[32:35], v[128:131]\n"
"v_mul_f32_e32 v29, s44, v29\n"
"v_cvt_pk_bf16_f32 v37, v26, v27\n"
"v_mfma_f32_16x16x32_bf16 v[132:135], a[164:167], v[32:35], v[132:135]\n"
"v_exp_f32_e32 v28, v28\n"
"v_mfma_f32_16x16x32_bf16 v[136:139], a[168:171], v[32:35], v[136:139]\n"
"ds_read_b64_tr_b16 a[212:213], v11 offset:25600\n"
"v_mul_f32_e32 v30, s44, v30\n"
"v_mfma_f32_16x16x32_bf16 v[140:143], a[172:175], v[32:35], v[140:143]\n"
"v_exp_f32_e32 v29, v29\n"
"v_mfma_f32_16x16x32_bf16 v[144:147], a[176:179], v[32:35], v[144:147]\n"
"s_add_u32 m0, s49, 2048\n"
"v_mul_f32_e32 v31, s44, v31\n"
"v_cvt_pk_bf16_f32 v38, v28, v29\n"
"v_mfma_f32_16x16x32_bf16 v[148:151], a[180:183], v[32:35], v[148:151]\n"
"s_add_u32 s61, s58, 0\n"
"v_exp_f32_e32 v30, v30\n"
"v_mfma_f32_16x16x32_bf16 v[152:155], a[184:187], v[32:35], v[152:155]\n"
"buffer_load_dwordx4 v8, s[36:39], s61 offen  nt lds\n"
"v_mfma_f32_16x16x32_bf16 v[156:159], a[188:191], v[32:35], v[156:159]\n"
"v_exp_f32_e32 v31, v31\n"
"v_mfma_f32_16x16x32_bf16 a[236:239], a[224:227], v[32:35], a[236:239]\n"
"ds_read_b64_tr_b16 a[214:215], v14 offset:25600\n"
"v_cvt_pk_bf16_f32 v39, v30, v31\n"
"v_mfma_f32_16x16x32_bf16 v[24:27], a[96:99], a[80:83], 0\n"
"ds_read_b64_tr_b16 a[222:223], v14 offset:26112\n"
"v_mul_f32_e32 v16, s44, v16\n"
"v_mul_f32_e32 v17, s44, v17\n"
"v_mfma_f32_16x16x32_bf16 v[24:27], a[100:103], a[84:87], v[24:27]\n"
"v_exp_f32_e32 v16, v16\n"
"v_mfma_f32_16x16x32_bf16 v[24:27], a[104:107], a[88:91], v[24:27]\n"
"ds_read_b64_tr_b16 a[216:217], v3 offset:26112\n"
"v_mul_f32_e32 v18, s44, v18\n"
"v_mfma_f32_16x16x32_bf16 v[24:27], a[108:111], a[92:95], v[24:27]\n"
"v_exp_f32_e32 v17, v17\n"
"v_mfma_f32_16x16x32_bf16 v[28:31], a[112:115], a[80:83], 0\n"
"v_mul_f32_e32 v19, s44, v19\n"
"v_cvt_pk_bf16_f32 v32, v16, v17\n"
"v_mfma_f32_16x16x32_bf16 v[28:31], a[116:119], a[84:87], v[28:31]\n"
"v_exp_f32_e32 v18, v18\n"
"v_mfma_f32_16x16x32_bf16 v[28:31], a[120:123], a[88:91], v[28:31]\n"
"ds_read_b64_tr_b16 a[218:219], v4 offset:26112\n"
"v_mul_f32_e32 v20, s44, v20\n"
"v_mfma_f32_16x16x32_bf16 v[28:31], a[124:127], a[92:95], v[28:31]\n"
"v_exp_f32_e32 v19, v19\n"
"v_mfma_f32_16x16x32_bf16 v[160:163], a[160:163], v[36:39], v[160:163]\n"
"v_mul_f32_e32 v21, s44, v21\n"
"v_cvt_pk_bf16_f32 v33, v18, v19\n"
"v_mfma_f32_16x16x32_bf16 v[164:167], a[164:167], v[36:39], v[164:167]\n"
"v_exp_f32_e32 v20, v20\n"
"v_mfma_f32_16x16x32_bf16 v[168:171], a[168:171], v[36:39], v[168:171]\n"
"ds_read_b64_tr_b16 a[220:221], v11 offset:26112\n"
"v_mul_f32_e32 v22, s44, v22\n"
"v_mfma_f32_16x16x32_bf16 v[172:175], a[172:175], v[36:39], v[172:175]\n"
"v_exp_f32_e32 v21, v21\n"
"v_mfma_f32_16x16x32_bf16 v[176:179], a[176:179], v[36:39], v[176:179]\n"
"s_add_u32 m0, s49, 3072\n"
"v_mul_f32_e32 v23, s44, v23\n"
"v_cvt_pk_bf16_f32 v34, v20, v21\n"
"v_mfma_f32_16x16x32_bf16 v[180:183], a[180:183], v[36:39], v[180:183]\n"
"s_add_u32 s61, s58, 128\n"
"v_exp_f32_e32 v22, v22\n"
"v_mfma_f32_16x16x32_bf16 v[184:187], a[184:187], v[36:39], v[184:187]\n"
"buffer_load_dwordx4 v8, s[36:39], s61 offen  nt lds\n"
"v_mfma_f32_16x16x32_bf16 v[188:191], a[188:191], v[36:39], v[188:191]\n"
"v_exp_f32_e32 v23, v23\n"
"v_mfma_f32_16x16x32_bf16 a[240:243], a[224:227], v[36:39], a[240:243]\n"
"v_cvt_pk_bf16_f32 v35, v22, v23\n"
"s_waitcnt lgkmcnt(15)\n"
"s_branch L_L6%=\n"
"L_T65%=:\n"
"v_mfma_f32_16x16x32_bf16 v[16:19], a[96:99], a[0:3], 0\n"
"v_mul_f32_e32 v24, s44, v24\n"
"v_mul_f32_e32 v25, s44, v25\n"
"v_mfma_f32_16x16x32_bf16 v[16:19], a[100:103], a[4:7], v[16:19]\n"
"v_exp_f32_e32 v24, v24\n"
"v_mfma_f32_16x16x32_bf16 v[16:19], a[104:107], a[8:11], v[16:19]\n"
"v_mul_f32_e32 v26, s44, v26\n"
"v_mfma_f32_16x16x32_bf16 v[16:19], a[108:111], a[12:15], v[16:19]\n"
"v_exp_f32_e32 v25, v25\n"
"v_mfma_f32_16x16x32_bf16 v[20:23], a[112:115], a[0:3], 0\n"
"v_mul_f32_e32 v27, s44, v27\n"
"v_cvt_pk_bf16_f32 v36, v24, v25\n"
"v_accvgpr_read_b32 v10, a240\n"
"v_subrev_u32_e32 v13, s51, v10\n"
"v_rcp_f32_e32 v12, v10\n"
"v_cmp_le_u32_e32 vcc, s52, v13\n"
"v_pk_mul_f32 v[160:161], v[160:161], v[12:13] op_sel_hi:[1,0]\n"
"v_pk_mul_f32 v[162:163], v[162:163], v[12:13] op_sel_hi:[1,0]\n"
"v_pk_mul_f32 v[164:165], v[164:165], v[12:13] op_sel_hi:[1,0]\n"
"v_pk_mul_f32 v[166:167], v[166:167], v[12:13] op_sel_hi:[1,0]\n"
"v_pk_mul_f32 v[168:169], v[168:169], v[12:13] op_sel_hi:[1,0]\n"
"v_pk_mul_f32 v[170:171], v[170:171], v[12:13] op_sel_hi:[1,0]\n"
"v_pk_mul_f32 v[172:173], v[172:173], v[12:13] op_sel_hi:[1,0]\n"
"v_pk_mul_f32 v[174:175], v[174:175], v[12:13] op_sel_hi:[1,0]\n"
"v_pk_mul_f32 v[176:177], v[176:177], v[12:13] op_sel_hi:[1,0]\n"
"v_pk_mul_f32 v[178:179], v[178:179], v[12:13] op_sel_hi:[1,0]\n"
"v_pk_mul_f32 v[180:181], v[180:181], v[12:13] op_sel_hi:[1,0]\n"
"v_pk_mul_f32 v[182:183], v[182:183], v[12:13] op_sel_hi:[1,0]\n"
"v_pk_mul_f32 v[184:185], v[184:185], v[12:13] op_sel_hi:[1,0]\n"
"v_pk_mul_f32 v[186:187], v[186:187], v[12:13] op_sel_hi:[1,0]\n"
"v_pk_mul_f32 v[188:189], v[188:189], v[12:13] op_sel_hi:[1,0]\n"
"v_pk_mul_f32 v[190:191], v[190:191], v[12:13] op_sel_hi:[1,0]\n"
"s_or_b64 s[54:55], s[54:55], vcc\n"
"v_mfma_f32_16x16x32_bf16 v[20:23], a[116:119], a[4:7], v[20:23]\n"
"s_waitcnt vmcnt(9) lgkmcnt(6)\n"
"s_barrier\n"
"v_exp_f32_e32 v26, v26\n"
"v_cvt_pk_bf16_f32 v40, v160, v161\n"
"v_cvt_pk_bf16_f32 v41, v162, v163\n"
"v_cvt_pk_bf16_f32 v42, v164, v165\n"
"v_cvt_pk_bf16_f32 v43, v166, v167\n"
"ds_write_b64 v56, v[40:41] offset:12288\n"
"ds_write_b64 v57, v[42:43] offset:12288\n"
"v_mfma_f32_16x16x32_bf16 v[20:23], a[120:123], a[8:11], v[20:23]\n"
"ds_read_b128 a[128:131], v1 offset:8192\n"
"ds_read_b128 a[140:143], v1 offset:9728\n"
"v_mul_f32_e32 v28, s44, v28\n"
"v_mfma_f32_16x16x32_bf16 v[20:23], a[124:127], a[12:15], v[20:23]\n"
"v_exp_f32_e32 v27, v27\n"
"v_cvt_pk_bf16_f32 v44, v168, v169\n"
"v_cvt_pk_bf16_f32 v45, v170, v171\n"
"v_cvt_pk_bf16_f32 v46, v172, v173\n"
"v_cvt_pk_bf16_f32 v47, v174, v175\n"
"ds_write_b64 v15, v[44:45] offset:12288\n"
"ds_write_b64 v60, v[46:47] offset:12288\n"
"v_mfma_f32_16x16x32_bf16 v[192:195], a[192:195], v[32:35], v[192:195]\n"
"v_mul_f32_e32 v29, s44, v29\n"
"v_cvt_pk_bf16_f32 v37, v26, v27\n"
"v_mfma_f32_16x16x32_bf16 v[196:199], a[196:199], v[32:35], v[196:199]\n"
"v_exp_f32_e32 v28, v28\n"
"v_cvt_pk_bf16_f32 v48, v176, v177\n"
"v_cvt_pk_bf16_f32 v49, v178, v179\n"
"v_cvt_pk_bf16_f32 v50, v180, v181\n"
"v_cvt_pk_bf16_f32 v51, v182, v183\n"
"ds_write_b64 v56, v[48:49] offset:12416\n"
"ds_write_b64 v57, v[50:51] offset:12416\n"
"v_mfma_f32_16x16x32_bf16 v[200:203], a[200:203], v[32:35], v[200:203]\n"
"ds_read_b128 a[132:135], v1 offset:8704\n"
"v_mul_f32_e32 v30, s44, v30\n"
"v_mfma_f32_16x16x32_bf16 v[204:207], a[204:207], v[32:35], v[204:207]\n"
"v_exp_f32_e32 v29, v29\n"
"v_cvt_pk_bf16_f32 v52, v184, v185\n"
"v_cvt_pk_bf16_f32 v53, v186, v187\n"
"v_cvt_pk_bf16_f32 v54, v188, v189\n"
"v_cvt_pk_bf16_f32 v55, v190, v191\n"
"ds_write_b64 v15, v[52:53] offset:12416\n"
"ds_write_b64 v60, v[54:55] offset:12416\n"
"v_mfma_f32_16x16x32_bf16 v[208:211], a[208:211], v[32:35], v[208:211]\n"
"s_add_u32 m0, s48, 0\n"
"v_mul_f32_e32 v31, s44, v31\n"
"v_cvt_pk_bf16_f32 v38, v28, v29\n"
"v_mfma_f32_16x16x32_bf16 v[212:215], a[212:215], v[32:35], v[212:215]\n"
"s_add_u32 s47, s45, 32768\n"
"v_exp_f32_e32 v30, v30\n"
"v_mfma_f32_16x16x32_bf16 v[216:219], a[216:219], v[32:35], v[216:219]\n"
"buffer_load_dwordx4 v6, s[24:27], s47 offen lds\n"
"v_mfma_f32_16x16x32_bf16 v[220:223], a[220:223], v[32:35], v[220:223]\n"
"v_exp_f32_e32 v31, v31\n"
"v_mfma_f32_16x16x32_bf16 a[244:247], a[224:227], v[32:35], a[244:247]\n"
"ds_read_b128 a[136:139], v1 offset:9216\n"
"v_cvt_pk_bf16_f32 v39, v30, v31\n"
"v_mfma_f32_16x16x32_bf16 v[24:27], a[96:99], a[16:19], 0\n"
"v_mul_f32_e32 v16, s44, v16\n"
"v_mul_f32_e32 v17, s44, v17\n"
"v_mfma_f32_16x16x32_bf16 v[24:27], a[100:103], a[20:23], v[24:27]\n"
"v_exp_f32_e32 v16, v16\n"
"v_mfma_f32_16x16x32_bf16 v[24:27], a[104:107], a[24:27], v[24:27]\n"
"ds_read_b128 a[144:147], v2 offset:8192\n"
"v_mul_f32_e32 v18, s44, v18\n"
"v_mfma_f32_16x16x32_bf16 v[24:27], a[108:111], a[28:31], v[24:27]\n"
"v_exp_f32_e32 v17, v17\n"
"v_mfma_f32_16x16x32_bf16 v[28:31], a[112:115], a[16:19], 0\n"
"v_mul_f32_e32 v19, s44, v19\n"
"v_cvt_pk_bf16_f32 v32, v16, v17\n"
"v_accvgpr_read_b32 v10, a244\n"
"v_subrev_u32_e32 v13, s51, v10\n"
"v_rcp_f32_e32 v12, v10\n"
"v_cmp_le_u32_e32 vcc, s52, v13\n"
"v_pk_mul_f32 v[192:193], v[192:193], v[12:13] op_sel_hi:[1,0]\n"
"v_pk_mul_f32 v[194:195], v[194:195], v[12:13] op_sel_hi:[1,0]\n"
"v_pk_mul_f32 v[196:197], v[196:197], v[12:13] op_sel_hi:[1,0]\n"
"v_pk_mul_f32 v[198:199], v[198:199], v[12:13] op_sel_hi:[1,0]\n"
"v_pk_mul_f32 v[200:201], v[200:201], v[12:13] op_sel_hi:[1,0]\n"
"v_pk_mul_f32 v[202:203], v[202:203], v[12:13] op_sel_hi:[1,0]\n"
"v_pk_mul_f32 v[204:205], v[204:205], v[12:13] op_sel_hi:[1,0]\n"
"v_pk_mul_f32 v[206:207], v[206:207], v[12:13] op_sel_hi:[1,0]\n"
"v_pk_mul_f32 v[208:209], v[208:209], v[12:13] op_sel_hi:[1,0]\n"
"v_pk_mul_f32 v[210:211], v[210:211], v[12:13] op_sel_hi:[1,0]\n"
"v_pk_mul_f32 v[212:213], v[212:213], v[12:13] op_sel_hi:[1,0]\n"
"v_pk_mul_f32 v[214:215], v[214:215], v[12:13] op_sel_hi:[1,0]\n"
"v_pk_mul_f32 v[216:217], v[216:217], v[12:13] op_sel_hi:[1,0]\n"
"v_pk_mul_f32 v[218:219], v[218:219], v[12:13] op_sel_hi:[1,0]\n"
"v_pk_mul_f32 v[220:221], v[220:221], v[12:13] op_sel_hi:[1,0]\n"
"v_pk_mul_f32 v[222:223], v[222:223], v[12:13] op_sel_hi:[1,0]\n"
"s_or_b64 s[54:55], s[54:55], vcc\n"
"v_mfma_f32_16x16x32_bf16 v[28:31], a[116:119], a[20:23], v[28:31]\n"
"v_exp_f32_e32 v18, v18\n"
"v_cvt_pk_bf16_f32 v40, v192, v193\n"
"v_cvt_pk_bf16_f32 v41, v194, v195\n"
"v_cvt_pk_bf16_f32 v42, v196, v197\n"
"v_cvt_pk_bf16_f32 v43, v198, v199\n"
"ds_write_b64 v56, v[40:41] offset:16384\n"
"ds_write_b64 v57, v[42:43] offset:16384\n"
"v_mfma_f32_16x16x32_bf16 v[28:31], a[120:123], a[24:27], v[28:31]\n"
"ds_read_b128 a[148:151], v2 offset:8704\n"
"v_mul_f32_e32 v20, s44, v20\n"
"v_mfma_f32_16x16x32_bf16 v[28:31], a[124:127], a[28:31], v[28:31]\n"
"v_exp_f32_e32 v19, v19\n"
"v_cvt_pk_bf16_f32 v44, v200, v201\n"
"v_cvt_pk_bf16_f32 v45, v202, v203\n"
"v_cvt_pk_bf16_f32 v46, v204, v205\n"
"v_cvt_pk_bf16_f32 v47, v206, v207\n"
"ds_write_b64 v15, v[44:45] offset:16384\n"
"ds_write_b64 v60, v[46:47] offset:16384\n"
"v_mfma_f32_16x16x32_bf16 v[224:227], a[192:195], v[36:39], v[224:227]\n"
"v_mul_f32_e32 v21, s44, v21\n"
"v_cvt_pk_bf16_f32 v33, v18, v19\n"
"v_mfma_f32_16x16x32_bf16 v[228:231], a[196:199], v[36:39], v[228:231]\n"
"v_exp_f32_e32 v20, v20\n"
"v_cvt_pk_bf16_f32 v48, v208, v209\n"
"v_cvt_pk_bf16_f32 v49, v210, v211\n"
"v_cvt_pk_bf16_f32 v50, v212, v213\n"
"v_cvt_pk_bf16_f32 v51, v214, v215\n"
"ds_write_b64 v56, v[48:49] offset:16512\n"
"ds_write_b64 v57, v[50:51] offset:16512\n"
"v_mfma_f32_16x16x32_bf16 v[232:235], a[200:203], v[36:39], v[232:235]\n"
"ds_read_b128 a[152:155], v2 offset:9216\n"
"v_mul_f32_e32 v22, s44, v22\n"
"v_mfma_f32_16x16x32_bf16 v[236:239], a[204:207], v[36:39], v[236:239]\n"
"v_exp_f32_e32 v21, v21\n"
"v_cvt_pk_bf16_f32 v52, v216, v217\n"
"v_cvt_pk_bf16_f32 v53, v218, v219\n"
"v_cvt_pk_bf16_f32 v54, v220, v221\n"
"v_cvt_pk_bf16_f32 v55, v222, v223\n"
"ds_write_b64 v15, v[52:53] offset:16512\n"
"ds_write_b64 v60, v[54:55] offset:16512\n"
"v_mfma_f32_16x16x32_bf16 v[240:243], a[208:211], v[36:39], v[240:243]\n"
"s_add_u32 m0, s48, 1024\n"
"v_mul_f32_e32 v23, s44, v23\n"
"v_cvt_pk_bf16_f32 v34, v20, v21\n"
"v_mfma_f32_16x16x32_bf16 v[244:247], a[212:215], v[36:39], v[244:247]\n"
"s_add_u32 s47, s45, 32896\n"
"v_exp_f32_e32 v22, v22\n"
"v_mfma_f32_16x16x32_bf16 v[248:251], a[216:219], v[36:39], v[248:251]\n"
"buffer_load_dwordx4 v6, s[24:27], s47 offen lds\n"
"v_mfma_f32_16x16x32_bf16 v[252:255], a[220:223], v[36:39], v[252:255]\n"
"v_exp_f32_e32 v23, v23\n"
"v_mfma_f32_16x16x32_bf16 a[248:251], a[224:227], v[36:39], a[248:251]\n"
"ds_read_b128 a[156:159], v2 offset:9728\n"
"v_cvt_pk_bf16_f32 v35, v22, v23\n"
"v_mfma_f32_16x16x32_bf16 v[16:19], a[96:99], a[32:35], 0\n"
"ds_read_b64_tr_b16 a[200:201], v3 offset:8704\n"
"v_mul_f32_e32 v24, s44, v24\n"
"v_mul_f32_e32 v25, s44, v25\n"
"v_mfma_f32_16x16x32_bf16 v[16:19], a[100:103], a[36:39], v[16:19]\n"
"ds_read_b64_tr_b16 a[202:203], v4 offset:8704\n"
"v_exp_f32_e32 v24, v24\n"
"v_mfma_f32_16x16x32_bf16 v[16:19], a[104:107], a[40:43], v[16:19]\n"
"ds_read_b64_tr_b16 a[192:193], v3 offset:8192\n"
"v_mul_f32_e32 v26, s44, v26\n"
"v_mfma_f32_16x16x32_bf16 v[16:19], a[108:111], a[44:47], v[16:19]\n"
"v_exp_f32_e32 v25, v25\n"
"v_mfma_f32_16x16x32_bf16 v[20:23], a[112:115], a[32:35], 0\n"
"v_mul_f32_e32 v27, s44, v27\n"
"v_cvt_pk_bf16_f32 v36, v24, v25\n"
"v_accvgpr_read_b32 v10, a248\n"
"v_subrev_u32_e32 v13, s51, v10\n"
"v_rcp_f32_e32 v12, v10\n"
"v_cmp_le_u32_e32 vcc, s52, v13\n"
"v_pk_mul_f32 v[224:225], v[224:225], v[12:13] op_sel_hi:[1,0]\n"
"v_pk_mul_f32 v[226:227], v[226:227], v[12:13] op_sel_hi:[1,0]\n"
"v_pk_mul_f32 v[228:229], v[228:229], v[12:13] op_sel_hi:[1,0]\n"
"v_pk_mul_f32 v[230:231], v[230:231], v[12:13] op_sel_hi:[1,0]\n"
"v_pk_mul_f32 v[232:233], v[232:233], v[12:13] op_sel_hi:[1,0]\n"
"v_pk_mul_f32 v[234:235], v[234:235], v[12:13] op_sel_hi:[1,0]\n"
"v_pk_mul_f32 v[236:237], v[236:237], v[12:13] op_sel_hi:[1,0]\n"
"v_pk_mul_f32 v[238:239], v[238:239], v[12:13] op_sel_hi:[1,0]\n"
"v_pk_mul_f32 v[240:241], v[240:241], v[12:13] op_sel_hi:[1,0]\n"
"v_pk_mul_f32 v[242:243], v[242:243], v[12:13] op_sel_hi:[1,0]\n"
"v_pk_mul_f32 v[244:245], v[244:245], v[12:13] op_sel_hi:[1,0]\n"
"v_pk_mul_f32 v[246:247], v[246:247], v[12:13] op_sel_hi:[1,0]\n"
"v_pk_mul_f32 v[248:249], v[248:249], v[12:13] op_sel_hi:[1,0]\n"
"v_pk_mul_f32 v[250:251], v[250:251], v[12:13] op_sel_hi:[1,0]\n"
"v_pk_mul_f32 v[252:253], v[252:253], v[12:13] op_sel_hi:[1,0]\n"
"v_pk_mul_f32 v[254:255], v[254:255], v[12:13] op_sel_hi:[1,0]\n"
"s_or_b64 s[54:55], s[54:55], vcc\n"
"v_mfma_f32_16x16x32_bf16 v[20:23], a[116:119], a[36:39], v[20:23]\n"
"v_exp_f32_e32 v26, v26\n"
"v_cvt_pk_bf16_f32 v40, v224, v225\n"
"v_cvt_pk_bf16_f32 v41, v226, v227\n"
"v_cvt_pk_bf16_f32 v42, v228, v229\n"
"v_cvt_pk_bf16_f32 v43, v230, v231\n"
"s_waitcnt lgkmcnt(15)\n"
"ds_write_b64 v56, v[40:41] offset:20480\n"
"ds_write_b64 v57, v[42:43] offset:20480\n"
"v_mfma_f32_16x16x32_bf16 v[20:23], a[120:123], a[40:43], v[20:23]\n"
"ds_read_b64_tr_b16 a[194:195], v4 offset:8192\n"
"v_mul_f32_e32 v28, s44, v28\n"
"v_mfma_f32_16x16x32_bf16 v[20:23], a[124:127], a[44:47], v[20:23]\n"
"v_exp_f32_e32 v27, v27\n"
"v_cvt_pk_bf16_f32 v44, v232, v233\n"
"v_cvt_pk_bf16_f32 v45, v234, v235\n"
"v_cvt_pk_bf16_f32 v46, v236, v237\n"
"v_cvt_pk_bf16_f32 v47, v238, v239\n"
"ds_write_b64 v15, v[44:45] offset:20480\n"
"ds_write_b64 v60, v[46:47] offset:20480\n"
"v_mfma_f32_16x16x32_bf16 v[64:67], a[160:163], v[32:35], 0\n"
"v_mul_f32_e32 v29, s44, v29\n"
"v_cvt_pk_bf16_f32 v37, v26, v27\n"
"v_mfma_f32_16x16x32_bf16 v[68:71], a[164:167], v[32:35], 0\n"
"v_exp_f32_e32 v28, v28\n"
"v_cvt_pk_bf16_f32 v48, v240, v241\n"
"v_cvt_pk_bf16_f32 v49, v242, v243\n"
"v_cvt_pk_bf16_f32 v50, v244, v245\n"
"v_cvt_pk_bf16_f32 v51, v246, v247\n"
"ds_write_b64 v56, v[48:49] offset:20608\n"
"ds_write_b64 v57, v[50:51] offset:20608\n"
"v_mfma_f32_16x16x32_bf16 v[72:75], a[168:171], v[32:35], 0\n"
"ds_read_b64_tr_b16 a[196:197], v11 offset:8192\n"
"v_mul_f32_e32 v30, s44, v30\n"
"v_mfma_f32_16x16x32_bf16 v[76:79], a[172:175], v[32:35], 0\n"
"v_exp_f32_e32 v29, v29\n"
"v_cvt_pk_bf16_f32 v52, v248, v249\n"
"v_cvt_pk_bf16_f32 v53, v250, v251\n"
"v_cvt_pk_bf16_f32 v54, v252, v253\n"
"v_cvt_pk_bf16_f32 v55, v254, v255\n"
"ds_write_b64 v15, v[52:53] offset:20608\n"
"ds_write_b64 v60, v[54:55] offset:20608\n"
"v_mfma_f32_16x16x32_bf16 v[80:83], a[176:179], v[32:35], 0\n"
"s_add_u32 m0, s48, 32768\n"
"v_mul_f32_e32 v31, s44, v31\n"
"v_cvt_pk_bf16_f32 v38, v28, v29\n"
"v_mfma_f32_16x16x32_bf16 v[84:87], a[180:183], v[32:35], 0\n"
"s_add_u32 s76, s45, 32768\n"
"v_exp_f32_e32 v30, v30\n"
"v_mfma_f32_16x16x32_bf16 v[88:91], a[184:187], v[32:35], 0\n"
"buffer_load_dwordx4 v6, s[28:31], s76 offen lds\n"
"v_mfma_f32_16x16x32_bf16 v[92:95], a[188:191], v[32:35], 0\n"
"v_exp_f32_e32 v31, v31\n"
"v_mfma_f32_16x16x32_bf16 a[228:231], a[224:227], v[32:35], 0\n"
"ds_read_b64_tr_b16 a[198:199], v14 offset:8192\n"
"v_cvt_pk_bf16_f32 v39, v30, v31\n"
"s_cmp_lg_u64 s[54:55], 0\n"
"s_cselect_b32 s57, s56, 0\n"
"s_or_b32 s53, s53, s57\n"
"s_lshl_b32 s56, s56, 1\n"
"s_mov_b64 s[54:55], 0\n"
"v_mfma_f32_16x16x32_bf16 v[24:27], a[96:99], a[48:51], 0\n"
"ds_read_b64_tr_b16 a[212:213], v11 offset:9216\n"
"v_mul_f32_e32 v16, s44, v16\n"
"v_mul_f32_e32 v17, s44, v17\n"
"v_mfma_f32_16x16x32_bf16 v[24:27], a[100:103], a[52:55], v[24:27]\n"
"ds_read_b64_tr_b16 a[214:215], v14 offset:9216\n"
"v_exp_f32_e32 v16, v16\n"
"v_mfma_f32_16x16x32_bf16 v[24:27], a[104:107], a[56:59], v[24:27]\n"
"ds_read_b64_tr_b16 a[204:205], v11 offset:8704\n"
"v_mul_f32_e32 v18, s44, v18\n"
"v_mfma_f32_16x16x32_bf16 v[24:27], a[108:111], a[60:63], v[24:27]\n"
"v_exp_f32_e32 v17, v17\n"
"v_mfma_f32_16x16x32_bf16 v[28:31], a[112:115], a[48:51], 0\n"
"v_mul_f32_e32 v19, s44, v19\n"
"v_cvt_pk_bf16_f32 v32, v16, v17\n"
"v_mfma_f32_16x16x32_bf16 v[28:31], a[116:119], a[52:55], v[28:31]\n"
"v_exp_f32_e32 v18, v18\n"
"v_mfma_f32_16x16x32_bf16 v[28:31], a[120:123], a[56:59], v[28:31]\n"
"ds_read_b64_tr_b16 a[206:207], v14 offset:8704\n"
"v_mul_f32_e32 v20, s44, v20\n"
"v_mfma_f32_16x16x32_bf16 v[28:31], a[124:127], a[60:63], v[28:31]\n"
"v_exp_f32_e32 v19, v19\n"
"v_mfma_f32_16x16x32_bf16 v[96:99], a[160:163], v[36:39], 0\n"
"v_mul_f32_e32 v21, s44, v21\n"
"v_cvt_pk_bf16_f32 v33, v18, v19\n"
"v_mfma_f32_16x16x32_bf16 v[100:103], a[164:167], v[36:39], 0\n"
"v_exp_f32_e32 v20, v20\n"
"v_mfma_f32_16x16x32_bf16 v[104:107], a[168:171], v[36:39], 0\n"
"ds_read_b64_tr_b16 a[208:209], v3 offset:9216\n"
"v_mul_f32_e32 v22, s44, v22\n"
"v_mfma_f32_16x16x32_bf16 v[108:111], a[172:175], v[36:39], 0\n"
"v_exp_f32_e32 v21, v21\n"
"v_mfma_f32_16x16x32_bf16 v[112:115], a[176:179], v[36:39], 0\n"
"s_add_u32 m0, s48, 33792\n"
"v_mul_f32_e32 v23, s44, v23\n"
"v_cvt_pk_bf16_f32 v34, v20, v21\n"
"v_mfma_f32_16x16x32_bf16 v[116:119], a[180:183], v[36:39], 0\n"
"s_add_u32 s76, s45, 32896\n"
"v_exp_f32_e32 v22, v22\n"
"v_mfma_f32_16x16x32_bf16 v[120:123], a[184:187], v[36:39], 0\n"
"buffer_load_dwordx4 v6, s[28:31], s76 offen lds\n"
"v_mfma_f32_16x16x32_bf16 v[124:127], a[188:191], v[36:39], 0\n"
"v_exp_f32_e32 v23, v23\n"
"v_mfma_f32_16x16x32_bf16 a[232:235], a[224:227], v[36:39], 0\n"
"ds_read_b64_tr_b16 a[210:211], v4 offset:9216\n"
"v_cvt_pk_bf16_f32 v35, v22, v23\n"
"v_mfma_f32_16x16x32_bf16 v[16:19], a[96:99], a[64:67], 0\n"
"ds_read_b64_tr_b16 a[222:223], v14 offset:9728\n"
"v_mul_f32_e32 v24, s44, v24\n"
"v_mul_f32_e32 v25, s44, v25\n"
"v_mfma_f32_16x16x32_bf16 v[16:19], a[100:103], a[68:71], v[16:19]\n"
"v_exp_f32_e32 v24, v24\n"
"v_mfma_f32_16x16x32_bf16 v[16:19], a[104:107], a[72:75], v[16:19]\n"
"ds_read_b64_tr_b16 a[216:217], v3 offset:9728\n"
"v_mul_f32_e32 v26, s44, v26\n"
"v_mfma_f32_16x16x32_bf16 v[16:19], a[108:111], a[76:79], v[16:19]\n"
"v_exp_f32_e32 v25, v25\n"
"v_mfma_f32_16x16x32_bf16 v[20:23], a[112:115], a[64:67], 0\n"
"v_mul_f32_e32 v27, s44, v27\n"
"v_cvt_pk_bf16_f32 v36, v24, v25\n"
"v_mfma_f32_16x16x32_bf16 v[20:23], a[116:119], a[68:71], v[20:23]\n"
"v_exp_f32_e32 v26, v26\n"
"v_mfma_f32_16x16x32_bf16 v[20:23], a[120:123], a[72:75], v[20:23]\n"
"ds_read_b64_tr_b16 a[218:219], v4 offset:9728\n"
"v_mul_f32_e32 v28, s44, v28\n"
"v_mfma_f32_16x16x32_bf16 v[20:23], a[124:127], a[76:79], v[20:23]\n"
"v_exp_f32_e32 v27, v27\n"
"v_mfma_f32_16x16x32_bf16 v[128:131], a[160:163], v[32:35], 0\n"
"v_mul_f32_e32 v29, s44, v29\n"
"v_cvt_pk_bf16_f32 v37, v26, v27\n"
"v_mfma_f32_16x16x32_bf16 v[132:135], a[164:167], v[32:35], 0\n"
"v_exp_f32_e32 v28, v28\n"
"v_mfma_f32_16x16x32_bf16 v[136:139], a[168:171], v[32:35], 0\n"
"ds_read_b64_tr_b16 a[220:221], v11 offset:9728\n"
"v_mul_f32_e32 v30, s44, v30\n"
"v_mfma_f32_16x16x32_bf16 v[140:143], a[172:175], v[32:35], 0\n"
"v_exp_f32_e32 v29, v29\n"
"v_mfma_f32_16x16x32_bf16 v[144:147], a[176:179], v[32:35], 0\n"
"v_mul_f32_e32 v31, s44, v31\n"
"v_cvt_pk_bf16_f32 v38, v28, v29\n"
"v_mfma_f32_16x16x32_bf16 v[148:151], a[180:183], v[32:35], 0\n"
"v_exp_f32_e32 v30, v30\n"
"v_mfma_f32_16x16x32_bf16 v[152:155], a[184:187], v[32:35], 0\n"
"v_mfma_f32_16x16x32_bf16 v[156:159], a[188:191], v[32:35], 0\n"
"v_exp_f32_e32 v31, v31\n"
"v_mfma_f32_16x16x32_bf16 a[236:239], a[224:227], v[32:35], 0\n"
"v_cvt_pk_bf16_f32 v39, v30, v31\n"
"v_mfma_f32_16x16x32_bf16 v[24:27], a[128:131], a[0:3], 0\n"
"v_mul_f32_e32 v16, s44, v16\n"
"v_mul_f32_e32 v17, s44, v17\n"
"v_mfma_f32_16x16x32_bf16 v[24:27], a[132:135], a[4:7], v[24:27]\n"
"v_exp_f32_e32 v16, v16\n"
"v_mfma_f32_16x16x32_bf16 v[24:27], a[136:139], a[8:11], v[24:27]\n"
"v_mul_f32_e32 v18, s44, v18\n"
"v_mfma_f32_16x16x32_bf16 v[24:27], a[140:143], a[12:15], v[24:27]\n"
"s_waitcnt lgkmcnt(15)\n"
"v_exp_f32_e32 v17, v17\n"
"v_mfma_f32_16x16x32_bf16 v[28:31], a[144:147], a[0:3], 0\n"
"v_mul_f32_e32 v19, s44, v19\n"
"v_cvt_pk_bf16_f32 v32, v16, v17\n"
"v_mfma_f32_16x16x32_bf16 v[28:31], a[148:151], a[4:7], v[28:31]\n"
"s_waitcnt vmcnt(9) lgkmcnt(0)\n"
"s_barrier\n"
"v_exp_f32_e32 v18, v18\n"
"v_mfma_f32_16x16x32_bf16 v[28:31], a[152:155], a[8:11], v[28:31]\n"
"ds_read_b128 a[96:99], v1 offset:16384\n"
"ds_read_b128 a[108:111], v1 offset:17920\n"
"v_mul_f32_e32 v20, s44, v20\n"
"v_mfma_f32_16x16x32_bf16 v[28:31], a[156:159], a[12:15], v[28:31]\n"
"ds_read_b128 v[40:43], v58 offset:0\n"
"v_exp_f32_e32 v19, v19\n"
"v_mfma_f32_16x16x32_bf16 v[160:163], a[160:163], v[36:39], 0\n"
"ds_read_b128 v[44:47], v59 offset:1024\n"
"v_mul_f32_e32 v21, s44, v21\n"
"v_cvt_pk_bf16_f32 v33, v18, v19\n"
"v_mfma_f32_16x16x32_bf16 v[164:167], a[164:167], v[36:39], 0\n"
"ds_read_b128 v[48:51], v58 offset:2048\n"
"v_exp_f32_e32 v20, v20\n"
"v_mfma_f32_16x16x32_bf16 v[168:171], a[168:171], v[36:39], 0\n"
"ds_read_b128 a[100:103], v1 offset:16896\n"
"ds_read_b128 v[52:55], v59 offset:3072\n"
"v_mul_f32_e32 v22, s44, v22\n"
"v_mfma_f32_16x16x32_bf16 v[172:175], a[172:175], v[36:39], 0\n"
"v_exp_f32_e32 v21, v21\n"
"v_mfma_f32_16x16x32_bf16 v[176:179], a[176:179], v[36:39], 0\n"
"s_add_u32 m0, s48, 8192\n"
"v_mul_f32_e32 v23, s44, v23\n"
"v_cvt_pk_bf16_f32 v34, v20, v21\n"
"v_mfma_f32_16x16x32_bf16 v[180:183], a[180:183], v[36:39], 0\n"
"s_add_u32 s47, s45, 40960\n"
"v_exp_f32_e32 v22, v22\n"
"v_mfma_f32_16x16x32_bf16 v[184:187], a[184:187], v[36:39], 0\n"
"buffer_load_dwordx4 v6, s[24:27], s47 offen lds\n"
"v_mfma_f32_16x16x32_bf16 v[188:191], a[188:191], v[36:39], 0\n"
"v_exp_f32_e32 v23, v23\n"
"v_mfma_f32_16x16x32_bf16 a[240:243], a[224:227], v[36:39], 0\n"
"ds_read_b128 a[104:107], v1 offset:17408\n"
"v_cvt_pk_bf16_f32 v35, v22, v23\n"
"v_mfma_f32_16x16x32_bf16 v[16:19], a[128:131], a[16:19], 0\n"
"ds_read_b128 a[124:127], v2 offset:17920\n"
"v_mul_f32_e32 v24, s44, v24\n"
"v_mul_f32_e32 v25, s44, v25\n"
"v_mfma_f32_16x16x32_bf16 v[16:19], a[132:135], a[20:23], v[16:19]\n"
"v_exp_f32_e32 v24, v24\n"
"s_add_u32 s77, s60, 0\n"
"v_mfma_f32_16x16x32_bf16 v[16:19], a[136:139], a[24:27], v[16:19]\n"
"ds_read_b128 a[112:115], v2 offset:16384\n"
"v_mul_f32_e32 v26, s44, v26\n"
"s_waitcnt lgkmcnt(7)\n"
"buffer_store_dwordx4 v[40:43], v9, s[40:43], s77 offen offset:0  sc1\n"
"v_mfma_f32_16x16x32_bf16 v[16:19], a[140:143], a[28:31], v[16:19]\n"
"v_exp_f32_e32 v25, v25\n"
"v_mfma_f32_16x16x32_bf16 v[20:23], a[144:147], a[16:19], 0\n"
"v_mul_f32_e32 v27, s44, v27\n"
"v_cvt_pk_bf16_f32 v36, v24, v25\n"
"v_mfma_f32_16x16x32_bf16 v[20:23], a[148:151], a[20:23], v[20:23]\n"
"v_exp_f32_e32 v26, v26\n"
"v_mfma_f32_16x16x32_bf16 v[20:23], a[152:155], a[24:27], v[20:23]\n"
"ds_read_b128 a[116:119], v2 offset:16896\n"
"v_mul_f32_e32 v28, s44, v28\n"
"s_waitcnt lgkmcnt(7)\n"
"buffer_store_dwordx4 v[44:47], v9, s[40:43], s77 offen offset:1024  sc1\n"
"v_mfma_f32_16x16x32_bf16 v[20:23], a[156:159], a[28:31], v[20:23]\n"
"v_exp_f32_e32 v27, v27\n"
"v_mfma_f32_16x16x32_bf16 v[192:195], a[160:163], v[32:35], 0\n"
"v_mul_f32_e32 v29, s44, v29\n"
"v_cvt_pk_bf16_f32 v37, v26, v27\n"
"v_mfma_f32_16x16x32_bf16 v[196:199], a[164:167], v[32:35], 0\n"
"v_exp_f32_e32 v28, v28\n"
"v_mfma_f32_16x16x32_bf16 v[200:203], a[168:171], v[32:35], 0\n"
"ds_read_b128 a[120:123], v2 offset:17408\n"
"v_mul_f32_e32 v30, s44, v30\n"
"s_waitcnt lgkmcnt(7)\n"
"buffer_store_dwordx4 v[48:51], v9, s[40:43], s77 offen offset:2048  sc1\n"
"v_mfma_f32_16x16x32_bf16 v[204:207], a[172:175], v[32:35], 0\n"
"v_exp_f32_e32 v29, v29\n"
"v_mfma_f32_16x16x32_bf16 v[208:211], a[176:179], v[32:35], 0\n"
"v_mul_f32_e32 v31, s44, v31\n"
"v_cvt_pk_bf16_f32 v38, v28, v29\n"
"v_mfma_f32_16x16x32_bf16 v[212:215], a[180:183], v[32:35], 0\n"
"s_mov_b32 s88, L_L5%=+8192-L_code%=\n"
"v_exp_f32_e32 v30, v30\n"
"v_mfma_f32_16x16x32_bf16 v[216:219], a[184:187], v[32:35], 0\n"
"s_add_u32 m0, s48, 9216\n"
"buffer_load_dword v63, v62, s[84:87], s88 offen\n"
"v_mfma_f32_16x16x32_bf16 v[220:223], a[188:191], v[32:35], 0\n"
"s_add_u32 s47, s45, 41088\n"
"v_exp_f32_e32 v31, v31\n"
"v_mfma_f32_16x16x32_bf16 a[244:247], a[224:227], v[32:35], 0\n"
"buffer_load_dwordx4 v6, s[24:27], s47 offen lds\n"
"v_cvt_pk_bf16_f32 v39, v30, v31\n"
"s_waitcnt lgkmcnt(5)\n"
"buffer_store_dwordx4 v[52:55], v9, s[40:43], s77 offen offset:3072  sc1\n"
"v_mfma_f32_16x16x32_bf16 v[24:27], a[128:131], a[32:35], 0\n"
"ds_read_b64_tr_b16 a[166:167], v14 offset:16384\n"
"v_mul_f32_e32 v16, s44, v16\n"
"v_mul_f32_e32 v17, s44, v17\n"
"v_mfma_f32_16x16x32_bf16 v[24:27], a[132:135], a[36:39], v[24:27]\n"
"ds_read_b64_tr_b16 a[168:169], v3 offset:16896\n"
"v_exp_f32_e32 v16, v16\n"
"v_mfma_f32_16x16x32_bf16 v[24:27], a[136:139], a[40:43], v[24:27]\n"
"ds_read_b64_tr_b16 a[160:161], v3 offset:16384\n"
"ds_read_b64_tr_b16 a[170:171], v4 offset:16896\n"
"v_mul_f32_e32 v18, s44, v18\n"
"v_mfma_f32_16x16x32_bf16 v[24:27], a[140:143], a[44:47], v[24:27]\n"
"v_exp_f32_e32 v17, v17\n"
"v_mfma_f32_16x16x32_bf16 v[28:31], a[144:147], a[32:35], 0\n"
"v_mul_f32_e32 v19, s44, v19\n"
"v_cvt_pk_bf16_f32 v32, v16, v17\n"
"v_mfma_f32_16x16x32_bf16 v[28:31], a[148:151], a[36:39], v[28:31]\n"
"v_exp_f32_e32 v18, v18\n"
"v_mfma_f32_16x16x32_bf16 v[28:31], a[152:155], a[40:43], v[28:31]\n"
"ds_read_b64_tr_b16 a[162:163], v4 offset:16384\n"
"v_mul_f32_e32 v20, s44, v20\n"
"v_mfma_f32_16x16x32_bf16 v[28:31], a[156:159], a[44:47], v[28:31]\n"
"v_exp_f32_e32 v19, v19\n"
"v_mfma_f32_16x16x32_bf16 v[64:67], a[192:195], v[36:39], v[64:67]\n"
"s_add_u32 m0, s49, 0\n"
"v_mul_f32_e32 v21, s44, v21\n"
"v_cvt_pk_bf16_f32 v33, v18, v19\n"
"v_mfma_f32_16x16x32_bf16 v[68:71], a[196:199], v[36:39], v[68:71]\n"
"s_add_u32 s61, s58, 0\n"
"v_exp_f32_e32 v20, v20\n"
"v_mfma_f32_16x16x32_bf16 v[72:75], a[200:203], v[36:39], v[72:75]\n"
"buffer_load_dwordx4 v7, s[36:39], s61 offen  nt lds\n"
"v_mul_f32_e32 v22, s44, v22\n"
"v_mfma_f32_16x16x32_bf16 v[76:79], a[204:207], v[36:39], v[76:79]\n"
"v_exp_f32_e32 v21, v21\n"
"v_mfma_f32_16x16x32_bf16 v[80:83], a[208:211], v[36:39], v[80:83]\n"
"s_add_u32 m0, s48, 40960\n"
"v_mul_f32_e32 v23, s44, v23\n"
"v_cvt_pk_bf16_f32 v34, v20, v21\n"
"v_mfma_f32_16x16x32_bf16 v[84:87], a[212:215], v[36:39], v[84:87]\n"
"s_add_u32 s76, s45, 40960\n"
"v_exp_f32_e32 v22, v22\n"
"v_mfma_f32_16x16x32_bf16 v[88:91], a[216:219], v[36:39], v[88:91]\n"
"buffer_load_dwordx4 v6, s[28:31], s76 offen lds\n"
"v_mfma_f32_16x16x32_bf16 v[92:95], a[220:223], v[36:39], v[92:95]\n"
"v_exp_f32_e32 v23, v23\n"
"v_mfma_f32_16x16x32_bf16 a[228:231], a[224:227], v[36:39], a[228:231]\n"
"ds_read_b64_tr_b16 a[164:165], v11 offset:16384\n"
"v_cvt_pk_bf16_f32 v35, v22, v23\n"
"v_mfma_f32_16x16x32_bf16 v[16:19], a[128:131], a[48:51], 0\n"
"ds_read_b64_tr_b16 a[180:181], v11 offset:17408\n"
"v_mul_f32_e32 v24, s44, v24\n"
"v_mul_f32_e32 v25, s44, v25\n"
"v_mfma_f32_16x16x32_bf16 v[16:19], a[132:135], a[52:55], v[16:19]\n"
"ds_read_b64_tr_b16 a[182:183], v14 offset:17408\n"
"v_exp_f32_e32 v24, v24\n"
"v_mfma_f32_16x16x32_bf16 v[16:19], a[136:139], a[56:59], v[16:19]\n"
"ds_read_b64_tr_b16 a[172:173], v11 offset:16896\n"
"v_mul_f32_e32 v26, s44, v26\n"
"v_mfma_f32_16x16x32_bf16 v[16:19], a[140:143], a[60:63], v[16:19]\n"
"v_exp_f32_e32 v25, v25\n"
"v_mfma_f32_16x16x32_bf16 v[20:23], a[144:147], a[48:51], 0\n"
"v_mul_f32_e32 v27, s44, v27\n"
"v_cvt_pk_bf16_f32 v36, v24, v25\n"
"v_mfma_f32_16x16x32_bf16 v[20:23], a[148:151], a[52:55], v[20:23]\n"
"v_exp_f32_e32 v26, v26\n"
"v_mfma_f32_16x16x32_bf16 v[20:23], a[152:155], a[56:59], v[20:23]\n"
"ds_read_b64_tr_b16 a[174:175], v14 offset:16896\n"
"v_mul_f32_e32 v28, s44, v28\n"
"v_mfma_f32_16x16x32_bf16 v[20:23], a[156:159], a[60:63], v[20:23]\n"
"v_exp_f32_e32 v27, v27\n"
"v_mfma_f32_16x16x32_bf16 v[96:99], a[192:195], v[32:35], v[96:99]\n"
"v_mul_f32_e32 v29, s44, v29\n"
"v_cvt_pk_bf16_f32 v37, v26, v27\n"
"v_mfma_f32_16x16x32_bf16 v[100:103], a[196:199], v[32:35], v[100:103]\n"
"v_exp_f32_e32 v28, v28\n"
"v_mfma_f32_16x16x32_bf16 v[104:107], a[200:203], v[32:35], v[104:107]\n"
"ds_read_b64_tr_b16 a[176:177], v3 offset:17408\n"
"v_mul_f32_e32 v30, s44, v30\n"
"v_mfma_f32_16x16x32_bf16 v[108:111], a[204:207], v[32:35], v[108:111]\n"
"v_exp_f32_e32 v29, v29\n"
"v_mfma_f32_16x16x32_bf16 v[112:115], a[208:211], v[32:35], v[112:115]\n"
"s_add_u32 m0, s48, 41984\n"
"v_mul_f32_e32 v31, s44, v31\n"
"v_cvt_pk_bf16_f32 v38, v28, v29\n"
"v_mfma_f32_16x16x32_bf16 v[116:119], a[212:215], v[32:35], v[116:119]\n"
"s_add_u32 s76, s45, 41088\n"
"v_exp_f32_e32 v30, v30\n"
"v_mfma_f32_16x16x32_bf16 v[120:123], a[216:219], v[32:35], v[120:123]\n"
"buffer_load_dwordx4 v6, s[28:31], s76 offen lds\n"
"v_mfma_f32_16x16x32_bf16 v[124:127], a[220:223], v[32:35], v[124:127]\n"
"v_exp_f32_e32 v31, v31\n"
"v_mfma_f32_16x16x32_bf16 a[232:235], a[224:227], v[32:35], a[232:235]\n"
"ds_read_b64_tr_b16 a[178:179], v4 offset:17408\n"
"v_cvt_pk_bf16_f32 v39, v30, v31\n"
"v_mfma_f32_16x16x32_bf16 v[24:27], a[128:131], a[64:67], 0\n"
"ds_read_b64_tr_b16 a[190:191], v14 offset:17920\n"
"v_mul_f32_e32 v16, s44, v16\n"
"v_mul_f32_e32 v17, s44, v17\n"
"v_mfma_f32_16x16x32_bf16 v[24:27], a[132:135], a[68:71], v[24:27]\n"
"v_exp_f32_e32 v16, v16\n"
"v_mfma_f32_16x16x32_bf16 v[24:27], a[136:139], a[72:75], v[24:27]\n"
"ds_read_b64_tr_b16 a[184:185], v3 offset:17920\n"
"v_mul_f32_e32 v18, s44, v18\n"
"v_mfma_f32_16x16x32_bf16 v[24:27], a[140:143], a[76:79], v[24:27]\n"
"v_exp_f32_e32 v17, v17\n"
"v_mfma_f32_16x16x32_bf16 v[28:31], a[144:147], a[64:67], 0\n"
"v_mul_f32_e32 v19, s44, v19\n"
"v_cvt_pk_bf16_f32 v32, v16, v17\n"
"v_mfma_f32_16x16x32_bf16 v[28:31], a[148:151], a[68:71], v[28:31]\n"
"v_exp_f32_e32 v18, v18\n"
"v_mfma_f32_16x16x32_bf16 v[28:31], a[152:155], a[72:75], v[28:31]\n"
"ds_read_b64_tr_b16 a[186:187], v4 offset:17920\n"
"v_mul_f32_e32 v20, s44, v20\n"
"v_mfma_f32_16x16x32_bf16 v[28:31], a[156:159], a[76:79], v[28:31]\n"
"v_exp_f32_e32 v19, v19\n"
"v_mfma_f32_16x16x32_bf16 v[128:131], a[192:195], v[36:39], v[128:131]\n"
"v_mul_f32_e32 v21, s44, v21\n"
"v_cvt_pk_bf16_f32 v33, v18, v19\n"
"v_mfma_f32_16x16x32_bf16 v[132:135], a[196:199], v[36:39], v[132:135]\n"
"v_exp_f32_e32 v20, v20\n"
"v_mfma_f32_16x16x32_bf16 v[136:139], a[200:203], v[36:39], v[136:139]\n"
"ds_read_b64_tr_b16 a[188:189], v11 offset:17920\n"
"v_mul_f32_e32 v22, s44, v22\n"
"v_mfma_f32_16x16x32_bf16 v[140:143], a[204:207], v[36:39], v[140:143]\n"
"v_exp_f32_e32 v21, v21\n"
"v_mfma_f32_16x16x32_bf16 v[144:147], a[208:211], v[36:39], v[144:147]\n"
"s_add_u32 m0, s49, 1024\n"
"v_mul_f32_e32 v23, s44, v23\n"
"v_cvt_pk_bf16_f32 v34, v20, v21\n"
"v_mfma_f32_16x16x32_bf16 v[148:151], a[212:215], v[36:39], v[148:151]\n"
"s_add_u32 s61, s58, 128\n"
"v_exp_f32_e32 v22, v22\n"
"v_mfma_f32_16x16x32_bf16 v[152:155], a[216:219], v[36:39], v[152:155]\n"
"buffer_load_dwordx4 v7, s[36:39], s61 offen  nt lds\n"
"v_mfma_f32_16x16x32_bf16 v[156:159], a[220:223], v[36:39], v[156:159]\n"
"v_exp_f32_e32 v23, v23\n"
"v_mfma_f32_16x16x32_bf16 a[236:239], a[224:227], v[36:39], a[236:239]\n"
"v_cvt_pk_bf16_f32 v35, v22, v23\n"
"v_mfma_f32_16x16x32_bf16 v[16:19], a[96:99], a[0:3], 0\n"
"v_mul_f32_e32 v24, s44, v24\n"
"v_mul_f32_e32 v25, s44, v25\n"
"v_mfma_f32_16x16x32_bf16 v[16:19], a[100:103], a[4:7], v[16:19]\n"
"s_waitcnt lgkmcnt(15)\n"
"v_exp_f32_e32 v24, v24\n"
"v_mfma_f32_16x16x32_bf16 v[16:19], a[104:107], a[8:11], v[16:19]\n"
"v_mul_f32_e32 v26, s44, v26\n"
"v_mfma_f32_16x16x32_bf16 v[16:19], a[108:111], a[12:15], v[16:19]\n"
"v_exp_f32_e32 v25, v25\n"
"v_mfma_f32_16x16x32_bf16 v[20:23], a[112:115], a[0:3], 0\n"
"v_mul_f32_e32 v27, s44, v27\n"
"v_cvt_pk_bf16_f32 v36, v24, v25\n"
"v_mfma_f32_16x16x32_bf16 v[20:23], a[116:119], a[4:7], v[20:23]\n"
"s_waitcnt vmcnt(15) lgkmcnt(0)\n"
"s_barrier\n"
"v_exp_f32_e32 v26, v26\n"
"v_mfma_f32_16x16x32_bf16 v[20:23], a[120:123], a[8:11], v[20:23]\n"
"ds_read_b128 a[128:131], v1 offset:24576\n"
"ds_read_b128 a[140:143], v1 offset:26112\n"
"v_mul_f32_e32 v28, s44, v28\n"
"v_mfma_f32_16x16x32_bf16 v[20:23], a[124:127], a[12:15], v[20:23]\n"
"v_exp_f32_e32 v27, v27\n"
"v_mfma_f32_16x16x32_bf16 v[160:163], a[192:195], v[32:35], v[160:163]\n"
"v_mul_f32_e32 v29, s44, v29\n"
"v_cvt_pk_bf16_f32 v37, v26, v27\n"
"v_mfma_f32_16x16x32_bf16 v[164:167], a[196:199], v[32:35], v[164:167]\n"
"v_exp_f32_e32 v28, v28\n"
"v_mfma_f32_16x16x32_bf16 v[168:171], a[200:203], v[32:35], v[168:171]\n"
"ds_read_b128 a[132:135], v1 offset:25088\n"
"v_mul_f32_e32 v30, s44, v30\n"
"v_mfma_f32_16x16x32_bf16 v[172:175], a[204:207], v[32:35], v[172:175]\n"
"v_exp_f32_e32 v29, v29\n"
"v_mfma_f32_16x16x32_bf16 v[176:179], a[208:211], v[32:35], v[176:179]\n"
"s_add_u32 m0, s48, 16384\n"
"v_mul_f32_e32 v31, s44, v31\n"
"v_cvt_pk_bf16_f32 v38, v28, v29\n"
"v_mfma_f32_16x16x32_bf16 v[180:183], a[212:215], v[32:35], v[180:183]\n"
"s_add_u32 s47, s45, 49152\n"
"v_exp_f32_e32 v30, v30\n"
"v_mfma_f32_16x16x32_bf16 v[184:187], a[216:219], v[32:35], v[184:187]\n"
"buffer_load_dwordx4 v6, s[24:27], s47 offen lds\n"
"v_mfma_f32_16x16x32_bf16 v[188:191], a[220:223], v[32:35], v[188:191]\n"
"v_exp_f32_e32 v31, v31\n"
"v_mfma_f32_16x16x32_bf16 a[240:243], a[224:227], v[32:35], a[240:243]\n"
"ds_read_b128 a[136:139], v1 offset:25600\n"
"v_cvt_pk_bf16_f32 v39, v30, v31\n"
"v_mfma_f32_16x16x32_bf16 v[24:27], a[96:99], a[16:19], 0\n"
"v_mul_f32_e32 v16, s44, v16\n"
"v_mul_f32_e32 v17, s44, v17\n"
"v_mfma_f32_16x16x32_bf16 v[24:27], a[100:103], a[20:23], v[24:27]\n"
"v_exp_f32_e32 v16, v16\n"
"v_mfma_f32_16x16x32_bf16 v[24:27], a[104:107], a[24:27], v[24:27]\n"
"ds_read_b128 a[144:147], v2 offset:24576\n"
"v_mul_f32_e32 v18, s44, v18\n"
"v_mfma_f32_16x16x32_bf16 v[24:27], a[108:111], a[28:31], v[24:27]\n"
"v_exp_f32_e32 v17, v17\n"
"v_mfma_f32_16x16x32_bf16 v[28:31], a[112:115], a[16:19], 0\n"
"v_mul_f32_e32 v19, s44, v19\n"
"v_cvt_pk_bf16_f32 v32, v16, v17\n"
"v_mfma_f32_16x16x32_bf16 v[28:31], a[116:119], a[20:23], v[28:31]\n"
"v_exp_f32_e32 v18, v18\n"
"v_mfma_f32_16x16x32_bf16 v[28:31], a[120:123], a[24:27], v[28:31]\n"
"ds_read_b128 a[148:151], v2 offset:25088\n"
"v_mul_f32_e32 v20, s44, v20\n"
"v_mfma_f32_16x16x32_bf16 v[28:31], a[124:127], a[28:31], v[28:31]\n"
"v_exp_f32_e32 v19, v19\n"
"v_mfma_f32_16x16x32_bf16 v[192:195], a[192:195], v[36:39], v[192:195]\n"
"v_mul_f32_e32 v21, s44, v21\n"
"v_cvt_pk_bf16_f32 v33, v18, v19\n"
"v_mfma_f32_16x16x32_bf16 v[196:199], a[196:199], v[36:39], v[196:199]\n"
"v_exp_f32_e32 v20, v20\n"
"v_mfma_f32_16x16x32_bf16 v[200:203], a[200:203], v[36:39], v[200:203]\n"
"ds_read_b128 a[152:155], v2 offset:25600\n"
"v_mul_f32_e32 v22, s44, v22\n"
"v_mfma_f32_16x16x32_bf16 v[204:207], a[204:207], v[36:39], v[204:207]\n"
"v_exp_f32_e32 v21, v21\n"
"v_mfma_f32_16x16x32_bf16 v[208:211], a[208:211], v[36:39], v[208:211]\n"
"s_add_u32 m0, s48, 17408\n"
"v_mul_f32_e32 v23, s44, v23\n"
"v_cvt_pk_bf16_f32 v34, v20, v21\n"
"v_mfma_f32_16x16x32_bf16 v[212:215], a[212:215], v[36:39], v[212:215]\n"
"s_add_u32 s47, s45, 49280\n"
"v_exp_f32_e32 v22, v22\n"
"v_mfma_f32_16x16x32_bf16 v[216:219], a[216:219], v[36:39], v[216:219]\n"
"buffer_load_dwordx4 v6, s[24:27], s47 offen lds\n"
"v_mfma_f32_16x16x32_bf16 v[220:223], a[220:223], v[36:39], v[220:223]\n"
"v_exp_f32_e32 v23, v23\n"
"v_mfma_f32_16x16x32_bf16 a[244:247], a[224:227], v[36:39], a[244:247]\n"
"ds_read_b128 a[156:159], v2 offset:26112\n"
"v_cvt_pk_bf16_f32 v35, v22, v23\n"
"v_mfma_f32_16x16x32_bf16 v[16:19], a[96:99], a[32:35], 0\n"
"ds_read_b64_tr_b16 a[198:199], v14 offset:24576\n"
"v_mul_f32_e32 v24, s44, v24\n"
"v_mul_f32_e32 v25, s44, v25\n"
"v_mfma_f32_16x16x32_bf16 v[16:19], a[100:103], a[36:39], v[16:19]\n"
"ds_read_b64_tr_b16 a[200:201], v3 offset:25088\n"
"v_exp_f32_e32 v24, v24\n"
"v_mfma_f32_16x16x32_bf16 v[16:19], a[104:107], a[40:43], v[16:19]\n"
"ds_read_b64_tr_b16 a[192:193], v3 offset:24576\n"
"ds_read_b64_tr_b16 a[202:203], v4 offset:25088\n"
"v_mul_f32_e32 v26, s44, v26\n"
"v_mfma_f32_16x16x32_bf16 v[16:19], a[108:111], a[44:47], v[16:19]\n"
"v_exp_f32_e32 v25, v25\n"
"v_mfma_f32_16x16x32_bf16 v[20:23], a[112:115], a[32:35], 0\n"
"v_mul_f32_e32 v27, s44, v27\n"
"v_cvt_pk_bf16_f32 v36, v24, v25\n"
"v_mfma_f32_16x16x32_bf16 v[20:23], a[116:119], a[36:39], v[20:23]\n"
"v_exp_f32_e32 v26, v26\n"
"v_mfma_f32_16x16x32_bf16 v[20:23], a[120:123], a[40:43], v[20:23]\n"
"ds_read_b64_tr_b16 a[194:195], v4 offset:24576\n"
"v_mul_f32_e32 v28, s44, v28\n"
"v_mfma_f32_16x16x32_bf16 v[20:23], a[124:127], a[44:47], v[20:23]\n"
"v_exp_f32_e32 v27, v27\n"
"v_mfma_f32_16x16x32_bf16 v[64:67], a[160:163], v[32:35], v[64:67]\n"
"s_add_u32 m0, s49, 2048\n"
"v_mul_f32_e32 v29, s44, v29\n"
"v_cvt_pk_bf16_f32 v37, v26, v27\n"
"v_mfma_f32_16x16x32_bf16 v[68:71], a[164:167], v[32:35], v[68:71]\n"
"s_add_u32 s61, s58, 0\n"
"v_exp_f32_e32 v28, v28\n"
"v_mfma_f32_16x16x32_bf16 v[72:75], a[168:171], v[32:35], v[72:75]\n"
"buffer_load_dwordx4 v8, s[36:39], s61 offen  nt lds\n"
"v_mul_f32_e32 v30, s44, v30\n"
"v_mfma_f32_16x16x32_bf16 v[76:79], a[172:175], v[32:35], v[76:79]\n"
"v_exp_f32_e32 v29, v29\n"
"v_mfma_f32_16x16x32_bf16 v[80:83], a[176:179], v[32:35], v[80:83]\n"
"s_add_u32 m0, s48, 49152\n"
"v_mul_f32_e32 v31, s44, v31\n"
"v_cvt_pk_bf16_f32 v38, v28, v29\n"
"v_mfma_f32_16x16x32_bf16 v[84:87], a[180:183], v[32:35], v[84:87]\n"
"s_add_u32 s76, s45, 49152\n"
"v_exp_f32_e32 v30, v30\n"
"v_mfma_f32_16x16x32_bf16 v[88:91], a[184:187], v[32:35], v[88:91]\n"
"buffer_load_dwordx4 v6, s[28:31], s76 offen lds\n"
"v_mfma_f32_16x16x32_bf16 v[92:95], a[188:191], v[32:35], v[92:95]\n"
"v_exp_f32_e32 v31, v31\n"
"v_mfma_f32_16x16x32_bf16 a[228:231], a[224:227], v[32:35], a[228:231]\n"
"ds_read_b64_tr_b16 a[196:197], v11 offset:24576\n"
"v_cvt_pk_bf16_f32 v39, v30, v31\n"
"v_mfma_f32_16x16x32_bf16 v[24:27], a[96:99], a[48:51], 0\n"
"ds_read_b64_tr_b16 a[212:213], v11 offset:25600\n"
"v_mul_f32_e32 v16, s44, v16\n"
"v_mul_f32_e32 v17, s44, v17\n"
"v_mfma_f32_16x16x32_bf16 v[24:27], a[100:103], a[52:55], v[24:27]\n"
"ds_read_b64_tr_b16 a[214:215], v14 offset:25600\n"
"v_exp_f32_e32 v16, v16\n"
"v_mfma_f32_16x16x32_bf16 v[24:27], a[104:107], a[56:59], v[24:27]\n"
"ds_read_b64_tr_b16 a[204:205], v11 offset:25088\n"
"v_mul_f32_e32 v18, s44, v18\n"
"v_mfma_f32_16x16x32_bf16 v[24:27], a[108:111], a[60:63], v[24:27]\n"
"v_exp_f32_e32 v17, v17\n"
"v_mfma_f32_16x16x32_bf16 v[28:31], a[112:115], a[48:51], 0\n"
"v_mul_f32_e32 v19, s44, v19\n"
"v_cvt_pk_bf16_f32 v32, v16, v17\n"
"v_mfma_f32_16x16x32_bf16 v[28:31], a[116:119], a[52:55], v[28:31]\n"
"v_exp_f32_e32 v18, v18\n"
"v_mfma_f32_16x16x32_bf16 v[28:31], a[120:123], a[56:59], v[28:31]\n"
"ds_read_b64_tr_b16 a[206:207], v14 offset:25088\n"
"v_mul_f32_e32 v20, s44, v20\n"
"v_mfma_f32_16x16x32_bf16 v[28:31], a[124:127], a[60:63], v[28:31]\n"
"v_exp_f32_e32 v19, v19\n"
"v_mfma_f32_16x16x32_bf16 v[96:99], a[160:163], v[36:39], v[96:99]\n"
"v_mul_f32_e32 v21, s44, v21\n"
"v_cvt_pk_bf16_f32 v33, v18, v19\n"
"v_mfma_f32_16x16x32_bf16 v[100:103], a[164:167], v[36:39], v[100:103]\n"
"v_exp_f32_e32 v20, v20\n"
"v_mfma_f32_16x16x32_bf16 v[104:107], a[168:171], v[36:39], v[104:107]\n"
"ds_read_b64_tr_b16 a[208:209], v3 offset:25600\n"
"v_mul_f32_e32 v22, s44, v22\n"
"v_mfma_f32_16x16x32_bf16 v[108:111], a[172:175], v[36:39], v[108:111]\n"
"v_exp_f32_e32 v21, v21\n"
"v_mfma_f32_16x16x32_bf16 v[112:115], a[176:179], v[36:39], v[112:115]\n"
"s_add_u32 m0, s48, 50176\n"
"v_mul_f32_e32 v23, s44, v23\n"
"v_cvt_pk_bf16_f32 v34, v20, v21\n"
"v_mfma_f32_16x16x32_bf16 v[116:119], a[180:183], v[36:39], v[116:119]\n"
"s_add_u32 s76, s45, 49280\n"
"v_exp_f32_e32 v22, v22\n"
"v_mfma_f32_16x16x32_bf16 v[120:123], a[184:187], v[36:39], v[120:123]\n"
"buffer_load_dwordx4 v6, s[28:31], s76 offen lds\n"
"v_mfma_f32_16x16x32_bf16 v[124:127], a[188:191], v[36:39], v[124:127]\n"
"v_exp_f32_e32 v23, v23\n"
"v_mfma_f32_16x16x32_bf16 a[232:235], a[224:227], v[36:39], a[232:235]\n"
"ds_read_b64_tr_b16 a[210:211], v4 offset:25600\n"
"v_cvt_pk_bf16_f32 v35, v22, v23\n"
"v_mfma_f32_16x16x32_bf16 v[16:19], a[96:99], a[64:67], 0\n"
"ds_read_b64_tr_b16 a[222:223], v14 offset:26112\n"
"v_mul_f32_e32 v24, s44, v24\n"
"v_mul_f32_e32 v25, s44, v25\n"
"v_mfma_f32_16x16x32_bf16 v[16:19], a[100:103], a[68:71], v[16:19]\n"
"v_exp_f32_e32 v24, v24\n"
"v_mfma_f32_16x16x32_bf16 v[16:19], a[104:107], a[72:75], v[16:19]\n"
"ds_read_b64_tr_b16 a[216:217], v3 offset:26112\n"
"v_mul_f32_e32 v26, s44, v26\n"
"v_mfma_f32_16x16x32_bf16 v[16:19], a[108:111], a[76:79], v[16:19]\n"
"v_exp_f32_e32 v25, v25\n"
"v_mfma_f32_16x16x32_bf16 v[20:23], a[112:115], a[64:67], 0\n"
"v_mul_f32_e32 v27, s44, v27\n"
"v_cvt_pk_bf16_f32 v36, v24, v25\n"
"v_mfma_f32_16x16x32_bf16 v[20:23], a[116:119], a[68:71], v[20:23]\n"
"v_exp_f32_e32 v26, v26\n"
"v_mfma_f32_16x16x32_bf16 v[20:23], a[120:123], a[72:75], v[20:23]\n"
"ds_read_b64_tr_b16 a[218:219], v4 offset:26112\n"
"v_mul_f32_e32 v28, s44, v28\n"
"v_mfma_f32_16x16x32_bf16 v[20:23], a[124:127], a[76:79], v[20:23]\n"
"v_exp_f32_e32 v27, v27\n"
"v_mfma_f32_16x16x32_bf16 v[128:131], a[160:163], v[32:35], v[128:131]\n"
"v_mul_f32_e32 v29, s44, v29\n"
"v_cvt_pk_bf16_f32 v37, v26, v27\n"
"v_mfma_f32_16x16x32_bf16 v[132:135], a[164:167], v[32:35], v[132:135]\n"
"v_exp_f32_e32 v28, v28\n"
"v_mfma_f32_16x16x32_bf16 v[136:139], a[168:171], v[32:35], v[136:139]\n"
"ds_read_b64_tr_b16 a[220:221], v11 offset:26112\n"
"v_mul_f32_e32 v30, s44, v30\n"
"v_mfma_f32_16x16x32_bf16 v[140:143], a[172:175], v[32:35], v[140:143]\n"
"v_exp_f32_e32 v29, v29\n"
"v_mfma_f32_16x16x32_bf16 v[144:147], a[176:179], v[32:35], v[144:147]\n"
"s_add_u32 m0, s49, 3072\n"
"v_mul_f32_e32 v31, s44, v31\n"
"v_cvt_pk_bf16_f32 v38, v28, v29\n"
"v_mfma_f32_16x16x32_bf16 v[148:151], a[180:183], v[32:35], v[148:151]\n"
"s_add_u32 s61, s58, 128\n"
"v_exp_f32_e32 v30, v30\n"
"v_mfma_f32_16x16x32_bf16 v[152:155], a[184:187], v[32:35], v[152:155]\n"
"buffer_load_dwordx4 v8, s[36:39], s61 offen  nt lds\n"
"v_mfma_f32_16x16x32_bf16 v[156:159], a[188:191], v[32:35], v[156:159]\n"
"v_exp_f32_e32 v31, v31\n"
"v_mfma_f32_16x16x32_bf16 a[236:239], a[224:227], v[32:35], a[236:239]\n"
"v_cvt_pk_bf16_f32 v39, v30, v31\n"
"s_waitcnt lgkmcnt(15)\n"
"L_L5%=:\n"
"v_mfma_f32_16x16x32_bf16 v[24:27], a[128:131], a[0:3], 0\n"
"v_mul_f32_e32 v16, s44, v16\n"
"v_mul_f32_e32 v17, s44, v17\n"
"v_mfma_f32_16x16x32_bf16 v[24:27], a[132:135], a[4:7], v[24:27]\n"
"v_exp_f32_e32 v16, v16\n"
"v_mfma_f32_16x16x32_bf16 v[24:27], a[136:139], a[8:11], v[24:27]\n"
"v_mul_f32_e32 v18, s44, v18\n"
"v_mfma_f32_16x16x32_bf16 v[24:27], a[140:143], a[12:15], v[24:27]\n"
"v_exp_f32_e32 v17, v17\n"
"v_mfma_f32_16x16x32_bf16 v[28:31], a[144:147], a[0:3], 0\n"
"v_mul_f32_e32 v19, s44, v19\n"
"v_cvt_pk_bf16_f32 v32, v16, v17\n"
"v_mfma_f32_16x16x32_bf16 v[28:31], a[148:151], a[4:7], v[28:31]\n"
"s_waitcnt vmcnt(13) lgkmcnt(0)\n"
"s_barrier\n"
"v_exp_f32_e32 v18, v18\n"
"v_mfma_f32_16x16x32_bf16 v[28:31], a[152:155], a[8:11], v[28:31]\n"
"ds_read_b128 a[96:99], v1 offset:0\n"
"ds_read_b128 a[108:111], v1 offset:1536\n"
"v_mul_f32_e32 v20, s44, v20\n"
"v_mfma_f32_16x16x32_bf16 v[28:31], a[156:159], a[12:15], v[28:31]\n"
"ds_read_b128 v[40:43], v58 offset:4096\n"
"v_exp_f32_e32 v19, v19\n"
"v_mfma_f32_16x16x32_bf16 v[160:163], a[160:163], v[36:39], v[160:163]\n"
"ds_read_b128 v[44:47], v59 offset:5120\n"
"v_mul_f32_e32 v21, s44, v21\n"
"v_cvt_pk_bf16_f32 v33, v18, v19\n"
"v_mfma_f32_16x16x32_bf16 v[164:167], a[164:167], v[36:39], v[164:167]\n"
"ds_read_b128 v[48:51], v58 offset:6144\n"
"v_exp_f32_e32 v20, v20\n"
"v_mfma_f32_16x16x32_bf16 v[168:171], a[168:171], v[36:39], v[168:171]\n"
"ds_read_b128 a[100:103], v1 offset:512\n"
"ds_read_b128 v[52:55], v59 offset:7168\n"
"v_mul_f32_e32 v22, s44, v22\n"
"v_mfma_f32_16x16x32_bf16 v[172:175], a[172:175], v[36:39], v[172:175]\n"
"v_exp_f32_e32 v21, v21\n"
"v_mfma_f32_16x16x32_bf16 v[176:179], a[176:179], v[36:39], v[176:179]\n"
"s_add_u32 m0, s48, 24576\n"
"v_mul_f32_e32 v23, s44, v23\n"
"v_cvt_pk_bf16_f32 v34, v20, v21\n"
"v_mfma_f32_16x16x32_bf16 v[180:183], a[180:183], v[36:39], v[180:183]\n"
"s_add_u32 s47, s45, 57344\n"
"v_exp_f32_e32 v22, v22\n"
"v_mfma_f32_16x16x32_bf16 v[184:187], a[184:187], v[36:39], v[184:187]\n"
"buffer_load_dwordx4 v6, s[24:27], s47 offen lds\n"
"v_mfma_f32_16x16x32_bf16 v[188:191], a[188:191], v[36:39], v[188:191]\n"
"v_exp_f32_e32 v23, v23\n"
"v_mfma_f32_16x16x32_bf16 a[240:243], a[224:227], v[36:39], a[240:243]\n"
"ds_read_b128 a[104:107], v1 offset:1024\n"
"v_cvt_pk_bf16_f32 v35, v22, v23\n"
"v_mfma_f32_16x16x32_bf16 v[16:19], a[128:131], a[16:19], 0\n"
"v_mul_f32_e32 v24, s44, v24\n"
"v_mul_f32_e32 v25, s44, v25\n"
"v_mfma_f32_16x16x32_bf16 v[16:19], a[132:135], a[20:23], v[16:19]\n"
"v_exp_f32_e32 v24, v24\n"
"s_add_u32 s77, s60, 4096\n"
"v_mfma_f32_16x16x32_bf16 v[16:19], a[136:139], a[24:27], v[16:19]\n"
"ds_read_b128 a[112:115], v2 offset:0\n"
"v_mul_f32_e32 v26, s44, v26\n"
"s_waitcnt lgkmcnt(6)\n"
"buffer_store_dwordx4 v[40:43], v9, s[40:43], s77 offen offset:0  sc1\n"
"v_mfma_f32_16x16x32_bf16 v[16:19], a[140:143], a[28:31], v[16:19]\n"
"v_exp_f32_e32 v25, v25\n"
"v_mfma_f32_16x16x32_bf16 v[20:23], a[144:147], a[16:19], 0\n"
"v_mul_f32_e32 v27, s44, v27\n"
"v_cvt_pk_bf16_f32 v36, v24, v25\n"
"v_mfma_f32_16x16x32_bf16 v[20:23], a[148:151], a[20:23], v[20:23]\n"
"v_exp_f32_e32 v26, v26\n"
"v_mfma_f32_16x16x32_bf16 v[20:23], a[152:155], a[24:27], v[20:23]\n"
"ds_read_b128 a[116:119], v2 offset:512\n"
"v_mul_f32_e32 v28, s44, v28\n"
"s_waitcnt lgkmcnt(6)\n"
"buffer_store_dwordx4 v[44:47], v9, s[40:43], s77 offen offset:1024  sc1\n"
"v_mfma_f32_16x16x32_bf16 v[20:23], a[156:159], a[28:31], v[20:23]\n"
"v_exp_f32_e32 v27, v27\n"
"v_mfma_f32_16x16x32_bf16 v[192:195], a[160:163], v[32:35], v[192:195]\n"
"v_mul_f32_e32 v29, s44, v29\n"
"v_cvt_pk_bf16_f32 v37, v26, v27\n"
"v_mfma_f32_16x16x32_bf16 v[196:199], a[164:167], v[32:35], v[196:199]\n"
"v_exp_f32_e32 v28, v28\n"
"v_mfma_f32_16x16x32_bf16 v[200:203], a[168:171], v[32:35], v[200:203]\n"
"ds_read_b128 a[120:123], v2 offset:1024\n"
"v_mul_f32_e32 v30, s44, v30\n"
"s_waitcnt lgkmcnt(6)\n"
"buffer_store_dwordx4 v[48:51], v9, s[40:43], s77 offen offset:2048  sc1\n"
"v_mfma_f32_16x16x32_bf16 v[204:207], a[172:175], v[32:35], v[204:207]\n"
"v_exp_f32_e32 v29, v29\n"
"v_mfma_f32_16x16x32_bf16 v[208:211], a[176:179], v[32:35], v[208:211]\n"
"s_add_u32 m0, s48, 25600\n"
"v_mul_f32_e32 v31, s44, v31\n"
"v_cvt_pk_bf16_f32 v38, v28, v29\n"
"v_mfma_f32_16x16x32_bf16 v[212:215], a[180:183], v[32:35], v[212:215]\n"
"s_add_u32 s47, s45, 57472\n"
"v_exp_f32_e32 v30, v30\n"
"v_mfma_f32_16x16x32_bf16 v[216:219], a[184:187], v[32:35], v[216:219]\n"
"buffer_load_dwordx4 v6, s[24:27], s47 offen lds\n"
"v_mfma_f32_16x16x32_bf16 v[220:223], a[188:191], v[32:35], v[220:223]\n"
"v_exp_f32_e32 v31, v31\n"
"v_mfma_f32_16x16x32_bf16 a[244:247], a[224:227], v[32:35], a[244:247]\n"
"ds_read_b128 a[124:127], v2 offset:1536\n"
"v_cvt_pk_bf16_f32 v39, v30, v31\n"
"s_waitcnt lgkmcnt(5)\n"
"buffer_store_dwordx4 v[52:55], v9, s[40:43], s77 offen offset:3072  sc1\n"
"v_mfma_f32_16x16x32_bf16 v[24:27], a[128:131], a[32:35], 0\n"
"ds_read_b64_tr_b16 a[166:167], v14 offset:0\n"
"v_mul_f32_e32 v16, s44, v16\n"
"v_mul_f32_e32 v17, s44, v17\n"
"v_mfma_f32_16x16x32_bf16 v[24:27], a[132:135], a[36:39], v[24:27]\n"
"ds_read_b64_tr_b16 a[168:169], v3 offset:512\n"
"v_exp_f32_e32 v16, v16\n"
"v_mfma_f32_16x16x32_bf16 v[24:27], a[136:139], a[40:43], v[24:27]\n"
"ds_read_b64_tr_b16 a[160:161], v3 offset:0\n"
"ds_read_b64_tr_b16 a[170:171], v4 offset:512\n"
"v_mul_f32_e32 v18, s44, v18\n"
"v_mfma_f32_16x16x32_bf16 v[24:27], a[140:143], a[44:47], v[24:27]\n"
"v_exp_f32_e32 v17, v17\n"
"v_mfma_f32_16x16x32_bf16 v[28:31], a[144:147], a[32:35], 0\n"
"v_mul_f32_e32 v19, s44, v19\n"
"v_cvt_pk_bf16_f32 v32, v16, v17\n"
"v_mfma_f32_16x16x32_bf16 v[28:31], a[148:151], a[36:39], v[28:31]\n"
"v_exp_f32_e32 v18, v18\n"
"v_mfma_f32_16x16x32_bf16 v[28:31], a[152:155], a[40:43], v[28:31]\n"
"ds_read_b64_tr_b16 a[162:163], v4 offset:0\n"
"v_mul_f32_e32 v20, s44, v20\n"
"v_mfma_f32_16x16x32_bf16 v[28:31], a[156:159], a[44:47], v[28:31]\n"
"v_exp_f32_e32 v19, v19\n"
"v_mfma_f32_16x16x32_bf16 v[64:67], a[192:195], v[36:39], v[64:67]\n"
"s_add_u32 m0, s49, 4096\n"
"v_mul_f32_e32 v21, s44, v21\n"
"v_cvt_pk_bf16_f32 v33, v18, v19\n"
"v_mfma_f32_16x16x32_bf16 v[68:71], a[196:199], v[36:39], v[68:71]\n"
"s_add_u32 s61, s58, 4096\n"
"v_exp_f32_e32 v20, v20\n"
"v_mfma_f32_16x16x32_bf16 v[72:75], a[200:203], v[36:39], v[72:75]\n"
"buffer_load_dwordx4 v7, s[36:39], s61 offen  nt lds\n"
"v_mul_f32_e32 v22, s44, v22\n"
"v_mfma_f32_16x16x32_bf16 v[76:79], a[204:207], v[36:39], v[76:79]\n"
"v_exp_f32_e32 v21, v21\n"
"v_mfma_f32_16x16x32_bf16 v[80:83], a[208:211], v[36:39], v[80:83]\n"
"s_add_u32 m0, s48, 57344\n"
"v_mul_f32_e32 v23, s44, v23\n"
"v_cvt_pk_bf16_f32 v34, v20, v21\n"
"v_mfma_f32_16x16x32_bf16 v[84:87], a[212:215], v[36:39], v[84:87]\n"
"s_add_u32 s76, s45, 57344\n"
"v_exp_f32_e32 v22, v22\n"
"v_mfma_f32_16x16x32_bf16 v[88:91], a[216:219], v[36:39], v[88:91]\n"
"buffer_load_dwordx4 v6, s[28:31], s76 offen lds\n"
"v_mfma_f32_16x16x32_bf16 v[92:95], a[220:223], v[36:39], v[92:95]\n"
"v_exp_f32_e32 v23, v23\n"
"v_mfma_f32_16x16x32_bf16 a[228:231], a[224:227], v[36:39], a[228:231]\n"
"ds_read_b64_tr_b16 a[164:165], v11 offset:0\n"
"v_cvt_pk_bf16_f32 v35, v22, v23\n"
"v_mfma_f32_16x16x32_bf16 v[16:19], a[128:131], a[48:51], 0\n"
"ds_read_b64_tr_b16 a[180:181], v11 offset:1024\n"
"v_mul_f32_e32 v24, s44, v24\n"
"v_mul_f32_e32 v25, s44, v25\n"
"v_mfma_f32_16x16x32_bf16 v[16:19], a[132:135], a[52:55], v[16:19]\n"
"ds_read_b64_tr_b16 a[182:183], v14 offset:1024\n"
"v_exp_f32_e32 v24, v24\n"
"v_mfma_f32_16x16x32_bf16 v[16:19], a[136:139], a[56:59], v[16:19]\n"
"ds_read_b64_tr_b16 a[172:173], v11 offset:512\n"
"v_mul_f32_e32 v26, s44, v26\n"
"v_mfma_f32_16x16x32_bf16 v[16:19], a[140:143], a[60:63], v[16:19]\n"
"v_exp_f32_e32 v25, v25\n"
"v_mfma_f32_16x16x32_bf16 v[20:23], a[144:147], a[48:51], 0\n"
"v_mul_f32_e32 v27, s44, v27\n"
"v_cvt_pk_bf16_f32 v36, v24, v25\n"
"v_mfma_f32_16x16x32_bf16 v[20:23], a[148:151], a[52:55], v[20:23]\n"
"v_exp_f32_e32 v26, v26\n"
"v_mfma_f32_16x16x32_bf16 v[20:23], a[152:155], a[56:59], v[20:23]\n"
"ds_read_b64_tr_b16 a[174:175], v14 offset:512\n"
"v_mul_f32_e32 v28, s44, v28\n"
"v_mfma_f32_16x16x32_bf16 v[20:23], a[156:159], a[60:63], v[20:23]\n"
"v_exp_f32_e32 v27, v27\n"
"v_mfma_f32_16x16x32_bf16 v[96:99], a[192:195], v[32:35], v[96:99]\n"
"v_mul_f32_e32 v29, s44, v29\n"
"v_cvt_pk_bf16_f32 v37, v26, v27\n"
"v_mfma_f32_16x16x32_bf16 v[100:103], a[196:199], v[32:35], v[100:103]\n"
"v_exp_f32_e32 v28, v28\n"
"v_mfma_f32_16x16x32_bf16 v[104:107], a[200:203], v[32:35], v[104:107]\n"
"ds_read_b64_tr_b16 a[176:177], v3 offset:1024\n"
"v_mul_f32_e32 v30, s44, v30\n"
"v_mfma_f32_16x16x32_bf16 v[108:111], a[204:207], v[32:35], v[108:111]\n"
"v_exp_f32_e32 v29, v29\n"
"v_mfma_f32_16x16x32_bf16 v[112:115], a[208:211], v[32:35], v[112:115]\n"
"s_add_u32 m0, s48, 58368\n"
"v_mul_f32_e32 v31, s44, v31\n"
"v_cvt_pk_bf16_f32 v38, v28, v29\n"
"v_mfma_f32_16x16x32_bf16 v[116:119], a[212:215], v[32:35], v[116:119]\n"
"s_add_u32 s76, s45, 57472\n"
"v_exp_f32_e32 v30, v30\n"
"v_mfma_f32_16x16x32_bf16 v[120:123], a[216:219], v[32:35], v[120:123]\n"
"buffer_load_dwordx4 v6, s[28:31], s76 offen lds\n"
"v_mfma_f32_16x16x32_bf16 v[124:127], a[220:223], v[32:35], v[124:127]\n"
"v_exp_f32_e32 v31, v31\n"
"v_mfma_f32_16x16x32_bf16 a[232:235], a[224:227], v[32:35], a[232:235]\n"
"ds_read_b64_tr_b16 a[178:179], v4 offset:1024\n"
"v_cvt_pk_bf16_f32 v39, v30, v31\n"
"v_mfma_f32_16x16x32_bf16 v[24:27], a[128:131], a[64:67], 0\n"
"ds_read_b64_tr_b16 a[190:191], v14 offset:1536\n"
"v_mul_f32_e32 v16, s44, v16\n"
"v_mul_f32_e32 v17, s44, v17\n"
"v_mfma_f32_16x16x32_bf16 v[24:27], a[132:135], a[68:71], v[24:27]\n"
"v_exp_f32_e32 v16, v16\n"
"v_mfma_f32_16x16x32_bf16 v[24:27], a[136:139], a[72:75], v[24:27]\n"
"ds_read_b64_tr_b16 a[184:185], v3 offset:1536\n"
"v_mul_f32_e32 v18, s44, v18\n"
"v_mfma_f32_16x16x32_bf16 v[24:27], a[140:143], a[76:79], v[24:27]\n"
"v_exp_f32_e32 v17, v17\n"
"v_mfma_f32_16x16x32_bf16 v[28:31], a[144:147], a[64:67], 0\n"
"v_mul_f32_e32 v19, s44, v19\n"
"v_cvt_pk_bf16_f32 v32, v16, v17\n"
"v_mfma_f32_16x16x32_bf16 v[28:31], a[148:151], a[68:71], v[28:31]\n"
"v_exp_f32_e32 v18, v18\n"
"v_mfma_f32_16x16x32_bf16 v[28:31], a[152:155], a[72:75], v[28:31]\n"
"ds_read_b64_tr_b16 a[186:187], v4 offset:1536\n"
"v_mul_f32_e32 v20, s44, v20\n"
"v_mfma_f32_16x16x32_bf16 v[28:31], a[156:159], a[76:79], v[28:31]\n"
"v_exp_f32_e32 v19, v19\n"
"v_mfma_f32_16x16x32_bf16 v[128:131], a[192:195], v[36:39], v[128:131]\n"
"v_mul_f32_e32 v21, s44, v21\n"
"v_cvt_pk_bf16_f32 v33, v18, v19\n"
"v_mfma_f32_16x16x32_bf16 v[132:135], a[196:199], v[36:39], v[132:135]\n"
"v_exp_f32_e32 v20, v20\n"
"v_mfma_f32_16x16x32_bf16 v[136:139], a[200:203], v[36:39], v[136:139]\n"
"ds_read_b64_tr_b16 a[188:189], v11 offset:1536\n"
"v_mul_f32_e32 v22, s44, v22\n"
"v_mfma_f32_16x16x32_bf16 v[140:143], a[204:207], v[36:39], v[140:143]\n"
"v_exp_f32_e32 v21, v21\n"
"v_mfma_f32_16x16x32_bf16 v[144:147], a[208:211], v[36:39], v[144:147]\n"
"s_add_u32 m0, s49, 5120\n"
"v_mul_f32_e32 v23, s44, v23\n"
"v_cvt_pk_bf16_f32 v34, v20, v21\n"
"v_mfma_f32_16x16x32_bf16 v[148:151], a[212:215], v[36:39], v[148:151]\n"
"s_add_u32 s61, s58, 4224\n"
"v_exp_f32_e32 v22, v22\n"
"v_mfma_f32_16x16x32_bf16 v[152:155], a[216:219], v[36:39], v[152:155]\n"
"buffer_load_dwordx4 v7, s[36:39], s61 offen  nt lds\n"
"v_mfma_f32_16x16x32_bf16 v[156:159], a[220:223], v[36:39], v[156:159]\n"
"v_exp_f32_e32 v23, v23\n"
"v_mfma_f32_16x16x32_bf16 a[236:239], a[224:227], v[36:39], a[236:239]\n"
"v_cvt_pk_bf16_f32 v35, v22, v23\n"
"v_mfma_f32_16x16x32_bf16 v[16:19], a[96:99], a[0:3], 0\n"
"v_mul_f32_e32 v24, s44, v24\n"
"v_mul_f32_e32 v25, s44, v25\n"
"v_mfma_f32_16x16x32_bf16 v[16:19], a[100:103], a[4:7], v[16:19]\n"
"s_waitcnt lgkmcnt(15)\n"
"v_exp_f32_e32 v24, v24\n"
"v_mfma_f32_16x16x32_bf16 v[16:19], a[104:107], a[8:11], v[16:19]\n"
"v_mul_f32_e32 v26, s44, v26\n"
"v_mfma_f32_16x16x32_bf16 v[16:19], a[108:111], a[12:15], v[16:19]\n"
"v_exp_f32_e32 v25, v25\n"
"v_mfma_f32_16x16x32_bf16 v[20:23], a[112:115], a[0:3], 0\n"
"v_mul_f32_e32 v27, s44, v27\n"
"v_cvt_pk_bf16_f32 v36, v24, v25\n"
"v_mfma_f32_16x16x32_bf16 v[20:23], a[116:119], a[4:7], v[20:23]\n"
"s_waitcnt vmcnt(17) lgkmcnt(0)\n"
"s_barrier\n"
"v_exp_f32_e32 v26, v26\n"
"v_mfma_f32_16x16x32_bf16 v[20:23], a[120:123], a[8:11], v[20:23]\n"
"ds_read_b128 a[128:131], v1 offset:8192\n"
"ds_read_b128 a[140:143], v1 offset:9728\n"
"v_mul_f32_e32 v28, s44, v28\n"
"v_mfma_f32_16x16x32_bf16 v[20:23], a[124:127], a[12:15], v[20:23]\n"
"v_exp_f32_e32 v27, v27\n"
"v_mfma_f32_16x16x32_bf16 v[160:163], a[192:195], v[32:35], v[160:163]\n"
"v_mul_f32_e32 v29, s44, v29\n"
"v_cvt_pk_bf16_f32 v37, v26, v27\n"
"v_mfma_f32_16x16x32_bf16 v[164:167], a[196:199], v[32:35], v[164:167]\n"
"v_exp_f32_e32 v28, v28\n"
"v_mfma_f32_16x16x32_bf16 v[168:171], a[200:203], v[32:35], v[168:171]\n"
"ds_read_b128 a[132:135], v1 offset:8704\n"
"v_mul_f32_e32 v30, s44, v30\n"
"v_mfma_f32_16x16x32_bf16 v[172:175], a[204:207], v[32:35], v[172:175]\n"
"v_exp_f32_e32 v29, v29\n"
"v_mfma_f32_16x16x32_bf16 v[176:179], a[208:211], v[32:35], v[176:179]\n"
"s_add_u32 m0, s48, 0\n"
"v_mul_f32_e32 v31, s44, v31\n"
"v_cvt_pk_bf16_f32 v38, v28, v29\n"
"v_mfma_f32_16x16x32_bf16 v[180:183], a[212:215], v[32:35], v[180:183]\n"
"s_add_u32 s47, s45, 65536\n"
"v_exp_f32_e32 v30, v30\n"
"v_mfma_f32_16x16x32_bf16 v[184:187], a[216:219], v[32:35], v[184:187]\n"
"buffer_load_dwordx4 v6, s[24:27], s47 offen lds\n"
"v_mfma_f32_16x16x32_bf16 v[188:191], a[220:223], v[32:35], v[188:191]\n"
"v_exp_f32_e32 v31, v31\n"
"v_mfma_f32_16x16x32_bf16 a[240:243], a[224:227], v[32:35], a[240:243]\n"
"ds_read_b128 a[136:139], v1 offset:9216\n"
"v_cvt_pk_bf16_f32 v39, v30, v31\n"
"v_mfma_f32_16x16x32_bf16 v[24:27], a[96:99], a[16:19], 0\n"
"v_mul_f32_e32 v16, s44, v16\n"
"v_mul_f32_e32 v17, s44, v17\n"
"v_mfma_f32_16x16x32_bf16 v[24:27], a[100:103], a[20:23], v[24:27]\n"
"v_exp_f32_e32 v16, v16\n"
"v_mfma_f32_16x16x32_bf16 v[24:27], a[104:107], a[24:27], v[24:27]\n"
"ds_read_b128 a[144:147], v2 offset:8192\n"
"v_mul_f32_e32 v18, s44, v18\n"
"v_mfma_f32_16x16x32_bf16 v[24:27], a[108:111], a[28:31], v[24:27]\n"
"v_exp_f32_e32 v17, v17\n"
"v_mfma_f32_16x16x32_bf16 v[28:31], a[112:115], a[16:19], 0\n"
"v_mul_f32_e32 v19, s44, v19\n"
"v_cvt_pk_bf16_f32 v32, v16, v17\n"
"v_mfma_f32_16x16x32_bf16 v[28:31], a[116:119], a[20:23], v[28:31]\n"
"v_exp_f32_e32 v18, v18\n"
"v_mfma_f32_16x16x32_bf16 v[28:31], a[120:123], a[24:27], v[28:31]\n"
"ds_read_b128 a[148:151], v2 offset:8704\n"
"v_mul_f32_e32 v20, s44, v20\n"
"v_mfma_f32_16x16x32_bf16 v[28:31], a[124:127], a[28:31], v[28:31]\n"
"v_exp_f32_e32 v19, v19\n"
"v_mfma_f32_16x16x32_bf16 v[192:195], a[192:195], v[36:39], v[192:195]\n"
"v_mul_f32_e32 v21, s44, v21\n"
"v_cvt_pk_bf16_f32 v33, v18, v19\n"
"v_mfma_f32_16x16x32_bf16 v[196:199], a[196:199], v[36:39], v[196:199]\n"
"v_exp_f32_e32 v20, v20\n"
"v_mfma_f32_16x16x32_bf16 v[200:203], a[200:203], v[36:39], v[200:203]\n"
"ds_read_b128 a[152:155], v2 offset:9216\n"
"v_mul_f32_e32 v22, s44, v22\n"
"v_mfma_f32_16x16x32_bf16 v[204:207], a[204:207], v[36:39], v[204:207]\n"
"v_exp_f32_e32 v21, v21\n"
"v_mfma_f32_16x16x32_bf16 v[208:211], a[208:211], v[36:39], v[208:211]\n"
"s_add_u32 m0, s48, 1024\n"
"v_mul_f32_e32 v23, s44, v23\n"
"v_cvt_pk_bf16_f32 v34, v20, v21\n"
"v_mfma_f32_16x16x32_bf16 v[212:215], a[212:215], v[36:39], v[212:215]\n"
"s_add_u32 s47, s45, 65664\n"
"v_exp_f32_e32 v22, v22\n"
"v_mfma_f32_16x16x32_bf16 v[216:219], a[216:219], v[36:39], v[216:219]\n"
"buffer_load_dwordx4 v6, s[24:27], s47 offen lds\n"
"v_mfma_f32_16x16x32_bf16 v[220:223], a[220:223], v[36:39], v[220:223]\n"
"v_exp_f32_e32 v23, v23\n"
"v_mfma_f32_16x16x32_bf16 a[244:247], a[224:227], v[36:39], a[244:247]\n"
"ds_read_b128 a[156:159], v2 offset:9728\n"
"v_cvt_pk_bf16_f32 v35, v22, v23\n"
"v_mfma_f32_16x16x32_bf16 v[16:19], a[96:99], a[32:35], 0\n"
"ds_read_b64_tr_b16 a[198:199], v14 offset:8192\n"
"v_mul_f32_e32 v24, s44, v24\n"
"v_mul_f32_e32 v25, s44, v25\n"
"v_mfma_f32_16x16x32_bf16 v[16:19], a[100:103], a[36:39], v[16:19]\n"
"ds_read_b64_tr_b16 a[200:201], v3 offset:8704\n"
"v_exp_f32_e32 v24, v24\n"
"v_mfma_f32_16x16x32_bf16 v[16:19], a[104:107], a[40:43], v[16:19]\n"
"ds_read_b64_tr_b16 a[192:193], v3 offset:8192\n"
"ds_read_b64_tr_b16 a[202:203], v4 offset:8704\n"
"v_mul_f32_e32 v26, s44, v26\n"
"v_mfma_f32_16x16x32_bf16 v[16:19], a[108:111], a[44:47], v[16:19]\n"
"v_exp_f32_e32 v25, v25\n"
"v_mfma_f32_16x16x32_bf16 v[20:23], a[112:115], a[32:35], 0\n"
"v_mul_f32_e32 v27, s44, v27\n"
"v_cvt_pk_bf16_f32 v36, v24, v25\n"
"v_mfma_f32_16x16x32_bf16 v[20:23], a[116:119], a[36:39], v[20:23]\n"
"v_exp_f32_e32 v26, v26\n"
"v_mfma_f32_16x16x32_bf16 v[20:23], a[120:123], a[40:43], v[20:23]\n"
"ds_read_b64_tr_b16 a[194:195], v4 offset:8192\n"
"v_mul_f32_e32 v28, s44, v28\n"
"v_mfma_f32_16x16x32_bf16 v[20:23], a[124:127], a[44:47], v[20:23]\n"
"v_exp_f32_e32 v27, v27\n"
"v_mfma_f32_16x16x32_bf16 v[64:67], a[160:163], v[32:35], v[64:67]\n"
"s_add_u32 m0, s49, 6144\n"
"v_mul_f32_e32 v29, s44, v29\n"
"v_cvt_pk_bf16_f32 v37, v26, v27\n"
"v_mfma_f32_16x16x32_bf16 v[68:71], a[164:167], v[32:35], v[68:71]\n"
"s_add_u32 s61, s58, 4096\n"
"v_exp_f32_e32 v28, v28\n"
"v_mfma_f32_16x16x32_bf16 v[72:75], a[168:171], v[32:35], v[72:75]\n"
"buffer_load_dwordx4 v8, s[36:39], s61 offen  nt lds\n"
"v_mul_f32_e32 v30, s44, v30\n"
"v_mfma_f32_16x16x32_bf16 v[76:79], a[172:175], v[32:35], v[76:79]\n"
"v_exp_f32_e32 v29, v29\n"
"v_mfma_f32_16x16x32_bf16 v[80:83], a[176:179], v[32:35], v[80:83]\n"
"s_add_u32 m0, s48, 32768\n"
"v_mul_f32_e32 v31, s44, v31\n"
"v_cvt_pk_bf16_f32 v38, v28, v29\n"
"v_mfma_f32_16x16x32_bf16 v[84:87], a[180:183], v[32:35], v[84:87]\n"
"s_add_u32 s76, s45, 65536\n"
"v_exp_f32_e32 v30, v30\n"
"v_mfma_f32_16x16x32_bf16 v[88:91], a[184:187], v[32:35], v[88:91]\n"
"buffer_load_dwordx4 v6, s[28:31], s76 offen lds\n"
"v_mfma_f32_16x16x32_bf16 v[92:95], a[188:191], v[32:35], v[92:95]\n"
"v_exp_f32_e32 v31, v31\n"
"v_mfma_f32_16x16x32_bf16 a[228:231], a[224:227], v[32:35], a[228:231]\n"
"ds_read_b64_tr_b16 a[196:197], v11 offset:8192\n"
"v_cvt_pk_bf16_f32 v39, v30, v31\n"
"v_mfma_f32_16x16x32_bf16 v[24:27], a[96:99], a[48:51], 0\n"
"ds_read_b64_tr_b16 a[212:213], v11 offset:9216\n"
"v_mul_f32_e32 v16, s44, v16\n"
"v_mul_f32_e32 v17, s44, v17\n"
"v_mfma_f32_16x16x32_bf16 v[24:27], a[100:103], a[52:55], v[24:27]\n"
"ds_read_b64_tr_b16 a[214:215], v14 offset:9216\n"
"v_exp_f32_e32 v16, v16\n"
"v_mfma_f32_16x16x32_bf16 v[24:27], a[104:107], a[56:59], v[24:27]\n"
"ds_read_b64_tr_b16 a[204:205], v11 offset:8704\n"
"v_mul_f32_e32 v18, s44, v18\n"
"v_mfma_f32_16x16x32_bf16 v[24:27], a[108:111], a[60:63], v[24:27]\n"
"v_exp_f32_e32 v17, v17\n"
"v_mfma_f32_16x16x32_bf16 v[28:31], a[112:115], a[48:51], 0\n"
"v_mul_f32_e32 v19, s44, v19\n"
"v_cvt_pk_bf16_f32 v32, v16, v17\n"
"v_mfma_f32_16x16x32_bf16 v[28:31], a[116:119], a[52:55], v[28:31]\n"
"v_exp_f32_e32 v18, v18\n"
"v_mfma_f32_16x16x32_bf16 v[28:31], a[120:123], a[56:59], v[28:31]\n"
"ds_read_b64_tr_b16 a[206:207], v14 offset:8704\n"
"v_mul_f32_e32 v20, s44, v20\n"
"v_mfma_f32_16x16x32_bf16 v[28:31], a[124:127], a[60:63], v[28:31]\n"
"v_exp_f32_e32 v19, v19\n"
"v_mfma_f32_16x16x32_bf16 v[96:99], a[160:163], v[36:39], v[96:99]\n"
"v_mul_f32_e32 v21, s44, v21\n"
"v_cvt_pk_bf16_f32 v33, v18, v19\n"
"v_mfma_f32_16x16x32_bf16 v[100:103], a[164:167], v[36:39], v[100:103]\n"
"v_exp_f32_e32 v20, v20\n"
"v_mfma_f32_16x16x32_bf16 v[104:107], a[168:171], v[36:39], v[104:107]\n"
"ds_read_b64_tr_b16 a[208:209], v3 offset:9216\n"
"v_mul_f32_e32 v22, s44, v22\n"
"v_mfma_f32_16x16x32_bf16 v[108:111], a[172:175], v[36:39], v[108:111]\n"
"v_exp_f32_e32 v21, v21\n"
"v_mfma_f32_16x16x32_bf16 v[112:115], a[176:179], v[36:39], v[112:115]\n"
"s_add_u32 m0, s48, 33792\n"
"v_mul_f32_e32 v23, s44, v23\n"
"v_cvt_pk_bf16_f32 v34, v20, v21\n"
"v_mfma_f32_16x16x32_bf16 v[116:119], a[180:183], v[36:39], v[116:119]\n"
"s_add_u32 s76, s45, 65664\n"
"v_exp_f32_e32 v22, v22\n"
"v_mfma_f32_16x16x32_bf16 v[120:123], a[184:187], v[36:39], v[120:123]\n"
"buffer_load_dwordx4 v6, s[28:31], s76 offen lds\n"
"v_mfma_f32_16x16x32_bf16 v[124:127], a[188:191], v[36:39], v[124:127]\n"
"v_exp_f32_e32 v23, v23\n"
"v_mfma_f32_16x16x32_bf16 a[232:235], a[224:227], v[36:39], a[232:235]\n"
"ds_read_b64_tr_b16 a[210:211], v4 offset:9216\n"
"v_cvt_pk_bf16_f32 v35, v22, v23\n"
"v_mfma_f32_16x16x32_bf16 v[16:19], a[96:99], a[64:67], 0\n"
"ds_read_b64_tr_b16 a[222:223], v14 offset:9728\n"
"v_mul_f32_e32 v24, s44, v24\n"
"v_mul_f32_e32 v25, s44, v25\n"
"v_mfma_f32_16x16x32_bf16 v[16:19], a[100:103], a[68:71], v[16:19]\n"
"v_exp_f32_e32 v24, v24\n"
"v_mfma_f32_16x16x32_bf16 v[16:19], a[104:107], a[72:75], v[16:19]\n"
"ds_read_b64_tr_b16 a[216:217], v3 offset:9728\n"
"v_mul_f32_e32 v26, s44, v26\n"
"v_mfma_f32_16x16x32_bf16 v[16:19], a[108:111], a[76:79], v[16:19]\n"
"v_exp_f32_e32 v25, v25\n"
"v_mfma_f32_16x16x32_bf16 v[20:23], a[112:115], a[64:67], 0\n"
"v_mul_f32_e32 v27, s44, v27\n"
"v_cvt_pk_bf16_f32 v36, v24, v25\n"
"v_mfma_f32_16x16x32_bf16 v[20:23], a[116:119], a[68:71], v[20:23]\n"
"v_exp_f32_e32 v26, v26\n"
"v_mfma_f32_16x16x32_bf16 v[20:23], a[120:123], a[72:75], v[20:23]\n"
"ds_read_b64_tr_b16 a[218:219], v4 offset:9728\n"
"v_mul_f32_e32 v28, s44, v28\n"
"v_mfma_f32_16x16x32_bf16 v[20:23], a[124:127], a[76:79], v[20:23]\n"
"v_exp_f32_e32 v27, v27\n"
"v_mfma_f32_16x16x32_bf16 v[128:131], a[160:163], v[32:35], v[128:131]\n"
"v_mul_f32_e32 v29, s44, v29\n"
"v_cvt_pk_bf16_f32 v37, v26, v27\n"
"v_mfma_f32_16x16x32_bf16 v[132:135], a[164:167], v[32:35], v[132:135]\n"
"v_exp_f32_e32 v28, v28\n"
"v_mfma_f32_16x16x32_bf16 v[136:139], a[168:171], v[32:35], v[136:139]\n"
"ds_read_b64_tr_b16 a[220:221], v11 offset:9728\n"
"v_mul_f32_e32 v30, s44, v30\n"
"v_mfma_f32_16x16x32_bf16 v[140:143], a[172:175], v[32:35], v[140:143]\n"
"v_exp_f32_e32 v29, v29\n"
"v_mfma_f32_16x16x32_bf16 v[144:147], a[176:179], v[32:35], v[144:147]\n"
"s_add_u32 m0, s49, 7168\n"
"v_mul_f32_e32 v31, s44, v31\n"
"v_cvt_pk_bf16_f32 v38, v28, v29\n"
"v_mfma_f32_16x16x32_bf16 v[148:151], a[180:183], v[32:35], v[148:151]\n"
"s_add_u32 s61, s58, 4224\n"
"v_exp_f32_e32 v30, v30\n"
"v_mfma_f32_16x16x32_bf16 v[152:155], a[184:187], v[32:35], v[152:155]\n"
"buffer_load_dwordx4 v8, s[36:39], s61 offen  nt lds\n"
"v_mfma_f32_16x16x32_bf16 v[156:159], a[188:191], v[32:35], v[156:159]\n"
"v_exp_f32_e32 v31, v31\n"
"v_mfma_f32_16x16x32_bf16 a[236:239], a[224:227], v[32:35], a[236:239]\n"
"s_waitcnt lgkmcnt(15)\n"
"v_cvt_pk_bf16_f32 v39, v30, v31\n"
"v_mfma_f32_16x16x32_bf16 v[24:27], a[128:131], a[0:3], 0\n"
"v_mul_f32_e32 v16, s44, v16\n"
"v_mul_f32_e32 v17, s44, v17\n"
"v_mfma_f32_16x16x32_bf16 v[24:27], a[132:135], a[4:7], v[24:27]\n"
"v_exp_f32_e32 v16, v16\n"
"v_mfma_f32_16x16x32_bf16 v[24:27], a[136:139], a[8:11], v[24:27]\n"
"v_mul_f32_e32 v18, s44, v18\n"
"v_mfma_f32_16x16x32_bf16 v[24:27], a[140:143], a[12:15], v[24:27]\n"
"v_exp_f32_e32 v17, v17\n"
"v_mfma_f32_16x16x32_bf16 v[28:31], a[144:147], a[0:3], 0\n"
"v_mul_f32_e32 v19, s44, v19\n"
"v_cvt_pk_bf16_f32 v32, v16, v17\n"
"v_mfma_f32_16x16x32_bf16 v[28:31], a[148:151], a[4:7], v[28:31]\n"
"s_waitcnt vmcnt(17) lgkmcnt(0)\n"
"s_barrier\n"
"v_exp_f32_e32 v18, v18\n"
"v_mfma_f32_16x16x32_bf16 v[28:31], a[152:155], a[8:11], v[28:31]\n"
"ds_read_b128 a[96:99], v1 offset:16384\n"
"ds_read_b128 a[108:111], v1 offset:17920\n"
"v_mul_f32_e32 v20, s44, v20\n"
"v_mfma_f32_16x16x32_bf16 v[28:31], a[156:159], a[12:15], v[28:31]\n"
"ds_read_b128 v[40:43], v58 offset:8192\n"
"v_exp_f32_e32 v19, v19\n"
"v_mfma_f32_16x16x32_bf16 v[160:163], a[160:163], v[36:39], v[160:163]\n"
"ds_read_b128 v[44:47], v59 offset:9216\n"
"v_mul_f32_e32 v21, s44, v21\n"
"v_cvt_pk_bf16_f32 v33, v18, v19\n"
"v_mfma_f32_16x16x32_bf16 v[164:167], a[164:167], v[36:39], v[164:167]\n"
"ds_read_b128 v[48:51], v58 offset:10240\n"
"v_exp_f32_e32 v20, v20\n"
"v_mfma_f32_16x16x32_bf16 v[168:171], a[168:171], v[36:39], v[168:171]\n"
"ds_read_b128 a[100:103], v1 offset:16896\n"
"ds_read_b128 v[52:55], v59 offset:11264\n"
"v_mul_f32_e32 v22, s44, v22\n"
"v_mfma_f32_16x16x32_bf16 v[172:175], a[172:175], v[36:39], v[172:175]\n"
"v_exp_f32_e32 v21, v21\n"
"v_mfma_f32_16x16x32_bf16 v[176:179], a[176:179], v[36:39], v[176:179]\n"
"s_add_u32 m0, s48, 8192\n"
"v_mul_f32_e32 v23, s44, v23\n"
"v_cvt_pk_bf16_f32 v34, v20, v21\n"
"v_mfma_f32_16x16x32_bf16 v[180:183], a[180:183], v[36:39], v[180:183]\n"
"s_add_u32 s47, s45, 73728\n"
"v_exp_f32_e32 v22, v22\n"
"v_mfma_f32_16x16x32_bf16 v[184:187], a[184:187], v[36:39], v[184:187]\n"
"buffer_load_dwordx4 v6, s[24:27], s47 offen lds\n"
"v_mfma_f32_16x16x32_bf16 v[188:191], a[188:191], v[36:39], v[188:191]\n"
"v_exp_f32_e32 v23, v23\n"
"v_mfma_f32_16x16x32_bf16 a[240:243], a[224:227], v[36:39], a[240:243]\n"
"ds_read_b128 a[104:107], v1 offset:17408\n"
"v_cvt_pk_bf16_f32 v35, v22, v23\n"
"v_mfma_f32_16x16x32_bf16 v[16:19], a[128:131], a[16:19], 0\n"
"ds_read_b128 a[124:127], v2 offset:17920\n"
"v_mul_f32_e32 v24, s44, v24\n"
"v_mul_f32_e32 v25, s44, v25\n"
"v_mfma_f32_16x16x32_bf16 v[16:19], a[132:135], a[20:23], v[16:19]\n"
"v_exp_f32_e32 v24, v24\n"
"s_add_u32 s77, s60, 8192\n"
"v_mfma_f32_16x16x32_bf16 v[16:19], a[136:139], a[24:27], v[16:19]\n"
"ds_read_b128 a[112:115], v2 offset:16384\n"
"v_mul_f32_e32 v26, s44, v26\n"
"s_waitcnt lgkmcnt(7)\n"
"buffer_store_dwordx4 v[40:43], v9, s[40:43], s77 offen offset:0  sc1\n"
"v_mfma_f32_16x16x32_bf16 v[16:19], a[140:143], a[28:31], v[16:19]\n"
"v_exp_f32_e32 v25, v25\n"
"v_mfma_f32_16x16x32_bf16 v[20:23], a[144:147], a[16:19], 0\n"
"v_mul_f32_e32 v27, s44, v27\n"
"v_cvt_pk_bf16_f32 v36, v24, v25\n"
"v_mfma_f32_16x16x32_bf16 v[20:23], a[148:151], a[20:23], v[20:23]\n"
"v_exp_f32_e32 v26, v26\n"
"v_mfma_f32_16x16x32_bf16 v[20:23], a[152:155], a[24:27], v[20:23]\n"
"ds_read_b128 a[116:119], v2 offset:16896\n"
"v_mul_f32_e32 v28, s44, v28\n"
"s_waitcnt lgkmcnt(7)\n"
"buffer_store_dwordx4 v[44:47], v9, s[40:43], s77 offen offset:1024  sc1\n"
"v_mfma_f32_16x16x32_bf16 v[20:23], a[156:159], a[28:31], v[20:23]\n"
"v_exp_f32_e32 v27, v27\n"
"v_mfma_f32_16x16x32_bf16 v[192:195], a[160:163], v[32:35], v[192:195]\n"
"v_mul_f32_e32 v29, s44, v29\n"
"v_cvt_pk_bf16_f32 v37, v26, v27\n"
"v_mfma_f32_16x16x32_bf16 v[196:199], a[164:167], v[32:35], v[196:199]\n"
"v_exp_f32_e32 v28, v28\n"
"v_mfma_f32_16x16x32_bf16 v[200:203], a[168:171], v[32:35], v[200:203]\n"
"ds_read_b128 a[120:123], v2 offset:17408\n"
"v_mul_f32_e32 v30, s44, v30\n"
"s_waitcnt lgkmcnt(7)\n"
"buffer_store_dwordx4 v[48:51], v9, s[40:43], s77 offen offset:2048  sc1\n"
"v_mfma_f32_16x16x32_bf16 v[204:207], a[172:175], v[32:35], v[204:207]\n"
"v_exp_f32_e32 v29, v29\n"
"v_mfma_f32_16x16x32_bf16 v[208:211], a[176:179], v[32:35], v[208:211]\n"
"v_mul_f32_e32 v31, s44, v31\n"
"v_cvt_pk_bf16_f32 v38, v28, v29\n"
"v_mfma_f32_16x16x32_bf16 v[212:215], a[180:183], v[32:35], v[212:215]\n"
"s_mov_b32 s88, L_S5%=+0-L_code%=\n"
"v_exp_f32_e32 v30, v30\n"
"v_mfma_f32_16x16x32_bf16 v[216:219], a[184:187], v[32:35], v[216:219]\n"
"s_add_u32 m0, s48, 9216\n"
"buffer_load_dword v63, v62, s[84:87], s88 offen\n"
"v_mfma_f32_16x16x32_bf16 v[220:223], a[188:191], v[32:35], v[220:223]\n"
"s_add_u32 s47, s45, 73856\n"
"v_exp_f32_e32 v31, v31\n"
"v_mfma_f32_16x16x32_bf16 a[244:247], a[224:227], v[32:35], a[244:247]\n"
"buffer_load_dwordx4 v6, s[24:27], s47 offen lds\n"
"v_cvt_pk_bf16_f32 v39, v30, v31\n"
"s_waitcnt lgkmcnt(5)\n"
"buffer_store_dwordx4 v[52:55], v9, s[40:43], s77 offen offset:3072  sc1\n"
"v_mfma_f32_16x16x32_bf16 v[24:27], a[128:131], a[32:35], 0\n"
"ds_read_b64_tr_b16 a[166:167], v14 offset:16384\n"
"v_mul_f32_e32 v16, s44, v16\n"
"v_mul_f32_e32 v17, s44, v17\n"
"v_mfma_f32_16x16x32_bf16 v[24:27], a[132:135], a[36:39], v[24:27]\n"
"ds_read_b64_tr_b16 a[168:169], v3 offset:16896\n"
"v_exp_f32_e32 v16, v16\n"
"v_mfma_f32_16x16x32_bf16 v[24:27], a[136:139], a[40:43], v[24:27]\n"
"ds_read_b64_tr_b16 a[160:161], v3 offset:16384\n"
"ds_read_b64_tr_b16 a[170:171], v4 offset:16896\n"
"v_mul_f32_e32 v18, s44, v18\n"
"v_mfma_f32_16x16x32_bf16 v[24:27], a[140:143], a[44:47], v[24:27]\n"
"v_exp_f32_e32 v17, v17\n"
"v_mfma_f32_16x16x32_bf16 v[28:31], a[144:147], a[32:35], 0\n"
"v_mul_f32_e32 v19, s44, v19\n"
"v_cvt_pk_bf16_f32 v32, v16, v17\n"
"v_mfma_f32_16x16x32_bf16 v[28:31], a[148:151], a[36:39], v[28:31]\n"
"v_exp_f32_e32 v18, v18\n"
"v_mfma_f32_16x16x32_bf16 v[28:31], a[152:155], a[40:43], v[28:31]\n"
"ds_read_b64_tr_b16 a[162:163], v4 offset:16384\n"
"v_mul_f32_e32 v20, s44, v20\n"
"v_mfma_f32_16x16x32_bf16 v[28:31], a[156:159], a[44:47], v[28:31]\n"
"v_exp_f32_e32 v19, v19\n"
"v_mfma_f32_16x16x32_bf16 v[64:67], a[192:195], v[36:39], v[64:67]\n"
"s_add_u32 m0, s49, 8192\n"
"v_mul_f32_e32 v21, s44, v21\n"
"v_cvt_pk_bf16_f32 v33, v18, v19\n"
"v_mfma_f32_16x16x32_bf16 v[68:71], a[196:199], v[36:39], v[68:71]\n"
"s_add_u32 s61, s58, 8192\n"
"v_exp_f32_e32 v20, v20\n"
"v_mfma_f32_16x16x32_bf16 v[72:75], a[200:203], v[36:39], v[72:75]\n"
"buffer_load_dwordx4 v7, s[36:39], s61 offen  nt lds\n"
"v_mul_f32_e32 v22, s44, v22\n"
"v_mfma_f32_16x16x32_bf16 v[76:79], a[204:207], v[36:39], v[76:79]\n"
"v_exp_f32_e32 v21, v21\n"
"v_mfma_f32_16x16x32_bf16 v[80:83], a[208:211], v[36:39], v[80:83]\n"
"s_add_u32 m0, s48, 40960\n"
"v_mul_f32_e32 v23, s44, v23\n"
"v_cvt_pk_bf16_f32 v34, v20, v21\n"
"v_mfma_f32_16x16x32_bf16 v[84:87], a[212:215], v[36:39], v[84:87]\n"
"s_add_u32 s76, s45, 73728\n"
"v_exp_f32_e32 v22, v22\n"
"v_mfma_f32_16x16x32_bf16 v[88:91], a[216:219], v[36:39], v[88:91]\n"
"buffer_load_dwordx4 v6, s[28:31], s76 offen lds\n"
"v_mfma_f32_16x16x32_bf16 v[92:95], a[220:223], v[36:39], v[92:95]\n"
"v_exp_f32_e32 v23, v23\n"
"v_mfma_f32_16x16x32_bf16 a[228:231], a[224:227], v[36:39], a[228:231]\n"
"ds_read_b64_tr_b16 a[164:165], v11 offset:16384\n"
"v_cvt_pk_bf16_f32 v35, v22, v23\n"
"v_mfma_f32_16x16x32_bf16 v[16:19], a[128:131], a[48:51], 0\n"
"ds_read_b64_tr_b16 a[180:181], v11 offset:17408\n"
"v_mul_f32_e32 v24, s44, v24\n"
"v_mul_f32_e32 v25, s44, v25\n"
"v_mfma_f32_16x16x32_bf16 v[16:19], a[132:135], a[52:55], v[16:19]\n"
"ds_read_b64_tr_b16 a[182:183], v14 offset:17408\n"
"v_exp_f32_e32 v24, v24\n"
"v_mfma_f32_16x16x32_bf16 v[16:19], a[136:139], a[56:59], v[16:19]\n"
"ds_read_b64_tr_b16 a[172:173], v11 offset:16896\n"
"v_mul_f32_e32 v26, s44, v26\n"
"v_mfma_f32_16x16x32_bf16 v[16:19], a[140:143], a[60:63], v[16:19]\n"
"v_exp_f32_e32 v25, v25\n"
"v_mfma_f32_16x16x32_bf16 v[20:23], a[144:147], a[48:51], 0\n"
"v_mul_f32_e32 v27, s44, v27\n"
"v_cvt_pk_bf16_f32 v36, v24, v25\n"
"v_mfma_f32_16x16x32_bf16 v[20:23], a[148:151], a[52:55], v[20:23]\n"
"v_exp_f32_e32 v26, v26\n"
"v_mfma_f32_16x16x32_bf16 v[20:23], a[152:155], a[56:59], v[20:23]\n"
"ds_read_b64_tr_b16 a[174:175], v14 offset:16896\n"
"v_mul_f32_e32 v28, s44, v28\n"
"v_mfma_f32_16x16x32_bf16 v[20:23], a[156:159], a[60:63], v[20:23]\n"
"v_exp_f32_e32 v27, v27\n"
"v_mfma_f32_16x16x32_bf16 v[96:99], a[192:195], v[32:35], v[96:99]\n"
"v_mul_f32_e32 v29, s44, v29\n"
"v_cvt_pk_bf16_f32 v37, v26, v27\n"
"v_mfma_f32_16x16x32_bf16 v[100:103], a[196:199], v[32:35], v[100:103]\n"
"v_exp_f32_e32 v28, v28\n"
"v_mfma_f32_16x16x32_bf16 v[104:107], a[200:203], v[32:35], v[104:107]\n"
"ds_read_b64_tr_b16 a[176:177], v3 offset:17408\n"
"v_mul_f32_e32 v30, s44, v30\n"
"v_mfma_f32_16x16x32_bf16 v[108:111], a[204:207], v[32:35], v[108:111]\n"
"v_exp_f32_e32 v29, v29\n"
"v_mfma_f32_16x16x32_bf16 v[112:115], a[208:211], v[32:35], v[112:115]\n"
"s_add_u32 m0, s48, 41984\n"
"v_mul_f32_e32 v31, s44, v31\n"
"v_cvt_pk_bf16_f32 v38, v28, v29\n"
"v_mfma_f32_16x16x32_bf16 v[116:119], a[212:215], v[32:35], v[116:119]\n"
"s_add_u32 s76, s45, 73856\n"
"v_exp_f32_e32 v30, v30\n"
"v_mfma_f32_16x16x32_bf16 v[120:123], a[216:219], v[32:35], v[120:123]\n"
"buffer_load_dwordx4 v6, s[28:31], s76 offen lds\n"
"v_mfma_f32_16x16x32_bf16 v[124:127], a[220:223], v[32:35], v[124:127]\n"
"v_exp_f32_e32 v31, v31\n"
"v_mfma_f32_16x16x32_bf16 a[232:235], a[224:227], v[32:35], a[232:235]\n"
"ds_read_b64_tr_b16 a[178:179], v4 offset:17408\n"
"v_cvt_pk_bf16_f32 v39, v30, v31\n"
"v_mfma_f32_16x16x32_bf16 v[24:27], a[128:131], a[64:67], 0\n"
"ds_read_b64_tr_b16 a[190:191], v14 offset:17920\n"
"v_mul_f32_e32 v16, s44, v16\n"
"v_mul_f32_e32 v17, s44, v17\n"
"v_mfma_f32_16x16x32_bf16 v[24:27], a[132:135], a[68:71], v[24:27]\n"
"v_exp_f32_e32 v16, v16\n"
"v_mfma_f32_16x16x32_bf16 v[24:27], a[136:139], a[72:75], v[24:27]\n"
"ds_read_b64_tr_b16 a[184:185], v3 offset:17920\n"
"v_mul_f32_e32 v18, s44, v18\n"
"v_mfma_f32_16x16x32_bf16 v[24:27], a[140:143], a[76:79], v[24:27]\n"
"v_exp_f32_e32 v17, v17\n"
"v_mfma_f32_16x16x32_bf16 v[28:31], a[144:147], a[64:67], 0\n"
"v_mul_f32_e32 v19, s44, v19\n"
"v_cvt_pk_bf16_f32 v32, v16, v17\n"
"v_mfma_f32_16x16x32_bf16 v[28:31], a[148:151], a[68:71], v[28:31]\n"
"v_exp_f32_e32 v18, v18\n"
"v_mfma_f32_16x16x32_bf16 v[28:31], a[152:155], a[72:75], v[28:31]\n"
"ds_read_b64_tr_b16 a[186:187], v4 offset:17920\n"
"v_mul_f32_e32 v20, s44, v20\n"
"v_mfma_f32_16x16x32_bf16 v[28:31], a[156:159], a[76:79], v[28:31]\n"
"v_exp_f32_e32 v19, v19\n"
"v_mfma_f32_16x16x32_bf16 v[128:131], a[192:195], v[36:39], v[128:131]\n"
"v_mul_f32_e32 v21, s44, v21\n"
"v_cvt_pk_bf16_f32 v33, v18, v19\n"
"v_mfma_f32_16x16x32_bf16 v[132:135], a[196:199], v[36:39], v[132:135]\n"
"v_exp_f32_e32 v20, v20\n"
"v_mfma_f32_16x16x32_bf16 v[136:139], a[200:203], v[36:39], v[136:139]\n"
"ds_read_b64_tr_b16 a[188:189], v11 offset:17920\n"
"v_mul_f32_e32 v22, s44, v22\n"
"v_mfma_f32_16x16x32_bf16 v[140:143], a[204:207], v[36:39], v[140:143]\n"
"v_exp_f32_e32 v21, v21\n"
"v_mfma_f32_16x16x32_bf16 v[144:147], a[208:211], v[36:39], v[144:147]\n"
"s_add_u32 m0, s49, 9216\n"
"v_mul_f32_e32 v23, s44, v23\n"
"v_cvt_pk_bf16_f32 v34, v20, v21\n"
"v_mfma_f32_16x16x32_bf16 v[148:151], a[212:215], v[36:39], v[148:151]\n"
"s_add_u32 s61, s58, 8320\n"
"v_exp_f32_e32 v22, v22\n"
"v_mfma_f32_16x16x32_bf16 v[152:155], a[216:219], v[36:39], v[152:155]\n"
"buffer_load_dwordx4 v7, s[36:39], s61 offen  nt lds\n"
"v_mfma_f32_16x16x32_bf16 v[156:159], a[220:223], v[36:39], v[156:159]\n"
"v_exp_f32_e32 v23, v23\n"
"v_mfma_f32_16x16x32_bf16 a[236:239], a[224:227], v[36:39], a[236:239]\n"
"v_cvt_pk_bf16_f32 v35, v22, v23\n"
"v_mfma_f32_16x16x32_bf16 v[16:19], a[96:99], a[0:3], 0\n"
"v_mul_f32_e32 v24, s44, v24\n"
"v_mul_f32_e32 v25, s44, v25\n"
"v_mfma_f32_16x16x32_bf16 v[16:19], a[100:103], a[4:7], v[16:19]\n"
"s_waitcnt lgkmcnt(15)\n"
"v_exp_f32_e32 v24, v24\n"
"v_mfma_f32_16x16x32_bf16 v[16:19], a[104:107], a[8:11], v[16:19]\n"
"v_mul_f32_e32 v26, s44, v26\n"
"v_mfma_f32_16x16x32_bf16 v[16:19], a[108:111], a[12:15], v[16:19]\n"
"v_exp_f32_e32 v25, v25\n"
"v_mfma_f32_16x16x32_bf16 v[20:23], a[112:115], a[0:3], 0\n"
"v_mul_f32_e32 v27, s44, v27\n"
"v_cvt_pk_bf16_f32 v36, v24, v25\n"
"v_mfma_f32_16x16x32_bf16 v[20:23], a[116:119], a[4:7], v[20:23]\n"
"s_waitcnt vmcnt(18) lgkmcnt(0)\n"
"s_barrier\n"
"v_exp_f32_e32 v26, v26\n"
"v_mfma_f32_16x16x32_bf16 v[20:23], a[120:123], a[8:11], v[20:23]\n"
"ds_read_b128 a[128:131], v1 offset:24576\n"
"ds_read_b128 a[140:143], v1 offset:26112\n"
"v_mul_f32_e32 v28, s44, v28\n"
"v_mfma_f32_16x16x32_bf16 v[20:23], a[124:127], a[12:15], v[20:23]\n"
"v_exp_f32_e32 v27, v27\n"
"v_mfma_f32_16x16x32_bf16 v[160:163], a[192:195], v[32:35], v[160:163]\n"
"v_mul_f32_e32 v29, s44, v29\n"
"v_cvt_pk_bf16_f32 v37, v26, v27\n"
"v_mfma_f32_16x16x32_bf16 v[164:167], a[196:199], v[32:35], v[164:167]\n"
"v_exp_f32_e32 v28, v28\n"
"v_mfma_f32_16x16x32_bf16 v[168:171], a[200:203], v[32:35], v[168:171]\n"
"ds_read_b128 a[132:135], v1 offset:25088\n"
"v_mul_f32_e32 v30, s44, v30\n"
"v_mfma_f32_16x16x32_bf16 v[172:175], a[204:207], v[32:35], v[172:175]\n"
"v_exp_f32_e32 v29, v29\n"
"v_mfma_f32_16x16x32_bf16 v[176:179], a[208:211], v[32:35], v[176:179]\n"
"s_add_u32 m0, s48, 16384\n"
"v_mul_f32_e32 v31, s44, v31\n"
"v_cvt_pk_bf16_f32 v38, v28, v29\n"
"v_mfma_f32_16x16x32_bf16 v[180:183], a[212:215], v[32:35], v[180:183]\n"
"s_add_u32 s47, s45, 81920\n"
"v_exp_f32_e32 v30, v30\n"
"v_mfma_f32_16x16x32_bf16 v[184:187], a[216:219], v[32:35], v[184:187]\n"
"buffer_load_dwordx4 v6, s[24:27], s47 offen lds\n"
"v_mfma_f32_16x16x32_bf16 v[188:191], a[220:223], v[32:35], v[188:191]\n"
"v_exp_f32_e32 v31, v31\n"
"v_mfma_f32_16x16x32_bf16 a[240:243], a[224:227], v[32:35], a[240:243]\n"
"ds_read_b128 a[136:139], v1 offset:25600\n"
"v_cvt_pk_bf16_f32 v39, v30, v31\n"
"v_mfma_f32_16x16x32_bf16 v[24:27], a[96:99], a[16:19], 0\n"
"v_mul_f32_e32 v16, s44, v16\n"
"v_mul_f32_e32 v17, s44, v17\n"
"v_mfma_f32_16x16x32_bf16 v[24:27], a[100:103], a[20:23], v[24:27]\n"
"v_exp_f32_e32 v16, v16\n"
"v_mfma_f32_16x16x32_bf16 v[24:27], a[104:107], a[24:27], v[24:27]\n"
"ds_read_b128 a[144:147], v2 offset:24576\n"
"v_mul_f32_e32 v18, s44, v18\n"
"v_mfma_f32_16x16x32_bf16 v[24:27], a[108:111], a[28:31], v[24:27]\n"
"v_exp_f32_e32 v17, v17\n"
"v_mfma_f32_16x16x32_bf16 v[28:31], a[112:115], a[16:19], 0\n"
"v_mul_f32_e32 v19, s44, v19\n"
"v_cvt_pk_bf16_f32 v32, v16, v17\n"
"v_mfma_f32_16x16x32_bf16 v[28:31], a[116:119], a[20:23], v[28:31]\n"
"v_exp_f32_e32 v18, v18\n"
"v_mfma_f32_16x16x32_bf16 v[28:31], a[120:123], a[24:27], v[28:31]\n"
"ds_read_b128 a[148:151], v2 offset:25088\n"
"v_mul_f32_e32 v20, s44, v20\n"
"v_mfma_f32_16x16x32_bf16 v[28:31], a[124:127], a[28:31], v[28:31]\n"
"v_exp_f32_e32 v19, v19\n"
"v_mfma_f32_16x16x32_bf16 v[192:195], a[192:195], v[36:39], v[192:195]\n"
"v_mul_f32_e32 v21, s44, v21\n"
"v_cvt_pk_bf16_f32 v33, v18, v19\n"
"v_mfma_f32_16x16x32_bf16 v[196:199], a[196:199], v[36:39], v[196:199]\n"
"v_exp_f32_e32 v20, v20\n"
"v_mfma_f32_16x16x32_bf16 v[200:203], a[200:203], v[36:39], v[200:203]\n"
"ds_read_b128 a[152:155], v2 offset:25600\n"
"v_mul_f32_e32 v22, s44, v22\n"
"v_mfma_f32_16x16x32_bf16 v[204:207], a[204:207], v[36:39], v[204:207]\n"
"v_exp_f32_e32 v21, v21\n"
"v_mfma_f32_16x16x32_bf16 v[208:211], a[208:211], v[36:39], v[208:211]\n"
"s_add_u32 m0, s48, 17408\n"
"v_mul_f32_e32 v23, s44, v23\n"
"v_cvt_pk_bf16_f32 v34, v20, v21\n"
"v_mfma_f32_16x16x32_bf16 v[212:215], a[212:215], v[36:39], v[212:215]\n"
"s_add_u32 s47, s45, 82048\n"
"v_exp_f32_e32 v22, v22\n"
"v_mfma_f32_16x16x32_bf16 v[216:219], a[216:219], v[36:39], v[216:219]\n"
"buffer_load_dwordx4 v6, s[24:27], s47 offen lds\n"
"v_mfma_f32_16x16x32_bf16 v[220:223], a[220:223], v[36:39], v[220:223]\n"
"v_exp_f32_e32 v23, v23\n"
"v_mfma_f32_16x16x32_bf16 a[244:247], a[224:227], v[36:39], a[244:247]\n"
"ds_read_b128 a[156:159], v2 offset:26112\n"
"v_cvt_pk_bf16_f32 v35, v22, v23\n"
"v_mfma_f32_16x16x32_bf16 v[16:19], a[96:99], a[32:35], 0\n"
"ds_read_b64_tr_b16 a[198:199], v14 offset:24576\n"
"v_mul_f32_e32 v24, s44, v24\n"
"v_mul_f32_e32 v25, s44, v25\n"
"v_mfma_f32_16x16x32_bf16 v[16:19], a[100:103], a[36:39], v[16:19]\n"
"ds_read_b64_tr_b16 a[200:201], v3 offset:25088\n"
"v_exp_f32_e32 v24, v24\n"
"v_mfma_f32_16x16x32_bf16 v[16:19], a[104:107], a[40:43], v[16:19]\n"
"ds_read_b64_tr_b16 a[192:193], v3 offset:24576\n"
"ds_read_b64_tr_b16 a[202:203], v4 offset:25088\n"
"v_mul_f32_e32 v26, s44, v26\n"
"v_mfma_f32_16x16x32_bf16 v[16:19], a[108:111], a[44:47], v[16:19]\n"
"v_exp_f32_e32 v25, v25\n"
"v_mfma_f32_16x16x32_bf16 v[20:23], a[112:115], a[32:35], 0\n"
"v_mul_f32_e32 v27, s44, v27\n"
"v_cvt_pk_bf16_f32 v36, v24, v25\n"
"v_mfma_f32_16x16x32_bf16 v[20:23], a[116:119], a[36:39], v[20:23]\n"
"v_exp_f32_e32 v26, v26\n"
"v_mfma_f32_16x16x32_bf16 v[20:23], a[120:123], a[40:43], v[20:23]\n"
"ds_read_b64_tr_b16 a[194:195], v4 offset:24576\n"
"v_mul_f32_e32 v28, s44, v28\n"
"v_mfma_f32_16x16x32_bf16 v[20:23], a[124:127], a[44:47], v[20:23]\n"
"v_exp_f32_e32 v27, v27\n"
"v_mfma_f32_16x16x32_bf16 v[64:67], a[160:163], v[32:35], v[64:67]\n"
"s_add_u32 m0, s49, 10240\n"
"v_mul_f32_e32 v29, s44, v29\n"
"v_cvt_pk_bf16_f32 v37, v26, v27\n"
"v_mfma_f32_16x16x32_bf16 v[68:71], a[164:167], v[32:35], v[68:71]\n"
"s_add_u32 s61, s58, 8192\n"
"v_exp_f32_e32 v28, v28\n"
"v_mfma_f32_16x16x32_bf16 v[72:75], a[168:171], v[32:35], v[72:75]\n"
"buffer_load_dwordx4 v8, s[36:39], s61 offen  nt lds\n"
"v_mul_f32_e32 v30, s44, v30\n"
"v_mfma_f32_16x16x32_bf16 v[76:79], a[172:175], v[32:35], v[76:79]\n"
"v_exp_f32_e32 v29, v29\n"
"v_mfma_f32_16x16x32_bf16 v[80:83], a[176:179], v[32:35], v[80:83]\n"
"s_add_u32 m0, s48, 49152\n"
"v_mul_f32_e32 v31, s44, v31\n"
"v_cvt_pk_bf16_f32 v38, v28, v29\n"
"v_mfma_f32_16x16x32_bf16 v[84:87], a[180:183], v[32:35], v[84:87]\n"
"s_add_u32 s76, s45, 81920\n"
"v_exp_f32_e32 v30, v30\n"
"v_mfma_f32_16x16x32_bf16 v[88:91], a[184:187], v[32:35], v[88:91]\n"
"buffer_load_dwordx4 v6, s[28:31], s76 offen lds\n"
"v_mfma_f32_16x16x32_bf16 v[92:95], a[188:191], v[32:35], v[92:95]\n"
"v_exp_f32_e32 v31, v31\n"
"v_mfma_f32_16x16x32_bf16 a[228:231], a[224:227], v[32:35], a[228:231]\n"
"ds_read_b64_tr_b16 a[196:197], v11 offset:24576\n"
"v_cvt_pk_bf16_f32 v39, v30, v31\n"
"v_mfma_f32_16x16x32_bf16 v[24:27], a[96:99], a[48:51], 0\n"
"ds_read_b64_tr_b16 a[212:213], v11 offset:25600\n"
"v_mul_f32_e32 v16, s44, v16\n"
"v_mul_f32_e32 v17, s44, v17\n"
"v_mfma_f32_16x16x32_bf16 v[24:27], a[100:103], a[52:55], v[24:27]\n"
"ds_read_b64_tr_b16 a[214:215], v14 offset:25600\n"
"v_exp_f32_e32 v16, v16\n"
"v_mfma_f32_16x16x32_bf16 v[24:27], a[104:107], a[56:59], v[24:27]\n"
"ds_read_b64_tr_b16 a[204:205], v11 offset:25088\n"
"v_mul_f32_e32 v18, s44, v18\n"
"v_mfma_f32_16x16x32_bf16 v[24:27], a[108:111], a[60:63], v[24:27]\n"
"v_exp_f32_e32 v17, v17\n"
"v_mfma_f32_16x16x32_bf16 v[28:31], a[112:115], a[48:51], 0\n"
"v_mul_f32_e32 v19, s44, v19\n"
"v_cvt_pk_bf16_f32 v32, v16, v17\n"
"v_mfma_f32_16x16x32_bf16 v[28:31], a[116:119], a[52:55], v[28:31]\n"
"v_exp_f32_e32 v18, v18\n"
"v_mfma_f32_16x16x32_bf16 v[28:31], a[120:123], a[56:59], v[28:31]\n"
"ds_read_b64_tr_b16 a[206:207], v14 offset:25088\n"
"v_mul_f32_e32 v20, s44, v20\n"
"v_mfma_f32_16x16x32_bf16 v[28:31], a[124:127], a[60:63], v[28:31]\n"
"v_exp_f32_e32 v19, v19\n"
"v_mfma_f32_16x16x32_bf16 v[96:99], a[160:163], v[36:39], v[96:99]\n"
"v_mul_f32_e32 v21, s44, v21\n"
"v_cvt_pk_bf16_f32 v33, v18, v19\n"
"v_mfma_f32_16x16x32_bf16 v[100:103], a[164:167], v[36:39], v[100:103]\n"
"v_exp_f32_e32 v20, v20\n"
"v_mfma_f32_16x16x32_bf16 v[104:107], a[168:171], v[36:39], v[104:107]\n"
"ds_read_b64_tr_b16 a[208:209], v3 offset:25600\n"
"v_mul_f32_e32 v22, s44, v22\n"
"v_mfma_f32_16x16x32_bf16 v[108:111], a[172:175], v[36:39], v[108:111]\n"
"v_exp_f32_e32 v21, v21\n"
"v_mfma_f32_16x16x32_bf16 v[112:115], a[176:179], v[36:39], v[112:115]\n"
"s_add_u32 m0, s48, 50176\n"
"v_mul_f32_e32 v23, s44, v23\n"
"v_cvt_pk_bf16_f32 v34, v20, v21\n"
"v_mfma_f32_16x16x32_bf16 v[116:119], a[180:183], v[36:39], v[116:119]\n"
"s_add_u32 s76, s45, 82048\n"
"v_exp_f32_e32 v22, v22\n"
"v_mfma_f32_16x16x32_bf16 v[120:123], a[184:187], v[36:39], v[120:123]\n"
"buffer_load_dwordx4 v6, s[28:31], s76 offen lds\n"
"v_mfma_f32_16x16x32_bf16 v[124:127], a[188:191], v[36:39], v[124:127]\n"
"v_exp_f32_e32 v23, v23\n"
"v_mfma_f32_16x16x32_bf16 a[232:235], a[224:227], v[36:39], a[232:235]\n"
"ds_read_b64_tr_b16 a[210:211], v4 offset:25600\n"
"v_cvt_pk_bf16_f32 v35, v22, v23\n"
"v_mfma_f32_16x16x32_bf16 v[16:19], a[96:99], a[64:67], 0\n"
"ds_read_b64_tr_b16 a[222:223], v14 offset:26112\n"
"v_mul_f32_e32 v24, s44, v24\n"
"v_mul_f32_e32 v25, s44, v25\n"
"v_mfma_f32_16x16x32_bf16 v[16:19], a[100:103], a[68:71], v[16:19]\n"
"v_exp_f32_e32 v24, v24\n"
"v_mfma_f32_16x16x32_bf16 v[16:19], a[104:107], a[72:75], v[16:19]\n"
"ds_read_b64_tr_b16 a[216:217], v3 offset:26112\n"
"v_mul_f32_e32 v26, s44, v26\n"
"v_mfma_f32_16x16x32_bf16 v[16:19], a[108:111], a[76:79], v[16:19]\n"
"v_exp_f32_e32 v25, v25\n"
"v_mfma_f32_16x16x32_bf16 v[20:23], a[112:115], a[64:67], 0\n"
"v_mul_f32_e32 v27, s44, v27\n"
"v_cvt_pk_bf16_f32 v36, v24, v25\n"
"v_mfma_f32_16x16x32_bf16 v[20:23], a[116:119], a[68:71], v[20:23]\n"
"v_exp_f32_e32 v26, v26\n"
"v_mfma_f32_16x16x32_bf16 v[20:23], a[120:123], a[72:75], v[20:23]\n"
"ds_read_b64_tr_b16 a[218:219], v4 offset:26112\n"
"v_mul_f32_e32 v28, s44, v28\n"
"v_mfma_f32_16x16x32_bf16 v[20:23], a[124:127], a[76:79], v[20:23]\n"
"v_exp_f32_e32 v27, v27\n"
"v_mfma_f32_16x16x32_bf16 v[128:131], a[160:163], v[32:35], v[128:131]\n"
"v_mul_f32_e32 v29, s44, v29\n"
"v_cvt_pk_bf16_f32 v37, v26, v27\n"
"v_mfma_f32_16x16x32_bf16 v[132:135], a[164:167], v[32:35], v[132:135]\n"
"v_exp_f32_e32 v28, v28\n"
"v_mfma_f32_16x16x32_bf16 v[136:139], a[168:171], v[32:35], v[136:139]\n"
"ds_read_b64_tr_b16 a[220:221], v11 offset:26112\n"
"v_mul_f32_e32 v30, s44, v30\n"
"v_mfma_f32_16x16x32_bf16 v[140:143], a[172:175], v[32:35], v[140:143]\n"
"v_exp_f32_e32 v29, v29\n"
"v_mfma_f32_16x16x32_bf16 v[144:147], a[176:179], v[32:35], v[144:147]\n"
"s_add_u32 m0, s49, 11264\n"
"v_mul_f32_e32 v31, s44, v31\n"
"v_cvt_pk_bf16_f32 v38, v28, v29\n"
"v_mfma_f32_16x16x32_bf16 v[148:151], a[180:183], v[32:35], v[148:151]\n"
"s_add_u32 s61, s58, 8320\n"
"v_exp_f32_e32 v30, v30\n"
"v_mfma_f32_16x16x32_bf16 v[152:155], a[184:187], v[32:35], v[152:155]\n"
"buffer_load_dwordx4 v8, s[36:39], s61 offen  nt lds\n"
"v_mfma_f32_16x16x32_bf16 v[156:159], a[188:191], v[32:35], v[156:159]\n"
"v_exp_f32_e32 v31, v31\n"
"v_mfma_f32_16x16x32_bf16 a[236:239], a[224:227], v[32:35], a[236:239]\n"
"s_waitcnt lgkmcnt(15)\n"
"v_cvt_pk_bf16_f32 v39, v30, v31\n"
"v_mfma_f32_16x16x32_bf16 v[24:27], a[128:131], a[0:3], 0\n"
"v_mul_f32_e32 v16, s44, v16\n"
"v_mul_f32_e32 v17, s44, v17\n"
"v_mfma_f32_16x16x32_bf16 v[24:27], a[132:135], a[4:7], v[24:27]\n"
"v_exp_f32_e32 v16, v16\n"
"v_mfma_f32_16x16x32_bf16 v[24:27], a[136:139], a[8:11], v[24:27]\n"
"v_mul_f32_e32 v18, s44, v18\n"
"v_mfma_f32_16x16x32_bf16 v[24:27], a[140:143], a[12:15], v[24:27]\n"
"v_exp_f32_e32 v17, v17\n"
"v_mfma_f32_16x16x32_bf16 v[28:31], a[144:147], a[0:3], 0\n"
"v_mul_f32_e32 v19, s44, v19\n"
"v_cvt_pk_bf16_f32 v32, v16, v17\n"
"v_mfma_f32_16x16x32_bf16 v[28:31], a[148:151], a[4:7], v[28:31]\n"
"s_waitcnt vmcnt(18) lgkmcnt(0)\n"
"s_barrier\n"
"v_exp_f32_e32 v18, v18\n"
"v_mfma_f32_16x16x32_bf16 v[28:31], a[152:155], a[8:11], v[28:31]\n"
"ds_read_b128 a[96:99], v1 offset:0\n"
"ds_read_b128 a[108:111], v1 offset:1536\n"
"v_mul_f32_e32 v20, s44, v20\n"
"v_mfma_f32_16x16x32_bf16 v[28:31], a[156:159], a[12:15], v[28:31]\n"
"ds_read_b128 v[40:43], v58 offset:12288\n"
"v_exp_f32_e32 v19, v19\n"
"v_mfma_f32_16x16x32_bf16 v[160:163], a[160:163], v[36:39], v[160:163]\n"
"ds_read_b128 v[44:47], v59 offset:13312\n"
"v_mul_f32_e32 v21, s44, v21\n"
"v_cvt_pk_bf16_f32 v33, v18, v19\n"
"v_mfma_f32_16x16x32_bf16 v[164:167], a[164:167], v[36:39], v[164:167]\n"
"ds_read_b128 v[48:51], v58 offset:14336\n"
"v_exp_f32_e32 v20, v20\n"
"v_mfma_f32_16x16x32_bf16 v[168:171], a[168:171], v[36:39], v[168:171]\n"
"ds_read_b128 a[100:103], v1 offset:512\n"
"ds_read_b128 v[52:55], v59 offset:15360\n"
"v_mul_f32_e32 v22, s44, v22\n"
"v_mfma_f32_16x16x32_bf16 v[172:175], a[172:175], v[36:39], v[172:175]\n"
"v_exp_f32_e32 v21, v21\n"
"v_mfma_f32_16x16x32_bf16 v[176:179], a[176:179], v[36:39], v[176:179]\n"
"s_add_u32 m0, s48, 24576\n"
"v_mul_f32_e32 v23, s44, v23\n"
"v_cvt_pk_bf16_f32 v34, v20, v21\n"
"v_mfma_f32_16x16x32_bf16 v[180:183], a[180:183], v[36:39], v[180:183]\n"
"s_add_u32 s47, s45, 90112\n"
"v_exp_f32_e32 v22, v22\n"
"v_mfma_f32_16x16x32_bf16 v[184:187], a[184:187], v[36:39], v[184:187]\n"
"buffer_load_dwordx4 v6, s[24:27], s47 offen lds\n"
"v_mfma_f32_16x16x32_bf16 v[188:191], a[188:191], v[36:39], v[188:191]\n"
"v_exp_f32_e32 v23, v23\n"
"v_mfma_f32_16x16x32_bf16 a[240:243], a[224:227], v[36:39], a[240:243]\n"
"ds_read_b128 a[104:107], v1 offset:1024\n"
"v_cvt_pk_bf16_f32 v35, v22, v23\n"
"v_mfma_f32_16x16x32_bf16 v[16:19], a[128:131], a[16:19], 0\n"
"ds_read_b128 a[124:127], v2 offset:1536\n"
"v_mul_f32_e32 v24, s44, v24\n"
"v_mul_f32_e32 v25, s44, v25\n"
"v_mfma_f32_16x16x32_bf16 v[16:19], a[132:135], a[20:23], v[16:19]\n"
"v_exp_f32_e32 v24, v24\n"
"s_add_u32 s77, s60, 12288\n"
"v_mfma_f32_16x16x32_bf16 v[16:19], a[136:139], a[24:27], v[16:19]\n"
"ds_read_b128 a[112:115], v2 offset:0\n"
"v_mul_f32_e32 v26, s44, v26\n"
"s_waitcnt lgkmcnt(7)\n"
"buffer_store_dwordx4 v[40:43], v9, s[40:43], s77 offen offset:0  sc1\n"
"v_mfma_f32_16x16x32_bf16 v[16:19], a[140:143], a[28:31], v[16:19]\n"
"v_exp_f32_e32 v25, v25\n"
"v_mfma_f32_16x16x32_bf16 v[20:23], a[144:147], a[16:19], 0\n"
"v_mul_f32_e32 v27, s44, v27\n"
"v_cvt_pk_bf16_f32 v36, v24, v25\n"
"v_mfma_f32_16x16x32_bf16 v[20:23], a[148:151], a[20:23], v[20:23]\n"
"v_exp_f32_e32 v26, v26\n"
"v_mfma_f32_16x16x32_bf16 v[20:23], a[152:155], a[24:27], v[20:23]\n"
"ds_read_b128 a[116:119], v2 offset:512\n"
"v_mul_f32_e32 v28, s44, v28\n"
"s_waitcnt lgkmcnt(7)\n"
"buffer_store_dwordx4 v[44:47], v9, s[40:43], s77 offen offset:1024  sc1\n"
"v_mfma_f32_16x16x32_bf16 v[20:23], a[156:159], a[28:31], v[20:23]\n"
"v_exp_f32_e32 v27, v27\n"
"v_mfma_f32_16x16x32_bf16 v[192:195], a[160:163], v[32:35], v[192:195]\n"
"v_mul_f32_e32 v29, s44, v29\n"
"v_cvt_pk_bf16_f32 v37, v26, v27\n"
"v_mfma_f32_16x16x32_bf16 v[196:199], a[164:167], v[32:35], v[196:199]\n"
"v_exp_f32_e32 v28, v28\n"
"v_mfma_f32_16x16x32_bf16 v[200:203], a[168:171], v[32:35], v[200:203]\n"
"ds_read_b128 a[120:123], v2 offset:1024\n"
"v_mul_f32_e32 v30, s44, v30\n"
"s_waitcnt lgkmcnt(7)\n"
"buffer_store_dwordx4 v[48:51], v9, s[40:43], s77 offen offset:2048  sc1\n"
"v_mfma_f32_16x16x32_bf16 v[204:207], a[172:175], v[32:35], v[204:207]\n"
"v_exp_f32_e32 v29, v29\n"
"v_mfma_f32_16x16x32_bf16 v[208:211], a[176:179], v[32:35], v[208:211]\n"
"v_mul_f32_e32 v31, s44, v31\n"
"v_cvt_pk_bf16_f32 v38, v28, v29\n"
"v_mfma_f32_16x16x32_bf16 v[212:215], a[180:183], v[32:35], v[212:215]\n"
"s_mov_b32 s88, L_T56%=+0-L_code%=\n"
"v_exp_f32_e32 v30, v30\n"
"v_mfma_f32_16x16x32_bf16 v[216:219], a[184:187], v[32:35], v[216:219]\n"
"s_add_u32 m0, s48, 25600\n"
"buffer_load_dword v63, v62, s[84:87], s88 offen\n"
"v_mfma_f32_16x16x32_bf16 v[220:223], a[188:191], v[32:35], v[220:223]\n"
"s_add_u32 s47, s45, 90240\n"
"v_exp_f32_e32 v31, v31\n"
"v_mfma_f32_16x16x32_bf16 a[244:247], a[224:227], v[32:35], a[244:247]\n"
"buffer_load_dwordx4 v6, s[24:27], s47 offen lds\n"
"v_cvt_pk_bf16_f32 v39, v30, v31\n"
"s_waitcnt lgkmcnt(5)\n"
"buffer_store_dwordx4 v[52:55], v9, s[40:43], s77 offen offset:3072  sc1\n"
"v_mfma_f32_16x16x32_bf16 v[24:27], a[128:131], a[32:35], 0\n"
"ds_read_b64_tr_b16 a[166:167], v14 offset:0\n"
"v_mul_f32_e32 v16, s44, v16\n"
"v_mul_f32_e32 v17, s44, v17\n"
"v_mfma_f32_16x16x32_bf16 v[24:27], a[132:135], a[36:39], v[24:27]\n"
"ds_read_b64_tr_b16 a[168:169], v3 offset:512\n"
"v_exp_f32_e32 v16, v16\n"
"v_mfma_f32_16x16x32_bf16 v[24:27], a[136:139], a[40:43], v[24:27]\n"
"ds_read_b64_tr_b16 a[160:161], v3 offset:0\n"
"ds_read_b64_tr_b16 a[170:171], v4 offset:512\n"
"v_mul_f32_e32 v18, s44, v18\n"
"v_mfma_f32_16x16x32_bf16 v[24:27], a[140:143], a[44:47], v[24:27]\n"
"v_exp_f32_e32 v17, v17\n"
"v_mfma_f32_16x16x32_bf16 v[28:31], a[144:147], a[32:35], 0\n"
"v_mul_f32_e32 v19, s44, v19\n"
"v_cvt_pk_bf16_f32 v32, v16, v17\n"
"v_mfma_f32_16x16x32_bf16 v[28:31], a[148:151], a[36:39], v[28:31]\n"
"v_exp_f32_e32 v18, v18\n"
"v_mfma_f32_16x16x32_bf16 v[28:31], a[152:155], a[40:43], v[28:31]\n"
"ds_read_b64_tr_b16 a[162:163], v4 offset:0\n"
"v_mul_f32_e32 v20, s44, v20\n"
"v_mfma_f32_16x16x32_bf16 v[28:31], a[156:159], a[44:47], v[28:31]\n"
"v_exp_f32_e32 v19, v19\n"
"v_mfma_f32_16x16x32_bf16 v[64:67], a[192:195], v[36:39], v[64:67]\n"
"s_add_u32 m0, s49, 12288\n"
"v_mul_f32_e32 v21, s44, v21\n"
"v_cvt_pk_bf16_f32 v33, v18, v19\n"
"v_mfma_f32_16x16x32_bf16 v[68:71], a[196:199], v[36:39], v[68:71]\n"
"s_add_u32 s61, s58, 12288\n"
"v_exp_f32_e32 v20, v20\n"
"v_mfma_f32_16x16x32_bf16 v[72:75], a[200:203], v[36:39], v[72:75]\n"
"buffer_load_dwordx4 v7, s[36:39], s61 offen  nt lds\n"
"v_mul_f32_e32 v22, s44, v22\n"
"v_mfma_f32_16x16x32_bf16 v[76:79], a[204:207], v[36:39], v[76:79]\n"
"v_exp_f32_e32 v21, v21\n"
"v_mfma_f32_16x16x32_bf16 v[80:83], a[208:211], v[36:39], v[80:83]\n"
"s_add_u32 m0, s48, 57344\n"
"v_mul_f32_e32 v23, s44, v23\n"
"v_cvt_pk_bf16_f32 v34, v20, v21\n"
"v_mfma_f32_16x16x32_bf16 v[84:87], a[212:215], v[36:39], v[84:87]\n"
"s_add_u32 s76, s45, 90112\n"
"v_exp_f32_e32 v22, v22\n"
"v_mfma_f32_16x16x32_bf16 v[88:91], a[216:219], v[36:39], v[88:91]\n"
"buffer_load_dwordx4 v6, s[28:31], s76 offen lds\n"
"v_mfma_f32_16x16x32_bf16 v[92:95], a[220:223], v[36:39], v[92:95]\n"
"v_exp_f32_e32 v23, v23\n"
"v_mfma_f32_16x16x32_bf16 a[228:231], a[224:227], v[36:39], a[228:231]\n"
"ds_read_b64_tr_b16 a[164:165], v11 offset:0\n"
"v_cvt_pk_bf16_f32 v35, v22, v23\n"
"v_mfma_f32_16x16x32_bf16 v[16:19], a[128:131], a[48:51], 0\n"
"ds_read_b64_tr_b16 a[180:181], v11 offset:1024\n"
"v_mul_f32_e32 v24, s44, v24\n"
"v_mul_f32_e32 v25, s44, v25\n"
"v_mfma_f32_16x16x32_bf16 v[16:19], a[132:135], a[52:55], v[16:19]\n"
"ds_read_b64_tr_b16 a[182:183], v14 offset:1024\n"
"v_exp_f32_e32 v24, v24\n"
"v_mfma_f32_16x16x32_bf16 v[16:19], a[136:139], a[56:59], v[16:19]\n"
"ds_read_b64_tr_b16 a[172:173], v11 offset:512\n"
"v_mul_f32_e32 v26, s44, v26\n"
"v_mfma_f32_16x16x32_bf16 v[16:19], a[140:143], a[60:63], v[16:19]\n"
"v_exp_f32_e32 v25, v25\n"
"v_mfma_f32_16x16x32_bf16 v[20:23], a[144:147], a[48:51], 0\n"
"v_mul_f32_e32 v27, s44, v27\n"
"v_cvt_pk_bf16_f32 v36, v24, v25\n"
"v_mfma_f32_16x16x32_bf16 v[20:23], a[148:151], a[52:55], v[20:23]\n"
"v_exp_f32_e32 v26, v26\n"
"v_mfma_f32_16x16x32_bf16 v[20:23], a[152:155], a[56:59], v[20:23]\n"
"ds_read_b64_tr_b16 a[174:175], v14 offset:512\n"
"v_mul_f32_e32 v28, s44, v28\n"
"v_mfma_f32_16x16x32_bf16 v[20:23], a[156:159], a[60:63], v[20:23]\n"
"v_exp_f32_e32 v27, v27\n"
"v_mfma_f32_16x16x32_bf16 v[96:99], a[192:195], v[32:35], v[96:99]\n"
"v_mul_f32_e32 v29, s44, v29\n"
"v_cvt_pk_bf16_f32 v37, v26, v27\n"
"v_mfma_f32_16x16x32_bf16 v[100:103], a[196:199], v[32:35], v[100:103]\n"
"v_exp_f32_e32 v28, v28\n"
"v_mfma_f32_16x16x32_bf16 v[104:107], a[200:203], v[32:35], v[104:107]\n"
"ds_read_b64_tr_b16 a[176:177], v3 offset:1024\n"
"v_mul_f32_e32 v30, s44, v30\n"
"v_mfma_f32_16x16x32_bf16 v[108:111], a[204:207], v[32:35], v[108:111]\n"
"v_exp_f32_e32 v29, v29\n"
"v_mfma_f32_16x16x32_bf16 v[112:115], a[208:211], v[32:35], v[112:115]\n"
"s_add_u32 m0, s48, 58368\n"
"v_mul_f32_e32 v31, s44, v31\n"
"v_cvt_pk_bf16_f32 v38, v28, v29\n"
"v_mfma_f32_16x16x32_bf16 v[116:119], a[212:215], v[32:35], v[116:119]\n"
"s_add_u32 s76, s45, 90240\n"
"v_exp_f32_e32 v30, v30\n"
"v_mfma_f32_16x16x32_bf16 v[120:123], a[216:219], v[32:35], v[120:123]\n"
"buffer_load_dwordx4 v6, s[28:31], s76 offen lds\n"
"v_mfma_f32_16x16x32_bf16 v[124:127], a[220:223], v[32:35], v[124:127]\n"
"v_exp_f32_e32 v31, v31\n"
"v_mfma_f32_16x16x32_bf16 a[232:235], a[224:227], v[32:35], a[232:235]\n"
"ds_read_b64_tr_b16 a[178:179], v4 offset:1024\n"
"v_cvt_pk_bf16_f32 v39, v30, v31\n"
"v_mfma_f32_16x16x32_bf16 v[24:27], a[128:131], a[64:67], 0\n"
"ds_read_b64_tr_b16 a[190:191], v14 offset:1536\n"
"v_mul_f32_e32 v16, s44, v16\n"
"v_mul_f32_e32 v17, s44, v17\n"
"v_mfma_f32_16x16x32_bf16 v[24:27], a[132:135], a[68:71], v[24:27]\n"
"v_exp_f32_e32 v16, v16\n"
"v_mfma_f32_16x16x32_bf16 v[24:27], a[136:139], a[72:75], v[24:27]\n"
"ds_read_b64_tr_b16 a[184:185], v3 offset:1536\n"
"v_mul_f32_e32 v18, s44, v18\n"
"v_mfma_f32_16x16x32_bf16 v[24:27], a[140:143], a[76:79], v[24:27]\n"
"v_exp_f32_e32 v17, v17\n"
"v_mfma_f32_16x16x32_bf16 v[28:31], a[144:147], a[64:67], 0\n"
"v_mul_f32_e32 v19, s44, v19\n"
"v_cvt_pk_bf16_f32 v32, v16, v17\n"
"v_mfma_f32_16x16x32_bf16 v[28:31], a[148:151], a[68:71], v[28:31]\n"
"v_exp_f32_e32 v18, v18\n"
"v_mfma_f32_16x16x32_bf16 v[28:31], a[152:155], a[72:75], v[28:31]\n"
"ds_read_b64_tr_b16 a[186:187], v4 offset:1536\n"
"v_mul_f32_e32 v20, s44, v20\n"
"v_mfma_f32_16x16x32_bf16 v[28:31], a[156:159], a[76:79], v[28:31]\n"
"v_exp_f32_e32 v19, v19\n"
"v_mfma_f32_16x16x32_bf16 v[128:131], a[192:195], v[36:39], v[128:131]\n"
"v_mul_f32_e32 v21, s44, v21\n"
"v_cvt_pk_bf16_f32 v33, v18, v19\n"
"v_mfma_f32_16x16x32_bf16 v[132:135], a[196:199], v[36:39], v[132:135]\n"
"v_exp_f32_e32 v20, v20\n"
"v_mfma_f32_16x16x32_bf16 v[136:139], a[200:203], v[36:39], v[136:139]\n"
"ds_read_b64_tr_b16 a[188:189], v11 offset:1536\n"
"v_mul_f32_e32 v22, s44, v22\n"
"v_mfma_f32_16x16x32_bf16 v[140:143], a[204:207], v[36:39], v[140:143]\n"
"v_exp_f32_e32 v21, v21\n"
"v_mfma_f32_16x16x32_bf16 v[144:147], a[208:211], v[36:39], v[144:147]\n"
"s_add_u32 m0, s49, 13312\n"
"v_mul_f32_e32 v23, s44, v23\n"
"v_cvt_pk_bf16_f32 v34, v20, v21\n"
"v_mfma_f32_16x16x32_bf16 v[148:151], a[212:215], v[36:39], v[148:151]\n"
"s_add_u32 s61, s58, 12416\n"
"v_exp_f32_e32 v22, v22\n"
"v_mfma_f32_16x16x32_bf16 v[152:155], a[216:219], v[36:39], v[152:155]\n"
"buffer_load_dwordx4 v7, s[36:39], s61 offen  nt lds\n"
"v_mfma_f32_16x16x32_bf16 v[156:159], a[220:223], v[36:39], v[156:159]\n"
"v_exp_f32_e32 v23, v23\n"
"v_mfma_f32_16x16x32_bf16 a[236:239], a[224:227], v[36:39], a[236:239]\n"
"v_cvt_pk_bf16_f32 v35, v22, v23\n"
"v_mfma_f32_16x16x32_bf16 v[16:19], a[96:99], a[0:3], 0\n"
"v_mul_f32_e32 v24, s44, v24\n"
"v_mul_f32_e32 v25, s44, v25\n"
"v_mfma_f32_16x16x32_bf16 v[16:19], a[100:103], a[4:7], v[16:19]\n"
"s_waitcnt lgkmcnt(15)\n"
"v_exp_f32_e32 v24, v24\n"
"v_mfma_f32_16x16x32_bf16 v[16:19], a[104:107], a[8:11], v[16:19]\n"
"v_mul_f32_e32 v26, s44, v26\n"
"v_mfma_f32_16x16x32_bf16 v[16:19], a[108:111], a[12:15], v[16:19]\n"
"v_exp_f32_e32 v25, v25\n"
"v_mfma_f32_16x16x32_bf16 v[20:23], a[112:115], a[0:3], 0\n"
"v_mul_f32_e32 v27, s44, v27\n"
"v_cvt_pk_bf16_f32 v36, v24, v25\n"
"v_mfma_f32_16x16x32_bf16 v[20:23], a[116:119], a[4:7], v[20:23]\n"
"s_waitcnt vmcnt(18) lgkmcnt(0)\n"
"s_barrier\n"
"v_exp_f32_e32 v26, v26\n"
"v_mfma_f32_16x16x32_bf16 v[20:23], a[120:123], a[8:11], v[20:23]\n"
"ds_read_b128 a[128:131], v1 offset:8192\n"
"ds_read_b128 a[140:143], v1 offset:9728\n"
"v_mul_f32_e32 v28, s44, v28\n"
"v_mfma_f32_16x16x32_bf16 v[20:23], a[124:127], a[12:15], v[20:23]\n"
"v_exp_f32_e32 v27, v27\n"
"v_mfma_f32_16x16x32_bf16 v[160:163], a[192:195], v[32:35], v[160:163]\n"
"v_mul_f32_e32 v29, s44, v29\n"
"v_cvt_pk_bf16_f32 v37, v26, v27\n"
"v_mfma_f32_16x16x32_bf16 v[164:167], a[196:199], v[32:35], v[164:167]\n"
"v_exp_f32_e32 v28, v28\n"
"v_mfma_f32_16x16x32_bf16 v[168:171], a[200:203], v[32:35], v[168:171]\n"
"ds_read_b128 a[132:135], v1 offset:8704\n"
"v_mul_f32_e32 v30, s44, v30\n"
"v_mfma_f32_16x16x32_bf16 v[172:175], a[204:207], v[32:35], v[172:175]\n"
"v_exp_f32_e32 v29, v29\n"
"v_mfma_f32_16x16x32_bf16 v[176:179], a[208:211], v[32:35], v[176:179]\n"
"s_add_u32 m0, s48, 0\n"
"v_mul_f32_e32 v31, s44, v31\n"
"v_cvt_pk_bf16_f32 v38, v28, v29\n"
"v_mfma_f32_16x16x32_bf16 v[180:183], a[212:215], v[32:35], v[180:183]\n"
"s_add_u32 s47, s45, 98304\n"
"v_exp_f32_e32 v30, v30\n"
"v_mfma_f32_16x16x32_bf16 v[184:187], a[216:219], v[32:35], v[184:187]\n"
"buffer_load_dwordx4 v6, s[24:27], s47 offen lds\n"
"v_mfma_f32_16x16x32_bf16 v[188:191], a[220:223], v[32:35], v[188:191]\n"
"v_exp_f32_e32 v31, v31\n"
"v_mfma_f32_16x16x32_bf16 a[240:243], a[224:227], v[32:35], a[240:243]\n"
"ds_read_b128 a[136:139], v1 offset:9216\n"
"v_cvt_pk_bf16_f32 v39, v30, v31\n"
"v_mfma_f32_16x16x32_bf16 v[24:27], a[96:99], a[16:19], 0\n"
"v_mul_f32_e32 v16, s44, v16\n"
"v_mul_f32_e32 v17, s44, v17\n"
"v_mfma_f32_16x16x32_bf16 v[24:27], a[100:103], a[20:23], v[24:27]\n"
"v_exp_f32_e32 v16, v16\n"
"v_mfma_f32_16x16x32_bf16 v[24:27], a[104:107], a[24:27], v[24:27]\n"
"ds_read_b128 a[144:147], v2 offset:8192\n"
"v_mul_f32_e32 v18, s44, v18\n"
"v_mfma_f32_16x16x32_bf16 v[24:27], a[108:111], a[28:31], v[24:27]\n"
"v_exp_f32_e32 v17, v17\n"
"v_mfma_f32_16x16x32_bf16 v[28:31], a[112:115], a[16:19], 0\n"
"v_mul_f32_e32 v19, s44, v19\n"
"v_cvt_pk_bf16_f32 v32, v16, v17\n"
"v_mfma_f32_16x16x32_bf16 v[28:31], a[116:119], a[20:23], v[28:31]\n"
"v_exp_f32_e32 v18, v18\n"
"v_mfma_f32_16x16x32_bf16 v[28:31], a[120:123], a[24:27], v[28:31]\n"
"ds_read_b128 a[148:151], v2 offset:8704\n"
"v_mul_f32_e32 v20, s44, v20\n"
"v_mfma_f32_16x16x32_bf16 v[28:31], a[124:127], a[28:31], v[28:31]\n"
"v_exp_f32_e32 v19, v19\n"
"v_mfma_f32_16x16x32_bf16 v[192:195], a[192:195], v[36:39], v[192:195]\n"
"v_mul_f32_e32 v21, s44, v21\n"
"v_cvt_pk_bf16_f32 v33, v18, v19\n"
"v_mfma_f32_16x16x32_bf16 v[196:199], a[196:199], v[36:39], v[196:199]\n"
"v_exp_f32_e32 v20, v20\n"
"v_mfma_f32_16x16x32_bf16 v[200:203], a[200:203], v[36:39], v[200:203]\n"
"ds_read_b128 a[152:155], v2 offset:9216\n"
"v_mul_f32_e32 v22, s44, v22\n"
"v_mfma_f32_16x16x32_bf16 v[204:207], a[204:207], v[36:39], v[204:207]\n"
"v_exp_f32_e32 v21, v21\n"
"v_mfma_f32_16x16x32_bf16 v[208:211], a[208:211], v[36:39], v[208:211]\n"
"s_add_u32 m0, s48, 1024\n"
"v_mul_f32_e32 v23, s44, v23\n"
"v_cvt_pk_bf16_f32 v34, v20, v21\n"
"v_mfma_f32_16x16x32_bf16 v[212:215], a[212:215], v[36:39], v[212:215]\n"
"s_add_u32 s47, s45, 98432\n"
"v_exp_f32_e32 v22, v22\n"
"v_mfma_f32_16x16x32_bf16 v[216:219], a[216:219], v[36:39], v[216:219]\n"
"buffer_load_dwordx4 v6, s[24:27], s47 offen lds\n"
"v_mfma_f32_16x16x32_bf16 v[220:223], a[220:223], v[36:39], v[220:223]\n"
"v_exp_f32_e32 v23, v23\n"
"v_mfma_f32_16x16x32_bf16 a[244:247], a[224:227], v[36:39], a[244:247]\n"
"ds_read_b128 a[156:159], v2 offset:9728\n"
"v_cvt_pk_bf16_f32 v35, v22, v23\n"
"v_mfma_f32_16x16x32_bf16 v[16:19], a[96:99], a[32:35], 0\n"
"ds_read_b64_tr_b16 a[198:199], v14 offset:8192\n"
"v_mul_f32_e32 v24, s44, v24\n"
"v_mul_f32_e32 v25, s44, v25\n"
"v_mfma_f32_16x16x32_bf16 v[16:19], a[100:103], a[36:39], v[16:19]\n"
"ds_read_b64_tr_b16 a[200:201], v3 offset:8704\n"
"v_exp_f32_e32 v24, v24\n"
"v_mfma_f32_16x16x32_bf16 v[16:19], a[104:107], a[40:43], v[16:19]\n"
"ds_read_b64_tr_b16 a[192:193], v3 offset:8192\n"
"ds_read_b64_tr_b16 a[202:203], v4 offset:8704\n"
"v_mul_f32_e32 v26, s44, v26\n"
"v_mfma_f32_16x16x32_bf16 v[16:19], a[108:111], a[44:47], v[16:19]\n"
"v_exp_f32_e32 v25, v25\n"
"v_mfma_f32_16x16x32_bf16 v[20:23], a[112:115], a[32:35], 0\n"
"v_mul_f32_e32 v27, s44, v27\n"
"v_cvt_pk_bf16_f32 v36, v24, v25\n"
"v_mfma_f32_16x16x32_bf16 v[20:23], a[116:119], a[36:39], v[20:23]\n"
"v_exp_f32_e32 v26, v26\n"
"v_mfma_f32_16x16x32_bf16 v[20:23], a[120:123], a[40:43], v[20:23]\n"
"ds_read_b64_tr_b16 a[194:195], v4 offset:8192\n"
"v_mul_f32_e32 v28, s44, v28\n"
"v_mfma_f32_16x16x32_bf16 v[20:23], a[124:127], a[44:47], v[20:23]\n"
"v_exp_f32_e32 v27, v27\n"
"v_mfma_f32_16x16x32_bf16 v[64:67], a[160:163], v[32:35], v[64:67]\n"
"s_add_u32 m0, s49, 14336\n"
"v_mul_f32_e32 v29, s44, v29\n"
"v_cvt_pk_bf16_f32 v37, v26, v27\n"
"v_mfma_f32_16x16x32_bf16 v[68:71], a[164:167], v[32:35], v[68:71]\n"
"s_add_u32 s61, s58, 12288\n"
"v_exp_f32_e32 v28, v28\n"
"v_mfma_f32_16x16x32_bf16 v[72:75], a[168:171], v[32:35], v[72:75]\n"
"buffer_load_dwordx4 v8, s[36:39], s61 offen  nt lds\n"
"v_mul_f32_e32 v30, s44, v30\n"
"v_mfma_f32_16x16x32_bf16 v[76:79], a[172:175], v[32:35], v[76:79]\n"
"v_exp_f32_e32 v29, v29\n"
"v_mfma_f32_16x16x32_bf16 v[80:83], a[176:179], v[32:35], v[80:83]\n"
"s_add_u32 m0, s48, 32768\n"
"v_mul_f32_e32 v31, s44, v31\n"
"v_cvt_pk_bf16_f32 v38, v28, v29\n"
"v_mfma_f32_16x16x32_bf16 v[84:87], a[180:183], v[32:35], v[84:87]\n"
"s_add_u32 s76, s45, 98304\n"
"v_exp_f32_e32 v30, v30\n"
"v_mfma_f32_16x16x32_bf16 v[88:91], a[184:187], v[32:35], v[88:91]\n"
"buffer_load_dwordx4 v6, s[28:31], s76 offen lds\n"
"v_mfma_f32_16x16x32_bf16 v[92:95], a[188:191], v[32:35], v[92:95]\n"
"v_exp_f32_e32 v31, v31\n"
"v_mfma_f32_16x16x32_bf16 a[228:231], a[224:227], v[32:35], a[228:231]\n"
"ds_read_b64_tr_b16 a[196:197], v11 offset:8192\n"
"v_cvt_pk_bf16_f32 v39, v30, v31\n"
"v_mfma_f32_16x16x32_bf16 v[24:27], a[96:99], a[48:51], 0\n"
"ds_read_b64_tr_b16 a[212:213], v11 offset:9216\n"
"v_mul_f32_e32 v16, s44, v16\n"
"v_mul_f32_e32 v17, s44, v17\n"
"v_mfma_f32_16x16x32_bf16 v[24:27], a[100:103], a[52:55], v[24:27]\n"
"ds_read_b64_tr_b16 a[214:215], v14 offset:9216\n"
"v_exp_f32_e32 v16, v16\n"
"v_mfma_f32_16x16x32_bf16 v[24:27], a[104:107], a[56:59], v[24:27]\n"
"ds_read_b64_tr_b16 a[204:205], v11 offset:8704\n"
"v_mul_f32_e32 v18, s44, v18\n"
"v_mfma_f32_16x16x32_bf16 v[24:27], a[108:111], a[60:63], v[24:27]\n"
"v_exp_f32_e32 v17, v17\n"
"v_mfma_f32_16x16x32_bf16 v[28:31], a[112:115], a[48:51], 0\n"
"v_mul_f32_e32 v19, s44, v19\n"
"v_cvt_pk_bf16_f32 v32, v16, v17\n"
"v_mfma_f32_16x16x32_bf16 v[28:31], a[116:119], a[52:55], v[28:31]\n"
"v_exp_f32_e32 v18, v18\n"
"v_mfma_f32_16x16x32_bf16 v[28:31], a[120:123], a[56:59], v[28:31]\n"
"ds_read_b64_tr_b16 a[206:207], v14 offset:8704\n"
"v_mul_f32_e32 v20, s44, v20\n"
"v_mfma_f32_16x16x32_bf16 v[28:31], a[124:127], a[60:63], v[28:31]\n"
"v_exp_f32_e32 v19, v19\n"
"v_mfma_f32_16x16x32_bf16 v[96:99], a[160:163], v[36:39], v[96:99]\n"
"v_mul_f32_e32 v21, s44, v21\n"
"v_cvt_pk_bf16_f32 v33, v18, v19\n"
"v_mfma_f32_16x16x32_bf16 v[100:103], a[164:167], v[36:39], v[100:103]\n"
"v_exp_f32_e32 v20, v20\n"
"v_mfma_f32_16x16x32_bf16 v[104:107], a[168:171], v[36:39], v[104:107]\n"
"ds_read_b64_tr_b16 a[208:209], v3 offset:9216\n"
"v_mul_f32_e32 v22, s44, v22\n"
"v_mfma_f32_16x16x32_bf16 v[108:111], a[172:175], v[36:39], v[108:111]\n"
"v_exp_f32_e32 v21, v21\n"
"v_mfma_f32_16x16x32_bf16 v[112:115], a[176:179], v[36:39], v[112:115]\n"
"s_add_u32 m0, s48, 33792\n"
"v_mul_f32_e32 v23, s44, v23\n"
"v_cvt_pk_bf16_f32 v34, v20, v21\n"
"v_mfma_f32_16x16x32_bf16 v[116:119], a[180:183], v[36:39], v[116:119]\n"
"s_add_u32 s76, s45, 98432\n"
"v_exp_f32_e32 v22, v22\n"
"v_mfma_f32_16x16x32_bf16 v[120:123], a[184:187], v[36:39], v[120:123]\n"
"buffer_load_dwordx4 v6, s[28:31], s76 offen lds\n"
"v_mfma_f32_16x16x32_bf16 v[124:127], a[188:191], v[36:39], v[124:127]\n"
"v_exp_f32_e32 v23, v23\n"
"v_mfma_f32_16x16x32_bf16 a[232:235], a[224:227], v[36:39], a[232:235]\n"
"ds_read_b64_tr_b16 a[210:211], v4 offset:9216\n"
"v_cvt_pk_bf16_f32 v35, v22, v23\n"
"v_mfma_f32_16x16x32_bf16 v[16:19], a[96:99], a[64:67], 0\n"
"ds_read_b64_tr_b16 a[222:223], v14 offset:9728\n"
"v_mul_f32_e32 v24, s44, v24\n"
"v_mul_f32_e32 v25, s44, v25\n"
"v_mfma_f32_16x16x32_bf16 v[16:19], a[100:103], a[68:71], v[16:19]\n"
"v_exp_f32_e32 v24, v24\n"
"v_mfma_f32_16x16x32_bf16 v[16:19], a[104:107], a[72:75], v[16:19]\n"
"ds_read_b64_tr_b16 a[216:217], v3 offset:9728\n"
"v_mul_f32_e32 v26, s44, v26\n"
"v_mfma_f32_16x16x32_bf16 v[16:19], a[108:111], a[76:79], v[16:19]\n"
"v_exp_f32_e32 v25, v25\n"
"v_mfma_f32_16x16x32_bf16 v[20:23], a[112:115], a[64:67], 0\n"
"v_mul_f32_e32 v27, s44, v27\n"
"v_cvt_pk_bf16_f32 v36, v24, v25\n"
"v_mfma_f32_16x16x32_bf16 v[20:23], a[116:119], a[68:71], v[20:23]\n"
"v_exp_f32_e32 v26, v26\n"
"v_mfma_f32_16x16x32_bf16 v[20:23], a[120:123], a[72:75], v[20:23]\n"
"ds_read_b64_tr_b16 a[218:219], v4 offset:9728\n"
"v_mul_f32_e32 v28, s44, v28\n"
"v_mfma_f32_16x16x32_bf16 v[20:23], a[124:127], a[76:79], v[20:23]\n"
"v_exp_f32_e32 v27, v27\n"
"v_mfma_f32_16x16x32_bf16 v[128:131], a[160:163], v[32:35], v[128:131]\n"
"v_mul_f32_e32 v29, s44, v29\n"
"v_cvt_pk_bf16_f32 v37, v26, v27\n"
"v_mfma_f32_16x16x32_bf16 v[132:135], a[164:167], v[32:35], v[132:135]\n"
"v_exp_f32_e32 v28, v28\n"
"v_mfma_f32_16x16x32_bf16 v[136:139], a[168:171], v[32:35], v[136:139]\n"
"ds_read_b64_tr_b16 a[220:221], v11 offset:9728\n"
"v_mul_f32_e32 v30, s44, v30\n"
"v_mfma_f32_16x16x32_bf16 v[140:143], a[172:175], v[32:35], v[140:143]\n"
"v_exp_f32_e32 v29, v29\n"
"v_mfma_f32_16x16x32_bf16 v[144:147], a[176:179], v[32:35], v[144:147]\n"
"s_add_u32 m0, s49, 15360\n"
"v_mul_f32_e32 v31, s44, v31\n"
"v_cvt_pk_bf16_f32 v38, v28, v29\n"
"v_mfma_f32_16x16x32_bf16 v[148:151], a[180:183], v[32:35], v[148:151]\n"
"s_add_u32 s61, s58, 12416\n"
"v_exp_f32_e32 v30, v30\n"
"v_mfma_f32_16x16x32_bf16 v[152:155], a[184:187], v[32:35], v[152:155]\n"
"buffer_load_dwordx4 v8, s[36:39], s61 offen  nt lds\n"
"v_mfma_f32_16x16x32_bf16 v[156:159], a[188:191], v[32:35], v[156:159]\n"
"v_exp_f32_e32 v31, v31\n"
"v_mfma_f32_16x16x32_bf16 a[236:239], a[224:227], v[32:35], a[236:239]\n"
"s_waitcnt lgkmcnt(15)\n"
"v_cvt_pk_bf16_f32 v39, v30, v31\n"
"v_mfma_f32_16x16x32_bf16 v[24:27], a[128:131], a[0:3], 0\n"
"v_mul_f32_e32 v16, s44, v16\n"
"v_mul_f32_e32 v17, s44, v17\n"
"v_mfma_f32_16x16x32_bf16 v[24:27], a[132:135], a[4:7], v[24:27]\n"
"v_exp_f32_e32 v16, v16\n"
"v_mfma_f32_16x16x32_bf16 v[24:27], a[136:139], a[8:11], v[24:27]\n"
"v_mul_f32_e32 v18, s44, v18\n"
"v_mfma_f32_16x16x32_bf16 v[24:27], a[140:143], a[12:15], v[24:27]\n"
"v_exp_f32_e32 v17, v17\n"
"v_mfma_f32_16x16x32_bf16 v[28:31], a[144:147], a[0:3], 0\n"
"v_mul_f32_e32 v19, s44, v19\n"
"v_cvt_pk_bf16_f32 v32, v16, v17\n"
"v_mfma_f32_16x16x32_bf16 v[28:31], a[148:151], a[4:7], v[28:31]\n"
"s_waitcnt vmcnt(18) lgkmcnt(0)\n"
"s_barrier\n"
"v_exp_f32_e32 v18, v18\n"
"v_mfma_f32_16x16x32_bf16 v[28:31], a[152:155], a[8:11], v[28:31]\n"
"ds_read_b128 a[96:99], v1 offset:16384\n"
"ds_read_b128 a[108:111], v1 offset:17920\n"
"v_mul_f32_e32 v20, s44, v20\n"
"v_mfma_f32_16x16x32_bf16 v[28:31], a[156:159], a[12:15], v[28:31]\n"
"ds_read_b128 v[40:43], v58 offset:16384\n"
"v_exp_f32_e32 v19, v19\n"
"v_mfma_f32_16x16x32_bf16 v[160:163], a[160:163], v[36:39], v[160:163]\n"
"ds_read_b128 v[44:47], v59 offset:17408\n"
"v_mul_f32_e32 v21, s44, v21\n"
"v_cvt_pk_bf16_f32 v33, v18, v19\n"
"v_mfma_f32_16x16x32_bf16 v[164:167], a[164:167], v[36:39], v[164:167]\n"
"ds_read_b128 v[48:51], v58 offset:18432\n"
"v_exp_f32_e32 v20, v20\n"
"v_mfma_f32_16x16x32_bf16 v[168:171], a[168:171], v[36:39], v[168:171]\n"
"ds_read_b128 a[100:103], v1 offset:16896\n"
"ds_read_b128 v[52:55], v59 offset:19456\n"
"v_mul_f32_e32 v22, s44, v22\n"
"v_mfma_f32_16x16x32_bf16 v[172:175], a[172:175], v[36:39], v[172:175]\n"
"v_exp_f32_e32 v21, v21\n"
"v_mfma_f32_16x16x32_bf16 v[176:179], a[176:179], v[36:39], v[176:179]\n"
"s_add_u32 m0, s48, 8192\n"
"v_mul_f32_e32 v23, s44, v23\n"
"v_cvt_pk_bf16_f32 v34, v20, v21\n"
"v_mfma_f32_16x16x32_bf16 v[180:183], a[180:183], v[36:39], v[180:183]\n"
"s_add_u32 s47, s45, 106496\n"
"v_exp_f32_e32 v22, v22\n"
"v_mfma_f32_16x16x32_bf16 v[184:187], a[184:187], v[36:39], v[184:187]\n"
"buffer_load_dwordx4 v6, s[24:27], s47 offen lds\n"
"v_mfma_f32_16x16x32_bf16 v[188:191], a[188:191], v[36:39], v[188:191]\n"
"v_exp_f32_e32 v23, v23\n"
"v_mfma_f32_16x16x32_bf16 a[240:243], a[224:227], v[36:39], a[240:243]\n"
"ds_read_b128 a[104:107], v1 offset:17408\n"
"v_cvt_pk_bf16_f32 v35, v22, v23\n"
"v_mfma_f32_16x16x32_bf16 v[16:19], a[128:131], a[16:19], 0\n"
"ds_read_b128 a[124:127], v2 offset:17920\n"
"v_mul_f32_e32 v24, s44, v24\n"
"v_mul_f32_e32 v25, s44, v25\n"
"v_mfma_f32_16x16x32_bf16 v[16:19], a[132:135], a[20:23], v[16:19]\n"
"v_exp_f32_e32 v24, v24\n"
"s_add_u32 s77, s60, 16384\n"
"v_mfma_f32_16x16x32_bf16 v[16:19], a[136:139], a[24:27], v[16:19]\n"
"ds_read_b128 a[112:115], v2 offset:16384\n"
"v_mul_f32_e32 v26, s44, v26\n"
"s_waitcnt lgkmcnt(7)\n"
"buffer_store_dwordx4 v[40:43], v9, s[40:43], s77 offen offset:0  sc1\n"
"v_mfma_f32_16x16x32_bf16 v[16:19], a[140:143], a[28:31], v[16:19]\n"
"v_exp_f32_e32 v25, v25\n"
"v_mfma_f32_16x16x32_bf16 v[20:23], a[144:147], a[16:19], 0\n"
"v_mul_f32_e32 v27, s44, v27\n"
"v_cvt_pk_bf16_f32 v36, v24, v25\n"
"v_mfma_f32_16x16x32_bf16 v[20:23], a[148:151], a[20:23], v[20:23]\n"
"v_exp_f32_e32 v26, v26\n"
"v_mfma_f32_16x16x32_bf16 v[20:23], a[152:155], a[24:27], v[20:23]\n"
"ds_read_b128 a[116:119], v2 offset:16896\n"
"v_mul_f32_e32 v28, s44, v28\n"
"s_waitcnt lgkmcnt(7)\n"
"buffer_store_dwordx4 v[44:47], v9, s[40:43], s77 offen offset:1024  sc1\n"
"v_mfma_f32_16x16x32_bf16 v[20:23], a[156:159], a[28:31], v[20:23]\n"
"v_exp_f32_e32 v27, v27\n"
"v_mfma_f32_16x16x32_bf16 v[192:195], a[160:163], v[32:35], v[192:195]\n"
"v_mul_f32_e32 v29, s44, v29\n"
"v_cvt_pk_bf16_f32 v37, v26, v27\n"
"v_mfma_f32_16x16x32_bf16 v[196:199], a[164:167], v[32:35], v[196:199]\n"
"v_exp_f32_e32 v28, v28\n"
"v_mfma_f32_16x16x32_bf16 v[200:203], a[168:171], v[32:35], v[200:203]\n"
"ds_read_b128 a[120:123], v2 offset:17408\n"
"v_mul_f32_e32 v30, s44, v30\n"
"s_waitcnt lgkmcnt(7)\n"
"buffer_store_dwordx4 v[48:51], v9, s[40:43], s77 offen offset:2048  sc1\n"
"v_mfma_f32_16x16x32_bf16 v[204:207], a[172:175], v[32:35], v[204:207]\n"
"v_exp_f32_e32 v29, v29\n"
"v_mfma_f32_16x16x32_bf16 v[208:211], a[176:179], v[32:35], v[208:211]\n"
"v_mul_f32_e32 v31, s44, v31\n"
"v_cvt_pk_bf16_f32 v38, v28, v29\n"
"v_mfma_f32_16x16x32_bf16 v[212:215], a[180:183], v[32:35], v[212:215]\n"
"s_mov_b32 s88, L_E5%=+0-L_code%=\n"
"v_exp_f32_e32 v30, v30\n"
"v_mfma_f32_16x16x32_bf16 v[216:219], a[184:187], v[32:35], v[216:219]\n"
"s_add_u32 m0, s48, 9216\n"
"buffer_load_dword v63, v62, s[84:87], s88 offen\n"
"v_mfma_f32_16x16x32_bf16 v[220:223], a[188:191], v[32:35], v[220:223]\n"
"s_add_u32 s47, s45, 106624\n"
"v_exp_f32_e32 v31, v31\n"
"v_mfma_f32_16x16x32_bf16 a[244:247], a[224:227], v[32:35], a[244:247]\n"
"buffer_load_dwordx4 v6, s[24:27], s47 offen lds\n"
"v_cvt_pk_bf16_f32 v39, v30, v31\n"
"s_waitcnt lgkmcnt(5)\n"
"buffer_store_dwordx4 v[52:55], v9, s[40:43], s77 offen offset:3072  sc1\n"
"v_mfma_f32_16x16x32_bf16 v[24:27], a[128:131], a[32:35], 0\n"
"ds_read_b64_tr_b16 a[166:167], v14 offset:16384\n"
"v_mul_f32_e32 v16, s44, v16\n"
"v_mul_f32_e32 v17, s44, v17\n"
"v_mfma_f32_16x16x32_bf16 v[24:27], a[132:135], a[36:39], v[24:27]\n"
"ds_read_b64_tr_b16 a[168:169], v3 offset:16896\n"
"v_exp_f32_e32 v16, v16\n"
"v_mfma_f32_16x16x32_bf16 v[24:27], a[136:139], a[40:43], v[24:27]\n"
"ds_read_b64_tr_b16 a[160:161], v3 offset:16384\n"
"ds_read_b64_tr_b16 a[170:171], v4 offset:16896\n"
"v_mul_f32_e32 v18, s44, v18\n"
"v_mfma_f32_16x16x32_bf16 v[24:27], a[140:143], a[44:47], v[24:27]\n"
"v_exp_f32_e32 v17, v17\n"
"v_mfma_f32_16x16x32_bf16 v[28:31], a[144:147], a[32:35], 0\n"
"v_mul_f32_e32 v19, s44, v19\n"
"v_cvt_pk_bf16_f32 v32, v16, v17\n"
"v_mfma_f32_16x16x32_bf16 v[28:31], a[148:151], a[36:39], v[28:31]\n"
"v_exp_f32_e32 v18, v18\n"
"v_mfma_f32_16x16x32_bf16 v[28:31], a[152:155], a[40:43], v[28:31]\n"
"ds_read_b64_tr_b16 a[162:163], v4 offset:16384\n"
"v_mul_f32_e32 v20, s44, v20\n"
"v_mfma_f32_16x16x32_bf16 v[28:31], a[156:159], a[44:47], v[28:31]\n"
"v_exp_f32_e32 v19, v19\n"
"v_mfma_f32_16x16x32_bf16 v[64:67], a[192:195], v[36:39], v[64:67]\n"
"s_add_u32 m0, s49, 16384\n"
"v_mul_f32_e32 v21, s44, v21\n"
"v_cvt_pk_bf16_f32 v33, v18, v19\n"
"v_mfma_f32_16x16x32_bf16 v[68:71], a[196:199], v[36:39], v[68:71]\n"
"s_add_u32 s61, s58, 16384\n"
"v_exp_f32_e32 v20, v20\n"
"v_mfma_f32_16x16x32_bf16 v[72:75], a[200:203], v[36:39], v[72:75]\n"
"buffer_load_dwordx4 v7, s[36:39], s61 offen  nt lds\n"
"v_mul_f32_e32 v22, s44, v22\n"
"v_mfma_f32_16x16x32_bf16 v[76:79], a[204:207], v[36:39], v[76:79]\n"
"v_exp_f32_e32 v21, v21\n"
"v_mfma_f32_16x16x32_bf16 v[80:83], a[208:211], v[36:39], v[80:83]\n"
"s_add_u32 m0, s48, 40960\n"
"v_mul_f32_e32 v23, s44, v23\n"
"v_cvt_pk_bf16_f32 v34, v20, v21\n"
"v_mfma_f32_16x16x32_bf16 v[84:87], a[212:215], v[36:39], v[84:87]\n"
"s_add_u32 s76, s45, 106496\n"
"v_exp_f32_e32 v22, v22\n"
"v_mfma_f32_16x16x32_bf16 v[88:91], a[216:219], v[36:39], v[88:91]\n"
"buffer_load_dwordx4 v6, s[28:31], s76 offen lds\n"
"v_mfma_f32_16x16x32_bf16 v[92:95], a[220:223], v[36:39], v[92:95]\n"
"v_exp_f32_e32 v23, v23\n"
"v_mfma_f32_16x16x32_bf16 a[228:231], a[224:227], v[36:39], a[228:231]\n"
"ds_read_b64_tr_b16 a[164:165], v11 offset:16384\n"
"v_cvt_pk_bf16_f32 v35, v22, v23\n"
"v_mfma_f32_16x16x32_bf16 v[16:19], a[128:131], a[48:51], 0\n"
"ds_read_b64_tr_b16 a[180:181], v11 offset:17408\n"
"v_mul_f32_e32 v24, s44, v24\n"
"v_mul_f32_e32 v25, s44, v25\n"
"v_mfma_f32_16x16x32_bf16 v[16:19], a[132:135], a[52:55], v[16:19]\n"
"ds_read_b64_tr_b16 a[182:183], v14 offset:17408\n"
"v_exp_f32_e32 v24, v24\n"
"v_mfma_f32_16x16x32_bf16 v[16:19], a[136:139], a[56:59], v[16:19]\n"
"ds_read_b64_tr_b16 a[172:173], v11 offset:16896\n"
"v_mul_f32_e32 v26, s44, v26\n"
"v_mfma_f32_16x16x32_bf16 v[16:19], a[140:143], a[60:63], v[16:19]\n"
"v_exp_f32_e32 v25, v25\n"
"v_mfma_f32_16x16x32_bf16 v[20:23], a[144:147], a[48:51], 0\n"
"v_mul_f32_e32 v27, s44, v27\n"
"v_cvt_pk_bf16_f32 v36, v24, v25\n"
"v_mfma_f32_16x16x32_bf16 v[20:23], a[148:151], a[52:55], v[20:23]\n"
"v_exp_f32_e32 v26, v26\n"
"v_mfma_f32_16x16x32_bf16 v[20:23], a[152:155], a[56:59], v[20:23]\n"
"ds_read_b64_tr_b16 a[174:175], v14 offset:16896\n"
"v_mul_f32_e32 v28, s44, v28\n"
"v_mfma_f32_16x16x32_bf16 v[20:23], a[156:159], a[60:63], v[20:23]\n"
"v_exp_f32_e32 v27, v27\n"
"v_mfma_f32_16x16x32_bf16 v[96:99], a[192:195], v[32:35], v[96:99]\n"
"v_mul_f32_e32 v29, s44, v29\n"
"v_cvt_pk_bf16_f32 v37, v26, v27\n"
"v_mfma_f32_16x16x32_bf16 v[100:103], a[196:199], v[32:35], v[100:103]\n"
"v_exp_f32_e32 v28, v28\n"
"v_mfma_f32_16x16x32_bf16 v[104:107], a[200:203], v[32:35], v[104:107]\n"
"ds_read_b64_tr_b16 a[176:177], v3 offset:17408\n"
"v_mul_f32_e32 v30, s44, v30\n"
"v_mfma_f32_16x16x32_bf16 v[108:111], a[204:207], v[32:35], v[108:111]\n"
"v_exp_f32_e32 v29, v29\n"
"v_mfma_f32_16x16x32_bf16 v[112:115], a[208:211], v[32:35], v[112:115]\n"
"s_add_u32 m0, s48, 41984\n"
"v_mul_f32_e32 v31, s44, v31\n"
"v_cvt_pk_bf16_f32 v38, v28, v29\n"
"v_mfma_f32_16x16x32_bf16 v[116:119], a[212:215], v[32:35], v[116:119]\n"
"s_add_u32 s76, s45, 106624\n"
"v_exp_f32_e32 v30, v30\n"
"v_mfma_f32_16x16x32_bf16 v[120:123], a[216:219], v[32:35], v[120:123]\n"
"buffer_load_dwordx4 v6, s[28:31], s76 offen lds\n"
"v_mfma_f32_16x16x32_bf16 v[124:127], a[220:223], v[32:35], v[124:127]\n"
"v_exp_f32_e32 v31, v31\n"
"v_mfma_f32_16x16x32_bf16 a[232:235], a[224:227], v[32:35], a[232:235]\n"
"ds_read_b64_tr_b16 a[178:179], v4 offset:17408\n"
"v_cvt_pk_bf16_f32 v39, v30, v31\n"
"v_mfma_f32_16x16x32_bf16 v[24:27], a[128:131], a[64:67], 0\n"
"ds_read_b64_tr_b16 a[190:191], v14 offset:17920\n"
"v_mul_f32_e32 v16, s44, v16\n"
"v_mul_f32_e32 v17, s44, v17\n"
"v_mfma_f32_16x16x32_bf16 v[24:27], a[132:135], a[68:71], v[24:27]\n"
"v_exp_f32_e32 v16, v16\n"
"v_mfma_f32_16x16x32_bf16 v[24:27], a[136:139], a[72:75], v[24:27]\n"
"ds_read_b64_tr_b16 a[184:185], v3 offset:17920\n"
"v_mul_f32_e32 v18, s44, v18\n"
"v_mfma_f32_16x16x32_bf16 v[24:27], a[140:143], a[76:79], v[24:27]\n"
"v_exp_f32_e32 v17, v17\n"
"v_mfma_f32_16x16x32_bf16 v[28:31], a[144:147], a[64:67], 0\n"
"v_mul_f32_e32 v19, s44, v19\n"
"v_cvt_pk_bf16_f32 v32, v16, v17\n"
"v_mfma_f32_16x16x32_bf16 v[28:31], a[148:151], a[68:71], v[28:31]\n"
"v_exp_f32_e32 v18, v18\n"
"v_mfma_f32_16x16x32_bf16 v[28:31], a[152:155], a[72:75], v[28:31]\n"
"ds_read_b64_tr_b16 a[186:187], v4 offset:17920\n"
"v_mul_f32_e32 v20, s44, v20\n"
"v_mfma_f32_16x16x32_bf16 v[28:31], a[156:159], a[76:79], v[28:31]\n"
"v_exp_f32_e32 v19, v19\n"
"v_mfma_f32_16x16x32_bf16 v[128:131], a[192:195], v[36:39], v[128:131]\n"
"v_mul_f32_e32 v21, s44, v21\n"
"v_cvt_pk_bf16_f32 v33, v18, v19\n"
"v_mfma_f32_16x16x32_bf16 v[132:135], a[196:199], v[36:39], v[132:135]\n"
"v_exp_f32_e32 v20, v20\n"
"v_mfma_f32_16x16x32_bf16 v[136:139], a[200:203], v[36:39], v[136:139]\n"
"ds_read_b64_tr_b16 a[188:189], v11 offset:17920\n"
"v_mul_f32_e32 v22, s44, v22\n"
"v_mfma_f32_16x16x32_bf16 v[140:143], a[204:207], v[36:39], v[140:143]\n"
"v_exp_f32_e32 v21, v21\n"
"v_mfma_f32_16x16x32_bf16 v[144:147], a[208:211], v[36:39], v[144:147]\n"
"s_add_u32 m0, s49, 17408\n"
"v_mul_f32_e32 v23, s44, v23\n"
"v_cvt_pk_bf16_f32 v34, v20, v21\n"
"v_mfma_f32_16x16x32_bf16 v[148:151], a[212:215], v[36:39], v[148:151]\n"
"s_add_u32 s61, s58, 16512\n"
"v_exp_f32_e32 v22, v22\n"
"v_mfma_f32_16x16x32_bf16 v[152:155], a[216:219], v[36:39], v[152:155]\n"
"buffer_load_dwordx4 v7, s[36:39], s61 offen  nt lds\n"
"v_mfma_f32_16x16x32_bf16 v[156:159], a[220:223], v[36:39], v[156:159]\n"
"v_exp_f32_e32 v23, v23\n"
"v_mfma_f32_16x16x32_bf16 a[236:239], a[224:227], v[36:39], a[236:239]\n"
"v_cvt_pk_bf16_f32 v35, v22, v23\n"
"v_mfma_f32_16x16x32_bf16 v[16:19], a[96:99], a[0:3], 0\n"
"v_mul_f32_e32 v24, s44, v24\n"
"v_mul_f32_e32 v25, s44, v25\n"
"v_mfma_f32_16x16x32_bf16 v[16:19], a[100:103], a[4:7], v[16:19]\n"
"s_waitcnt lgkmcnt(15)\n"
"v_exp_f32_e32 v24, v24\n"
"v_mfma_f32_16x16x32_bf16 v[16:19], a[104:107], a[8:11], v[16:19]\n"
"v_mul_f32_e32 v26, s44, v26\n"
"v_mfma_f32_16x16x32_bf16 v[16:19], a[108:111], a[12:15], v[16:19]\n"
"v_exp_f32_e32 v25, v25\n"
"v_mfma_f32_16x16x32_bf16 v[20:23], a[112:115], a[0:3], 0\n"
"v_mul_f32_e32 v27, s44, v27\n"
"v_cvt_pk_bf16_f32 v36, v24, v25\n"
"v_mfma_f32_16x16x32_bf16 v[20:23], a[116:119], a[4:7], v[20:23]\n"
"s_waitcnt vmcnt(18) lgkmcnt(0)\n"
"s_barrier\n"
"v_exp_f32_e32 v26, v26\n"
"v_mfma_f32_16x16x32_bf16 v[20:23], a[120:123], a[8:11], v[20:23]\n"
"ds_read_b128 a[128:131], v1 offset:24576\n"
"ds_read_b128 a[140:143], v1 offset:26112\n"
"v_mul_f32_e32 v28, s44, v28\n"
"v_mfma_f32_16x16x32_bf16 v[20:23], a[124:127], a[12:15], v[20:23]\n"
"v_exp_f32_e32 v27, v27\n"
"v_mfma_f32_16x16x32_bf16 v[160:163], a[192:195], v[32:35], v[160:163]\n"
"v_mul_f32_e32 v29, s44, v29\n"
"v_cvt_pk_bf16_f32 v37, v26, v27\n"
"v_mfma_f32_16x16x32_bf16 v[164:167], a[196:199], v[32:35], v[164:167]\n"
"v_exp_f32_e32 v28, v28\n"
"v_mfma_f32_16x16x32_bf16 v[168:171], a[200:203], v[32:35], v[168:171]\n"
"ds_read_b128 a[132:135], v1 offset:25088\n"
"v_mul_f32_e32 v30, s44, v30\n"
"v_mfma_f32_16x16x32_bf16 v[172:175], a[204:207], v[32:35], v[172:175]\n"
"v_exp_f32_e32 v29, v29\n"
"v_mfma_f32_16x16x32_bf16 v[176:179], a[208:211], v[32:35], v[176:179]\n"
"s_add_u32 m0, s48, 16384\n"
"v_mul_f32_e32 v31, s44, v31\n"
"v_cvt_pk_bf16_f32 v38, v28, v29\n"
"v_mfma_f32_16x16x32_bf16 v[180:183], a[212:215], v[32:35], v[180:183]\n"
"s_add_u32 s47, s45, 114688\n"
"v_exp_f32_e32 v30, v30\n"
"v_mfma_f32_16x16x32_bf16 v[184:187], a[216:219], v[32:35], v[184:187]\n"
"buffer_load_dwordx4 v6, s[24:27], s47 offen lds\n"
"v_mfma_f32_16x16x32_bf16 v[188:191], a[220:223], v[32:35], v[188:191]\n"
"v_exp_f32_e32 v31, v31\n"
"v_mfma_f32_16x16x32_bf16 a[240:243], a[224:227], v[32:35], a[240:243]\n"
"ds_read_b128 a[136:139], v1 offset:25600\n"
"v_cvt_pk_bf16_f32 v39, v30, v31\n"
"v_mfma_f32_16x16x32_bf16 v[24:27], a[96:99], a[16:19], 0\n"
"v_mul_f32_e32 v16, s44, v16\n"
"v_mul_f32_e32 v17, s44, v17\n"
"v_mfma_f32_16x16x32_bf16 v[24:27], a[100:103], a[20:23], v[24:27]\n"
"v_exp_f32_e32 v16, v16\n"
"v_mfma_f32_16x16x32_bf16 v[24:27], a[104:107], a[24:27], v[24:27]\n"
"ds_read_b128 a[144:147], v2 offset:24576\n"
"v_mul_f32_e32 v18, s44, v18\n"
"v_mfma_f32_16x16x32_bf16 v[24:27], a[108:111], a[28:31], v[24:27]\n"
"v_exp_f32_e32 v17, v17\n"
"v_mfma_f32_16x16x32_bf16 v[28:31], a[112:115], a[16:19], 0\n"
"v_mul_f32_e32 v19, s44, v19\n"
"v_cvt_pk_bf16_f32 v32, v16, v17\n"
"v_mfma_f32_16x16x32_bf16 v[28:31], a[116:119], a[20:23], v[28:31]\n"
"v_exp_f32_e32 v18, v18\n"
"v_mfma_f32_16x16x32_bf16 v[28:31], a[120:123], a[24:27], v[28:31]\n"
"ds_read_b128 a[148:151], v2 offset:25088\n"
"v_mul_f32_e32 v20, s44, v20\n"
"v_mfma_f32_16x16x32_bf16 v[28:31], a[124:127], a[28:31], v[28:31]\n"
"v_exp_f32_e32 v19, v19\n"
"v_mfma_f32_16x16x32_bf16 v[192:195], a[192:195], v[36:39], v[192:195]\n"
"v_mul_f32_e32 v21, s44, v21\n"
"v_cvt_pk_bf16_f32 v33, v18, v19\n"
"v_mfma_f32_16x16x32_bf16 v[196:199], a[196:199], v[36:39], v[196:199]\n"
"v_exp_f32_e32 v20, v20\n"
"v_mfma_f32_16x16x32_bf16 v[200:203], a[200:203], v[36:39], v[200:203]\n"
"ds_read_b128 a[152:155], v2 offset:25600\n"
"v_mul_f32_e32 v22, s44, v22\n"
"v_mfma_f32_16x16x32_bf16 v[204:207], a[204:207], v[36:39], v[204:207]\n"
"v_exp_f32_e32 v21, v21\n"
"v_mfma_f32_16x16x32_bf16 v[208:211], a[208:211], v[36:39], v[208:211]\n"
"s_add_u32 m0, s48, 17408\n"
"v_mul_f32_e32 v23, s44, v23\n"
"v_cvt_pk_bf16_f32 v34, v20, v21\n"
"v_mfma_f32_16x16x32_bf16 v[212:215], a[212:215], v[36:39], v[212:215]\n"
"s_add_u32 s47, s45, 114816\n"
"v_exp_f32_e32 v22, v22\n"
"v_mfma_f32_16x16x32_bf16 v[216:219], a[216:219], v[36:39], v[216:219]\n"
"buffer_load_dwordx4 v6, s[24:27], s47 offen lds\n"
"v_mfma_f32_16x16x32_bf16 v[220:223], a[220:223], v[36:39], v[220:223]\n"
"v_exp_f32_e32 v23, v23\n"
"v_mfma_f32_16x16x32_bf16 a[244:247], a[224:227], v[36:39], a[244:247]\n"
"ds_read_b128 a[156:159], v2 offset:26112\n"
"v_cvt_pk_bf16_f32 v35, v22, v23\n"
"v_mfma_f32_16x16x32_bf16 v[16:19], a[96:99], a[32:35], 0\n"
"ds_read_b64_tr_b16 a[198:199], v14 offset:24576\n"
"v_mul_f32_e32 v24, s44, v24\n"
"v_mul_f32_e32 v25, s44, v25\n"
"v_mfma_f32_16x16x32_bf16 v[16:19], a[100:103], a[36:39], v[16:19]\n"
"ds_read_b64_tr_b16 a[200:201], v3 offset:25088\n"
"v_exp_f32_e32 v24, v24\n"
"v_mfma_f32_16x16x32_bf16 v[16:19], a[104:107], a[40:43], v[16:19]\n"
"ds_read_b64_tr_b16 a[192:193], v3 offset:24576\n"
"ds_read_b64_tr_b16 a[202:203], v4 offset:25088\n"
"v_mul_f32_e32 v26, s44, v26\n"
"v_mfma_f32_16x16x32_bf16 v[16:19], a[108:111], a[44:47], v[16:19]\n"
"v_exp_f32_e32 v25, v25\n"
"v_mfma_f32_16x16x32_bf16 v[20:23], a[112:115], a[32:35], 0\n"
"v_mul_f32_e32 v27, s44, v27\n"
"v_cvt_pk_bf16_f32 v36, v24, v25\n"
"v_mfma_f32_16x16x32_bf16 v[20:23], a[116:119], a[36:39], v[20:23]\n"
"v_exp_f32_e32 v26, v26\n"
"v_mfma_f32_16x16x32_bf16 v[20:23], a[120:123], a[40:43], v[20:23]\n"
"ds_read_b64_tr_b16 a[194:195], v4 offset:24576\n"
"v_mul_f32_e32 v28, s44, v28\n"
"v_mfma_f32_16x16x32_bf16 v[20:23], a[124:127], a[44:47], v[20:23]\n"
"v_exp_f32_e32 v27, v27\n"
"v_mfma_f32_16x16x32_bf16 v[64:67], a[160:163], v[32:35], v[64:67]\n"
"s_add_u32 m0, s49, 18432\n"
"v_mul_f32_e32 v29, s44, v29\n"
"v_cvt_pk_bf16_f32 v37, v26, v27\n"
"v_mfma_f32_16x16x32_bf16 v[68:71], a[164:167], v[32:35], v[68:71]\n"
"s_add_u32 s61, s58, 16384\n"
"v_exp_f32_e32 v28, v28\n"
"v_mfma_f32_16x16x32_bf16 v[72:75], a[168:171], v[32:35], v[72:75]\n"
"buffer_load_dwordx4 v8, s[36:39], s61 offen  nt lds\n"
"v_mul_f32_e32 v30, s44, v30\n"
"v_mfma_f32_16x16x32_bf16 v[76:79], a[172:175], v[32:35], v[76:79]\n"
"v_exp_f32_e32 v29, v29\n"
"v_mfma_f32_16x16x32_bf16 v[80:83], a[176:179], v[32:35], v[80:83]\n"
"s_add_u32 m0, s48, 49152\n"
"v_mul_f32_e32 v31, s44, v31\n"
"v_cvt_pk_bf16_f32 v38, v28, v29\n"
"v_mfma_f32_16x16x32_bf16 v[84:87], a[180:183], v[32:35], v[84:87]\n"
"s_add_u32 s76, s45, 114688\n"
"v_exp_f32_e32 v30, v30\n"
"v_mfma_f32_16x16x32_bf16 v[88:91], a[184:187], v[32:35], v[88:91]\n"
"buffer_load_dwordx4 v6, s[28:31], s76 offen lds\n"
"v_mfma_f32_16x16x32_bf16 v[92:95], a[188:191], v[32:35], v[92:95]\n"
"v_exp_f32_e32 v31, v31\n"
"v_mfma_f32_16x16x32_bf16 a[228:231], a[224:227], v[32:35], a[228:231]\n"
"ds_read_b64_tr_b16 a[196:197], v11 offset:24576\n"
"v_cvt_pk_bf16_f32 v39, v30, v31\n"
"v_mfma_f32_16x16x32_bf16 v[24:27], a[96:99], a[48:51], 0\n"
"ds_read_b64_tr_b16 a[212:213], v11 offset:25600\n"
"v_mul_f32_e32 v16, s44, v16\n"
"v_mul_f32_e32 v17, s44, v17\n"
"v_mfma_f32_16x16x32_bf16 v[24:27], a[100:103], a[52:55], v[24:27]\n"
"ds_read_b64_tr_b16 a[214:215], v14 offset:25600\n"
"v_exp_f32_e32 v16, v16\n"
"v_mfma_f32_16x16x32_bf16 v[24:27], a[104:107], a[56:59], v[24:27]\n"
"ds_read_b64_tr_b16 a[204:205], v11 offset:25088\n"
"v_mul_f32_e32 v18, s44, v18\n"
"v_mfma_f32_16x16x32_bf16 v[24:27], a[108:111], a[60:63], v[24:27]\n"
"v_exp_f32_e32 v17, v17\n"
"v_mfma_f32_16x16x32_bf16 v[28:31], a[112:115], a[48:51], 0\n"
"v_mul_f32_e32 v19, s44, v19\n"
"v_cvt_pk_bf16_f32 v32, v16, v17\n"
"v_mfma_f32_16x16x32_bf16 v[28:31], a[116:119], a[52:55], v[28:31]\n"
"v_exp_f32_e32 v18, v18\n"
"v_mfma_f32_16x16x32_bf16 v[28:31], a[120:123], a[56:59], v[28:31]\n"
"ds_read_b64_tr_b16 a[206:207], v14 offset:25088\n"
"v_mul_f32_e32 v20, s44, v20\n"
"v_mfma_f32_16x16x32_bf16 v[28:31], a[124:127], a[60:63], v[28:31]\n"
"v_exp_f32_e32 v19, v19\n"
"v_mfma_f32_16x16x32_bf16 v[96:99], a[160:163], v[36:39], v[96:99]\n"
"v_mul_f32_e32 v21, s44, v21\n"
"v_cvt_pk_bf16_f32 v33, v18, v19\n"
"v_mfma_f32_16x16x32_bf16 v[100:103], a[164:167], v[36:39], v[100:103]\n"
"v_exp_f32_e32 v20, v20\n"
"v_mfma_f32_16x16x32_bf16 v[104:107], a[168:171], v[36:39], v[104:107]\n"
"ds_read_b64_tr_b16 a[208:209], v3 offset:25600\n"
"v_mul_f32_e32 v22, s44, v22\n"
"v_mfma_f32_16x16x32_bf16 v[108:111], a[172:175], v[36:39], v[108:111]\n"
"v_exp_f32_e32 v21, v21\n"
"v_mfma_f32_16x16x32_bf16 v[112:115], a[176:179], v[36:39], v[112:115]\n"
"s_add_u32 m0, s48, 50176\n"
"v_mul_f32_e32 v23, s44, v23\n"
"v_cvt_pk_bf16_f32 v34, v20, v21\n"
"v_mfma_f32_16x16x32_bf16 v[116:119], a[180:183], v[36:39], v[116:119]\n"
"s_add_u32 s76, s45, 114816\n"
"v_exp_f32_e32 v22, v22\n"
"v_mfma_f32_16x16x32_bf16 v[120:123], a[184:187], v[36:39], v[120:123]\n"
"buffer_load_dwordx4 v6, s[28:31], s76 offen lds\n"
"v_mfma_f32_16x16x32_bf16 v[124:127], a[188:191], v[36:39], v[124:127]\n"
"v_exp_f32_e32 v23, v23\n"
"v_mfma_f32_16x16x32_bf16 a[232:235], a[224:227], v[36:39], a[232:235]\n"
"ds_read_b64_tr_b16 a[210:211], v4 offset:25600\n"
"v_cvt_pk_bf16_f32 v35, v22, v23\n"
"v_mfma_f32_16x16x32_bf16 v[16:19], a[96:99], a[64:67], 0\n"
"ds_read_b64_tr_b16 a[222:223], v14 offset:26112\n"
"v_mul_f32_e32 v24, s44, v24\n"
"v_mul_f32_e32 v25, s44, v25\n"
"v_mfma_f32_16x16x32_bf16 v[16:19], a[100:103], a[68:71], v[16:19]\n"
"v_exp_f32_e32 v24, v24\n"
"v_mfma_f32_16x16x32_bf16 v[16:19], a[104:107], a[72:75], v[16:19]\n"
"ds_read_b64_tr_b16 a[216:217], v3 offset:26112\n"
"v_mul_f32_e32 v26, s44, v26\n"
"v_mfma_f32_16x16x32_bf16 v[16:19], a[108:111], a[76:79], v[16:19]\n"
"v_exp_f32_e32 v25, v25\n"
"v_mfma_f32_16x16x32_bf16 v[20:23], a[112:115], a[64:67], 0\n"
"v_mul_f32_e32 v27, s44, v27\n"
"v_cvt_pk_bf16_f32 v36, v24, v25\n"
"v_mfma_f32_16x16x32_bf16 v[20:23], a[116:119], a[68:71], v[20:23]\n"
"v_exp_f32_e32 v26, v26\n"
"v_mfma_f32_16x16x32_bf16 v[20:23], a[120:123], a[72:75], v[20:23]\n"
"ds_read_b64_tr_b16 a[218:219], v4 offset:26112\n"
"v_mul_f32_e32 v28, s44, v28\n"
"v_mfma_f32_16x16x32_bf16 v[20:23], a[124:127], a[76:79], v[20:23]\n"
"v_exp_f32_e32 v27, v27\n"
"v_mfma_f32_16x16x32_bf16 v[128:131], a[160:163], v[32:35], v[128:131]\n"
"v_mul_f32_e32 v29, s44, v29\n"
"v_cvt_pk_bf16_f32 v37, v26, v27\n"
"v_mfma_f32_16x16x32_bf16 v[132:135], a[164:167], v[32:35], v[132:135]\n"
"v_exp_f32_e32 v28, v28\n"
"v_mfma_f32_16x16x32_bf16 v[136:139], a[168:171], v[32:35], v[136:139]\n"
"ds_read_b64_tr_b16 a[220:221], v11 offset:26112\n"
"v_mul_f32_e32 v30, s44, v30\n"
"v_mfma_f32_16x16x32_bf16 v[140:143], a[172:175], v[32:35], v[140:143]\n"
"v_exp_f32_e32 v29, v29\n"
"v_mfma_f32_16x16x32_bf16 v[144:147], a[176:179], v[32:35], v[144:147]\n"
"s_add_u32 m0, s49, 19456\n"
"v_mul_f32_e32 v31, s44, v31\n"
"v_cvt_pk_bf16_f32 v38, v28, v29\n"
"v_mfma_f32_16x16x32_bf16 v[148:151], a[180:183], v[32:35], v[148:151]\n"
"s_add_u32 s61, s58, 16512\n"
"v_exp_f32_e32 v30, v30\n"
"v_mfma_f32_16x16x32_bf16 v[152:155], a[184:187], v[32:35], v[152:155]\n"
"buffer_load_dwordx4 v8, s[36:39], s61 offen  nt lds\n"
"v_mfma_f32_16x16x32_bf16 v[156:159], a[188:191], v[32:35], v[156:159]\n"
"v_exp_f32_e32 v31, v31\n"
"v_mfma_f32_16x16x32_bf16 a[236:239], a[224:227], v[32:35], a[236:239]\n"
"s_waitcnt lgkmcnt(15)\n"
"v_cvt_pk_bf16_f32 v39, v30, v31\n"
"v_mfma_f32_16x16x32_bf16 v[24:27], a[128:131], a[0:3], 0\n"
"v_mul_f32_e32 v16, s44, v16\n"
"v_mul_f32_e32 v17, s44, v17\n"
"v_mfma_f32_16x16x32_bf16 v[24:27], a[132:135], a[4:7], v[24:27]\n"
"v_exp_f32_e32 v16, v16\n"
"v_mfma_f32_16x16x32_bf16 v[24:27], a[136:139], a[8:11], v[24:27]\n"
"v_mul_f32_e32 v18, s44, v18\n"
"v_mfma_f32_16x16x32_bf16 v[24:27], a[140:143], a[12:15], v[24:27]\n"
"v_exp_f32_e32 v17, v17\n"
"v_mfma_f32_16x16x32_bf16 v[28:31], a[144:147], a[0:3], 0\n"
"v_mul_f32_e32 v19, s44, v19\n"
"v_cvt_pk_bf16_f32 v32, v16, v17\n"
"v_mfma_f32_16x16x32_bf16 v[28:31], a[148:151], a[4:7], v[28:31]\n"
"s_waitcnt vmcnt(18) lgkmcnt(0)\n"
"s_barrier\n"
"v_exp_f32_e32 v18, v18\n"
"v_mfma_f32_16x16x32_bf16 v[28:31], a[152:155], a[8:11], v[28:31]\n"
"ds_read_b128 a[96:99], v1 offset:0\n"
"ds_read_b128 a[108:111], v1 offset:1536\n"
"v_mul_f32_e32 v20, s44, v20\n"
"v_mfma_f32_16x16x32_bf16 v[28:31], a[156:159], a[12:15], v[28:31]\n"
"ds_read_b128 v[40:43], v58 offset:20480\n"
"v_exp_f32_e32 v19, v19\n"
"v_mfma_f32_16x16x32_bf16 v[160:163], a[160:163], v[36:39], v[160:163]\n"
"ds_read_b128 v[44:47], v59 offset:21504\n"
"v_mul_f32_e32 v21, s44, v21\n"
"v_cvt_pk_bf16_f32 v33, v18, v19\n"
"v_mfma_f32_16x16x32_bf16 v[164:167], a[164:167], v[36:39], v[164:167]\n"
"ds_read_b128 v[48:51], v58 offset:22528\n"
"v_exp_f32_e32 v20, v20\n"
"v_mfma_f32_16x16x32_bf16 v[168:171], a[168:171], v[36:39], v[168:171]\n"
"ds_read_b128 a[100:103], v1 offset:512\n"
"ds_read_b128 v[52:55], v59 offset:23552\n"
"v_mul_f32_e32 v22, s44, v22\n"
"v_mfma_f32_16x16x32_bf16 v[172:175], a[172:175], v[36:39], v[172:175]\n"
"v_exp_f32_e32 v21, v21\n"
"v_mfma_f32_16x16x32_bf16 v[176:179], a[176:179], v[36:39], v[176:179]\n"
"s_add_u32 m0, s48, 24576\n"
"v_mul_f32_e32 v23, s44, v23\n"
"v_cvt_pk_bf16_f32 v34, v20, v21\n"
"v_mfma_f32_16x16x32_bf16 v[180:183], a[180:183], v[36:39], v[180:183]\n"
"s_add_u32 s47, s45, 122880\n"
"v_exp_f32_e32 v22, v22\n"
"v_mfma_f32_16x16x32_bf16 v[184:187], a[184:187], v[36:39], v[184:187]\n"
"buffer_load_dwordx4 v6, s[24:27], s47 offen lds\n"
"v_mfma_f32_16x16x32_bf16 v[188:191], a[188:191], v[36:39], v[188:191]\n"
"v_exp_f32_e32 v23, v23\n"
"v_mfma_f32_16x16x32_bf16 a[240:243], a[224:227], v[36:39], a[240:243]\n"
"ds_read_b128 a[104:107], v1 offset:1024\n"
"v_cvt_pk_bf16_f32 v35, v22, v23\n"
"v_mfma_f32_16x16x32_bf16 v[16:19], a[128:131], a[16:19], 0\n"
"v_mul_f32_e32 v24, s44, v24\n"
"v_mul_f32_e32 v25, s44, v25\n"
"v_mfma_f32_16x16x32_bf16 v[16:19], a[132:135], a[20:23], v[16:19]\n"
"v_exp_f32_e32 v24, v24\n"
"s_add_u32 s77, s62, 20480\n"
"v_mfma_f32_16x16x32_bf16 v[16:19], a[136:139], a[24:27], v[16:19]\n"
"ds_read_b128 a[112:115], v2 offset:0\n"
"v_mul_f32_e32 v26, s44, v26\n"
"s_waitcnt lgkmcnt(6)\n"
"buffer_store_dwordx4 v[40:43], v9, s[40:43], s77 offen offset:0  sc1\n"
"v_mfma_f32_16x16x32_bf16 v[16:19], a[140:143], a[28:31], v[16:19]\n"
"v_exp_f32_e32 v25, v25\n"
"v_mfma_f32_16x16x32_bf16 v[20:23], a[144:147], a[16:19], 0\n"
"v_mul_f32_e32 v27, s44, v27\n"
"v_cvt_pk_bf16_f32 v36, v24, v25\n"
"v_mfma_f32_16x16x32_bf16 v[20:23], a[148:151], a[20:23], v[20:23]\n"
"v_exp_f32_e32 v26, v26\n"
"v_mfma_f32_16x16x32_bf16 v[20:23], a[152:155], a[24:27], v[20:23]\n"
"ds_read_b128 a[116:119], v2 offset:512\n"
"v_mul_f32_e32 v28, s44, v28\n"
"s_waitcnt lgkmcnt(6)\n"
"buffer_store_dwordx4 v[44:47], v9, s[40:43], s77 offen offset:1024  sc1\n"
"v_mfma_f32_16x16x32_bf16 v[20:23], a[156:159], a[28:31], v[20:23]\n"
"v_exp_f32_e32 v27, v27\n"
"v_mfma_f32_16x16x32_bf16 v[192:195], a[160:163], v[32:35], v[192:195]\n"
"v_mul_f32_e32 v29, s44, v29\n"
"v_cvt_pk_bf16_f32 v37, v26, v27\n"
"v_mfma_f32_16x16x32_bf16 v[196:199], a[164:167], v[32:35], v[196:199]\n"
"v_exp_f32_e32 v28, v28\n"
"v_mfma_f32_16x16x32_bf16 v[200:203], a[168:171], v[32:35], v[200:203]\n"
"ds_read_b128 a[120:123], v2 offset:1024\n"
"v_mul_f32_e32 v30, s44, v30\n"
"s_waitcnt lgkmcnt(6)\n"
"buffer_store_dwordx4 v[48:51], v9, s[40:43], s77 offen offset:2048  sc1\n"
"v_mfma_f32_16x16x32_bf16 v[204:207], a[172:175], v[32:35], v[204:207]\n"
"v_exp_f32_e32 v29, v29\n"
"v_mfma_f32_16x16x32_bf16 v[208:211], a[176:179], v[32:35], v[208:211]\n"
"s_add_u32 m0, s48, 25600\n"
"v_mul_f32_e32 v31, s44, v31\n"
"v_cvt_pk_bf16_f32 v38, v28, v29\n"
"v_mfma_f32_16x16x32_bf16 v[212:215], a[180:183], v[32:35], v[212:215]\n"
"s_add_u32 s47, s45, 123008\n"
"v_exp_f32_e32 v30, v30\n"
"v_mfma_f32_16x16x32_bf16 v[216:219], a[184:187], v[32:35], v[216:219]\n"
"buffer_load_dwordx4 v6, s[24:27], s47 offen lds\n"
"v_mfma_f32_16x16x32_bf16 v[220:223], a[188:191], v[32:35], v[220:223]\n"
"v_exp_f32_e32 v31, v31\n"
"v_mfma_f32_16x16x32_bf16 a[244:247], a[224:227], v[32:35], a[244:247]\n"
"ds_read_b128 a[124:127], v2 offset:1536\n"
"v_cvt_pk_bf16_f32 v39, v30, v31\n"
"s_waitcnt lgkmcnt(5)\n"
"buffer_store_dwordx4 v[52:55], v9, s[40:43], s77 offen offset:3072  sc1\n"
"v_mfma_f32_16x16x32_bf16 v[24:27], a[128:131], a[32:35], 0\n"
"ds_read_b64_tr_b16 a[166:167], v14 offset:0\n"
"v_mul_f32_e32 v16, s44, v16\n"
"v_mul_f32_e32 v17, s44, v17\n"
"v_mfma_f32_16x16x32_bf16 v[24:27], a[132:135], a[36:39], v[24:27]\n"
"ds_read_b64_tr_b16 a[168:169], v3 offset:512\n"
"v_exp_f32_e32 v16, v16\n"
"v_mfma_f32_16x16x32_bf16 v[24:27], a[136:139], a[40:43], v[24:27]\n"
"ds_read_b64_tr_b16 a[160:161], v3 offset:0\n"
"ds_read_b64_tr_b16 a[170:171], v4 offset:512\n"
"v_mul_f32_e32 v18, s44, v18\n"
"v_mfma_f32_16x16x32_bf16 v[24:27], a[140:143], a[44:47], v[24:27]\n"
"v_exp_f32_e32 v17, v17\n"
"v_mfma_f32_16x16x32_bf16 v[28:31], a[144:147], a[32:35], 0\n"
"v_mul_f32_e32 v19, s44, v19\n"
"v_cvt_pk_bf16_f32 v32, v16, v17\n"
"v_mfma_f32_16x16x32_bf16 v[28:31], a[148:151], a[36:39], v[28:31]\n"
"v_exp_f32_e32 v18, v18\n"
"v_mfma_f32_16x16x32_bf16 v[28:31], a[152:155], a[40:43], v[28:31]\n"
"ds_read_b64_tr_b16 a[162:163], v4 offset:0\n"
"v_mul_f32_e32 v20, s44, v20\n"
"v_mfma_f32_16x16x32_bf16 v[28:31], a[156:159], a[44:47], v[28:31]\n"
"v_exp_f32_e32 v19, v19\n"
"v_mfma_f32_16x16x32_bf16 v[64:67], a[192:195], v[36:39], v[64:67]\n"
"s_add_u32 m0, s49, 20480\n"
"v_mul_f32_e32 v21, s44, v21\n"
"v_cvt_pk_bf16_f32 v33, v18, v19\n"
"v_mfma_f32_16x16x32_bf16 v[68:71], a[196:199], v[36:39], v[68:71]\n"
"s_add_u32 s61, s59, 20480\n"
"v_exp_f32_e32 v20, v20\n"
"v_mfma_f32_16x16x32_bf16 v[72:75], a[200:203], v[36:39], v[72:75]\n"
"buffer_load_dwordx4 v7, s[36:39], s61 offen  nt lds\n"
"v_mul_f32_e32 v22, s44, v22\n"
"v_mfma_f32_16x16x32_bf16 v[76:79], a[204:207], v[36:39], v[76:79]\n"
"v_exp_f32_e32 v21, v21\n"
"v_mfma_f32_16x16x32_bf16 v[80:83], a[208:211], v[36:39], v[80:83]\n"
"s_add_u32 m0, s48, 57344\n"
"v_mul_f32_e32 v23, s44, v23\n"
"v_cvt_pk_bf16_f32 v34, v20, v21\n"
"v_mfma_f32_16x16x32_bf16 v[84:87], a[212:215], v[36:39], v[84:87]\n"
"s_add_u32 s76, s45, 122880\n"
"v_exp_f32_e32 v22, v22\n"
"v_mfma_f32_16x16x32_bf16 v[88:91], a[216:219], v[36:39], v[88:91]\n"
"buffer_load_dwordx4 v6, s[28:31], s76 offen lds\n"
"v_mfma_f32_16x16x32_bf16 v[92:95], a[220:223], v[36:39], v[92:95]\n"
"v_exp_f32_e32 v23, v23\n"
"v_mfma_f32_16x16x32_bf16 a[228:231], a[224:227], v[36:39], a[228:231]\n"
"ds_read_b64_tr_b16 a[164:165], v11 offset:0\n"
"v_cvt_pk_bf16_f32 v35, v22, v23\n"
"v_mfma_f32_16x16x32_bf16 v[16:19], a[128:131], a[48:51], 0\n"
"ds_read_b64_tr_b16 a[180:181], v11 offset:1024\n"
"v_mul_f32_e32 v24, s44, v24\n"
"v_mul_f32_e32 v25, s44, v25\n"
"v_mfma_f32_16x16x32_bf16 v[16:19], a[132:135], a[52:55], v[16:19]\n"
"ds_read_b64_tr_b16 a[182:183], v14 offset:1024\n"
"v_exp_f32_e32 v24, v24\n"
"v_mfma_f32_16x16x32_bf16 v[16:19], a[136:139], a[56:59], v[16:19]\n"
"ds_read_b64_tr_b16 a[172:173], v11 offset:512\n"
"v_mul_f32_e32 v26, s44, v26\n"
"v_mfma_f32_16x16x32_bf16 v[16:19], a[140:143], a[60:63], v[16:19]\n"
"v_exp_f32_e32 v25, v25\n"
"v_mfma_f32_16x16x32_bf16 v[20:23], a[144:147], a[48:51], 0\n"
"v_mul_f32_e32 v27, s44, v27\n"
"v_cvt_pk_bf16_f32 v36, v24, v25\n"
"v_mfma_f32_16x16x32_bf16 v[20:23], a[148:151], a[52:55], v[20:23]\n"
"v_exp_f32_e32 v26, v26\n"
"v_mfma_f32_16x16x32_bf16 v[20:23], a[152:155], a[56:59], v[20:23]\n"
"ds_read_b64_tr_b16 a[174:175], v14 offset:512\n"
"v_mul_f32_e32 v28, s44, v28\n"
"v_mfma_f32_16x16x32_bf16 v[20:23], a[156:159], a[60:63], v[20:23]\n"
"v_exp_f32_e32 v27, v27\n"
"v_mfma_f32_16x16x32_bf16 v[96:99], a[192:195], v[32:35], v[96:99]\n"
"v_mul_f32_e32 v29, s44, v29\n"
"v_cvt_pk_bf16_f32 v37, v26, v27\n"
"v_mfma_f32_16x16x32_bf16 v[100:103], a[196:199], v[32:35], v[100:103]\n"
"v_exp_f32_e32 v28, v28\n"
"v_mfma_f32_16x16x32_bf16 v[104:107], a[200:203], v[32:35], v[104:107]\n"
"ds_read_b64_tr_b16 a[176:177], v3 offset:1024\n"
"v_mul_f32_e32 v30, s44, v30\n"
"v_mfma_f32_16x16x32_bf16 v[108:111], a[204:207], v[32:35], v[108:111]\n"
"v_exp_f32_e32 v29, v29\n"
"v_mfma_f32_16x16x32_bf16 v[112:115], a[208:211], v[32:35], v[112:115]\n"
"s_add_u32 m0, s48, 58368\n"
"v_mul_f32_e32 v31, s44, v31\n"
"v_cvt_pk_bf16_f32 v38, v28, v29\n"
"v_mfma_f32_16x16x32_bf16 v[116:119], a[212:215], v[32:35], v[116:119]\n"
"s_add_u32 s76, s45, 123008\n"
"v_exp_f32_e32 v30, v30\n"
"v_mfma_f32_16x16x32_bf16 v[120:123], a[216:219], v[32:35], v[120:123]\n"
"buffer_load_dwordx4 v6, s[28:31], s76 offen lds\n"
"v_mfma_f32_16x16x32_bf16 v[124:127], a[220:223], v[32:35], v[124:127]\n"
"v_exp_f32_e32 v31, v31\n"
"v_mfma_f32_16x16x32_bf16 a[232:235], a[224:227], v[32:35], a[232:235]\n"
"ds_read_b64_tr_b16 a[178:179], v4 offset:1024\n"
"v_cvt_pk_bf16_f32 v39, v30, v31\n"
"v_mfma_f32_16x16x32_bf16 v[24:27], a[128:131], a[64:67], 0\n"
"ds_read_b64_tr_b16 a[190:191], v14 offset:1536\n"
"v_mul_f32_e32 v16, s44, v16\n"
"v_mul_f32_e32 v17, s44, v17\n"
"v_mfma_f32_16x16x32_bf16 v[24:27], a[132:135], a[68:71], v[24:27]\n"
"v_exp_f32_e32 v16, v16\n"
"v_mfma_f32_16x16x32_bf16 v[24:27], a[136:139], a[72:75], v[24:27]\n"
"ds_read_b64_tr_b16 a[184:185], v3 offset:1536\n"
"v_mul_f32_e32 v18, s44, v18\n"
"v_mfma_f32_16x16x32_bf16 v[24:27], a[140:143], a[76:79], v[24:27]\n"
"v_exp_f32_e32 v17, v17\n"
"v_mfma_f32_16x16x32_bf16 v[28:31], a[144:147], a[64:67], 0\n"
"v_mul_f32_e32 v19, s44, v19\n"
"v_cvt_pk_bf16_f32 v32, v16, v17\n"
"v_mfma_f32_16x16x32_bf16 v[28:31], a[148:151], a[68:71], v[28:31]\n"
"v_exp_f32_e32 v18, v18\n"
"v_mfma_f32_16x16x32_bf16 v[28:31], a[152:155], a[72:75], v[28:31]\n"
"ds_read_b64_tr_b16 a[186:187], v4 offset:1536\n"
"v_mul_f32_e32 v20, s44, v20\n"
"v_mfma_f32_16x16x32_bf16 v[28:31], a[156:159], a[76:79], v[28:31]\n"
"v_exp_f32_e32 v19, v19\n"
"v_mfma_f32_16x16x32_bf16 v[128:131], a[192:195], v[36:39], v[128:131]\n"
"v_mul_f32_e32 v21, s44, v21\n"
"v_cvt_pk_bf16_f32 v33, v18, v19\n"
"v_mfma_f32_16x16x32_bf16 v[132:135], a[196:199], v[36:39], v[132:135]\n"
"v_exp_f32_e32 v20, v20\n"
"v_mfma_f32_16x16x32_bf16 v[136:139], a[200:203], v[36:39], v[136:139]\n"
"ds_read_b64_tr_b16 a[188:189], v11 offset:1536\n"
"v_mul_f32_e32 v22, s44, v22\n"
"v_mfma_f32_16x16x32_bf16 v[140:143], a[204:207], v[36:39], v[140:143]\n"
"v_exp_f32_e32 v21, v21\n"
"v_mfma_f32_16x16x32_bf16 v[144:147], a[208:211], v[36:39], v[144:147]\n"
"s_add_u32 m0, s49, 21504\n"
"v_mul_f32_e32 v23, s44, v23\n"
"v_cvt_pk_bf16_f32 v34, v20, v21\n"
"v_mfma_f32_16x16x32_bf16 v[148:151], a[212:215], v[36:39], v[148:151]\n"
"s_add_u32 s61, s59, 20608\n"
"v_exp_f32_e32 v22, v22\n"
"v_mfma_f32_16x16x32_bf16 v[152:155], a[216:219], v[36:39], v[152:155]\n"
"buffer_load_dwordx4 v7, s[36:39], s61 offen  nt lds\n"
"v_mfma_f32_16x16x32_bf16 v[156:159], a[220:223], v[36:39], v[156:159]\n"
"v_exp_f32_e32 v23, v23\n"
"v_mfma_f32_16x16x32_bf16 a[236:239], a[224:227], v[36:39], a[236:239]\n"
"v_cvt_pk_bf16_f32 v35, v22, v23\n"
"s_mul_i32 s70, s50, 3014656\n"
"s_cmp_gt_u32 s63, s50\n"
"s_cselect_b32 s71, 0, 16384\n"
"s_add_u32 s70, s70, s71\n"
"s_add_u32 s40, s68, s70\n"
"s_addc_u32 s41, s69, 0\n"
"s_cmp_eq_u32 s63, s50\n"
"s_cselect_b32 s71, 5, 6\n"
"s_mul_i32 s42, s71, 16384\n"
"s_mul_i32 s60, s64, s71\n"
"s_mul_i32 s60, s60, 4096\n"
"s_cselect_b32 s62, 1073741824, s60\n"
"v_mfma_f32_16x16x32_bf16 v[16:19], a[96:99], a[0:3], 0\n"
"v_mul_f32_e32 v24, s44, v24\n"
"v_mul_f32_e32 v25, s44, v25\n"
"v_mfma_f32_16x16x32_bf16 v[16:19], a[100:103], a[4:7], v[16:19]\n"
"s_waitcnt lgkmcnt(15)\n"
"v_exp_f32_e32 v24, v24\n"
"v_mfma_f32_16x16x32_bf16 v[16:19], a[104:107], a[8:11], v[16:19]\n"
"v_mul_f32_e32 v26, s44, v26\n"
"v_mfma_f32_16x16x32_bf16 v[16:19], a[108:111], a[12:15], v[16:19]\n"
"v_exp_f32_e32 v25, v25\n"
"v_mfma_f32_16x16x32_bf16 v[20:23], a[112:115], a[0:3], 0\n"
"v_mul_f32_e32 v27, s44, v27\n"
"v_cvt_pk_bf16_f32 v36, v24, v25\n"
"v_mfma_f32_16x16x32_bf16 v[20:23], a[116:119], a[4:7], v[20:23]\n"
"s_waitcnt vmcnt(17) lgkmcnt(0)\n"
"s_barrier\n"
"v_exp_f32_e32 v26, v26\n"
"v_mfma_f32_16x16x32_bf16 v[20:23], a[120:123], a[8:11], v[20:23]\n"
"ds_read_b128 a[128:131], v1 offset:8192\n"
"ds_read_b128 a[140:143], v1 offset:9728\n"
"v_mul_f32_e32 v28, s44, v28\n"
"v_mfma_f32_16x16x32_bf16 v[20:23], a[124:127], a[12:15], v[20:23]\n"
"v_exp_f32_e32 v27, v27\n"
"v_mfma_f32_16x16x32_bf16 v[160:163], a[192:195], v[32:35], v[160:163]\n"
"v_mul_f32_e32 v29, s44, v29\n"
"v_cvt_pk_bf16_f32 v37, v26, v27\n"
"v_mfma_f32_16x16x32_bf16 v[164:167], a[196:199], v[32:35], v[164:167]\n"
"v_exp_f32_e32 v28, v28\n"
"v_mfma_f32_16x16x32_bf16 v[168:171], a[200:203], v[32:35], v[168:171]\n"
"ds_read_b128 a[132:135], v1 offset:8704\n"
"v_mul_f32_e32 v30, s44, v30\n"
"v_mfma_f32_16x16x32_bf16 v[172:175], a[204:207], v[32:35], v[172:175]\n"
"v_exp_f32_e32 v29, v29\n"
"v_mfma_f32_16x16x32_bf16 v[176:179], a[208:211], v[32:35], v[176:179]\n"
"s_add_u32 m0, s48, 0\n"
"v_mul_f32_e32 v31, s44, v31\n"
"v_cvt_pk_bf16_f32 v38, v28, v29\n"
"v_mfma_f32_16x16x32_bf16 v[180:183], a[212:215], v[32:35], v[180:183]\n"
"s_add_u32 s47, s46, 0\n"
"v_exp_f32_e32 v30, v30\n"
"v_mfma_f32_16x16x32_bf16 v[184:187], a[216:219], v[32:35], v[184:187]\n"
"buffer_load_dwordx4 v6, s[24:27], s47 offen lds\n"
"v_mfma_f32_16x16x32_bf16 v[188:191], a[220:223], v[32:35], v[188:191]\n"
"v_exp_f32_e32 v31, v31\n"
"v_mfma_f32_16x16x32_bf16 a[240:243], a[224:227], v[32:35], a[240:243]\n"
"ds_read_b128 a[136:139], v1 offset:9216\n"
"v_cvt_pk_bf16_f32 v39, v30, v31\n"
"v_mfma_f32_16x16x32_bf16 v[24:27], a[96:99], a[16:19], 0\n"
"v_mul_f32_e32 v16, s44, v16\n"
"v_mul_f32_e32 v17, s44, v17\n"
"v_mfma_f32_16x16x32_bf16 v[24:27], a[100:103], a[20:23], v[24:27]\n"
"v_exp_f32_e32 v16, v16\n"
"v_mfma_f32_16x16x32_bf16 v[24:27], a[104:107], a[24:27], v[24:27]\n"
"ds_read_b128 a[144:147], v2 offset:8192\n"
"v_mul_f32_e32 v18, s44, v18\n"
"v_mfma_f32_16x16x32_bf16 v[24:27], a[108:111], a[28:31], v[24:27]\n"
"v_exp_f32_e32 v17, v17\n"
"v_mfma_f32_16x16x32_bf16 v[28:31], a[112:115], a[16:19], 0\n"
"v_mul_f32_e32 v19, s44, v19\n"
"v_cvt_pk_bf16_f32 v32, v16, v17\n"
"v_mfma_f32_16x16x32_bf16 v[28:31], a[116:119], a[20:23], v[28:31]\n"
"v_exp_f32_e32 v18, v18\n"
"v_mfma_f32_16x16x32_bf16 v[28:31], a[120:123], a[24:27], v[28:31]\n"
"ds_read_b128 a[148:151], v2 offset:8704\n"
"v_mul_f32_e32 v20, s44, v20\n"
"v_mfma_f32_16x16x32_bf16 v[28:31], a[124:127], a[28:31], v[28:31]\n"
"v_exp_f32_e32 v19, v19\n"
"v_mfma_f32_16x16x32_bf16 v[192:195], a[192:195], v[36:39], v[192:195]\n"
"v_mul_f32_e32 v21, s44, v21\n"
"v_cvt_pk_bf16_f32 v33, v18, v19\n"
"v_mfma_f32_16x16x32_bf16 v[196:199], a[196:199], v[36:39], v[196:199]\n"
"v_exp_f32_e32 v20, v20\n"
"v_mfma_f32_16x16x32_bf16 v[200:203], a[200:203], v[36:39], v[200:203]\n"
"ds_read_b128 a[152:155], v2 offset:9216\n"
"v_mul_f32_e32 v22, s44, v22\n"
"v_mfma_f32_16x16x32_bf16 v[204:207], a[204:207], v[36:39], v[204:207]\n"
"v_exp_f32_e32 v21, v21\n"
"v_mfma_f32_16x16x32_bf16 v[208:211], a[208:211], v[36:39], v[208:211]\n"
"s_add_u32 m0, s48, 1024\n"
"v_mul_f32_e32 v23, s44, v23\n"
"v_cvt_pk_bf16_f32 v34, v20, v21\n"
"v_mfma_f32_16x16x32_bf16 v[212:215], a[212:215], v[36:39], v[212:215]\n"
"s_add_u32 s47, s46, 128\n"
"v_exp_f32_e32 v22, v22\n"
"v_mfma_f32_16x16x32_bf16 v[216:219], a[216:219], v[36:39], v[216:219]\n"
"buffer_load_dwordx4 v6, s[24:27], s47 offen lds\n"
"v_mfma_f32_16x16x32_bf16 v[220:223], a[220:223], v[36:39], v[220:223]\n"
"v_exp_f32_e32 v23, v23\n"
"v_mfma_f32_16x16x32_bf16 a[244:247], a[224:227], v[36:39], a[244:247]\n"
"ds_read_b128 a[156:159], v2 offset:9728\n"
"v_cvt_pk_bf16_f32 v35, v22, v23\n"
"v_mfma_f32_16x16x32_bf16 v[16:19], a[96:99], a[32:35], 0\n"
"ds_read_b64_tr_b16 a[198:199], v14 offset:8192\n"
"v_mul_f32_e32 v24, s44, v24\n"
"v_mul_f32_e32 v25, s44, v25\n"
"v_mfma_f32_16x16x32_bf16 v[16:19], a[100:103], a[36:39], v[16:19]\n"
"ds_read_b64_tr_b16 a[200:201], v3 offset:8704\n"
"v_exp_f32_e32 v24, v24\n"
"v_mfma_f32_16x16x32_bf16 v[16:19], a[104:107], a[40:43], v[16:19]\n"
"ds_read_b64_tr_b16 a[192:193], v3 offset:8192\n"
"ds_read_b64_tr_b16 a[202:203], v4 offset:8704\n"
"v_mul_f32_e32 v26, s44, v26\n"
"v_mfma_f32_16x16x32_bf16 v[16:19], a[108:111], a[44:47], v[16:19]\n"
"v_exp_f32_e32 v25, v25\n"
"v_mfma_f32_16x16x32_bf16 v[20:23], a[112:115], a[32:35], 0\n"
"v_mul_f32_e32 v27, s44, v27\n"
"v_cvt_pk_bf16_f32 v36, v24, v25\n"
"v_mfma_f32_16x16x32_bf16 v[20:23], a[116:119], a[36:39], v[20:23]\n"
"v_exp_f32_e32 v26, v26\n"
"v_mfma_f32_16x16x32_bf16 v[20:23], a[120:123], a[40:43], v[20:23]\n"
"ds_read_b64_tr_b16 a[194:195], v4 offset:8192\n"
"v_mul_f32_e32 v28, s44, v28\n"
"v_mfma_f32_16x16x32_bf16 v[20:23], a[124:127], a[44:47], v[20:23]\n"
"v_exp_f32_e32 v27, v27\n"
"v_mfma_f32_16x16x32_bf16 v[64:67], a[160:163], v[32:35], v[64:67]\n"
"s_add_u32 m0, s49, 22528\n"
"v_mul_f32_e32 v29, s44, v29\n"
"v_cvt_pk_bf16_f32 v37, v26, v27\n"
"v_mfma_f32_16x16x32_bf16 v[68:71], a[164:167], v[32:35], v[68:71]\n"
"s_add_u32 s61, s59, 20480\n"
"v_exp_f32_e32 v28, v28\n"
"v_mfma_f32_16x16x32_bf16 v[72:75], a[168:171], v[32:35], v[72:75]\n"
"buffer_load_dwordx4 v8, s[36:39], s61 offen  nt lds\n"
"v_mul_f32_e32 v30, s44, v30\n"
"v_mfma_f32_16x16x32_bf16 v[76:79], a[172:175], v[32:35], v[76:79]\n"
"v_exp_f32_e32 v29, v29\n"
"v_mfma_f32_16x16x32_bf16 v[80:83], a[176:179], v[32:35], v[80:83]\n"
"s_add_u32 m0, s48, 32768\n"
"v_mul_f32_e32 v31, s44, v31\n"
"v_cvt_pk_bf16_f32 v38, v28, v29\n"
"v_mfma_f32_16x16x32_bf16 v[84:87], a[180:183], v[32:35], v[84:87]\n"
"s_add_u32 s76, s46, 0\n"
"v_exp_f32_e32 v30, v30\n"
"v_mfma_f32_16x16x32_bf16 v[88:91], a[184:187], v[32:35], v[88:91]\n"
"buffer_load_dwordx4 v6, s[28:31], s76 offen lds\n"
"v_mfma_f32_16x16x32_bf16 v[92:95], a[188:191], v[32:35], v[92:95]\n"
"v_exp_f32_e32 v31, v31\n"
"v_mfma_f32_16x16x32_bf16 a[228:231], a[224:227], v[32:35], a[228:231]\n"
"ds_read_b64_tr_b16 a[196:197], v11 offset:8192\n"
"v_cvt_pk_bf16_f32 v39, v30, v31\n"
"v_mfma_f32_16x16x32_bf16 v[24:27], a[96:99], a[48:51], 0\n"
"ds_read_b64_tr_b16 a[212:213], v11 offset:9216\n"
"v_mul_f32_e32 v16, s44, v16\n"
"v_mul_f32_e32 v17, s44, v17\n"
"v_mfma_f32_16x16x32_bf16 v[24:27], a[100:103], a[52:55], v[24:27]\n"
"ds_read_b64_tr_b16 a[214:215], v14 offset:9216\n"
"v_exp_f32_e32 v16, v16\n"
"v_mfma_f32_16x16x32_bf16 v[24:27], a[104:107], a[56:59], v[24:27]\n"
"ds_read_b64_tr_b16 a[204:205], v11 offset:8704\n"
"v_mul_f32_e32 v18, s44, v18\n"
"v_mfma_f32_16x16x32_bf16 v[24:27], a[108:111], a[60:63], v[24:27]\n"
"v_exp_f32_e32 v17, v17\n"
"v_mfma_f32_16x16x32_bf16 v[28:31], a[112:115], a[48:51], 0\n"
"v_mul_f32_e32 v19, s44, v19\n"
"v_cvt_pk_bf16_f32 v32, v16, v17\n"
"v_mfma_f32_16x16x32_bf16 v[28:31], a[116:119], a[52:55], v[28:31]\n"
"v_exp_f32_e32 v18, v18\n"
"v_mfma_f32_16x16x32_bf16 v[28:31], a[120:123], a[56:59], v[28:31]\n"
"ds_read_b64_tr_b16 a[206:207], v14 offset:8704\n"
"v_mul_f32_e32 v20, s44, v20\n"
"v_mfma_f32_16x16x32_bf16 v[28:31], a[124:127], a[60:63], v[28:31]\n"
"v_exp_f32_e32 v19, v19\n"
"v_mfma_f32_16x16x32_bf16 v[96:99], a[160:163], v[36:39], v[96:99]\n"
"v_mul_f32_e32 v21, s44, v21\n"
"v_cvt_pk_bf16_f32 v33, v18, v19\n"
"v_mfma_f32_16x16x32_bf16 v[100:103], a[164:167], v[36:39], v[100:103]\n"
"v_exp_f32_e32 v20, v20\n"
"v_mfma_f32_16x16x32_bf16 v[104:107], a[168:171], v[36:39], v[104:107]\n"
"ds_read_b64_tr_b16 a[208:209], v3 offset:9216\n"
"v_mul_f32_e32 v22, s44, v22\n"
"v_mfma_f32_16x16x32_bf16 v[108:111], a[172:175], v[36:39], v[108:111]\n"
"v_exp_f32_e32 v21, v21\n"
"v_mfma_f32_16x16x32_bf16 v[112:115], a[176:179], v[36:39], v[112:115]\n"
"s_add_u32 m0, s48, 33792\n"
"v_mul_f32_e32 v23, s44, v23\n"
"v_cvt_pk_bf16_f32 v34, v20, v21\n"
"v_mfma_f32_16x16x32_bf16 v[116:119], a[180:183], v[36:39], v[116:119]\n"
"s_add_u32 s76, s46, 128\n"
"v_exp_f32_e32 v22, v22\n"
"v_mfma_f32_16x16x32_bf16 v[120:123], a[184:187], v[36:39], v[120:123]\n"
"buffer_load_dwordx4 v6, s[28:31], s76 offen lds\n"
"v_mfma_f32_16x16x32_bf16 v[124:127], a[188:191], v[36:39], v[124:127]\n"
"v_exp_f32_e32 v23, v23\n"
"v_mfma_f32_16x16x32_bf16 a[232:235], a[224:227], v[36:39], a[232:235]\n"
"ds_read_b64_tr_b16 a[210:211], v4 offset:9216\n"
"v_cvt_pk_bf16_f32 v35, v22, v23\n"
"v_mfma_f32_16x16x32_bf16 v[16:19], a[96:99], a[64:67], 0\n"
"ds_read_b64_tr_b16 a[222:223], v14 offset:9728\n"
"v_mul_f32_e32 v24, s44, v24\n"
"v_mul_f32_e32 v25, s44, v25\n"
"v_mfma_f32_16x16x32_bf16 v[16:19], a[100:103], a[68:71], v[16:19]\n"
"v_exp_f32_e32 v24, v24\n"
"v_mfma_f32_16x16x32_bf16 v[16:19], a[104:107], a[72:75], v[16:19]\n"
"ds_read_b64_tr_b16 a[216:217], v3 offset:9728\n"
"v_mul_f32_e32 v26, s44, v26\n"
"v_mfma_f32_16x16x32_bf16 v[16:19], a[108:111], a[76:79], v[16:19]\n"
"v_exp_f32_e32 v25, v25\n"
"v_mfma_f32_16x16x32_bf16 v[20:23], a[112:115], a[64:67], 0\n"
"v_mul_f32_e32 v27, s44, v27\n"
"v_cvt_pk_bf16_f32 v36, v24, v25\n"
"v_mfma_f32_16x16x32_bf16 v[20:23], a[116:119], a[68:71], v[20:23]\n"
"v_exp_f32_e32 v26, v26\n"
"v_mfma_f32_16x16x32_bf16 v[20:23], a[120:123], a[72:75], v[20:23]\n"
"ds_read_b64_tr_b16 a[218:219], v4 offset:9728\n"
"v_mul_f32_e32 v28, s44, v28\n"
"v_mfma_f32_16x16x32_bf16 v[20:23], a[124:127], a[76:79], v[20:23]\n"
"v_exp_f32_e32 v27, v27\n"
"v_mfma_f32_16x16x32_bf16 v[128:131], a[160:163], v[32:35], v[128:131]\n"
"v_mul_f32_e32 v29, s44, v29\n"
"v_cvt_pk_bf16_f32 v37, v26, v27\n"
"v_mfma_f32_16x16x32_bf16 v[132:135], a[164:167], v[32:35], v[132:135]\n"
"v_exp_f32_e32 v28, v28\n"
"v_mfma_f32_16x16x32_bf16 v[136:139], a[168:171], v[32:35], v[136:139]\n"
"ds_read_b64_tr_b16 a[220:221], v11 offset:9728\n"
"v_mul_f32_e32 v30, s44, v30\n"
"v_mfma_f32_16x16x32_bf16 v[140:143], a[172:175], v[32:35], v[140:143]\n"
"v_exp_f32_e32 v29, v29\n"
"v_mfma_f32_16x16x32_bf16 v[144:147], a[176:179], v[32:35], v[144:147]\n"
"s_add_u32 m0, s49, 23552\n"
"v_mul_f32_e32 v31, s44, v31\n"
"v_cvt_pk_bf16_f32 v38, v28, v29\n"
"v_mfma_f32_16x16x32_bf16 v[148:151], a[180:183], v[32:35], v[148:151]\n"
"s_add_u32 s61, s59, 20608\n"
"v_exp_f32_e32 v30, v30\n"
"v_mfma_f32_16x16x32_bf16 v[152:155], a[184:187], v[32:35], v[152:155]\n"
"buffer_load_dwordx4 v8, s[36:39], s61 offen  nt lds\n"
"v_mfma_f32_16x16x32_bf16 v[156:159], a[188:191], v[32:35], v[156:159]\n"
"v_exp_f32_e32 v31, v31\n"
"v_mfma_f32_16x16x32_bf16 a[236:239], a[224:227], v[32:35], a[236:239]\n"
"s_waitcnt lgkmcnt(15)\n"
"v_cvt_pk_bf16_f32 v39, v30, v31\n"
"v_mfma_f32_16x16x32_bf16 v[24:27], a[128:131], a[0:3], 0\n"
"v_mul_f32_e32 v16, s44, v16\n"
"v_mul_f32_e32 v17, s44, v17\n"
"v_mfma_f32_16x16x32_bf16 v[24:27], a[132:135], a[4:7], v[24:27]\n"
"v_exp_f32_e32 v16, v16\n"
"v_mfma_f32_16x16x32_bf16 v[24:27], a[136:139], a[8:11], v[24:27]\n"
"v_mul_f32_e32 v18, s44, v18\n"
"v_mfma_f32_16x16x32_bf16 v[24:27], a[140:143], a[12:15], v[24:27]\n"
"v_exp_f32_e32 v17, v17\n"
"v_mfma_f32_16x16x32_bf16 v[28:31], a[144:147], a[0:3], 0\n"
"v_mul_f32_e32 v19, s44, v19\n"
"v_cvt_pk_bf16_f32 v32, v16, v17\n"
"v_mfma_f32_16x16x32_bf16 v[28:31], a[148:151], a[4:7], v[28:31]\n"
"s_waitcnt vmcnt(17) lgkmcnt(0)\n"
"s_barrier\n"
"v_exp_f32_e32 v18, v18\n"
"v_mfma_f32_16x16x32_bf16 v[28:31], a[152:155], a[8:11], v[28:31]\n"
"ds_read_b128 a[96:99], v1 offset:16384\n"
"ds_read_b128 a[108:111], v1 offset:17920\n"
"v_mul_f32_e32 v20, s44, v20\n"
"v_mfma_f32_16x16x32_bf16 v[28:31], a[156:159], a[12:15], v[28:31]\n"
"v_exp_f32_e32 v19, v19\n"
"v_mfma_f32_16x16x32_bf16 v[160:163], a[160:163], v[36:39], v[160:163]\n"
"v_mul_f32_e32 v21, s44, v21\n"
"v_cvt_pk_bf16_f32 v33, v18, v19\n"
"v_mfma_f32_16x16x32_bf16 v[164:167], a[164:167], v[36:39], v[164:167]\n"
"v_exp_f32_e32 v20, v20\n"
"v_mfma_f32_16x16x32_bf16 v[168:171], a[168:171], v[36:39], v[168:171]\n"
"ds_read_b128 a[100:103], v1 offset:16896\n"
"v_mul_f32_e32 v22, s44, v22\n"
"v_mfma_f32_16x16x32_bf16 v[172:175], a[172:175], v[36:39], v[172:175]\n"
"v_exp_f32_e32 v21, v21\n"
"v_mfma_f32_16x16x32_bf16 v[176:179], a[176:179], v[36:39], v[176:179]\n"
"s_add_u32 m0, s48, 8192\n"
"v_mul_f32_e32 v23, s44, v23\n"
"v_cvt_pk_bf16_f32 v34, v20, v21\n"
"v_mfma_f32_16x16x32_bf16 v[180:183], a[180:183], v[36:39], v[180:183]\n"
"s_add_u32 s47, s46, 8192\n"
"v_exp_f32_e32 v22, v22\n"
"v_mfma_f32_16x16x32_bf16 v[184:187], a[184:187], v[36:39], v[184:187]\n"
"buffer_load_dwordx4 v6, s[24:27], s47 offen lds\n"
"v_mfma_f32_16x16x32_bf16 v[188:191], a[188:191], v[36:39], v[188:191]\n"
"v_exp_f32_e32 v23, v23\n"
"v_mfma_f32_16x16x32_bf16 a[240:243], a[224:227], v[36:39], a[240:243]\n"
"ds_read_b128 a[104:107], v1 offset:17408\n"
"v_cvt_pk_bf16_f32 v35, v22, v23\n"
"v_mfma_f32_16x16x32_bf16 v[16:19], a[128:131], a[16:19], 0\n"
"v_mul_f32_e32 v24, s44, v24\n"
"v_mul_f32_e32 v25, s44, v25\n"
"v_mfma_f32_16x16x32_bf16 v[16:19], a[132:135], a[20:23], v[16:19]\n"
"v_exp_f32_e32 v24, v24\n"
"v_mfma_f32_16x16x32_bf16 v[16:19], a[136:139], a[24:27], v[16:19]\n"
"ds_read_b128 a[112:115], v2 offset:16384\n"
"v_mul_f32_e32 v26, s44, v26\n"
"v_mfma_f32_16x16x32_bf16 v[16:19], a[140:143], a[28:31], v[16:19]\n"
"v_exp_f32_e32 v25, v25\n"
"v_mfma_f32_16x16x32_bf16 v[20:23], a[144:147], a[16:19], 0\n"
"v_mul_f32_e32 v27, s44, v27\n"
"v_cvt_pk_bf16_f32 v36, v24, v25\n"
"v_mfma_f32_16x16x32_bf16 v[20:23], a[148:151], a[20:23], v[20:23]\n"
"v_exp_f32_e32 v26, v26\n"
"v_mfma_f32_16x16x32_bf16 v[20:23], a[152:155], a[24:27], v[20:23]\n"
"ds_read_b128 a[116:119], v2 offset:16896\n"
"v_mul_f32_e32 v28, s44, v28\n"
"v_mfma_f32_16x16x32_bf16 v[20:23], a[156:159], a[28:31], v[20:23]\n"
"v_exp_f32_e32 v27, v27\n"
"v_mfma_f32_16x16x32_bf16 v[192:195], a[160:163], v[32:35], v[192:195]\n"
"v_mul_f32_e32 v29, s44, v29\n"
"v_cvt_pk_bf16_f32 v37, v26, v27\n"
"v_mfma_f32_16x16x32_bf16 v[196:199], a[164:167], v[32:35], v[196:199]\n"
"v_exp_f32_e32 v28, v28\n"
"v_mfma_f32_16x16x32_bf16 v[200:203], a[168:171], v[32:35], v[200:203]\n"
"ds_read_b128 a[120:123], v2 offset:17408\n"
"v_mul_f32_e32 v30, s44, v30\n"
"v_mfma_f32_16x16x32_bf16 v[204:207], a[172:175], v[32:35], v[204:207]\n"
"v_exp_f32_e32 v29, v29\n"
"v_mfma_f32_16x16x32_bf16 v[208:211], a[176:179], v[32:35], v[208:211]\n"
"s_add_u32 m0, s48, 9216\n"
"v_mul_f32_e32 v31, s44, v31\n"
"v_cvt_pk_bf16_f32 v38, v28, v29\n"
"v_mfma_f32_16x16x32_bf16 v[212:215], a[180:183], v[32:35], v[212:215]\n"
"s_add_u32 s47, s46, 8320\n"
"v_exp_f32_e32 v30, v30\n"
"v_mfma_f32_16x16x32_bf16 v[216:219], a[184:187], v[32:35], v[216:219]\n"
"buffer_load_dwordx4 v6, s[24:27], s47 offen lds\n"
"v_mfma_f32_16x16x32_bf16 v[220:223], a[188:191], v[32:35], v[220:223]\n"
"v_exp_f32_e32 v31, v31\n"
"v_mfma_f32_16x16x32_bf16 a[244:247], a[224:227], v[32:35], a[244:247]\n"
"ds_read_b128 a[124:127], v2 offset:17920\n"
"v_cvt_pk_bf16_f32 v39, v30, v31\n"
"v_mfma_f32_16x16x32_bf16 v[24:27], a[128:131], a[32:35], 0\n"
"ds_read_b64_tr_b16 a[168:169], v3 offset:16896\n"
"v_mul_f32_e32 v16, s44, v16\n"
"v_mul_f32_e32 v17, s44, v17\n"
"v_mfma_f32_16x16x32_bf16 v[24:27], a[132:135], a[36:39], v[24:27]\n"
"ds_read_b64_tr_b16 a[170:171], v4 offset:16896\n"
"v_exp_f32_e32 v16, v16\n"
"v_mfma_f32_16x16x32_bf16 v[24:27], a[136:139], a[40:43], v[24:27]\n"
"ds_read_b64_tr_b16 a[160:161], v3 offset:16384\n"
"v_mul_f32_e32 v18, s44, v18\n"
"v_mfma_f32_16x16x32_bf16 v[24:27], a[140:143], a[44:47], v[24:27]\n"
"v_exp_f32_e32 v17, v17\n"
"v_mfma_f32_16x16x32_bf16 v[28:31], a[144:147], a[32:35], 0\n"
"v_mul_f32_e32 v19, s44, v19\n"
"v_cvt_pk_bf16_f32 v32, v16, v17\n"
"v_mfma_f32_16x16x32_bf16 v[28:31], a[148:151], a[36:39], v[28:31]\n"
"v_exp_f32_e32 v18, v18\n"
"v_mfma_f32_16x16x32_bf16 v[28:31], a[152:155], a[40:43], v[28:31]\n"
"ds_read_b64_tr_b16 a[162:163], v4 offset:16384\n"
"v_mul_f32_e32 v20, s44, v20\n"
"v_mfma_f32_16x16x32_bf16 v[28:31], a[156:159], a[44:47], v[28:31]\n"
"v_exp_f32_e32 v19, v19\n"
"v_mfma_f32_16x16x32_bf16 v[64:67], a[192:195], v[36:39], v[64:67]\n"
"v_mul_f32_e32 v21, s44, v21\n"
"v_cvt_pk_bf16_f32 v33, v18, v19\n"
"v_mfma_f32_16x16x32_bf16 v[68:71], a[196:199], v[36:39], v[68:71]\n"
"v_exp_f32_e32 v20, v20\n"
"v_mfma_f32_16x16x32_bf16 v[72:75], a[200:203], v[36:39], v[72:75]\n"
"ds_read_b64_tr_b16 a[164:165], v11 offset:16384\n"
"v_mul_f32_e32 v22, s44, v22\n"
"v_mfma_f32_16x16x32_bf16 v[76:79], a[204:207], v[36:39], v[76:79]\n"
"v_exp_f32_e32 v21, v21\n"
"v_mfma_f32_16x16x32_bf16 v[80:83], a[208:211], v[36:39], v[80:83]\n"
"s_add_u32 m0, s48, 40960\n"
"v_mul_f32_e32 v23, s44, v23\n"
"v_cvt_pk_bf16_f32 v34, v20, v21\n"
"v_mfma_f32_16x16x32_bf16 v[84:87], a[212:215], v[36:39], v[84:87]\n"
"s_add_u32 s76, s46, 8192\n"
"v_exp_f32_e32 v22, v22\n"
"v_mfma_f32_16x16x32_bf16 v[88:91], a[216:219], v[36:39], v[88:91]\n"
"buffer_load_dwordx4 v6, s[28:31], s76 offen lds\n"
"v_mfma_f32_16x16x32_bf16 v[92:95], a[220:223], v[36:39], v[92:95]\n"
"v_exp_f32_e32 v23, v23\n"
"v_mfma_f32_16x16x32_bf16 a[228:231], a[224:227], v[36:39], a[228:231]\n"
"ds_read_b64_tr_b16 a[166:167], v14 offset:16384\n"
"v_cvt_pk_bf16_f32 v35, v22, v23\n"
"v_mfma_f32_16x16x32_bf16 v[16:19], a[128:131], a[48:51], 0\n"
"ds_read_b64_tr_b16 a[180:181], v11 offset:17408\n"
"v_mul_f32_e32 v24, s44, v24\n"
"v_mul_f32_e32 v25, s44, v25\n"
"v_mfma_f32_16x16x32_bf16 v[16:19], a[132:135], a[52:55], v[16:19]\n"
"ds_read_b64_tr_b16 a[182:183], v14 offset:17408\n"
"v_exp_f32_e32 v24, v24\n"
"v_mfma_f32_16x16x32_bf16 v[16:19], a[136:139], a[56:59], v[16:19]\n"
"ds_read_b64_tr_b16 a[172:173], v11 offset:16896\n"
"v_mul_f32_e32 v26, s44, v26\n"
"v_mfma_f32_16x16x32_bf16 v[16:19], a[140:143], a[60:63], v[16:19]\n"
"v_exp_f32_e32 v25, v25\n"
"v_mfma_f32_16x16x32_bf16 v[20:23], a[144:147], a[48:51], 0\n"
"v_mul_f32_e32 v27, s44, v27\n"
"v_cvt_pk_bf16_f32 v36, v24, v25\n"
"v_mfma_f32_16x16x32_bf16 v[20:23], a[148:151], a[52:55], v[20:23]\n"
"v_exp_f32_e32 v26, v26\n"
"v_mfma_f32_16x16x32_bf16 v[20:23], a[152:155], a[56:59], v[20:23]\n"
"ds_read_b64_tr_b16 a[174:175], v14 offset:16896\n"
"v_mul_f32_e32 v28, s44, v28\n"
"v_mfma_f32_16x16x32_bf16 v[20:23], a[156:159], a[60:63], v[20:23]\n"
"v_exp_f32_e32 v27, v27\n"
"v_mfma_f32_16x16x32_bf16 v[96:99], a[192:195], v[32:35], v[96:99]\n"
"v_mul_f32_e32 v29, s44, v29\n"
"v_cvt_pk_bf16_f32 v37, v26, v27\n"
"v_mfma_f32_16x16x32_bf16 v[100:103], a[196:199], v[32:35], v[100:103]\n"
"v_exp_f32_e32 v28, v28\n"
"v_mfma_f32_16x16x32_bf16 v[104:107], a[200:203], v[32:35], v[104:107]\n"
"ds_read_b64_tr_b16 a[176:177], v3 offset:17408\n"
"v_mul_f32_e32 v30, s44, v30\n"
"v_mfma_f32_16x16x32_bf16 v[108:111], a[204:207], v[32:35], v[108:111]\n"
"v_exp_f32_e32 v29, v29\n"
"v_mfma_f32_16x16x32_bf16 v[112:115], a[208:211], v[32:35], v[112:115]\n"
"s_add_u32 m0, s48, 41984\n"
"v_mul_f32_e32 v31, s44, v31\n"
"v_cvt_pk_bf16_f32 v38, v28, v29\n"
"v_mfma_f32_16x16x32_bf16 v[116:119], a[212:215], v[32:35], v[116:119]\n"
"s_add_u32 s76, s46, 8320\n"
"v_exp_f32_e32 v30, v30\n"
"v_mfma_f32_16x16x32_bf16 v[120:123], a[216:219], v[32:35], v[120:123]\n"
"buffer_load_dwordx4 v6, s[28:31], s76 offen lds\n"
"v_mfma_f32_16x16x32_bf16 v[124:127], a[220:223], v[32:35], v[124:127]\n"
"v_exp_f32_e32 v31, v31\n"
"v_mfma_f32_16x16x32_bf16 a[232:235], a[224:227], v[32:35], a[232:235]\n"
"ds_read_b64_tr_b16 a[178:179], v4 offset:17408\n"
"v_cvt_pk_bf16_f32 v39, v30, v31\n"
"v_mfma_f32_16x16x32_bf16 v[24:27], a[128:131], a[64:67], 0\n"
"ds_read_b64_tr_b16 a[190:191], v14 offset:17920\n"
"v_mul_f32_e32 v16, s44, v16\n"
"v_mul_f32_e32 v17, s44, v17\n"
"v_mfma_f32_16x16x32_bf16 v[24:27], a[132:135], a[68:71], v[24:27]\n"
"v_exp_f32_e32 v16, v16\n"
"v_mfma_f32_16x16x32_bf16 v[24:27], a[136:139], a[72:75], v[24:27]\n"
"ds_read_b64_tr_b16 a[184:185], v3 offset:17920\n"
"v_mul_f32_e32 v18, s44, v18\n"
"v_mfma_f32_16x16x32_bf16 v[24:27], a[140:143], a[76:79], v[24:27]\n"
"v_exp_f32_e32 v17, v17\n"
"v_mfma_f32_16x16x32_bf16 v[28:31], a[144:147], a[64:67], 0\n"
"v_mul_f32_e32 v19, s44, v19\n"
"v_cvt_pk_bf16_f32 v32, v16, v17\n"
"v_mfma_f32_16x16x32_bf16 v[28:31], a[148:151], a[68:71], v[28:31]\n"
"v_exp_f32_e32 v18, v18\n"
"v_mfma_f32_16x16x32_bf16 v[28:31], a[152:155], a[72:75], v[28:31]\n"
"ds_read_b64_tr_b16 a[186:187], v4 offset:17920\n"
"v_mul_f32_e32 v20, s44, v20\n"
"v_mfma_f32_16x16x32_bf16 v[28:31], a[156:159], a[76:79], v[28:31]\n"
"v_exp_f32_e32 v19, v19\n"
"v_mfma_f32_16x16x32_bf16 v[128:131], a[192:195], v[36:39], v[128:131]\n"
"v_mul_f32_e32 v21, s44, v21\n"
"v_cvt_pk_bf16_f32 v33, v18, v19\n"
"v_mfma_f32_16x16x32_bf16 v[132:135], a[196:199], v[36:39], v[132:135]\n"
"v_exp_f32_e32 v20, v20\n"
"v_mfma_f32_16x16x32_bf16 v[136:139], a[200:203], v[36:39], v[136:139]\n"
"ds_read_b64_tr_b16 a[188:189], v11 offset:17920\n"
"v_mul_f32_e32 v22, s44, v22\n"
"v_mfma_f32_16x16x32_bf16 v[140:143], a[204:207], v[36:39], v[140:143]\n"
"v_exp_f32_e32 v21, v21\n"
"v_mfma_f32_16x16x32_bf16 v[144:147], a[208:211], v[36:39], v[144:147]\n"
"v_mul_f32_e32 v23, s44, v23\n"
"v_cvt_pk_bf16_f32 v34, v20, v21\n"
"v_mfma_f32_16x16x32_bf16 v[148:151], a[212:215], v[36:39], v[148:151]\n"
"v_exp_f32_e32 v22, v22\n"
"v_mfma_f32_16x16x32_bf16 v[152:155], a[216:219], v[36:39], v[152:155]\n"
"v_mfma_f32_16x16x32_bf16 v[156:159], a[220:223], v[36:39], v[156:159]\n"
"v_exp_f32_e32 v23, v23\n"
"v_mfma_f32_16x16x32_bf16 a[236:239], a[224:227], v[36:39], a[236:239]\n"
"v_cvt_pk_bf16_f32 v35, v22, v23\n"
"s_waitcnt lgkmcnt(15)\n"
"s_cmp_eq_u32 s50, 3\n"
"s_cbranch_scc1 L_E5%=\n"
"L_S5%=:\n"
"v_mfma_f32_16x16x32_bf16 v[16:19], a[96:99], a[0:3], 0\n"
"v_mul_f32_e32 v24, s44, v24\n"
"v_mul_f32_e32 v25, s44, v25\n"
"v_mfma_f32_16x16x32_bf16 v[16:19], a[100:103], a[4:7], v[16:19]\n"
"v_exp_f32_e32 v24, v24\n"
"v_mfma_f32_16x16x32_bf16 v[16:19], a[104:107], a[8:11], v[16:19]\n"
"v_mul_f32_e32 v26, s44, v26\n"
"v_mfma_f32_16x16x32_bf16 v[16:19], a[108:111], a[12:15], v[16:19]\n"
"v_exp_f32_e32 v25, v25\n"
"v_mfma_f32_16x16x32_bf16 v[20:23], a[112:115], a[0:3], 0\n"
"v_mul_f32_e32 v27, s44, v27\n"
"v_cvt_pk_bf16_f32 v36, v24, v25\n"
"v_mfma_f32_16x16x32_bf16 v[20:23], a[116:119], a[4:7], v[20:23]\n"
"s_waitcnt vmcnt(11) lgkmcnt(0)\n"
"s_barrier\n"
"v_exp_f32_e32 v26, v26\n"
"v_mfma_f32_16x16x32_bf16 v[20:23], a[120:123], a[8:11], v[20:23]\n"
"ds_read_b128 a[128:131], v1 offset:24576\n"
"ds_read_b128 a[140:143], v1 offset:26112\n"
"v_mul_f32_e32 v28, s44, v28\n"
"v_mfma_f32_16x16x32_bf16 v[20:23], a[124:127], a[12:15], v[20:23]\n"
"v_exp_f32_e32 v27, v27\n"
"v_mfma_f32_16x16x32_bf16 v[160:163], a[192:195], v[32:35], v[160:163]\n"
"v_mul_f32_e32 v29, s44, v29\n"
"v_cvt_pk_bf16_f32 v37, v26, v27\n"
"v_mfma_f32_16x16x32_bf16 v[164:167], a[196:199], v[32:35], v[164:167]\n"
"v_exp_f32_e32 v28, v28\n"
"v_mfma_f32_16x16x32_bf16 v[168:171], a[200:203], v[32:35], v[168:171]\n"
"ds_read_b128 a[132:135], v1 offset:25088\n"
"v_mul_f32_e32 v30, s44, v30\n"
"v_mfma_f32_16x16x32_bf16 v[172:175], a[204:207], v[32:35], v[172:175]\n"
"v_exp_f32_e32 v29, v29\n"
"v_mfma_f32_16x16x32_bf16 v[176:179], a[208:211], v[32:35], v[176:179]\n"
"s_add_u32 m0, s48, 16384\n"
"v_mul_f32_e32 v31, s44, v31\n"
"v_cvt_pk_bf16_f32 v38, v28, v29\n"
"v_mfma_f32_16x16x32_bf16 v[180:183], a[212:215], v[32:35], v[180:183]\n"
"s_add_u32 s47, s46, 16384\n"
"v_exp_f32_e32 v30, v30\n"
"v_mfma_f32_16x16x32_bf16 v[184:187], a[216:219], v[32:35], v[184:187]\n"
"buffer_load_dwordx4 v6, s[24:27], s47 offen lds\n"
"v_mfma_f32_16x16x32_bf16 v[188:191], a[220:223], v[32:35], v[188:191]\n"
"v_exp_f32_e32 v31, v31\n"
"v_mfma_f32_16x16x32_bf16 a[240:243], a[224:227], v[32:35], a[240:243]\n"
"ds_read_b128 a[136:139], v1 offset:25600\n"
"v_cvt_pk_bf16_f32 v39, v30, v31\n"
"v_mfma_f32_16x16x32_bf16 v[24:27], a[96:99], a[16:19], 0\n"
"ds_read_b128 a[156:159], v2 offset:26112\n"
"v_mul_f32_e32 v16, s44, v16\n"
"v_mul_f32_e32 v17, s44, v17\n"
"v_mfma_f32_16x16x32_bf16 v[24:27], a[100:103], a[20:23], v[24:27]\n"
"s_waitcnt vmcnt(5)\n"
"ds_read_b128 a[80:83], v5 offset:20480\n"
"v_exp_f32_e32 v16, v16\n"
"v_mfma_f32_16x16x32_bf16 v[24:27], a[104:107], a[24:27], v[24:27]\n"
"ds_read_b128 a[144:147], v2 offset:24576\n"
"ds_read_b128 a[84:87], v5 offset:20992\n"
"v_mul_f32_e32 v18, s44, v18\n"
"v_mfma_f32_16x16x32_bf16 v[24:27], a[108:111], a[28:31], v[24:27]\n"
"ds_read_b128 a[88:91], v5 offset:21504\n"
"v_exp_f32_e32 v17, v17\n"
"v_mfma_f32_16x16x32_bf16 v[28:31], a[112:115], a[16:19], 0\n"
"ds_read_b128 a[92:95], v5 offset:22016\n"
"v_mul_f32_e32 v19, s44, v19\n"
"v_cvt_pk_bf16_f32 v32, v16, v17\n"
"v_mfma_f32_16x16x32_bf16 v[28:31], a[116:119], a[20:23], v[28:31]\n"
"v_exp_f32_e32 v18, v18\n"
"v_mfma_f32_16x16x32_bf16 v[28:31], a[120:123], a[24:27], v[28:31]\n"
"ds_read_b128 a[148:151], v2 offset:25088\n"
"v_mul_f32_e32 v20, s44, v20\n"
"v_mfma_f32_16x16x32_bf16 v[28:31], a[124:127], a[28:31], v[28:31]\n"
"v_exp_f32_e32 v19, v19\n"
"v_mfma_f32_16x16x32_bf16 v[192:195], a[192:195], v[36:39], v[192:195]\n"
"v_mul_f32_e32 v21, s44, v21\n"
"v_cvt_pk_bf16_f32 v33, v18, v19\n"
"v_mfma_f32_16x16x32_bf16 v[196:199], a[196:199], v[36:39], v[196:199]\n"
"v_exp_f32_e32 v20, v20\n"
"v_mfma_f32_16x16x32_bf16 v[200:203], a[200:203], v[36:39], v[200:203]\n"
"ds_read_b128 a[152:155], v2 offset:25600\n"
"v_mul_f32_e32 v22, s44, v22\n"
"v_mfma_f32_16x16x32_bf16 v[204:207], a[204:207], v[36:39], v[204:207]\n"
"v_exp_f32_e32 v21, v21\n"
"v_mfma_f32_16x16x32_bf16 v[208:211], a[208:211], v[36:39], v[208:211]\n"
"v_mul_f32_e32 v23, s44, v23\n"
"v_cvt_pk_bf16_f32 v34, v20, v21\n"
"v_mfma_f32_16x16x32_bf16 v[212:215], a[212:215], v[36:39], v[212:215]\n"
"s_mov_b32 s88, L_L6%=+0-L_code%=\n"
"v_exp_f32_e32 v22, v22\n"
"v_mfma_f32_16x16x32_bf16 v[216:219], a[216:219], v[36:39], v[216:219]\n"
"s_add_u32 m0, s48, 17408\n"
"buffer_load_dword v63, v62, s[84:87], s88 offen\n"
"v_mfma_f32_16x16x32_bf16 v[220:223], a[220:223], v[36:39], v[220:223]\n"
"s_add_u32 s47, s46, 16512\n"
"v_exp_f32_e32 v23, v23\n"
"v_mfma_f32_16x16x32_bf16 a[244:247], a[224:227], v[36:39], a[244:247]\n"
"buffer_load_dwordx4 v6, s[24:27], s47 offen lds\n"
"v_cvt_pk_bf16_f32 v35, v22, v23\n"
"v_mfma_f32_16x16x32_bf16 v[16:19], a[96:99], a[32:35], 0\n"
"ds_read_b64_tr_b16 a[200:201], v3 offset:25088\n"
"v_mul_f32_e32 v24, s44, v24\n"
"v_mul_f32_e32 v25, s44, v25\n"
"v_mfma_f32_16x16x32_bf16 v[16:19], a[100:103], a[36:39], v[16:19]\n"
"ds_read_b64_tr_b16 a[202:203], v4 offset:25088\n"
"v_exp_f32_e32 v24, v24\n"
"v_mfma_f32_16x16x32_bf16 v[16:19], a[104:107], a[40:43], v[16:19]\n"
"ds_read_b64_tr_b16 a[192:193], v3 offset:24576\n"
"v_mul_f32_e32 v26, s44, v26\n"
"v_mfma_f32_16x16x32_bf16 v[16:19], a[108:111], a[44:47], v[16:19]\n"
"v_exp_f32_e32 v25, v25\n"
"v_mfma_f32_16x16x32_bf16 v[20:23], a[112:115], a[32:35], 0\n"
"v_mul_f32_e32 v27, s44, v27\n"
"v_cvt_pk_bf16_f32 v36, v24, v25\n"
"v_mfma_f32_16x16x32_bf16 v[20:23], a[116:119], a[36:39], v[20:23]\n"
"v_exp_f32_e32 v26, v26\n"
"v_mfma_f32_16x16x32_bf16 v[20:23], a[120:123], a[40:43], v[20:23]\n"
"ds_read_b64_tr_b16 a[194:195], v4 offset:24576\n"
"v_mul_f32_e32 v28, s44, v28\n"
"v_mfma_f32_16x16x32_bf16 v[20:23], a[124:127], a[44:47], v[20:23]\n"
"v_exp_f32_e32 v27, v27\n"
"v_mfma_f32_16x16x32_bf16 v[64:67], a[160:163], v[32:35], v[64:67]\n"
"v_mul_f32_e32 v29, s44, v29\n"
"v_cvt_pk_bf16_f32 v37, v26, v27\n"
"v_mfma_f32_16x16x32_bf16 v[68:71], a[164:167], v[32:35], v[68:71]\n"
"v_exp_f32_e32 v28, v28\n"
"v_mfma_f32_16x16x32_bf16 v[72:75], a[168:171], v[32:35], v[72:75]\n"
"ds_read_b64_tr_b16 a[196:197], v11 offset:24576\n"
"v_mul_f32_e32 v30, s44, v30\n"
"v_mfma_f32_16x16x32_bf16 v[76:79], a[172:175], v[32:35], v[76:79]\n"
"v_exp_f32_e32 v29, v29\n"
"v_mfma_f32_16x16x32_bf16 v[80:83], a[176:179], v[32:35], v[80:83]\n"
"s_add_u32 m0, s48, 49152\n"
"v_mul_f32_e32 v31, s44, v31\n"
"v_cvt_pk_bf16_f32 v38, v28, v29\n"
"v_mfma_f32_16x16x32_bf16 v[84:87], a[180:183], v[32:35], v[84:87]\n"
"s_add_u32 s76, s46, 16384\n"
"v_exp_f32_e32 v30, v30\n"
"v_mfma_f32_16x16x32_bf16 v[88:91], a[184:187], v[32:35], v[88:91]\n"
"buffer_load_dwordx4 v6, s[28:31], s76 offen lds\n"
"v_mfma_f32_16x16x32_bf16 v[92:95], a[188:191], v[32:35], v[92:95]\n"
"v_exp_f32_e32 v31, v31\n"
"v_mfma_f32_16x16x32_bf16 a[228:231], a[224:227], v[32:35], a[228:231]\n"
"ds_read_b64_tr_b16 a[198:199], v14 offset:24576\n"
"v_cvt_pk_bf16_f32 v39, v30, v31\n"
"v_mfma_f32_16x16x32_bf16 v[24:27], a[96:99], a[48:51], 0\n"
"ds_read_b64_tr_b16 a[212:213], v11 offset:25600\n"
"v_mul_f32_e32 v16, s44, v16\n"
"v_mul_f32_e32 v17, s44, v17\n"
"v_mfma_f32_16x16x32_bf16 v[24:27], a[100:103], a[52:55], v[24:27]\n"
"ds_read_b64_tr_b16 a[214:215], v14 offset:25600\n"
"v_exp_f32_e32 v16, v16\n"
"v_mfma_f32_16x16x32_bf16 v[24:27], a[104:107], a[56:59], v[24:27]\n"
"ds_read_b64_tr_b16 a[204:205], v11 offset:25088\n"
"v_mul_f32_e32 v18, s44, v18\n"
"v_mfma_f32_16x16x32_bf16 v[24:27], a[108:111], a[60:63], v[24:27]\n"
"v_exp_f32_e32 v17, v17\n"
"v_mfma_f32_16x16x32_bf16 v[28:31], a[112:115], a[48:51], 0\n"
"v_mul_f32_e32 v19, s44, v19\n"
"v_cvt_pk_bf16_f32 v32, v16, v17\n"
"v_mfma_f32_16x16x32_bf16 v[28:31], a[116:119], a[52:55], v[28:31]\n"
"v_exp_f32_e32 v18, v18\n"
"v_mfma_f32_16x16x32_bf16 v[28:31], a[120:123], a[56:59], v[28:31]\n"
"ds_read_b64_tr_b16 a[206:207], v14 offset:25088\n"
"v_mul_f32_e32 v20, s44, v20\n"
"v_mfma_f32_16x16x32_bf16 v[28:31], a[124:127], a[60:63], v[28:31]\n"
"v_exp_f32_e32 v19, v19\n"
"v_mfma_f32_16x16x32_bf16 v[96:99], a[160:163], v[36:39], v[96:99]\n"
"v_mul_f32_e32 v21, s44, v21\n"
"v_cvt_pk_bf16_f32 v33, v18, v19\n"
"v_mfma_f32_16x16x32_bf16 v[100:103], a[164:167], v[36:39], v[100:103]\n"
"v_exp_f32_e32 v20, v20\n"
"v_mfma_f32_16x16x32_bf16 v[104:107], a[168:171], v[36:39], v[104:107]\n"
"ds_read_b64_tr_b16 a[208:209], v3 offset:25600\n"
"v_mul_f32_e32 v22, s44, v22\n"
"v_mfma_f32_16x16x32_bf16 v[108:111], a[172:175], v[36:39], v[108:111]\n"
"v_exp_f32_e32 v21, v21\n"
"v_mfma_f32_16x16x32_bf16 v[112:115], a[176:179], v[36:39], v[112:115]\n"
"s_add_u32 m0, s48, 50176\n"
"v_mul_f32_e32 v23, s44, v23\n"
"v_cvt_pk_bf16_f32 v34, v20, v21\n"
"v_mfma_f32_16x16x32_bf16 v[116:119], a[180:183], v[36:39], v[116:119]\n"
"s_add_u32 s76, s46, 16512\n"
"v_exp_f32_e32 v22, v22\n"
"v_mfma_f32_16x16x32_bf16 v[120:123], a[184:187], v[36:39], v[120:123]\n"
"buffer_load_dwordx4 v6, s[28:31], s76 offen lds\n"
"v_mfma_f32_16x16x32_bf16 v[124:127], a[188:191], v[36:39], v[124:127]\n"
"v_exp_f32_e32 v23, v23\n"
"v_mfma_f32_16x16x32_bf16 a[232:235], a[224:227], v[36:39], a[232:235]\n"
"ds_read_b64_tr_b16 a[210:211], v4 offset:25600\n"
"v_cvt_pk_bf16_f32 v35, v22, v23\n"
"v_mfma_f32_16x16x32_bf16 v[16:19], a[96:99], a[64:67], 0\n"
"ds_read_b64_tr_b16 a[222:223], v14 offset:26112\n"
"v_mul_f32_e32 v24, s44, v24\n"
"v_mul_f32_e32 v25, s44, v25\n"
"v_mfma_f32_16x16x32_bf16 v[16:19], a[100:103], a[68:71], v[16:19]\n"
"v_exp_f32_e32 v24, v24\n"
"v_mfma_f32_16x16x32_bf16 v[16:19], a[104:107], a[72:75], v[16:19]\n"
"ds_read_b64_tr_b16 a[216:217], v3 offset:26112\n"
"v_mul_f32_e32 v26, s44, v26\n"
"v_mfma_f32_16x16x32_bf16 v[16:19], a[108:111], a[76:79], v[16:19]\n"
"v_exp_f32_e32 v25, v25\n"
"v_mfma_f32_16x16x32_bf16 v[20:23], a[112:115], a[64:67], 0\n"
"v_mul_f32_e32 v27, s44, v27\n"
"v_cvt_pk_bf16_f32 v36, v24, v25\n"
"v_mfma_f32_16x16x32_bf16 v[20:23], a[116:119], a[68:71], v[20:23]\n"
"v_exp_f32_e32 v26, v26\n"
"v_mfma_f32_16x16x32_bf16 v[20:23], a[120:123], a[72:75], v[20:23]\n"
"ds_read_b64_tr_b16 a[218:219], v4 offset:26112\n"
"v_mul_f32_e32 v28, s44, v28\n"
"v_mfma_f32_16x16x32_bf16 v[20:23], a[124:127], a[76:79], v[20:23]\n"
"v_exp_f32_e32 v27, v27\n"
"v_mfma_f32_16x16x32_bf16 v[128:131], a[160:163], v[32:35], v[128:131]\n"
"v_mul_f32_e32 v29, s44, v29\n"
"v_cvt_pk_bf16_f32 v37, v26, v27\n"
"v_mfma_f32_16x16x32_bf16 v[132:135], a[164:167], v[32:35], v[132:135]\n"
"v_exp_f32_e32 v28, v28\n"
"v_mfma_f32_16x16x32_bf16 v[136:139], a[168:171], v[32:35], v[136:139]\n"
"ds_read_b64_tr_b16 a[220:221], v11 offset:26112\n"
"v_mul_f32_e32 v30, s44, v30\n"
"v_mfma_f32_16x16x32_bf16 v[140:143], a[172:175], v[32:35], v[140:143]\n"
"v_exp_f32_e32 v29, v29\n"
"v_mfma_f32_16x16x32_bf16 v[144:147], a[176:179], v[32:35], v[144:147]\n"
"v_mul_f32_e32 v31, s44, v31\n"
"v_cvt_pk_bf16_f32 v38, v28, v29\n"
"v_mfma_f32_16x16x32_bf16 v[148:151], a[180:183], v[32:35], v[148:151]\n"
"v_exp_f32_e32 v30, v30\n"
"v_mfma_f32_16x16x32_bf16 v[152:155], a[184:187], v[32:35], v[152:155]\n"
"v_mfma_f32_16x16x32_bf16 v[156:159], a[188:191], v[32:35], v[156:159]\n"
"v_exp_f32_e32 v31, v31\n"
"v_mfma_f32_16x16x32_bf16 a[236:239], a[224:227], v[32:35], a[236:239]\n"
"s_waitcnt lgkmcnt(15)\n"
"v_cvt_pk_bf16_f32 v39, v30, v31\n"
"v_mfma_f32_16x16x32_bf16 v[24:27], a[128:131], a[0:3], 0\n"
"v_mul_f32_e32 v16, s44, v16\n"
"v_mul_f32_e32 v17, s44, v17\n"
"v_mfma_f32_16x16x32_bf16 v[24:27], a[132:135], a[4:7], v[24:27]\n"
"v_exp_f32_e32 v16, v16\n"
"v_mfma_f32_16x16x32_bf16 v[24:27], a[136:139], a[8:11], v[24:27]\n"
"v_mul_f32_e32 v18, s44, v18\n"
"v_mfma_f32_16x16x32_bf16 v[24:27], a[140:143], a[12:15], v[24:27]\n"
"v_exp_f32_e32 v17, v17\n"
"v_mfma_f32_16x16x32_bf16 v[28:31], a[144:147], a[0:3], 0\n"
"v_mul_f32_e32 v19, s44, v19\n"
"v_cvt_pk_bf16_f32 v32, v16, v17\n"
"v_mfma_f32_16x16x32_bf16 v[28:31], a[148:151], a[4:7], v[28:31]\n"
"s_waitcnt lgkmcnt(0)\n"
"s_barrier\n"
"v_exp_f32_e32 v18, v18\n"
"v_mfma_f32_16x16x32_bf16 v[28:31], a[152:155], a[8:11], v[28:31]\n"
"ds_read_b128 a[96:99], v1 offset:0\n"
"ds_read_b128 a[108:111], v1 offset:1536\n"
"v_mul_f32_e32 v20, s44, v20\n"
"v_mfma_f32_16x16x32_bf16 v[28:31], a[156:159], a[12:15], v[28:31]\n"
"v_exp_f32_e32 v19, v19\n"
"v_mfma_f32_16x16x32_bf16 v[160:163], a[160:163], v[36:39], v[160:163]\n"
"v_mul_f32_e32 v21, s44, v21\n"
"v_cvt_pk_bf16_f32 v33, v18, v19\n"
"v_mfma_f32_16x16x32_bf16 v[164:167], a[164:167], v[36:39], v[164:167]\n"
"ds_read_b128 a[0:3], v5 offset:0\n"
"v_exp_f32_e32 v20, v20\n"
"v_mfma_f32_16x16x32_bf16 v[168:171], a[168:171], v[36:39], v[168:171]\n"
"ds_read_b128 a[100:103], v1 offset:512\n"
"v_mul_f32_e32 v22, s44, v22\n"
"v_mfma_f32_16x16x32_bf16 v[172:175], a[172:175], v[36:39], v[172:175]\n"
"ds_read_b128 a[4:7], v5 offset:512\n"
"v_exp_f32_e32 v21, v21\n"
"v_mfma_f32_16x16x32_bf16 v[176:179], a[176:179], v[36:39], v[176:179]\n"
"s_add_u32 m0, s48, 24576\n"
"v_mul_f32_e32 v23, s44, v23\n"
"v_cvt_pk_bf16_f32 v34, v20, v21\n"
"v_mfma_f32_16x16x32_bf16 v[180:183], a[180:183], v[36:39], v[180:183]\n"
"s_add_u32 s47, s46, 24576\n"
"ds_read_b128 a[8:11], v5 offset:1024\n"
"v_exp_f32_e32 v22, v22\n"
"v_mfma_f32_16x16x32_bf16 v[184:187], a[184:187], v[36:39], v[184:187]\n"
"buffer_load_dwordx4 v6, s[24:27], s47 offen lds\n"
"v_mfma_f32_16x16x32_bf16 v[188:191], a[188:191], v[36:39], v[188:191]\n"
"ds_read_b128 a[12:15], v5 offset:1536\n"
"v_exp_f32_e32 v23, v23\n"
"v_mfma_f32_16x16x32_bf16 a[240:243], a[224:227], v[36:39], a[240:243]\n"
"ds_read_b128 a[104:107], v1 offset:1024\n"
"v_cvt_pk_bf16_f32 v35, v22, v23\n"
"v_mfma_f32_16x16x32_bf16 v[16:19], a[128:131], a[16:19], 0\n"
"v_mul_f32_e32 v24, s44, v24\n"
"v_mul_f32_e32 v25, s44, v25\n"
"v_mfma_f32_16x16x32_bf16 v[16:19], a[132:135], a[20:23], v[16:19]\n"
"v_exp_f32_e32 v24, v24\n"
"v_mfma_f32_16x16x32_bf16 v[16:19], a[136:139], a[24:27], v[16:19]\n"
"ds_read_b128 a[112:115], v2 offset:0\n"
"v_mul_f32_e32 v26, s44, v26\n"
"v_mfma_f32_16x16x32_bf16 v[16:19], a[140:143], a[28:31], v[16:19]\n"
"v_exp_f32_e32 v25, v25\n"
"v_mfma_f32_16x16x32_bf16 v[20:23], a[144:147], a[16:19], 0\n"
"v_mul_f32_e32 v27, s44, v27\n"
"v_cvt_pk_bf16_f32 v36, v24, v25\n"
"v_mfma_f32_16x16x32_bf16 v[20:23], a[148:151], a[20:23], v[20:23]\n"
"v_exp_f32_e32 v26, v26\n"
"v_mfma_f32_16x16x32_bf16 v[20:23], a[152:155], a[24:27], v[20:23]\n"
"ds_read_b128 a[116:119], v2 offset:512\n"
"v_mul_f32_e32 v28, s44, v28\n"
"v_mfma_f32_16x16x32_bf16 v[20:23], a[156:159], a[28:31], v[20:23]\n"
"v_exp_f32_e32 v27, v27\n"
"v_mfma_f32_16x16x32_bf16 v[192:195], a[160:163], v[32:35], v[192:195]\n"
"v_mul_f32_e32 v29, s44, v29\n"
"v_cvt_pk_bf16_f32 v37, v26, v27\n"
"v_mfma_f32_16x16x32_bf16 v[196:199], a[164:167], v[32:35], v[196:199]\n"
"ds_read_b128 a[16:19], v5 offset:4096\n"
"v_exp_f32_e32 v28, v28\n"
"v_mfma_f32_16x16x32_bf16 v[200:203], a[168:171], v[32:35], v[200:203]\n"
"ds_read_b128 a[120:123], v2 offset:1024\n"
"v_mul_f32_e32 v30, s44, v30\n"
"v_mfma_f32_16x16x32_bf16 v[204:207], a[172:175], v[32:35], v[204:207]\n"
"ds_read_b128 a[20:23], v5 offset:4608\n"
"v_exp_f32_e32 v29, v29\n"
"v_mfma_f32_16x16x32_bf16 v[208:211], a[176:179], v[32:35], v[208:211]\n"
"s_add_u32 m0, s48, 25600\n"
"v_mul_f32_e32 v31, s44, v31\n"
"v_cvt_pk_bf16_f32 v38, v28, v29\n"
"v_mfma_f32_16x16x32_bf16 v[212:215], a[180:183], v[32:35], v[212:215]\n"
"s_add_u32 s47, s46, 24704\n"
"ds_read_b128 a[24:27], v5 offset:5120\n"
"v_exp_f32_e32 v30, v30\n"
"v_mfma_f32_16x16x32_bf16 v[216:219], a[184:187], v[32:35], v[216:219]\n"
"buffer_load_dwordx4 v6, s[24:27], s47 offen lds\n"
"v_mfma_f32_16x16x32_bf16 v[220:223], a[188:191], v[32:35], v[220:223]\n"
"ds_read_b128 a[28:31], v5 offset:5632\n"
"v_exp_f32_e32 v31, v31\n"
"v_mfma_f32_16x16x32_bf16 a[244:247], a[224:227], v[32:35], a[244:247]\n"
"ds_read_b128 a[124:127], v2 offset:1536\n"
"v_cvt_pk_bf16_f32 v39, v30, v31\n"
"v_mfma_f32_16x16x32_bf16 v[24:27], a[128:131], a[32:35], 0\n"
"ds_read_b64_tr_b16 a[168:169], v3 offset:512\n"
"v_mul_f32_e32 v16, s44, v16\n"
"v_mul_f32_e32 v17, s44, v17\n"
"v_mfma_f32_16x16x32_bf16 v[24:27], a[132:135], a[36:39], v[24:27]\n"
"ds_read_b64_tr_b16 a[170:171], v4 offset:512\n"
"v_exp_f32_e32 v16, v16\n"
"v_mfma_f32_16x16x32_bf16 v[24:27], a[136:139], a[40:43], v[24:27]\n"
"ds_read_b64_tr_b16 a[160:161], v3 offset:0\n"
"v_mul_f32_e32 v18, s44, v18\n"
"v_mfma_f32_16x16x32_bf16 v[24:27], a[140:143], a[44:47], v[24:27]\n"
"v_exp_f32_e32 v17, v17\n"
"v_mfma_f32_16x16x32_bf16 v[28:31], a[144:147], a[32:35], 0\n"
"v_mul_f32_e32 v19, s44, v19\n"
"v_cvt_pk_bf16_f32 v32, v16, v17\n"
"v_mfma_f32_16x16x32_bf16 v[28:31], a[148:151], a[36:39], v[28:31]\n"
"v_exp_f32_e32 v18, v18\n"
"v_mfma_f32_16x16x32_bf16 v[28:31], a[152:155], a[40:43], v[28:31]\n"
"ds_read_b64_tr_b16 a[162:163], v4 offset:0\n"
"v_mul_f32_e32 v20, s44, v20\n"
"v_mfma_f32_16x16x32_bf16 v[28:31], a[156:159], a[44:47], v[28:31]\n"
"v_exp_f32_e32 v19, v19\n"
"v_mfma_f32_16x16x32_bf16 v[64:67], a[192:195], v[36:39], v[64:67]\n"
"v_mul_f32_e32 v21, s44, v21\n"
"v_cvt_pk_bf16_f32 v33, v18, v19\n"
"v_mfma_f32_16x16x32_bf16 v[68:71], a[196:199], v[36:39], v[68:71]\n"
"ds_read_b128 a[32:35], v5 offset:8192\n"
"v_exp_f32_e32 v20, v20\n"
"v_mfma_f32_16x16x32_bf16 v[72:75], a[200:203], v[36:39], v[72:75]\n"
"ds_read_b64_tr_b16 a[164:165], v11 offset:0\n"
"v_mul_f32_e32 v22, s44, v22\n"
"v_mfma_f32_16x16x32_bf16 v[76:79], a[204:207], v[36:39], v[76:79]\n"
"ds_read_b128 a[36:39], v5 offset:8704\n"
"v_exp_f32_e32 v21, v21\n"
"v_mfma_f32_16x16x32_bf16 v[80:83], a[208:211], v[36:39], v[80:83]\n"
"s_add_u32 m0, s48, 57344\n"
"v_mul_f32_e32 v23, s44, v23\n"
"v_cvt_pk_bf16_f32 v34, v20, v21\n"
"v_mfma_f32_16x16x32_bf16 v[84:87], a[212:215], v[36:39], v[84:87]\n"
"s_add_u32 s76, s46, 24576\n"
"ds_read_b128 a[40:43], v5 offset:9216\n"
"v_exp_f32_e32 v22, v22\n"
"v_mfma_f32_16x16x32_bf16 v[88:91], a[216:219], v[36:39], v[88:91]\n"
"buffer_load_dwordx4 v6, s[28:31], s76 offen lds\n"
"v_mfma_f32_16x16x32_bf16 v[92:95], a[220:223], v[36:39], v[92:95]\n"
"ds_read_b128 a[44:47], v5 offset:9728\n"
"v_exp_f32_e32 v23, v23\n"
"v_mfma_f32_16x16x32_bf16 a[228:231], a[224:227], v[36:39], a[228:231]\n"
"ds_read_b64_tr_b16 a[166:167], v14 offset:0\n"
"v_cvt_pk_bf16_f32 v35, v22, v23\n"
"v_mfma_f32_16x16x32_bf16 v[16:19], a[128:131], a[48:51], 0\n"
"ds_read_b64_tr_b16 a[180:181], v11 offset:1024\n"
"v_mul_f32_e32 v24, s44, v24\n"
"v_mul_f32_e32 v25, s44, v25\n"
"v_mfma_f32_16x16x32_bf16 v[16:19], a[132:135], a[52:55], v[16:19]\n"
"ds_read_b64_tr_b16 a[182:183], v14 offset:1024\n"
"v_exp_f32_e32 v24, v24\n"
"v_mfma_f32_16x16x32_bf16 v[16:19], a[136:139], a[56:59], v[16:19]\n"
"ds_read_b64_tr_b16 a[172:173], v11 offset:512\n"
"v_mul_f32_e32 v26, s44, v26\n"
"v_mfma_f32_16x16x32_bf16 v[16:19], a[140:143], a[60:63], v[16:19]\n"
"v_exp_f32_e32 v25, v25\n"
"v_mfma_f32_16x16x32_bf16 v[20:23], a[144:147], a[48:51], 0\n"
"v_mul_f32_e32 v27, s44, v27\n"
"v_cvt_pk_bf16_f32 v36, v24, v25\n"
"v_accvgpr_read_b32 v10, a228\n"
"v_subrev_u32_e32 v13, s51, v10\n"
"v_rcp_f32_e32 v12, v10\n"
"v_cmp_le_u32_e32 vcc, s52, v13\n"
"v_pk_mul_f32 v[64:65], v[64:65], v[12:13] op_sel_hi:[1,0]\n"
"v_pk_mul_f32 v[66:67], v[66:67], v[12:13] op_sel_hi:[1,0]\n"
"v_pk_mul_f32 v[68:69], v[68:69], v[12:13] op_sel_hi:[1,0]\n"
"v_pk_mul_f32 v[70:71], v[70:71], v[12:13] op_sel_hi:[1,0]\n"
"v_pk_mul_f32 v[72:73], v[72:73], v[12:13] op_sel_hi:[1,0]\n"
"v_pk_mul_f32 v[74:75], v[74:75], v[12:13] op_sel_hi:[1,0]\n"
"v_pk_mul_f32 v[76:77], v[76:77], v[12:13] op_sel_hi:[1,0]\n"
"v_pk_mul_f32 v[78:79], v[78:79], v[12:13] op_sel_hi:[1,0]\n"
"v_pk_mul_f32 v[80:81], v[80:81], v[12:13] op_sel_hi:[1,0]\n"
"v_pk_mul_f32 v[82:83], v[82:83], v[12:13] op_sel_hi:[1,0]\n"
"v_pk_mul_f32 v[84:85], v[84:85], v[12:13] op_sel_hi:[1,0]\n"
"v_pk_mul_f32 v[86:87], v[86:87], v[12:13] op_sel_hi:[1,0]\n"
"v_pk_mul_f32 v[88:89], v[88:89], v[12:13] op_sel_hi:[1,0]\n"
"v_pk_mul_f32 v[90:91], v[90:91], v[12:13] op_sel_hi:[1,0]\n"
"v_pk_mul_f32 v[92:93], v[92:93], v[12:13] op_sel_hi:[1,0]\n"
"v_pk_mul_f32 v[94:95], v[94:95], v[12:13] op_sel_hi:[1,0]\n"
"s_or_b64 s[54:55], s[54:55], vcc\n"
"v_mfma_f32_16x16x32_bf16 v[20:23], a[148:151], a[52:55], v[20:23]\n"
"v_exp_f32_e32 v26, v26\n"
"v_cvt_pk_bf16_f32 v40, v64, v65\n"
"v_cvt_pk_bf16_f32 v41, v66, v67\n"
"v_cvt_pk_bf16_f32 v42, v68, v69\n"
"v_cvt_pk_bf16_f32 v43, v70, v71\n"
"s_waitcnt lgkmcnt(15)\n"
"ds_write_b64 v56, v[40:41] offset:0\n"
"ds_write_b64 v57, v[42:43] offset:0\n"
"v_mfma_f32_16x16x32_bf16 v[20:23], a[152:155], a[56:59], v[20:23]\n"
"ds_read_b64_tr_b16 a[174:175], v14 offset:512\n"
"v_mul_f32_e32 v28, s44, v28\n"
"v_mfma_f32_16x16x32_bf16 v[20:23], a[156:159], a[60:63], v[20:23]\n"
"v_exp_f32_e32 v27, v27\n"
"v_cvt_pk_bf16_f32 v44, v72, v73\n"
"v_cvt_pk_bf16_f32 v45, v74, v75\n"
"v_cvt_pk_bf16_f32 v46, v76, v77\n"
"v_cvt_pk_bf16_f32 v47, v78, v79\n"
"ds_write_b64 v15, v[44:45] offset:0\n"
"ds_write_b64 v60, v[46:47] offset:0\n"
"v_mfma_f32_16x16x32_bf16 v[96:99], a[192:195], v[32:35], v[96:99]\n"
"v_mul_f32_e32 v29, s44, v29\n"
"v_cvt_pk_bf16_f32 v37, v26, v27\n"
"v_mfma_f32_16x16x32_bf16 v[100:103], a[196:199], v[32:35], v[100:103]\n"
"ds_read_b128 a[48:51], v5 offset:12288\n"
"v_exp_f32_e32 v28, v28\n"
"v_cvt_pk_bf16_f32 v48, v80, v81\n"
"v_cvt_pk_bf16_f32 v49, v82, v83\n"
"v_cvt_pk_bf16_f32 v50, v84, v85\n"
"v_cvt_pk_bf16_f32 v51, v86, v87\n"
"ds_write_b64 v56, v[48:49] offset:128\n"
"ds_write_b64 v57, v[50:51] offset:128\n"
"v_mfma_f32_16x16x32_bf16 v[104:107], a[200:203], v[32:35], v[104:107]\n"
"ds_read_b64_tr_b16 a[176:177], v3 offset:1024\n"
"v_mul_f32_e32 v30, s44, v30\n"
"v_mfma_f32_16x16x32_bf16 v[108:111], a[204:207], v[32:35], v[108:111]\n"
"ds_read_b128 a[52:55], v5 offset:12800\n"
"v_exp_f32_e32 v29, v29\n"
"v_cvt_pk_bf16_f32 v52, v88, v89\n"
"v_cvt_pk_bf16_f32 v53, v90, v91\n"
"v_cvt_pk_bf16_f32 v54, v92, v93\n"
"v_cvt_pk_bf16_f32 v55, v94, v95\n"
"ds_write_b64 v15, v[52:53] offset:128\n"
"ds_write_b64 v60, v[54:55] offset:128\n"
"v_mfma_f32_16x16x32_bf16 v[112:115], a[208:211], v[32:35], v[112:115]\n"
"s_add_u32 m0, s48, 58368\n"
"v_mul_f32_e32 v31, s44, v31\n"
"v_cvt_pk_bf16_f32 v38, v28, v29\n"
"v_mfma_f32_16x16x32_bf16 v[116:119], a[212:215], v[32:35], v[116:119]\n"
"s_add_u32 s76, s46, 24704\n"
"ds_read_b128 a[56:59], v5 offset:13312\n"
"v_exp_f32_e32 v30, v30\n"
"v_mfma_f32_16x16x32_bf16 v[120:123], a[216:219], v[32:35], v[120:123]\n"
"buffer_load_dwordx4 v6, s[28:31], s76 offen lds\n"
"v_mfma_f32_16x16x32_bf16 v[124:127], a[220:223], v[32:35], v[124:127]\n"
"ds_read_b128 a[60:63], v5 offset:13824\n"
"v_exp_f32_e32 v31, v31\n"
"v_mfma_f32_16x16x32_bf16 a[232:235], a[224:227], v[32:35], a[232:235]\n"
"ds_read_b64_tr_b16 a[178:179], v4 offset:1024\n"
"v_cvt_pk_bf16_f32 v39, v30, v31\n"
"v_mfma_f32_16x16x32_bf16 v[24:27], a[128:131], a[64:67], 0\n"
"ds_read_b64_tr_b16 a[190:191], v14 offset:1536\n"
"v_mul_f32_e32 v16, s44, v16\n"
"v_mul_f32_e32 v17, s44, v17\n"
"v_mfma_f32_16x16x32_bf16 v[24:27], a[132:135], a[68:71], v[24:27]\n"
"v_exp_f32_e32 v16, v16\n"
"v_mfma_f32_16x16x32_bf16 v[24:27], a[136:139], a[72:75], v[24:27]\n"
"ds_read_b64_tr_b16 a[184:185], v3 offset:1536\n"
"v_mul_f32_e32 v18, s44, v18\n"
"v_mfma_f32_16x16x32_bf16 v[24:27], a[140:143], a[76:79], v[24:27]\n"
"v_exp_f32_e32 v17, v17\n"
"v_mfma_f32_16x16x32_bf16 v[28:31], a[144:147], a[64:67], 0\n"
"v_mul_f32_e32 v19, s44, v19\n"
"v_cvt_pk_bf16_f32 v32, v16, v17\n"
"v_accvgpr_read_b32 v10, a232\n"
"v_subrev_u32_e32 v13, s51, v10\n"
"v_rcp_f32_e32 v12, v10\n"
"v_cmp_le_u32_e32 vcc, s52, v13\n"
"v_pk_mul_f32 v[96:97], v[96:97], v[12:13] op_sel_hi:[1,0]\n"
"v_pk_mul_f32 v[98:99], v[98:99], v[12:13] op_sel_hi:[1,0]\n"
"v_pk_mul_f32 v[100:101], v[100:101], v[12:13] op_sel_hi:[1,0]\n"
"v_pk_mul_f32 v[102:103], v[102:103], v[12:13] op_sel_hi:[1,0]\n"
"v_pk_mul_f32 v[104:105], v[104:105], v[12:13] op_sel_hi:[1,0]\n"
"v_pk_mul_f32 v[106:107], v[106:107], v[12:13] op_sel_hi:[1,0]\n"
"v_pk_mul_f32 v[108:109], v[108:109], v[12:13] op_sel_hi:[1,0]\n"
"v_pk_mul_f32 v[110:111], v[110:111], v[12:13] op_sel_hi:[1,0]\n"
"v_pk_mul_f32 v[112:113], v[112:113], v[12:13] op_sel_hi:[1,0]\n"
"v_pk_mul_f32 v[114:115], v[114:115], v[12:13] op_sel_hi:[1,0]\n"
"v_pk_mul_f32 v[116:117], v[116:117], v[12:13] op_sel_hi:[1,0]\n"
"v_pk_mul_f32 v[118:119], v[118:119], v[12:13] op_sel_hi:[1,0]\n"
"v_pk_mul_f32 v[120:121], v[120:121], v[12:13] op_sel_hi:[1,0]\n"
"v_pk_mul_f32 v[122:123], v[122:123], v[12:13] op_sel_hi:[1,0]\n"
"v_pk_mul_f32 v[124:125], v[124:125], v[12:13] op_sel_hi:[1,0]\n"
"v_pk_mul_f32 v[126:127], v[126:127], v[12:13] op_sel_hi:[1,0]\n"
"s_or_b64 s[54:55], s[54:55], vcc\n"
"v_mfma_f32_16x16x32_bf16 v[28:31], a[148:151], a[68:71], v[28:31]\n"
"v_exp_f32_e32 v18, v18\n"
"v_cvt_pk_bf16_f32 v40, v96, v97\n"
"v_cvt_pk_bf16_f32 v41, v98, v99\n"
"v_cvt_pk_bf16_f32 v42, v100, v101\n"
"v_cvt_pk_bf16_f32 v43, v102, v103\n"
"s_waitcnt lgkmcnt(15)\n"
"ds_write_b64 v56, v[40:41] offset:4096\n"
"ds_write_b64 v57, v[42:43] offset:4096\n"
"v_mfma_f32_16x16x32_bf16 v[28:31], a[152:155], a[72:75], v[28:31]\n"
"ds_read_b64_tr_b16 a[186:187], v4 offset:1536\n"
"v_mul_f32_e32 v20, s44, v20\n"
"v_mfma_f32_16x16x32_bf16 v[28:31], a[156:159], a[76:79], v[28:31]\n"
"v_exp_f32_e32 v19, v19\n"
"v_cvt_pk_bf16_f32 v44, v104, v105\n"
"v_cvt_pk_bf16_f32 v45, v106, v107\n"
"v_cvt_pk_bf16_f32 v46, v108, v109\n"
"v_cvt_pk_bf16_f32 v47, v110, v111\n"
"ds_write_b64 v15, v[44:45] offset:4096\n"
"ds_write_b64 v60, v[46:47] offset:4096\n"
"v_mfma_f32_16x16x32_bf16 v[128:131], a[192:195], v[36:39], v[128:131]\n"
"v_mul_f32_e32 v21, s44, v21\n"
"v_cvt_pk_bf16_f32 v33, v18, v19\n"
"v_mfma_f32_16x16x32_bf16 v[132:135], a[196:199], v[36:39], v[132:135]\n"
"v_exp_f32_e32 v20, v20\n"
"v_cvt_pk_bf16_f32 v48, v112, v113\n"
"v_cvt_pk_bf16_f32 v49, v114, v115\n"
"v_cvt_pk_bf16_f32 v50, v116, v117\n"
"v_cvt_pk_bf16_f32 v51, v118, v119\n"
"ds_write_b64 v56, v[48:49] offset:4224\n"
"ds_write_b64 v57, v[50:51] offset:4224\n"
"v_mfma_f32_16x16x32_bf16 v[136:139], a[200:203], v[36:39], v[136:139]\n"
"ds_read_b64_tr_b16 a[188:189], v11 offset:1536\n"
"v_mul_f32_e32 v22, s44, v22\n"
"v_mfma_f32_16x16x32_bf16 v[140:143], a[204:207], v[36:39], v[140:143]\n"
"v_exp_f32_e32 v21, v21\n"
"v_cvt_pk_bf16_f32 v52, v120, v121\n"
"v_cvt_pk_bf16_f32 v53, v122, v123\n"
"v_cvt_pk_bf16_f32 v54, v124, v125\n"
"v_cvt_pk_bf16_f32 v55, v126, v127\n"
"ds_write_b64 v15, v[52:53] offset:4224\n"
"ds_write_b64 v60, v[54:55] offset:4224\n"
"v_mfma_f32_16x16x32_bf16 v[144:147], a[208:211], v[36:39], v[144:147]\n"
"v_mul_f32_e32 v23, s44, v23\n"
"v_cvt_pk_bf16_f32 v34, v20, v21\n"
"v_mfma_f32_16x16x32_bf16 v[148:151], a[212:215], v[36:39], v[148:151]\n"
"v_exp_f32_e32 v22, v22\n"
"v_mfma_f32_16x16x32_bf16 v[152:155], a[216:219], v[36:39], v[152:155]\n"
"ds_read_b128 a[64:67], v5 offset:16384\n"
"ds_read_b128 a[68:71], v5 offset:16896\n"
"v_mfma_f32_16x16x32_bf16 v[156:159], a[220:223], v[36:39], v[156:159]\n"
"ds_read_b128 a[76:79], v5 offset:17920\n"
"v_exp_f32_e32 v23, v23\n"
"v_mfma_f32_16x16x32_bf16 a[236:239], a[224:227], v[36:39], a[236:239]\n"
"ds_read_b128 a[72:75], v5 offset:17408\n"
"v_cvt_pk_bf16_f32 v35, v22, v23\n"
"s_mov_b32 s45, s46\n"
"s_add_u32 s46, s46, 1048576\n"
"s_add_u32 s50, s50, 1\n"
"s_cmp_ge_u32 s50, 3\n"
"s_cselect_b32 s46, 1073741824, s46\n"
"s_add_u32 s72, s50, 1\n"
"s_mul_i32 s70, s72, 3014656\n"
"s_cmp_gt_u32 s63, s72\n"
"s_cselect_b32 s71, 0, 16384\n"
"s_add_u32 s70, s70, s71\n"
"s_add_u32 s36, s66, s70\n"
"s_addc_u32 s37, s67, 0\n"
"s_cmp_eq_u32 s63, s72\n"
"s_cselect_b32 s71, 5, 6\n"
"s_mul_i32 s38, s71, 16384\n"
"s_mul_i32 s58, s64, s71\n"
"s_mul_i32 s58, s58, 4096\n"
"s_cselect_b32 s59, 1073741824, s58\n"
"s_cmp_gt_u32 s72, 3\n"
"s_cselect_b32 s38, 0, s38\n"
"L_T56%=:\n"
"v_mfma_f32_16x16x32_bf16 v[16:19], a[96:99], a[0:3], 0\n"
"v_mul_f32_e32 v24, s44, v24\n"
"v_mul_f32_e32 v25, s44, v25\n"
"v_mfma_f32_16x16x32_bf16 v[16:19], a[100:103], a[4:7], v[16:19]\n"
"v_exp_f32_e32 v24, v24\n"
"v_mfma_f32_16x16x32_bf16 v[16:19], a[104:107], a[8:11], v[16:19]\n"
"v_mul_f32_e32 v26, s44, v26\n"
"v_mfma_f32_16x16x32_bf16 v[16:19], a[108:111], a[12:15], v[16:19]\n"
"v_exp_f32_e32 v25, v25\n"
"v_mfma_f32_16x16x32_bf16 v[20:23], a[112:115], a[0:3], 0\n"
"v_mul_f32_e32 v27, s44, v27\n"
"v_cvt_pk_bf16_f32 v36, v24, v25\n"
"v_accvgpr_read_b32 v10, a236\n"
"v_subrev_u32_e32 v13, s51, v10\n"
"v_rcp_f32_e32 v12, v10\n"
"v_cmp_le_u32_e32 vcc, s52, v13\n"
"v_pk_mul_f32 v[128:129], v[128:129], v[12:13] op_sel_hi:[1,0]\n"
"v_pk_mul_f32 v[130:131], v[130:131], v[12:13] op_sel_hi:[1,0]\n"
"v_pk_mul_f32 v[132:133], v[132:133], v[12:13] op_sel_hi:[1,0]\n"
"v_pk_mul_f32 v[134:135], v[134:135], v[12:13] op_sel_hi:[1,0]\n"
"v_pk_mul_f32 v[136:137], v[136:137], v[12:13] op_sel_hi:[1,0]\n"
"v_pk_mul_f32 v[138:139], v[138:139], v[12:13] op_sel_hi:[1,0]\n"
"v_pk_mul_f32 v[140:141], v[140:141], v[12:13] op_sel_hi:[1,0]\n"
"v_pk_mul_f32 v[142:143], v[142:143], v[12:13] op_sel_hi:[1,0]\n"
"v_pk_mul_f32 v[144:145], v[144:145], v[12:13] op_sel_hi:[1,0]\n"
"v_pk_mul_f32 v[146:147], v[146:147], v[12:13] op_sel_hi:[1,0]\n"
"v_pk_mul_f32 v[148:149], v[148:149], v[12:13] op_sel_hi:[1,0]\n"
"v_pk_mul_f32 v[150:151], v[150:151], v[12:13] op_sel_hi:[1,0]\n"
"v_pk_mul_f32 v[152:153], v[152:153], v[12:13] op_sel_hi:[1,0]\n"
"v_pk_mul_f32 v[154:155], v[154:155], v[12:13] op_sel_hi:[1,0]\n"
"v_pk_mul_f32 v[156:157], v[156:157], v[12:13] op_sel_hi:[1,0]\n"
"v_pk_mul_f32 v[158:159], v[158:159], v[12:13] op_sel_hi:[1,0]\n"
"s_or_b64 s[54:55], s[54:55], vcc\n"
"v_mfma_f32_16x16x32_bf16 v[20:23], a[116:119], a[4:7], v[20:23]\n"
"s_waitcnt vmcnt(9) lgkmcnt(6)\n"
"s_barrier\n"
"v_exp_f32_e32 v26, v26\n"
"v_cvt_pk_bf16_f32 v40, v128, v129\n"
"v_cvt_pk_bf16_f32 v41, v130, v131\n"
"v_cvt_pk_bf16_f32 v42, v132, v133\n"
"v_cvt_pk_bf16_f32 v43, v134, v135\n"
"ds_write_b64 v56, v[40:41] offset:8192\n"
"ds_write_b64 v57, v[42:43] offset:8192\n"
"v_mfma_f32_16x16x32_bf16 v[20:23], a[120:123], a[8:11], v[20:23]\n"
"ds_read_b128 a[128:131], v1 offset:8192\n"
"ds_read_b128 a[140:143], v1 offset:9728\n"
"v_mul_f32_e32 v28, s44, v28\n"
"v_mfma_f32_16x16x32_bf16 v[20:23], a[124:127], a[12:15], v[20:23]\n"
"v_exp_f32_e32 v27, v27\n"
"v_cvt_pk_bf16_f32 v44, v136, v137\n"
"v_cvt_pk_bf16_f32 v45, v138, v139\n"
"v_cvt_pk_bf16_f32 v46, v140, v141\n"
"v_cvt_pk_bf16_f32 v47, v142, v143\n"
"ds_write_b64 v15, v[44:45] offset:8192\n"
"ds_write_b64 v60, v[46:47] offset:8192\n"
"v_mfma_f32_16x16x32_bf16 v[160:163], a[192:195], v[32:35], v[160:163]\n"
"v_mul_f32_e32 v29, s44, v29\n"
"v_cvt_pk_bf16_f32 v37, v26, v27\n"
"v_mfma_f32_16x16x32_bf16 v[164:167], a[196:199], v[32:35], v[164:167]\n"
"v_exp_f32_e32 v28, v28\n"
"v_cvt_pk_bf16_f32 v48, v144, v145\n"
"v_cvt_pk_bf16_f32 v49, v146, v147\n"
"v_cvt_pk_bf16_f32 v50, v148, v149\n"
"v_cvt_pk_bf16_f32 v51, v150, v151\n"
"ds_write_b64 v56, v[48:49] offset:8320\n"
"ds_write_b64 v57, v[50:51] offset:8320\n"
"v_mfma_f32_16x16x32_bf16 v[168:171], a[200:203], v[32:35], v[168:171]\n"
"ds_read_b128 a[132:135], v1 offset:8704\n"
"v_mul_f32_e32 v30, s44, v30\n"
"v_mfma_f32_16x16x32_bf16 v[172:175], a[204:207], v[32:35], v[172:175]\n"
"v_exp_f32_e32 v29, v29\n"
"v_cvt_pk_bf16_f32 v52, v152, v153\n"
"v_cvt_pk_bf16_f32 v53, v154, v155\n"
"v_cvt_pk_bf16_f32 v54, v156, v157\n"
"v_cvt_pk_bf16_f32 v55, v158, v159\n"
"ds_write_b64 v15, v[52:53] offset:8320\n"
"ds_write_b64 v60, v[54:55] offset:8320\n"
"v_mfma_f32_16x16x32_bf16 v[176:179], a[208:211], v[32:35], v[176:179]\n"
"s_add_u32 m0, s48, 0\n"
"v_mul_f32_e32 v31, s44, v31\n"
"v_cvt_pk_bf16_f32 v38, v28, v29\n"
"v_mfma_f32_16x16x32_bf16 v[180:183], a[212:215], v[32:35], v[180:183]\n"
"s_add_u32 s47, s45, 32768\n"
"v_exp_f32_e32 v30, v30\n"
"v_mfma_f32_16x16x32_bf16 v[184:187], a[216:219], v[32:35], v[184:187]\n"
"buffer_load_dwordx4 v6, s[24:27], s47 offen lds\n"
"v_mfma_f32_16x16x32_bf16 v[188:191], a[220:223], v[32:35], v[188:191]\n"
"v_exp_f32_e32 v31, v31\n"
"v_mfma_f32_16x16x32_bf16 a[240:243], a[224:227], v[32:35], a[240:243]\n"
"ds_read_b128 a[136:139], v1 offset:9216\n"
"v_cvt_pk_bf16_f32 v39, v30, v31\n"
"v_mfma_f32_16x16x32_bf16 v[24:27], a[96:99], a[16:19], 0\n"
"v_mul_f32_e32 v16, s44, v16\n"
"v_mul_f32_e32 v17, s44, v17\n"
"v_mfma_f32_16x16x32_bf16 v[24:27], a[100:103], a[20:23], v[24:27]\n"
"v_exp_f32_e32 v16, v16\n"
"v_mfma_f32_16x16x32_bf16 v[24:27], a[104:107], a[24:27], v[24:27]\n"
"ds_read_b128 a[144:147], v2 offset:8192\n"
"v_mul_f32_e32 v18, s44, v18\n"
"v_mfma_f32_16x16x32_bf16 v[24:27], a[108:111], a[28:31], v[24:27]\n"
"v_exp_f32_e32 v17, v17\n"
"v_mfma_f32_16x16x32_bf16 v[28:31], a[112:115], a[16:19], 0\n"
"v_mul_f32_e32 v19, s44, v19\n"
"v_cvt_pk_bf16_f32 v32, v16, v17\n"
"v_accvgpr_read_b32 v10, a240\n"
"v_subrev_u32_e32 v13, s51, v10\n"
"v_rcp_f32_e32 v12, v10\n"
"v_cmp_le_u32_e32 vcc, s52, v13\n"
"v_pk_mul_f32 v[160:161], v[160:161], v[12:13] op_sel_hi:[1,0]\n"
"v_pk_mul_f32 v[162:163], v[162:163], v[12:13] op_sel_hi:[1,0]\n"
"v_pk_mul_f32 v[164:165], v[164:165], v[12:13] op_sel_hi:[1,0]\n"
"v_pk_mul_f32 v[166:167], v[166:167], v[12:13] op_sel_hi:[1,0]\n"
"v_pk_mul_f32 v[168:169], v[168:169], v[12:13] op_sel_hi:[1,0]\n"
"v_pk_mul_f32 v[170:171], v[170:171], v[12:13] op_sel_hi:[1,0]\n"
"v_pk_mul_f32 v[172:173], v[172:173], v[12:13] op_sel_hi:[1,0]\n"
"v_pk_mul_f32 v[174:175], v[174:175], v[12:13] op_sel_hi:[1,0]\n"
"v_pk_mul_f32 v[176:177], v[176:177], v[12:13] op_sel_hi:[1,0]\n"
"v_pk_mul_f32 v[178:179], v[178:179], v[12:13] op_sel_hi:[1,0]\n"
"v_pk_mul_f32 v[180:181], v[180:181], v[12:13] op_sel_hi:[1,0]\n"
"v_pk_mul_f32 v[182:183], v[182:183], v[12:13] op_sel_hi:[1,0]\n"
"v_pk_mul_f32 v[184:185], v[184:185], v[12:13] op_sel_hi:[1,0]\n"
"v_pk_mul_f32 v[186:187], v[186:187], v[12:13] op_sel_hi:[1,0]\n"
"v_pk_mul_f32 v[188:189], v[188:189], v[12:13] op_sel_hi:[1,0]\n"
"v_pk_mul_f32 v[190:191], v[190:191], v[12:13] op_sel_hi:[1,0]\n"
"s_or_b64 s[54:55], s[54:55], vcc\n"
"v_mfma_f32_16x16x32_bf16 v[28:31], a[116:119], a[20:23], v[28:31]\n"
"v_exp_f32_e32 v18, v18\n"
"v_cvt_pk_bf16_f32 v40, v160, v161\n"
"v_cvt_pk_bf16_f32 v41, v162, v163\n"
"v_cvt_pk_bf16_f32 v42, v164, v165\n"
"v_cvt_pk_bf16_f32 v43, v166, v167\n"
"ds_write_b64 v56, v[40:41] offset:12288\n"
"ds_write_b64 v57, v[42:43] offset:12288\n"
"v_mfma_f32_16x16x32_bf16 v[28:31], a[120:123], a[24:27], v[28:31]\n"
"ds_read_b128 a[148:151], v2 offset:8704\n"
"v_mul_f32_e32 v20, s44, v20\n"
"v_mfma_f32_16x16x32_bf16 v[28:31], a[124:127], a[28:31], v[28:31]\n"
"v_exp_f32_e32 v19, v19\n"
"v_cvt_pk_bf16_f32 v44, v168, v169\n"
"v_cvt_pk_bf16_f32 v45, v170, v171\n"
"v_cvt_pk_bf16_f32 v46, v172, v173\n"
"v_cvt_pk_bf16_f32 v47, v174, v175\n"
"ds_write_b64 v15, v[44:45] offset:12288\n"
"ds_write_b64 v60, v[46:47] offset:12288\n"
"v_mfma_f32_16x16x32_bf16 v[192:195], a[192:195], v[36:39], v[192:195]\n"
"v_mul_f32_e32 v21, s44, v21\n"
"v_cvt_pk_bf16_f32 v33, v18, v19\n"
"v_mfma_f32_16x16x32_bf16 v[196:199], a[196:199], v[36:39], v[196:199]\n"
"v_exp_f32_e32 v20, v20\n"
"v_cvt_pk_bf16_f32 v48, v176, v177\n"
"v_cvt_pk_bf16_f32 v49, v178, v179\n"
"v_cvt_pk_bf16_f32 v50, v180, v181\n"
"v_cvt_pk_bf16_f32 v51, v182, v183\n"
"ds_write_b64 v56, v[48:49] offset:12416\n"
"ds_write_b64 v57, v[50:51] offset:12416\n"
"v_mfma_f32_16x16x32_bf16 v[200:203], a[200:203], v[36:39], v[200:203]\n"
"ds_read_b128 a[152:155], v2 offset:9216\n"
"v_mul_f32_e32 v22, s44, v22\n"
"v_mfma_f32_16x16x32_bf16 v[204:207], a[204:207], v[36:39], v[204:207]\n"
"v_exp_f32_e32 v21, v21\n"
"v_cvt_pk_bf16_f32 v52, v184, v185\n"
"v_cvt_pk_bf16_f32 v53, v186, v187\n"
"v_cvt_pk_bf16_f32 v54, v188, v189\n"
"v_cvt_pk_bf16_f32 v55, v190, v191\n"
"ds_write_b64 v15, v[52:53] offset:12416\n"
"ds_write_b64 v60, v[54:55] offset:12416\n"
"v_mfma_f32_16x16x32_bf16 v[208:211], a[208:211], v[36:39], v[208:211]\n"
"s_add_u32 m0, s48, 1024\n"
"v_mul_f32_e32 v23, s44, v23\n"
"v_cvt_pk_bf16_f32 v34, v20, v21\n"
"v_mfma_f32_16x16x32_bf16 v[212:215], a[212:215], v[36:39], v[212:215]\n"
"s_add_u32 s47, s45, 32896\n"
"v_exp_f32_e32 v22, v22\n"
"v_mfma_f32_16x16x32_bf16 v[216:219], a[216:219], v[36:39], v[216:219]\n"
"buffer_load_dwordx4 v6, s[24:27], s47 offen lds\n"
"v_mfma_f32_16x16x32_bf16 v[220:223], a[220:223], v[36:39], v[220:223]\n"
"v_exp_f32_e32 v23, v23\n"
"v_mfma_f32_16x16x32_bf16 a[244:247], a[224:227], v[36:39], a[244:247]\n"
"ds_read_b128 a[156:159], v2 offset:9728\n"
"v_cvt_pk_bf16_f32 v35, v22, v23\n"
"v_mfma_f32_16x16x32_bf16 v[16:19], a[96:99], a[32:35], 0\n"
"v_mul_f32_e32 v24, s44, v24\n"
"v_mul_f32_e32 v25, s44, v25\n"
"v_mfma_f32_16x16x32_bf16 v[16:19], a[100:103], a[36:39], v[16:19]\n"
"v_exp_f32_e32 v24, v24\n"
"v_mfma_f32_16x16x32_bf16 v[16:19], a[104:107], a[40:43], v[16:19]\n"
"ds_read_b64_tr_b16 a[192:193], v3 offset:8192\n"
"v_mul_f32_e32 v26, s44, v26\n"
"v_mfma_f32_16x16x32_bf16 v[16:19], a[108:111], a[44:47], v[16:19]\n"
"v_exp_f32_e32 v25, v25\n"
"v_mfma_f32_16x16x32_bf16 v[20:23], a[112:115], a[32:35], 0\n"
"v_mul_f32_e32 v27, s44, v27\n"
"v_cvt_pk_bf16_f32 v36, v24, v25\n"
"v_accvgpr_read_b32 v10, a244\n"
"v_subrev_u32_e32 v13, s51, v10\n"
"v_rcp_f32_e32 v12, v10\n"
"v_cmp_le_u32_e32 vcc, s52, v13\n"
"v_pk_mul_f32 v[192:193], v[192:193], v[12:13] op_sel_hi:[1,0]\n"
"v_pk_mul_f32 v[194:195], v[194:195], v[12:13] op_sel_hi:[1,0]\n"
"v_pk_mul_f32 v[196:197], v[196:197], v[12:13] op_sel_hi:[1,0]\n"
"v_pk_mul_f32 v[198:199], v[198:199], v[12:13] op_sel_hi:[1,0]\n"
"v_pk_mul_f32 v[200:201], v[200:201], v[12:13] op_sel_hi:[1,0]\n"
"v_pk_mul_f32 v[202:203], v[202:203], v[12:13] op_sel_hi:[1,0]\n"
"v_pk_mul_f32 v[204:205], v[204:205], v[12:13] op_sel_hi:[1,0]\n"
"v_pk_mul_f32 v[206:207], v[206:207], v[12:13] op_sel_hi:[1,0]\n"
"v_pk_mul_f32 v[208:209], v[208:209], v[12:13] op_sel_hi:[1,0]\n"
"v_pk_mul_f32 v[210:211], v[210:211], v[12:13] op_sel_hi:[1,0]\n"
"v_pk_mul_f32 v[212:213], v[212:213], v[12:13] op_sel_hi:[1,0]\n"
"v_pk_mul_f32 v[214:215], v[214:215], v[12:13] op_sel_hi:[1,0]\n"
"v_pk_mul_f32 v[216:217], v[216:217], v[12:13] op_sel_hi:[1,0]\n"
"v_pk_mul_f32 v[218:219], v[218:219], v[12:13] op_sel_hi:[1,0]\n"
"v_pk_mul_f32 v[220:221], v[220:221], v[12:13] op_sel_hi:[1,0]\n"
"v_pk_mul_f32 v[222:223], v[222:223], v[12:13] op_sel_hi:[1,0]\n"
"s_or_b64 s[54:55], s[54:55], vcc\n"
"v_mfma_f32_16x16x32_bf16 v[20:23], a[116:119], a[36:39], v[20:23]\n"
"v_exp_f32_e32 v26, v26\n"
"v_cvt_pk_bf16_f32 v40, v192, v193\n"
"v_cvt_pk_bf16_f32 v41, v194, v195\n"
"v_cvt_pk_bf16_f32 v42, v196, v197\n"
"v_cvt_pk_bf16_f32 v43, v198, v199\n"
"s_waitcnt lgkmcnt(15)\n"
"ds_write_b64 v56, v[40:41] offset:16384\n"
"ds_write_b64 v57, v[42:43] offset:16384\n"
"v_mfma_f32_16x16x32_bf16 v[20:23], a[120:123], a[40:43], v[20:23]\n"
"ds_read_b64_tr_b16 a[194:195], v4 offset:8192\n"
"v_mul_f32_e32 v28, s44, v28\n"
"v_mfma_f32_16x16x32_bf16 v[20:23], a[124:127], a[44:47], v[20:23]\n"
"v_exp_f32_e32 v27, v27\n"
"v_cvt_pk_bf16_f32 v44, v200, v201\n"
"v_cvt_pk_bf16_f32 v45, v202, v203\n"
"v_cvt_pk_bf16_f32 v46, v204, v205\n"
"v_cvt_pk_bf16_f32 v47, v206, v207\n"
"ds_write_b64 v15, v[44:45] offset:16384\n"
"ds_write_b64 v60, v[46:47] offset:16384\n"
"v_mfma_f32_16x16x32_bf16 v[64:67], a[160:163], v[32:35], 0\n"
"v_mul_f32_e32 v29, s44, v29\n"
"v_cvt_pk_bf16_f32 v37, v26, v27\n"
"v_mfma_f32_16x16x32_bf16 v[68:71], a[164:167], v[32:35], 0\n"
"v_exp_f32_e32 v28, v28\n"
"v_cvt_pk_bf16_f32 v48, v208, v209\n"
"v_cvt_pk_bf16_f32 v49, v210, v211\n"
"v_cvt_pk_bf16_f32 v50, v212, v213\n"
"v_cvt_pk_bf16_f32 v51, v214, v215\n"
"ds_write_b64 v56, v[48:49] offset:16512\n"
"ds_write_b64 v57, v[50:51] offset:16512\n"
"v_mfma_f32_16x16x32_bf16 v[72:75], a[168:171], v[32:35], 0\n"
"ds_read_b64_tr_b16 a[196:197], v11 offset:8192\n"
"v_mul_f32_e32 v30, s44, v30\n"
"v_mfma_f32_16x16x32_bf16 v[76:79], a[172:175], v[32:35], 0\n"
"v_exp_f32_e32 v29, v29\n"
"v_cvt_pk_bf16_f32 v52, v216, v217\n"
"v_cvt_pk_bf16_f32 v53, v218, v219\n"
"v_cvt_pk_bf16_f32 v54, v220, v221\n"
"v_cvt_pk_bf16_f32 v55, v222, v223\n"
"ds_write_b64 v15, v[52:53] offset:16512\n"
"ds_write_b64 v60, v[54:55] offset:16512\n"
"v_mfma_f32_16x16x32_bf16 v[80:83], a[176:179], v[32:35], 0\n"
"s_add_u32 m0, s48, 32768\n"
"v_mul_f32_e32 v31, s44, v31\n"
"v_cvt_pk_bf16_f32 v38, v28, v29\n"
"v_mfma_f32_16x16x32_bf16 v[84:87], a[180:183], v[32:35], 0\n"
"s_add_u32 s76, s45, 32768\n"
"v_exp_f32_e32 v30, v30\n"
"v_mfma_f32_16x16x32_bf16 v[88:91], a[184:187], v[32:35], 0\n"
"buffer_load_dwordx4 v6, s[28:31], s76 offen lds\n"
"v_mfma_f32_16x16x32_bf16 v[92:95], a[188:191], v[32:35], 0\n"
"v_exp_f32_e32 v31, v31\n"
"v_mfma_f32_16x16x32_bf16 a[228:231], a[224:227], v[32:35], 0\n"
"ds_read_b64_tr_b16 a[198:199], v14 offset:8192\n"
"v_cvt_pk_bf16_f32 v39, v30, v31\n"
"s_cmp_lg_u64 s[54:55], 0\n"
"s_cselect_b32 s57, s56, 0\n"
"s_or_b32 s53, s53, s57\n"
"s_lshl_b32 s56, s56, 1\n"
"s_mov_b64 s[54:55], 0\n"
"v_mfma_f32_16x16x32_bf16 v[24:27], a[96:99], a[48:51], 0\n"
"v_mul_f32_e32 v16, s44, v16\n"
"v_mul_f32_e32 v17, s44, v17\n"
"v_mfma_f32_16x16x32_bf16 v[24:27], a[100:103], a[52:55], v[24:27]\n"
"v_exp_f32_e32 v16, v16\n"
"v_mfma_f32_16x16x32_bf16 v[24:27], a[104:107], a[56:59], v[24:27]\n"
"ds_read_b64_tr_b16 a[200:201], v3 offset:8704\n"
"v_mul_f32_e32 v18, s44, v18\n"
"v_mfma_f32_16x16x32_bf16 v[24:27], a[108:111], a[60:63], v[24:27]\n"
"v_exp_f32_e32 v17, v17\n"
"v_mfma_f32_16x16x32_bf16 v[28:31], a[112:115], a[48:51], 0\n"
"v_mul_f32_e32 v19, s44, v19\n"
"v_cvt_pk_bf16_f32 v32, v16, v17\n"
"v_mfma_f32_16x16x32_bf16 v[28:31], a[116:119], a[52:55], v[28:31]\n"
"v_exp_f32_e32 v18, v18\n"
"v_mfma_f32_16x16x32_bf16 v[28:31], a[120:123], a[56:59], v[28:31]\n"
"ds_read_b64_tr_b16 a[202:203], v4 offset:8704\n"
"v_mul_f32_e32 v20, s44, v20\n"
"v_mfma_f32_16x16x32_bf16 v[28:31], a[124:127], a[60:63], v[28:31]\n"
"v_exp_f32_e32 v19, v19\n"
"v_mfma_f32_16x16x32_bf16 v[96:99], a[160:163], v[36:39], 0\n"
"v_mul_f32_e32 v21, s44, v21\n"
"v_cvt_pk_bf16_f32 v33, v18, v19\n"
"v_mfma_f32_16x16x32_bf16 v[100:103], a[164:167], v[36:39], 0\n"
"v_exp_f32_e32 v20, v20\n"
"v_mfma_f32_16x16x32_bf16 v[104:107], a[168:171], v[36:39], 0\n"
"ds_read_b64_tr_b16 a[204:205], v11 offset:8704\n"
"v_mul_f32_e32 v22, s44, v22\n"
"v_mfma_f32_16x16x32_bf16 v[108:111], a[172:175], v[36:39], 0\n"
"v_exp_f32_e32 v21, v21\n"
"v_mfma_f32_16x16x32_bf16 v[112:115], a[176:179], v[36:39], 0\n"
"s_add_u32 m0, s48, 33792\n"
"v_mul_f32_e32 v23, s44, v23\n"
"v_cvt_pk_bf16_f32 v34, v20, v21\n"
"v_mfma_f32_16x16x32_bf16 v[116:119], a[180:183], v[36:39], 0\n"
"s_add_u32 s76, s45, 32896\n"
"v_exp_f32_e32 v22, v22\n"
"v_mfma_f32_16x16x32_bf16 v[120:123], a[184:187], v[36:39], 0\n"
"buffer_load_dwordx4 v6, s[28:31], s76 offen lds\n"
"v_mfma_f32_16x16x32_bf16 v[124:127], a[188:191], v[36:39], 0\n"
"v_exp_f32_e32 v23, v23\n"
"v_mfma_f32_16x16x32_bf16 a[232:235], a[224:227], v[36:39], 0\n"
"ds_read_b64_tr_b16 a[206:207], v14 offset:8704\n"
"v_cvt_pk_bf16_f32 v35, v22, v23\n"
"v_mfma_f32_16x16x32_bf16 v[16:19], a[96:99], a[64:67], 0\n"
"v_mul_f32_e32 v24, s44, v24\n"
"v_mul_f32_e32 v25, s44, v25\n"
"v_mfma_f32_16x16x32_bf16 v[16:19], a[100:103], a[68:71], v[16:19]\n"
"v_exp_f32_e32 v24, v24\n"
"v_mfma_f32_16x16x32_bf16 v[16:19], a[104:107], a[72:75], v[16:19]\n"
"ds_read_b64_tr_b16 a[210:211], v4 offset:9216\n"
"v_mul_f32_e32 v26, s44, v26\n"
"v_mfma_f32_16x16x32_bf16 v[16:19], a[108:111], a[76:79], v[16:19]\n"
"v_exp_f32_e32 v25, v25\n"
"v_mfma_f32_16x16x32_bf16 v[20:23], a[112:115], a[64:67], 0\n"
"v_mul_f32_e32 v27, s44, v27\n"
"v_cvt_pk_bf16_f32 v36, v24, v25\n"
"v_mfma_f32_16x16x32_bf16 v[20:23], a[116:119], a[68:71], v[20:23]\n"
"v_exp_f32_e32 v26, v26\n"
"v_mfma_f32_16x16x32_bf16 v[20:23], a[120:123], a[72:75], v[20:23]\n"
"ds_read_b64_tr_b16 a[212:213], v11 offset:9216\n"
"v_mul_f32_e32 v28, s44, v28\n"
"v_mfma_f32_16x16x32_bf16 v[20:23], a[124:127], a[76:79], v[20:23]\n"
"v_exp_f32_e32 v27, v27\n"
"v_mfma_f32_16x16x32_bf16 v[128:131], a[160:163], v[32:35], 0\n"
"v_mul_f32_e32 v29, s44, v29\n"
"v_cvt_pk_bf16_f32 v37, v26, v27\n"
"v_mfma_f32_16x16x32_bf16 v[132:135], a[164:167], v[32:35], 0\n"
"v_exp_f32_e32 v28, v28\n"
"v_mfma_f32_16x16x32_bf16 v[136:139], a[168:171], v[32:35], 0\n"
"ds_read_b64_tr_b16 a[214:215], v14 offset:9216\n"
"v_mul_f32_e32 v30, s44, v30\n"
"v_mfma_f32_16x16x32_bf16 v[140:143], a[172:175], v[32:35], 0\n"
"v_exp_f32_e32 v29, v29\n"
"v_mfma_f32_16x16x32_bf16 v[144:147], a[176:179], v[32:35], 0\n"
"v_mul_f32_e32 v31, s44, v31\n"
"v_cvt_pk_bf16_f32 v38, v28, v29\n"
"v_mfma_f32_16x16x32_bf16 v[148:151], a[180:183], v[32:35], 0\n"
"v_exp_f32_e32 v30, v30\n"
"v_mfma_f32_16x16x32_bf16 v[152:155], a[184:187], v[32:35], 0\n"
"ds_read_b64_tr_b16 a[208:209], v3 offset:9216\n"
"v_mfma_f32_16x16x32_bf16 v[156:159], a[188:191], v[32:35], 0\n"
"v_exp_f32_e32 v31, v31\n"
"v_mfma_f32_16x16x32_bf16 a[236:239], a[224:227], v[32:35], 0\n"
"v_cvt_pk_bf16_f32 v39, v30, v31\n"
"v_mfma_f32_16x16x32_bf16 v[24:27], a[96:99], a[80:83], 0\n"
"ds_read_b64_tr_b16 a[222:223], v14 offset:9728\n"
"v_mul_f32_e32 v16, s44, v16\n"
"v_mul_f32_e32 v17, s44, v17\n"
"v_mfma_f32_16x16x32_bf16 v[24:27], a[100:103], a[84:87], v[24:27]\n"
"v_exp_f32_e32 v16, v16\n"
"v_mfma_f32_16x16x32_bf16 v[24:27], a[104:107], a[88:91], v[24:27]\n"
"ds_read_b64_tr_b16 a[216:217], v3 offset:9728\n"
"v_mul_f32_e32 v18, s44, v18\n"
"v_mfma_f32_16x16x32_bf16 v[24:27], a[108:111], a[92:95], v[24:27]\n"
"v_exp_f32_e32 v17, v17\n"
"v_mfma_f32_16x16x32_bf16 v[28:31], a[112:115], a[80:83], 0\n"
"v_mul_f32_e32 v19, s44, v19\n"
"v_cvt_pk_bf16_f32 v32, v16, v17\n"
"v_mfma_f32_16x16x32_bf16 v[28:31], a[116:119], a[84:87], v[28:31]\n"
"v_exp_f32_e32 v18, v18\n"
"v_mfma_f32_16x16x32_bf16 v[28:31], a[120:123], a[88:91], v[28:31]\n"
"ds_read_b64_tr_b16 a[218:219], v4 offset:9728\n"
"v_mul_f32_e32 v20, s44, v20\n"
"v_mfma_f32_16x16x32_bf16 v[28:31], a[124:127], a[92:95], v[28:31]\n"
"v_exp_f32_e32 v19, v19\n"
"v_mfma_f32_16x16x32_bf16 v[160:163], a[160:163], v[36:39], 0\n"
"v_mul_f32_e32 v21, s44, v21\n"
"v_cvt_pk_bf16_f32 v33, v18, v19\n"
"v_mfma_f32_16x16x32_bf16 v[164:167], a[164:167], v[36:39], 0\n"
"v_exp_f32_e32 v20, v20\n"
"v_mfma_f32_16x16x32_bf16 v[168:171], a[168:171], v[36:39], 0\n"
"ds_read_b64_tr_b16 a[220:221], v11 offset:9728\n"
"v_mul_f32_e32 v22, s44, v22\n"
"v_mfma_f32_16x16x32_bf16 v[172:175], a[172:175], v[36:39], 0\n"
"v_exp_f32_e32 v21, v21\n"
"v_mfma_f32_16x16x32_bf16 v[176:179], a[176:179], v[36:39], 0\n"
"v_mul_f32_e32 v23, s44, v23\n"
"v_cvt_pk_bf16_f32 v34, v20, v21\n"
"v_mfma_f32_16x16x32_bf16 v[180:183], a[180:183], v[36:39], 0\n"
"v_exp_f32_e32 v22, v22\n"
"v_mfma_f32_16x16x32_bf16 v[184:187], a[184:187], v[36:39], 0\n"
"v_mfma_f32_16x16x32_bf16 v[188:191], a[188:191], v[36:39], 0\n"
"v_exp_f32_e32 v23, v23\n"
"v_mfma_f32_16x16x32_bf16 a[240:243], a[224:227], v[36:39], 0\n"
"v_cvt_pk_bf16_f32 v35, v22, v23\n"
"v_mfma_f32_16x16x32_bf16 v[16:19], a[128:131], a[0:3], 0\n"
"v_mul_f32_e32 v24, s44, v24\n"
"v_mul_f32_e32 v25, s44, v25\n"
"v_mfma_f32_16x16x32_bf16 v[16:19], a[132:135], a[4:7], v[16:19]\n"
"s_waitcnt lgkmcnt(15)\n"
"v_exp_f32_e32 v24, v24\n"
"v_mfma_f32_16x16x32_bf16 v[16:19], a[136:139], a[8:11], v[16:19]\n"
"v_mul_f32_e32 v26, s44, v26\n"
"v_mfma_f32_16x16x32_bf16 v[16:19], a[140:143], a[12:15], v[16:19]\n"
"v_exp_f32_e32 v25, v25\n"
"v_mfma_f32_16x16x32_bf16 v[20:23], a[144:147], a[0:3], 0\n"
"v_mul_f32_e32 v27, s44, v27\n"
"v_cvt_pk_bf16_f32 v36, v24, v25\n"
"v_mfma_f32_16x16x32_bf16 v[20:23], a[148:151], a[4:7], v[20:23]\n"
"s_waitcnt vmcnt(8) lgkmcnt(0)\n"
"s_barrier\n"
"v_exp_f32_e32 v26, v26\n"
"v_mfma_f32_16x16x32_bf16 v[20:23], a[152:155], a[8:11], v[20:23]\n"
"ds_read_b128 a[96:99], v1 offset:16384\n"
"ds_read_b128 a[108:111], v1 offset:17920\n"
"v_mul_f32_e32 v28, s44, v28\n"
"v_mfma_f32_16x16x32_bf16 v[20:23], a[156:159], a[12:15], v[20:23]\n"
"ds_read_b128 v[40:43], v58 offset:0\n"
"v_exp_f32_e32 v27, v27\n"
"v_mfma_f32_16x16x32_bf16 v[192:195], a[160:163], v[32:35], 0\n"
"ds_read_b128 v[44:47], v59 offset:1024\n"
"v_mul_f32_e32 v29, s44, v29\n"
"v_cvt_pk_bf16_f32 v37, v26, v27\n"
"v_mfma_f32_16x16x32_bf16 v[196:199], a[164:167], v[32:35], 0\n"
"ds_read_b128 v[48:51], v58 offset:2048\n"
"v_exp_f32_e32 v28, v28\n"
"v_mfma_f32_16x16x32_bf16 v[200:203], a[168:171], v[32:35], 0\n"
"ds_read_b128 a[100:103], v1 offset:16896\n"
"ds_read_b128 v[52:55], v59 offset:3072\n"
"v_mul_f32_e32 v30, s44, v30\n"
"v_mfma_f32_16x16x32_bf16 v[204:207], a[172:175], v[32:35], 0\n"
"v_exp_f32_e32 v29, v29\n"
"v_mfma_f32_16x16x32_bf16 v[208:211], a[176:179], v[32:35], 0\n"
"s_add_u32 m0, s48, 8192\n"
"v_mul_f32_e32 v31, s44, v31\n"
"v_cvt_pk_bf16_f32 v38, v28, v29\n"
"v_mfma_f32_16x16x32_bf16 v[212:215], a[180:183], v[32:35], 0\n"
"s_add_u32 s47, s45, 40960\n"
"v_exp_f32_e32 v30, v30\n"
"v_mfma_f32_16x16x32_bf16 v[216:219], a[184:187], v[32:35], 0\n"
"buffer_load_dwordx4 v6, s[24:27], s47 offen lds\n"
"v_mfma_f32_16x16x32_bf16 v[220:223], a[188:191], v[32:35], 0\n"
"v_exp_f32_e32 v31, v31\n"
"v_mfma_f32_16x16x32_bf16 a[244:247], a[224:227], v[32:35], 0\n"
"ds_read_b128 a[104:107], v1 offset:17408\n"
"v_cvt_pk_bf16_f32 v39, v30, v31\n"
"v_mfma_f32_16x16x32_bf16 v[24:27], a[128:131], a[16:19], 0\n"
"v_mul_f32_e32 v16, s44, v16\n"
"v_mul_f32_e32 v17, s44, v17\n"
"v_mfma_f32_16x16x32_bf16 v[24:27], a[132:135], a[20:23], v[24:27]\n"
"v_exp_f32_e32 v16, v16\n"
"s_add_u32 s77, s60, 0\n"
"v_mfma_f32_16x16x32_bf16 v[24:27], a[136:139], a[24:27], v[24:27]\n"
"ds_read_b128 a[112:115], v2 offset:16384\n"
"v_mul_f32_e32 v18, s44, v18\n"
"s_waitcnt lgkmcnt(6)\n"
"buffer_store_dwordx4 v[40:43], v9, s[40:43], s77 offen offset:0  sc1\n"
"v_mfma_f32_16x16x32_bf16 v[24:27], a[140:143], a[28:31], v[24:27]\n"
"v_exp_f32_e32 v17, v17\n"
"v_mfma_f32_16x16x32_bf16 v[28:31], a[144:147], a[16:19], 0\n"
"v_mul_f32_e32 v19, s44, v19\n"
"v_cvt_pk_bf16_f32 v32, v16, v17\n"
"v_mfma_f32_16x16x32_bf16 v[28:31], a[148:151], a[20:23], v[28:31]\n"
"v_exp_f32_e32 v18, v18\n"
"v_mfma_f32_16x16x32_bf16 v[28:31], a[152:155], a[24:27], v[28:31]\n"
"ds_read_b128 a[116:119], v2 offset:16896\n"
"v_mul_f32_e32 v20, s44, v20\n"
"s_waitcnt lgkmcnt(6)\n"
"buffer_store_dwordx4 v[44:47], v9, s[40:43], s77 offen offset:1024  sc1\n"
"v_mfma_f32_16x16x32_bf16 v[28:31], a[156:159], a[28:31], v[28:31]\n"
"v_exp_f32_e32 v19, v19\n"
"v_mfma_f32_16x16x32_bf16 v[224:227], a[160:163], v[36:39], 0\n"
"v_mul_f32_e32 v21, s44, v21\n"
"v_cvt_pk_bf16_f32 v33, v18, v19\n"
"v_mfma_f32_16x16x32_bf16 v[228:231], a[164:167], v[36:39], 0\n"
"v_exp_f32_e32 v20, v20\n"
"v_mfma_f32_16x16x32_bf16 v[232:235], a[168:171], v[36:39], 0\n"
"ds_read_b128 a[120:123], v2 offset:17408\n"
"v_mul_f32_e32 v22, s44, v22\n"
"s_waitcnt lgkmcnt(6)\n"
"buffer_store_dwordx4 v[48:51], v9, s[40:43], s77 offen offset:2048  sc1\n"
"v_mfma_f32_16x16x32_bf16 v[236:239], a[172:175], v[36:39], 0\n"
"v_exp_f32_e32 v21, v21\n"
"v_mfma_f32_16x16x32_bf16 v[240:243], a[176:179], v[36:39], 0\n"
"s_add_u32 m0, s48, 9216\n"
"v_mul_f32_e32 v23, s44, v23\n"
"v_cvt_pk_bf16_f32 v34, v20, v21\n"
"v_mfma_f32_16x16x32_bf16 v[244:247], a[180:183], v[36:39], 0\n"
"s_add_u32 s47, s45, 41088\n"
"v_exp_f32_e32 v22, v22\n"
"v_mfma_f32_16x16x32_bf16 v[248:251], a[184:187], v[36:39], 0\n"
"buffer_load_dwordx4 v6, s[24:27], s47 offen lds\n"
"v_mfma_f32_16x16x32_bf16 v[252:255], a[188:191], v[36:39], 0\n"
"v_exp_f32_e32 v23, v23\n"
"v_mfma_f32_16x16x32_bf16 a[248:251], a[224:227], v[36:39], 0\n"
"ds_read_b128 a[124:127], v2 offset:17920\n"
"v_cvt_pk_bf16_f32 v35, v22, v23\n"
"s_waitcnt lgkmcnt(5)\n"
"buffer_store_dwordx4 v[52:55], v9, s[40:43], s77 offen offset:3072  sc1\n"
"v_mfma_f32_16x16x32_bf16 v[16:19], a[128:131], a[32:35], 0\n"
"v_mul_f32_e32 v24, s44, v24\n"
"v_mul_f32_e32 v25, s44, v25\n"
"v_mfma_f32_16x16x32_bf16 v[16:19], a[132:135], a[36:39], v[16:19]\n"
"v_exp_f32_e32 v24, v24\n"
"v_mfma_f32_16x16x32_bf16 v[16:19], a[136:139], a[40:43], v[16:19]\n"
"ds_read_b64_tr_b16 a[160:161], v3 offset:16384\n"
"v_mul_f32_e32 v26, s44, v26\n"
"v_mfma_f32_16x16x32_bf16 v[16:19], a[140:143], a[44:47], v[16:19]\n"
"v_exp_f32_e32 v25, v25\n"
"v_mfma_f32_16x16x32_bf16 v[20:23], a[144:147], a[32:35], 0\n"
"v_mul_f32_e32 v27, s44, v27\n"
"v_cvt_pk_bf16_f32 v36, v24, v25\n"
"v_mfma_f32_16x16x32_bf16 v[20:23], a[148:151], a[36:39], v[20:23]\n"
"v_exp_f32_e32 v26, v26\n"
"v_mfma_f32_16x16x32_bf16 v[20:23], a[152:155], a[40:43], v[20:23]\n"
"ds_read_b64_tr_b16 a[162:163], v4 offset:16384\n"
"v_mul_f32_e32 v28, s44, v28\n"
"v_mfma_f32_16x16x32_bf16 v[20:23], a[156:159], a[44:47], v[20:23]\n"
"v_exp_f32_e32 v27, v27\n"
"v_mfma_f32_16x16x32_bf16 v[64:67], a[192:195], v[32:35], v[64:67]\n"
"v_mul_f32_e32 v29, s44, v29\n"
"v_cvt_pk_bf16_f32 v37, v26, v27\n"
"v_mfma_f32_16x16x32_bf16 v[68:71], a[196:199], v[32:35], v[68:71]\n"
"v_exp_f32_e32 v28, v28\n"
"v_mfma_f32_16x16x32_bf16 v[72:75], a[200:203], v[32:35], v[72:75]\n"
"ds_read_b64_tr_b16 a[164:165], v11 offset:16384\n"
"v_mul_f32_e32 v30, s44, v30\n"
"v_mfma_f32_16x16x32_bf16 v[76:79], a[204:207], v[32:35], v[76:79]\n"
"v_exp_f32_e32 v29, v29\n"
"v_mfma_f32_16x16x32_bf16 v[80:83], a[208:211], v[32:35], v[80:83]\n"
"s_add_u32 m0, s48, 40960\n"
"v_mul_f32_e32 v31, s44, v31\n"
"v_cvt_pk_bf16_f32 v38, v28, v29\n"
"v_mfma_f32_16x16x32_bf16 v[84:87], a[212:215], v[32:35], v[84:87]\n"
"s_add_u32 s76, s45, 40960\n"
"v_exp_f32_e32 v30, v30\n"
"v_mfma_f32_16x16x32_bf16 v[88:91], a[216:219], v[32:35], v[88:91]\n"
"buffer_load_dwordx4 v6, s[28:31], s76 offen lds\n"
"v_mfma_f32_16x16x32_bf16 v[92:95], a[220:223], v[32:35], v[92:95]\n"
"v_exp_f32_e32 v31, v31\n"
"v_mfma_f32_16x16x32_bf16 a[228:231], a[224:227], v[32:35], a[228:231]\n"
"ds_read_b64_tr_b16 a[166:167], v14 offset:16384\n"
"v_cvt_pk_bf16_f32 v39, v30, v31\n"
"v_mfma_f32_16x16x32_bf16 v[24:27], a[128:131], a[48:51], 0\n"
"v_mul_f32_e32 v16, s44, v16\n"
"v_mul_f32_e32 v17, s44, v17\n"
"v_mfma_f32_16x16x32_bf16 v[24:27], a[132:135], a[52:55], v[24:27]\n"
"v_exp_f32_e32 v16, v16\n"
"v_mfma_f32_16x16x32_bf16 v[24:27], a[136:139], a[56:59], v[24:27]\n"
"ds_read_b64_tr_b16 a[168:169], v3 offset:16896\n"
"v_mul_f32_e32 v18, s44, v18\n"
"v_mfma_f32_16x16x32_bf16 v[24:27], a[140:143], a[60:63], v[24:27]\n"
"v_exp_f32_e32 v17, v17\n"
"v_mfma_f32_16x16x32_bf16 v[28:31], a[144:147], a[48:51], 0\n"
"v_mul_f32_e32 v19, s44, v19\n"
"v_cvt_pk_bf16_f32 v32, v16, v17\n"
"v_mfma_f32_16x16x32_bf16 v[28:31], a[148:151], a[52:55], v[28:31]\n"
"v_exp_f32_e32 v18, v18\n"
"v_mfma_f32_16x16x32_bf16 v[28:31], a[152:155], a[56:59], v[28:31]\n"
"ds_read_b64_tr_b16 a[170:171], v4 offset:16896\n"
"v_mul_f32_e32 v20, s44, v20\n"
"v_mfma_f32_16x16x32_bf16 v[28:31], a[156:159], a[60:63], v[28:31]\n"
"v_exp_f32_e32 v19, v19\n"
"v_mfma_f32_16x16x32_bf16 v[96:99], a[192:195], v[36:39], v[96:99]\n"
"v_mul_f32_e32 v21, s44, v21\n"
"v_cvt_pk_bf16_f32 v33, v18, v19\n"
"v_mfma_f32_16x16x32_bf16 v[100:103], a[196:199], v[36:39], v[100:103]\n"
"v_exp_f32_e32 v20, v20\n"
"v_mfma_f32_16x16x32_bf16 v[104:107], a[200:203], v[36:39], v[104:107]\n"
"ds_read_b64_tr_b16 a[172:173], v11 offset:16896\n"
"v_mul_f32_e32 v22, s44, v22\n"
"v_mfma_f32_16x16x32_bf16 v[108:111], a[204:207], v[36:39], v[108:111]\n"
"v_exp_f32_e32 v21, v21\n"
"v_mfma_f32_16x16x32_bf16 v[112:115], a[208:211], v[36:39], v[112:115]\n"
"s_add_u32 m0, s48, 41984\n"
"v_mul_f32_e32 v23, s44, v23\n"
"v_cvt_pk_bf16_f32 v34, v20, v21\n"
"v_mfma_f32_16x16x32_bf16 v[116:119], a[212:215], v[36:39], v[116:119]\n"
"s_add_u32 s76, s45, 41088\n"
"v_exp_f32_e32 v22, v22\n"
"v_mfma_f32_16x16x32_bf16 v[120:123], a[216:219], v[36:39], v[120:123]\n"
"buffer_load_dwordx4 v6, s[28:31], s76 offen lds\n"
"v_mfma_f32_16x16x32_bf16 v[124:127], a[220:223], v[36:39], v[124:127]\n"
"v_exp_f32_e32 v23, v23\n"
"v_mfma_f32_16x16x32_bf16 a[232:235], a[224:227], v[36:39], a[232:235]\n"
"ds_read_b64_tr_b16 a[174:175], v14 offset:16896\n"
"v_cvt_pk_bf16_f32 v35, v22, v23\n"
"v_mfma_f32_16x16x32_bf16 v[16:19], a[128:131], a[64:67], 0\n"
"ds_read_b64_tr_b16 a[182:183], v14 offset:17408\n"
"v_mul_f32_e32 v24, s44, v24\n"
"v_mul_f32_e32 v25, s44, v25\n"
"v_mfma_f32_16x16x32_bf16 v[16:19], a[132:135], a[68:71], v[16:19]\n"
"v_exp_f32_e32 v24, v24\n"
"v_mfma_f32_16x16x32_bf16 v[16:19], a[136:139], a[72:75], v[16:19]\n"
"ds_read_b64_tr_b16 a[176:177], v3 offset:17408\n"
"v_mul_f32_e32 v26, s44, v26\n"
"v_mfma_f32_16x16x32_bf16 v[16:19], a[140:143], a[76:79], v[16:19]\n"
"v_exp_f32_e32 v25, v25\n"
"v_mfma_f32_16x16x32_bf16 v[20:23], a[144:147], a[64:67], 0\n"
"v_mul_f32_e32 v27, s44, v27\n"
"v_cvt_pk_bf16_f32 v36, v24, v25\n"
"v_mfma_f32_16x16x32_bf16 v[20:23], a[148:151], a[68:71], v[20:23]\n"
"v_exp_f32_e32 v26, v26\n"
"v_mfma_f32_16x16x32_bf16 v[20:23], a[152:155], a[72:75], v[20:23]\n"
"ds_read_b64_tr_b16 a[178:179], v4 offset:17408\n"
"v_mul_f32_e32 v28, s44, v28\n"
"v_mfma_f32_16x16x32_bf16 v[20:23], a[156:159], a[76:79], v[20:23]\n"
"v_exp_f32_e32 v27, v27\n"
"v_mfma_f32_16x16x32_bf16 v[128:131], a[192:195], v[32:35], v[128:131]\n"
"v_mul_f32_e32 v29, s44, v29\n"
"v_cvt_pk_bf16_f32 v37, v26, v27\n"
"v_mfma_f32_16x16x32_bf16 v[132:135], a[196:199], v[32:35], v[132:135]\n"
"v_exp_f32_e32 v28, v28\n"
"v_mfma_f32_16x16x32_bf16 v[136:139], a[200:203], v[32:35], v[136:139]\n"
"ds_read_b64_tr_b16 a[180:181], v11 offset:17408\n"
"v_mul_f32_e32 v30, s44, v30\n"
"v_mfma_f32_16x16x32_bf16 v[140:143], a[204:207], v[32:35], v[140:143]\n"
"v_exp_f32_e32 v29, v29\n"
"v_mfma_f32_16x16x32_bf16 v[144:147], a[208:211], v[32:35], v[144:147]\n"
"v_mul_f32_e32 v31, s44, v31\n"
"v_cvt_pk_bf16_f32 v38, v28, v29\n"
"v_mfma_f32_16x16x32_bf16 v[148:151], a[212:215], v[32:35], v[148:151]\n"
"s_mov_b32 s88, L_L6%=+8192-L_code%=\n"
"v_exp_f32_e32 v30, v30\n"
"v_mfma_f32_16x16x32_bf16 v[152:155], a[216:219], v[32:35], v[152:155]\n"
"s_add_u32 m0, s49, 0\n"
"buffer_load_dword v63, v62, s[84:87], s88 offen\n"
"v_mfma_f32_16x16x32_bf16 v[156:159], a[220:223], v[32:35], v[156:159]\n"
"s_add_u32 s61, s58, 0\n"
"v_exp_f32_e32 v31, v31\n"
"v_mfma_f32_16x16x32_bf16 a[236:239], a[224:227], v[32:35], a[236:239]\n"
"buffer_load_dwordx4 v7, s[36:39], s61 offen  nt lds\n"
"v_cvt_pk_bf16_f32 v39, v30, v31\n"
"v_mfma_f32_16x16x32_bf16 v[24:27], a[128:131], a[80:83], 0\n"
"ds_read_b64_tr_b16 a[190:191], v14 offset:17920\n"
"v_mul_f32_e32 v16, s44, v16\n"
"v_mul_f32_e32 v17, s44, v17\n"
"v_mfma_f32_16x16x32_bf16 v[24:27], a[132:135], a[84:87], v[24:27]\n"
"v_exp_f32_e32 v16, v16\n"
"v_mfma_f32_16x16x32_bf16 v[24:27], a[136:139], a[88:91], v[24:27]\n"
"ds_read_b64_tr_b16 a[184:185], v3 offset:17920\n"
"v_mul_f32_e32 v18, s44, v18\n"
"v_mfma_f32_16x16x32_bf16 v[24:27], a[140:143], a[92:95], v[24:27]\n"
"v_exp_f32_e32 v17, v17\n"
"v_mfma_f32_16x16x32_bf16 v[28:31], a[144:147], a[80:83], 0\n"
"v_mul_f32_e32 v19, s44, v19\n"
"v_cvt_pk_bf16_f32 v32, v16, v17\n"
"v_mfma_f32_16x16x32_bf16 v[28:31], a[148:151], a[84:87], v[28:31]\n"
"v_exp_f32_e32 v18, v18\n"
"v_mfma_f32_16x16x32_bf16 v[28:31], a[152:155], a[88:91], v[28:31]\n"
"ds_read_b64_tr_b16 a[186:187], v4 offset:17920\n"
"v_mul_f32_e32 v20, s44, v20\n"
"v_mfma_f32_16x16x32_bf16 v[28:31], a[156:159], a[92:95], v[28:31]\n"
"v_exp_f32_e32 v19, v19\n"
"v_mfma_f32_16x16x32_bf16 v[160:163], a[192:195], v[36:39], v[160:163]\n"
"v_mul_f32_e32 v21, s44, v21\n"
"v_cvt_pk_bf16_f32 v33, v18, v19\n"
"v_mfma_f32_16x16x32_bf16 v[164:167], a[196:199], v[36:39], v[164:167]\n"
"v_exp_f32_e32 v20, v20\n"
"v_mfma_f32_16x16x32_bf16 v[168:171], a[200:203], v[36:39], v[168:171]\n"
"ds_read_b64_tr_b16 a[188:189], v11 offset:17920\n"
"v_mul_f32_e32 v22, s44, v22\n"
"v_mfma_f32_16x16x32_bf16 v[172:175], a[204:207], v[36:39], v[172:175]\n"
"v_exp_f32_e32 v21, v21\n"
"v_mfma_f32_16x16x32_bf16 v[176:179], a[208:211], v[36:39], v[176:179]\n"
"s_add_u32 m0, s49, 1024\n"
"v_mul_f32_e32 v23, s44, v23\n"
"v_cvt_pk_bf16_f32 v34, v20, v21\n"
"v_mfma_f32_16x16x32_bf16 v[180:183], a[212:215], v[36:39], v[180:183]\n"
"s_add_u32 s61, s58, 128\n"
"v_exp_f32_e32 v22, v22\n"
"v_mfma_f32_16x16x32_bf16 v[184:187], a[216:219], v[36:39], v[184:187]\n"
"buffer_load_dwordx4 v7, s[36:39], s61 offen  nt lds\n"
"v_mfma_f32_16x16x32_bf16 v[188:191], a[220:223], v[36:39], v[188:191]\n"
"v_exp_f32_e32 v23, v23\n"
"v_mfma_f32_16x16x32_bf16 a[240:243], a[224:227], v[36:39], a[240:243]\n"
"v_cvt_pk_bf16_f32 v35, v22, v23\n"
"v_mfma_f32_16x16x32_bf16 v[16:19], a[96:99], a[0:3], 0\n"
"v_mul_f32_e32 v24, s44, v24\n"
"v_mul_f32_e32 v25, s44, v25\n"
"v_mfma_f32_16x16x32_bf16 v[16:19], a[100:103], a[4:7], v[16:19]\n"
"s_waitcnt lgkmcnt(15)\n"
"v_exp_f32_e32 v24, v24\n"
"v_mfma_f32_16x16x32_bf16 v[16:19], a[104:107], a[8:11], v[16:19]\n"
"v_mul_f32_e32 v26, s44, v26\n"
"v_mfma_f32_16x16x32_bf16 v[16:19], a[108:111], a[12:15], v[16:19]\n"
"v_exp_f32_e32 v25, v25\n"
"v_mfma_f32_16x16x32_bf16 v[20:23], a[112:115], a[0:3], 0\n"
"v_mul_f32_e32 v27, s44, v27\n"
"v_cvt_pk_bf16_f32 v36, v24, v25\n"
"v_mfma_f32_16x16x32_bf16 v[20:23], a[116:119], a[4:7], v[20:23]\n"
"s_waitcnt vmcnt(15) lgkmcnt(0)\n"
"s_barrier\n"
"v_exp_f32_e32 v26, v26\n"
"v_mfma_f32_16x16x32_bf16 v[20:23], a[120:123], a[8:11], v[20:23]\n"
"ds_read_b128 a[128:131], v1 offset:24576\n"
"ds_read_b128 a[140:143], v1 offset:26112\n"
"v_mul_f32_e32 v28, s44, v28\n"
"v_mfma_f32_16x16x32_bf16 v[20:23], a[124:127], a[12:15], v[20:23]\n"
"v_exp_f32_e32 v27, v27\n"
"v_mfma_f32_16x16x32_bf16 v[192:195], a[192:195], v[32:35], v[192:195]\n"
"v_mul_f32_e32 v29, s44, v29\n"
"v_cvt_pk_bf16_f32 v37, v26, v27\n"
"v_mfma_f32_16x16x32_bf16 v[196:199], a[196:199], v[32:35], v[196:199]\n"
"v_exp_f32_e32 v28, v28\n"
"v_mfma_f32_16x16x32_bf16 v[200:203], a[200:203], v[32:35], v[200:203]\n"
"ds_read_b128 a[132:135], v1 offset:25088\n"
"v_mul_f32_e32 v30, s44, v30\n"
"v_mfma_f32_16x16x32_bf16 v[204:207], a[204:207], v[32:35], v[204:207]\n"
"v_exp_f32_e32 v29, v29\n"
"v_mfma_f32_16x16x32_bf16 v[208:211], a[208:211], v[32:35], v[208:211]\n"
"s_add_u32 m0, s48, 16384\n"
"v_mul_f32_e32 v31, s44, v31\n"
"v_cvt_pk_bf16_f32 v38, v28, v29\n"
"v_mfma_f32_16x16x32_bf16 v[212:215], a[212:215], v[32:35], v[212:215]\n"
"s_add_u32 s47, s45, 49152\n"
"v_exp_f32_e32 v30, v30\n"
"v_mfma_f32_16x16x32_bf16 v[216:219], a[216:219], v[32:35], v[216:219]\n"
"buffer_load_dwordx4 v6, s[24:27], s47 offen lds\n"
"v_mfma_f32_16x16x32_bf16 v[220:223], a[220:223], v[32:35], v[220:223]\n"
"v_exp_f32_e32 v31, v31\n"
"v_mfma_f32_16x16x32_bf16 a[244:247], a[224:227], v[32:35], a[244:247]\n"
"ds_read_b128 a[136:139], v1 offset:25600\n"
"v_cvt_pk_bf16_f32 v39, v30, v31\n"
"v_mfma_f32_16x16x32_bf16 v[24:27], a[96:99], a[16:19], 0\n"
"v_mul_f32_e32 v16, s44, v16\n"
"v_mul_f32_e32 v17, s44, v17\n"
"v_mfma_f32_16x16x32_bf16 v[24:27], a[100:103], a[20:23], v[24:27]\n"
"v_exp_f32_e32 v16, v16\n"
"v_mfma_f32_16x16x32_bf16 v[24:27], a[104:107], a[24:27], v[24:27]\n"
"ds_read_b128 a[144:147], v2 offset:24576\n"
"v_mul_f32_e32 v18, s44, v18\n"
"v_mfma_f32_16x16x32_bf16 v[24:27], a[108:111], a[28:31], v[24:27]\n"
"v_exp_f32_e32 v17, v17\n"
"v_mfma_f32_16x16x32_bf16 v[28:31], a[112:115], a[16:19], 0\n"
"v_mul_f32_e32 v19, s44, v19\n"
"v_cvt_pk_bf16_f32 v32, v16, v17\n"
"v_mfma_f32_16x16x32_bf16 v[28:31], a[116:119], a[20:23], v[28:31]\n"
"v_exp_f32_e32 v18, v18\n"
"v_mfma_f32_16x16x32_bf16 v[28:31], a[120:123], a[24:27], v[28:31]\n"
"ds_read_b128 a[148:151], v2 offset:25088\n"
"v_mul_f32_e32 v20, s44, v20\n"
"v_mfma_f32_16x16x32_bf16 v[28:31], a[124:127], a[28:31], v[28:31]\n"
"v_exp_f32_e32 v19, v19\n"
"v_mfma_f32_16x16x32_bf16 v[224:227], a[192:195], v[36:39], v[224:227]\n"
"v_mul_f32_e32 v21, s44, v21\n"
"v_cvt_pk_bf16_f32 v33, v18, v19\n"
"v_mfma_f32_16x16x32_bf16 v[228:231], a[196:199], v[36:39], v[228:231]\n"
"v_exp_f32_e32 v20, v20\n"
"v_mfma_f32_16x16x32_bf16 v[232:235], a[200:203], v[36:39], v[232:235]\n"
"ds_read_b128 a[152:155], v2 offset:25600\n"
"v_mul_f32_e32 v22, s44, v22\n"
"v_mfma_f32_16x16x32_bf16 v[236:239], a[204:207], v[36:39], v[236:239]\n"
"v_exp_f32_e32 v21, v21\n"
"v_mfma_f32_16x16x32_bf16 v[240:243], a[208:211], v[36:39], v[240:243]\n"
"s_add_u32 m0, s48, 17408\n"
"v_mul_f32_e32 v23, s44, v23\n"
"v_cvt_pk_bf16_f32 v34, v20, v21\n"
"v_mfma_f32_16x16x32_bf16 v[244:247], a[212:215], v[36:39], v[244:247]\n"
"s_add_u32 s47, s45, 49280\n"
"v_exp_f32_e32 v22, v22\n"
"v_mfma_f32_16x16x32_bf16 v[248:251], a[216:219], v[36:39], v[248:251]\n"
"buffer_load_dwordx4 v6, s[24:27], s47 offen lds\n"
"v_mfma_f32_16x16x32_bf16 v[252:255], a[220:223], v[36:39], v[252:255]\n"
"v_exp_f32_e32 v23, v23\n"
"v_mfma_f32_16x16x32_bf16 a[248:251], a[224:227], v[36:39], a[248:251]\n"
"ds_read_b128 a[156:159], v2 offset:26112\n"
"v_cvt_pk_bf16_f32 v35, v22, v23\n"
"v_mfma_f32_16x16x32_bf16 v[16:19], a[96:99], a[32:35], 0\n"
"v_mul_f32_e32 v24, s44, v24\n"
"v_mul_f32_e32 v25, s44, v25\n"
"v_mfma_f32_16x16x32_bf16 v[16:19], a[100:103], a[36:39], v[16:19]\n"
"v_exp_f32_e32 v24, v24\n"
"v_mfma_f32_16x16x32_bf16 v[16:19], a[104:107], a[40:43], v[16:19]\n"
"ds_read_b64_tr_b16 a[192:193], v3 offset:24576\n"
"v_mul_f32_e32 v26, s44, v26\n"
"v_mfma_f32_16x16x32_bf16 v[16:19], a[108:111], a[44:47], v[16:19]\n"
"v_exp_f32_e32 v25, v25\n"
"v_mfma_f32_16x16x32_bf16 v[20:23], a[112:115], a[32:35], 0\n"
"v_mul_f32_e32 v27, s44, v27\n"
"v_cvt_pk_bf16_f32 v36, v24, v25\n"
"v_mfma_f32_16x16x32_bf16 v[20:23], a[116:119], a[36:39], v[20:23]\n"
"v_exp_f32_e32 v26, v26\n"
"v_mfma_f32_16x16x32_bf16 v[20:23], a[120:123], a[40:43], v[20:23]\n"
"ds_read_b64_tr_b16 a[194:195], v4 offset:24576\n"
"v_mul_f32_e32 v28, s44, v28\n"
"v_mfma_f32_16x16x32_bf16 v[20:23], a[124:127], a[44:47], v[20:23]\n"
"v_exp_f32_e32 v27, v27\n"
"v_mfma_f32_16x16x32_bf16 v[64:67], a[160:163], v[32:35], v[64:67]\n"
"v_mul_f32_e32 v29, s44, v29\n"
"v_cvt_pk_bf16_f32 v37, v26, v27\n"
"v_mfma_f32_16x16x32_bf16 v[68:71], a[164:167], v[32:35], v[68:71]\n"
"v_exp_f32_e32 v28, v28\n"
"v_mfma_f32_16x16x32_bf16 v[72:75], a[168:171], v[32:35], v[72:75]\n"
"ds_read_b64_tr_b16 a[196:197], v11 offset:24576\n"
"v_mul_f32_e32 v30, s44, v30\n"
"v_mfma_f32_16x16x32_bf16 v[76:79], a[172:175], v[32:35], v[76:79]\n"
"v_exp_f32_e32 v29, v29\n"
"v_mfma_f32_16x16x32_bf16 v[80:83], a[176:179], v[32:35], v[80:83]\n"
"s_add_u32 m0, s48, 49152\n"
"v_mul_f32_e32 v31, s44, v31\n"
"v_cvt_pk_bf16_f32 v38, v28, v29\n"
"v_mfma_f32_16x16x32_bf16 v[84:87], a[180:183], v[32:35], v[84:87]\n"
"s_add_u32 s76, s45, 49152\n"
"v_exp_f32_e32 v30, v30\n"
"v_mfma_f32_16x16x32_bf16 v[88:91], a[184:187], v[32:35], v[88:91]\n"
"buffer_load_dwordx4 v6, s[28:31], s76 offen lds\n"
"v_mfma_f32_16x16x32_bf16 v[92:95], a[188:191], v[32:35], v[92:95]\n"
"v_exp_f32_e32 v31, v31\n"
"v_mfma_f32_16x16x32_bf16 a[228:231], a[224:227], v[32:35], a[228:231]\n"
"ds_read_b64_tr_b16 a[198:199], v14 offset:24576\n"
"v_cvt_pk_bf16_f32 v39, v30, v31\n"
"v_mfma_f32_16x16x32_bf16 v[24:27], a[96:99], a[48:51], 0\n"
"v_mul_f32_e32 v16, s44, v16\n"
"v_mul_f32_e32 v17, s44, v17\n"
"v_mfma_f32_16x16x32_bf16 v[24:27], a[100:103], a[52:55], v[24:27]\n"
"v_exp_f32_e32 v16, v16\n"
"v_mfma_f32_16x16x32_bf16 v[24:27], a[104:107], a[56:59], v[24:27]\n"
"ds_read_b64_tr_b16 a[200:201], v3 offset:25088\n"
"v_mul_f32_e32 v18, s44, v18\n"
"v_mfma_f32_16x16x32_bf16 v[24:27], a[108:111], a[60:63], v[24:27]\n"
"v_exp_f32_e32 v17, v17\n"
"v_mfma_f32_16x16x32_bf16 v[28:31], a[112:115], a[48:51], 0\n"
"v_mul_f32_e32 v19, s44, v19\n"
"v_cvt_pk_bf16_f32 v32, v16, v17\n"
"v_mfma_f32_16x16x32_bf16 v[28:31], a[116:119], a[52:55], v[28:31]\n"
"v_exp_f32_e32 v18, v18\n"
"v_mfma_f32_16x16x32_bf16 v[28:31], a[120:123], a[56:59], v[28:31]\n"
"ds_read_b64_tr_b16 a[202:203], v4 offset:25088\n"
"v_mul_f32_e32 v20, s44, v20\n"
"v_mfma_f32_16x16x32_bf16 v[28:31], a[124:127], a[60:63], v[28:31]\n"
"v_exp_f32_e32 v19, v19\n"
"v_mfma_f32_16x16x32_bf16 v[96:99], a[160:163], v[36:39], v[96:99]\n"
"v_mul_f32_e32 v21, s44, v21\n"
"v_cvt_pk_bf16_f32 v33, v18, v19\n"
"v_mfma_f32_16x16x32_bf16 v[100:103], a[164:167], v[36:39], v[100:103]\n"
"v_exp_f32_e32 v20, v20\n"
"v_mfma_f32_16x16x32_bf16 v[104:107], a[168:171], v[36:39], v[104:107]\n"
"ds_read_b64_tr_b16 a[204:205], v11 offset:25088\n"
"v_mul_f32_e32 v22, s44, v22\n"
"v_mfma_f32_16x16x32_bf16 v[108:111], a[172:175], v[36:39], v[108:111]\n"
"v_exp_f32_e32 v21, v21\n"
"v_mfma_f32_16x16x32_bf16 v[112:115], a[176:179], v[36:39], v[112:115]\n"
"s_add_u32 m0, s48, 50176\n"
"v_mul_f32_e32 v23, s44, v23\n"
"v_cvt_pk_bf16_f32 v34, v20, v21\n"
"v_mfma_f32_16x16x32_bf16 v[116:119], a[180:183], v[36:39], v[116:119]\n"
"s_add_u32 s76, s45, 49280\n"
"v_exp_f32_e32 v22, v22\n"
"v_mfma_f32_16x16x32_bf16 v[120:123], a[184:187], v[36:39], v[120:123]\n"
"buffer_load_dwordx4 v6, s[28:31], s76 offen lds\n"
"v_mfma_f32_16x16x32_bf16 v[124:127], a[188:191], v[36:39], v[124:127]\n"
"v_exp_f32_e32 v23, v23\n"
"v_mfma_f32_16x16x32_bf16 a[232:235], a[224:227], v[36:39], a[232:235]\n"
"ds_read_b64_tr_b16 a[206:207], v14 offset:25088\n"
"v_cvt_pk_bf16_f32 v35, v22, v23\n"
"v_mfma_f32_16x16x32_bf16 v[16:19], a[96:99], a[64:67], 0\n"
"ds_read_b64_tr_b16 a[214:215], v14 offset:25600\n"
"v_mul_f32_e32 v24, s44, v24\n"
"v_mul_f32_e32 v25, s44, v25\n"
"v_mfma_f32_16x16x32_bf16 v[16:19], a[100:103], a[68:71], v[16:19]\n"
"v_exp_f32_e32 v24, v24\n"
"v_mfma_f32_16x16x32_bf16 v[16:19], a[104:107], a[72:75], v[16:19]\n"
"ds_read_b64_tr_b16 a[208:209], v3 offset:25600\n"
"v_mul_f32_e32 v26, s44, v26\n"
"v_mfma_f32_16x16x32_bf16 v[16:19], a[108:111], a[76:79], v[16:19]\n"
"v_exp_f32_e32 v25, v25\n"
"v_mfma_f32_16x16x32_bf16 v[20:23], a[112:115], a[64:67], 0\n"
"v_mul_f32_e32 v27, s44, v27\n"
"v_cvt_pk_bf16_f32 v36, v24, v25\n"
"v_mfma_f32_16x16x32_bf16 v[20:23], a[116:119], a[68:71], v[20:23]\n"
"v_exp_f32_e32 v26, v26\n"
"v_mfma_f32_16x16x32_bf16 v[20:23], a[120:123], a[72:75], v[20:23]\n"
"ds_read_b64_tr_b16 a[210:211], v4 offset:25600\n"
"v_mul_f32_e32 v28, s44, v28\n"
"v_mfma_f32_16x16x32_bf16 v[20:23], a[124:127], a[76:79], v[20:23]\n"
"v_exp_f32_e32 v27, v27\n"
"v_mfma_f32_16x16x32_bf16 v[128:131], a[160:163], v[32:35], v[128:131]\n"
"v_mul_f32_e32 v29, s44, v29\n"
"v_cvt_pk_bf16_f32 v37, v26, v27\n"
"v_mfma_f32_16x16x32_bf16 v[132:135], a[164:167], v[32:35], v[132:135]\n"
"v_exp_f32_e32 v28, v28\n"
"v_mfma_f32_16x16x32_bf16 v[136:139], a[168:171], v[32:35], v[136:139]\n"
"ds_read_b64_tr_b16 a[212:213], v11 offset:25600\n"
"v_mul_f32_e32 v30, s44, v30\n"
"v_mfma_f32_16x16x32_bf16 v[140:143], a[172:175], v[32:35], v[140:143]\n"
"v_exp_f32_e32 v29, v29\n"
"v_mfma_f32_16x16x32_bf16 v[144:147], a[176:179], v[32:35], v[144:147]\n"
"v_mul_f32_e32 v31, s44, v31\n"
"v_cvt_pk_bf16_f32 v38, v28, v29\n"
"v_mfma_f32_16x16x32_bf16 v[148:151], a[180:183], v[32:35], v[148:151]\n"
"s_mov_b32 s88, L_L6%=+16384-L_code%=\n"
"v_exp_f32_e32 v30, v30\n"
"v_mfma_f32_16x16x32_bf16 v[152:155], a[184:187], v[32:35], v[152:155]\n"
"s_add_u32 m0, s49, 2048\n"
"buffer_load_dword v63, v62, s[84:87], s88 offen\n"
"v_mfma_f32_16x16x32_bf16 v[156:159], a[188:191], v[32:35], v[156:159]\n"
"s_add_u32 s61, s58, 0\n"
"v_exp_f32_e32 v31, v31\n"
"v_mfma_f32_16x16x32_bf16 a[236:239], a[224:227], v[32:35], a[236:239]\n"
"buffer_load_dwordx4 v8, s[36:39], s61 offen  nt lds\n"
"v_cvt_pk_bf16_f32 v39, v30, v31\n"
"v_mfma_f32_16x16x32_bf16 v[24:27], a[96:99], a[80:83], 0\n"
"ds_read_b64_tr_b16 a[222:223], v14 offset:26112\n"
"v_mul_f32_e32 v16, s44, v16\n"
"v_mul_f32_e32 v17, s44, v17\n"
"v_mfma_f32_16x16x32_bf16 v[24:27], a[100:103], a[84:87], v[24:27]\n"
"v_exp_f32_e32 v16, v16\n"
"v_mfma_f32_16x16x32_bf16 v[24:27], a[104:107], a[88:91], v[24:27]\n"
"ds_read_b64_tr_b16 a[216:217], v3 offset:26112\n"
"v_mul_f32_e32 v18, s44, v18\n"
"v_mfma_f32_16x16x32_bf16 v[24:27], a[108:111], a[92:95], v[24:27]\n"
"v_exp_f32_e32 v17, v17\n"
"v_mfma_f32_16x16x32_bf16 v[28:31], a[112:115], a[80:83], 0\n"
"v_mul_f32_e32 v19, s44, v19\n"
"v_cvt_pk_bf16_f32 v32, v16, v17\n"
"v_mfma_f32_16x16x32_bf16 v[28:31], a[116:119], a[84:87], v[28:31]\n"
"v_exp_f32_e32 v18, v18\n"
"v_mfma_f32_16x16x32_bf16 v[28:31], a[120:123], a[88:91], v[28:31]\n"
"ds_read_b64_tr_b16 a[218:219], v4 offset:26112\n"
"v_mul_f32_e32 v20, s44, v20\n"
"v_mfma_f32_16x16x32_bf16 v[28:31], a[124:127], a[92:95], v[28:31]\n"
"v_exp_f32_e32 v19, v19\n"
"v_mfma_f32_16x16x32_bf16 v[160:163], a[160:163], v[36:39], v[160:163]\n"
"v_mul_f32_e32 v21, s44, v21\n"
"v_cvt_pk_bf16_f32 v33, v18, v19\n"
"v_mfma_f32_16x16x32_bf16 v[164:167], a[164:167], v[36:39], v[164:167]\n"
"v_exp_f32_e32 v20, v20\n"
"v_mfma_f32_16x16x32_bf16 v[168:171], a[168:171], v[36:39], v[168:171]\n"
"ds_read_b64_tr_b16 a[220:221], v11 offset:26112\n"
"v_mul_f32_e32 v22, s44, v22\n"
"v_mfma_f32_16x16x32_bf16 v[172:175], a[172:175], v[36:39], v[172:175]\n"
"v_exp_f32_e32 v21, v21\n"
"v_mfma_f32_16x16x32_bf16 v[176:179], a[176:179], v[36:39], v[176:179]\n"
"s_add_u32 m0, s49, 3072\n"
"v_mul_f32_e32 v23, s44, v23\n"
"v_cvt_pk_bf16_f32 v34, v20, v21\n"
"v_mfma_f32_16x16x32_bf16 v[180:183], a[180:183], v[36:39], v[180:183]\n"
"s_add_u32 s61, s58, 128\n"
"v_exp_f32_e32 v22, v22\n"
"v_mfma_f32_16x16x32_bf16 v[184:187], a[184:187], v[36:39], v[184:187]\n"
"buffer_load_dwordx4 v8, s[36:39], s61 offen  nt lds\n"
"v_mfma_f32_16x16x32_bf16 v[188:191], a[188:191], v[36:39], v[188:191]\n"
"v_exp_f32_e32 v23, v23\n"
"v_mfma_f32_16x16x32_bf16 a[240:243], a[224:227], v[36:39], a[240:243]\n"
"v_cvt_pk_bf16_f32 v35, v22, v23\n"
"s_waitcnt lgkmcnt(15)\n"
"s_branch L_L6%=\n"
"L_H5%=:\n"
"v_mfma_f32_16x16x32_bf16 v[16:19], a[96:99], a[0:3], 0\n"
"s_waitcnt lgkmcnt(2)\n"
"v_mfma_f32_16x16x32_bf16 v[16:19], a[100:103], a[4:7], v[16:19]\n"
"s_waitcnt lgkmcnt(1)\n"
"v_mfma_f32_16x16x32_bf16 v[16:19], a[104:107], a[8:11], v[16:19]\n"
"s_waitcnt lgkmcnt(0)\n"
"v_mfma_f32_16x16x32_bf16 v[16:19], a[108:111], a[12:15], v[16:19]\n"
"v_mfma_f32_16x16x32_bf16 v[20:23], a[112:115], a[0:3], 0\n"
"v_mfma_f32_16x16x32_bf16 v[20:23], a[116:119], a[4:7], v[20:23]\n"
"v_mfma_f32_16x16x32_bf16 v[20:23], a[120:123], a[8:11], v[20:23]\n"
"s_waitcnt vmcnt(10)\n"
"s_barrier\n"
"v_mfma_f32_16x16x32_bf16 v[20:23], a[124:127], a[12:15], v[20:23]\n"
"ds_read_b128 a[128:131], v1 offset:8192\n"
"s_add_u32 m0, s49, 8192\n"
"s_add_u32 s61, s58, 8192\n"
"buffer_load_dwordx4 v7, s[36:39], s61 offen lds\n"
"s_add_u32 m0, s49, 9216\n"
"s_add_u32 s61, s58, 8320\n"
"buffer_load_dwordx4 v7, s[36:39], s61 offen lds\n"
"s_add_u32 m0, s49, 10240\n"
"s_add_u32 s61, s58, 8192\n"
"buffer_load_dwordx4 v8, s[36:39], s61 offen lds\n"
"s_add_u32 m0, s49, 11264\n"
"s_add_u32 s61, s58, 8320\n"
"buffer_load_dwordx4 v8, s[36:39], s61 offen lds\n"
"ds_read_b128 a[132:135], v1 offset:8704\n"
"ds_read_b128 a[136:139], v1 offset:9216\n"
"ds_read_b128 a[140:143], v1 offset:9728\n"
"ds_read_b128 a[144:147], v2 offset:8192\n"
"ds_read_b128 a[148:151], v2 offset:8704\n"
"ds_read_b128 a[152:155], v2 offset:9216\n"
"ds_read_b128 a[156:159], v2 offset:9728\n"
"s_waitcnt lgkmcnt(7)\n"
"v_mfma_f32_16x16x32_bf16 v[24:27], a[128:131], a[0:3], 0\n"
"s_waitcnt vmcnt(10) lgkmcnt(6)\n"
"s_barrier\n"
"s_waitcnt vmcnt(6)\n"
"ds_read_b128 a[16:19], v5 offset:4096\n"
"v_mul_f32_e32 v16, s44, v16\n"
"v_mul_f32_e32 v17, s44, v17\n"
"v_mfma_f32_16x16x32_bf16 v[24:27], a[132:135], a[4:7], v[24:27]\n"
"s_waitcnt lgkmcnt(6)\n"
"ds_read_b64_tr_b16 a[172:173], v11 offset:512\n"
"v_exp_f32_e32 v16, v16\n"
"v_mfma_f32_16x16x32_bf16 v[24:27], a[136:139], a[8:11], v[24:27]\n"
"s_waitcnt lgkmcnt(6)\n"
"ds_read_b64_tr_b16 a[162:163], v4 offset:0\n"
"ds_read_b64_tr_b16 a[174:175], v14 offset:512\n"
"v_mul_f32_e32 v18, s44, v18\n"
"v_mfma_f32_16x16x32_bf16 v[24:27], a[140:143], a[12:15], v[24:27]\n"
"s_waitcnt lgkmcnt(7)\n"
"ds_read_b64_tr_b16 a[176:177], v3 offset:1024\n"
"v_exp_f32_e32 v17, v17\n"
"v_mfma_f32_16x16x32_bf16 v[28:31], a[144:147], a[0:3], 0\n"
"s_waitcnt lgkmcnt(7)\n"
"ds_read_b64_tr_b16 a[178:179], v4 offset:1024\n"
"v_mul_f32_e32 v19, s44, v19\n"
"v_cvt_pk_bf16_f32 v32, v16, v17\n"
"v_mfma_f32_16x16x32_bf16 v[28:31], a[148:151], a[4:7], v[28:31]\n"
"s_waitcnt lgkmcnt(7)\n"
"ds_read_b64_tr_b16 a[180:181], v11 offset:1024\n"
"v_exp_f32_e32 v18, v18\n"
"v_mfma_f32_16x16x32_bf16 v[28:31], a[152:155], a[8:11], v[28:31]\n"
"s_waitcnt lgkmcnt(7)\n"
"ds_read_b64_tr_b16 a[164:165], v11 offset:0\n"
"ds_read_b64_tr_b16 a[182:183], v14 offset:1024\n"
"v_mul_f32_e32 v20, s44, v20\n"
"v_mfma_f32_16x16x32_bf16 v[28:31], a[156:159], a[12:15], v[28:31]\n"
"s_waitcnt lgkmcnt(8)\n"
"ds_read_b64_tr_b16 a[184:185], v3 offset:1536\n"
"v_exp_f32_e32 v19, v19\n"
"s_add_u32 m0, s49, 12288\n"
"s_add_u32 s61, s58, 12288\n"
"buffer_load_dwordx4 v7, s[36:39], s61 offen lds\n"
"s_add_u32 m0, s49, 13312\n"
"s_add_u32 s61, s58, 12416\n"
"buffer_load_dwordx4 v7, s[36:39], s61 offen lds\n"
"s_add_u32 m0, s49, 14336\n"
"s_add_u32 s61, s58, 12288\n"
"buffer_load_dwordx4 v8, s[36:39], s61 offen lds\n"
"s_add_u32 m0, s49, 15360\n"
"s_add_u32 s61, s58, 12416\n"
"buffer_load_dwordx4 v8, s[36:39], s61 offen lds\n"
"ds_read_b64_tr_b16 a[186:187], v4 offset:1536\n"
"v_mul_f32_e32 v21, s44, v21\n"
"v_cvt_pk_bf16_f32 v33, v18, v19\n"
"ds_read_b64_tr_b16 a[188:189], v11 offset:1536\n"
"v_exp_f32_e32 v20, v20\n"
"ds_read_b64_tr_b16 a[166:167], v14 offset:0\n"
"ds_read_b64_tr_b16 a[190:191], v14 offset:1536\n"
"v_mul_f32_e32 v22, s44, v22\n"
"ds_read_b128 a[20:23], v5 offset:4608\n"
"v_exp_f32_e32 v21, v21\n"
"ds_read_b128 a[24:27], v5 offset:5120\n"
"v_mul_f32_e32 v23, s44, v23\n"
"v_cvt_pk_bf16_f32 v34, v20, v21\n"
"ds_read_b128 a[28:31], v5 offset:5632\n"
"v_exp_f32_e32 v22, v22\n"
"ds_read_b64_tr_b16 a[160:161], v3 offset:0\n"
"ds_read_b64_tr_b16 a[168:169], v3 offset:512\n"
"v_exp_f32_e32 v23, v23\n"
"ds_read_b64_tr_b16 a[170:171], v4 offset:512\n"
"v_cvt_pk_bf16_f32 v35, v22, v23\n"
"v_mfma_f32_16x16x32_bf16 v[16:19], a[96:99], a[16:19], 0\n"
"s_waitcnt lgkmcnt(5)\n"
"ds_read_b64_tr_b16 a[200:201], v3 offset:8704\n"
"v_mul_f32_e32 v24, s44, v24\n"
"v_mul_f32_e32 v25, s44, v25\n"
"v_mfma_f32_16x16x32_bf16 v[16:19], a[100:103], a[20:23], v[16:19]\n"
"s_waitcnt lgkmcnt(5)\n"
"ds_read_b64_tr_b16 a[202:203], v4 offset:8704\n"
"v_exp_f32_e32 v24, v24\n"
"v_mfma_f32_16x16x32_bf16 v[16:19], a[104:107], a[24:27], v[16:19]\n"
"s_waitcnt lgkmcnt(5)\n"
"ds_read_b64_tr_b16 a[192:193], v3 offset:8192\n"
"ds_read_b64_tr_b16 a[204:205], v11 offset:8704\n"
"v_mul_f32_e32 v26, s44, v26\n"
"v_mfma_f32_16x16x32_bf16 v[16:19], a[108:111], a[28:31], v[16:19]\n"
"ds_read_b64_tr_b16 a[206:207], v14 offset:8704\n"
"v_exp_f32_e32 v25, v25\n"
"v_mfma_f32_16x16x32_bf16 v[20:23], a[112:115], a[16:19], 0\n"
"ds_read_b64_tr_b16 a[208:209], v3 offset:9216\n"
"v_mul_f32_e32 v27, s44, v27\n"
"v_cvt_pk_bf16_f32 v36, v24, v25\n"
"v_mfma_f32_16x16x32_bf16 v[20:23], a[116:119], a[20:23], v[20:23]\n"
"ds_read_b64_tr_b16 a[210:211], v4 offset:9216\n"
"v_exp_f32_e32 v26, v26\n"
"v_mfma_f32_16x16x32_bf16 v[20:23], a[120:123], a[24:27], v[20:23]\n"
"ds_read_b64_tr_b16 a[194:195], v4 offset:8192\n"
"ds_read_b64_tr_b16 a[212:213], v11 offset:9216\n"
"v_mul_f32_e32 v28, s44, v28\n"
"v_mfma_f32_16x16x32_bf16 v[20:23], a[124:127], a[28:31], v[20:23]\n"
"s_waitcnt lgkmcnt(11)\n"
"ds_read_b64_tr_b16 a[214:215], v14 offset:9216\n"
"v_exp_f32_e32 v27, v27\n"
"s_add_u32 m0, s49, 16384\n"
"s_add_u32 s61, s58, 16384\n"
"buffer_load_dwordx4 v7, s[36:39], s61 offen lds\n"
"s_add_u32 m0, s49, 17408\n"
"s_add_u32 s61, s58, 16512\n"
"buffer_load_dwordx4 v7, s[36:39], s61 offen lds\n"
"s_add_u32 m0, s49, 18432\n"
"s_add_u32 s61, s58, 16384\n"
"buffer_load_dwordx4 v8, s[36:39], s61 offen lds\n"
"s_add_u32 m0, s49, 19456\n"
"s_add_u32 s61, s58, 16512\n"
"buffer_load_dwordx4 v8, s[36:39], s61 offen lds\n"
"v_mfma_f32_16x16x32_bf16 v[64:67], a[160:163], v[32:35], 0\n"
"ds_read_b64_tr_b16 a[216:217], v3 offset:9728\n"
"v_mul_f32_e32 v29, s44, v29\n"
"v_cvt_pk_bf16_f32 v37, v26, v27\n"
"v_mfma_f32_16x16x32_bf16 v[68:71], a[164:167], v[32:35], 0\n"
"s_waitcnt lgkmcnt(11)\n"
"ds_read_b64_tr_b16 a[218:219], v4 offset:9728\n"
"v_exp_f32_e32 v28, v28\n"
"v_mfma_f32_16x16x32_bf16 v[72:75], a[168:171], v[32:35], 0\n"
"ds_read_b64_tr_b16 a[196:197], v11 offset:8192\n"
"ds_read_b64_tr_b16 a[220:221], v11 offset:9728\n"
"v_mul_f32_e32 v30, s44, v30\n"
"v_mfma_f32_16x16x32_bf16 v[76:79], a[172:175], v[32:35], 0\n"
"ds_read_b64_tr_b16 a[222:223], v14 offset:9728\n"
"v_exp_f32_e32 v29, v29\n"
"v_mfma_f32_16x16x32_bf16 v[80:83], a[176:179], v[32:35], 0\n"
"v_mul_f32_e32 v31, s44, v31\n"
"v_cvt_pk_bf16_f32 v38, v28, v29\n"
"v_mfma_f32_16x16x32_bf16 v[84:87], a[180:183], v[32:35], 0\n"
"s_mov_b32 s88, L_L5%=+0-L_code%=\n"
"v_exp_f32_e32 v30, v30\n"
"v_mfma_f32_16x16x32_bf16 v[88:91], a[184:187], v[32:35], 0\n"
"buffer_load_dword v63, v62, s[84:87], s88 offen\n"
"v_mfma_f32_16x16x32_bf16 v[92:95], a[188:191], v[32:35], 0\n"
"v_exp_f32_e32 v31, v31\n"
"v_mfma_f32_16x16x32_bf16 a[228:231], a[224:227], v[32:35], 0\n"
"ds_read_b64_tr_b16 a[198:199], v14 offset:8192\n"
"v_cvt_pk_bf16_f32 v39, v30, v31\n"
"v_mfma_f32_16x16x32_bf16 v[24:27], a[128:131], a[16:19], 0\n"
"v_mul_f32_e32 v16, s44, v16\n"
"v_mul_f32_e32 v17, s44, v17\n"
"v_mfma_f32_16x16x32_bf16 v[24:27], a[132:135], a[20:23], v[24:27]\n"
"v_exp_f32_e32 v16, v16\n"
"v_mfma_f32_16x16x32_bf16 v[24:27], a[136:139], a[24:27], v[24:27]\n"
"s_waitcnt vmcnt(9)\n"
"ds_read_b128 a[36:39], v5 offset:8704\n"
"v_mul_f32_e32 v18, s44, v18\n"
"v_mfma_f32_16x16x32_bf16 v[24:27], a[140:143], a[28:31], v[24:27]\n"
"v_exp_f32_e32 v17, v17\n"
"v_mfma_f32_16x16x32_bf16 v[28:31], a[144:147], a[16:19], 0\n"
"v_mul_f32_e32 v19, s44, v19\n"
"v_cvt_pk_bf16_f32 v32, v16, v17\n"
"v_mfma_f32_16x16x32_bf16 v[28:31], a[148:151], a[20:23], v[28:31]\n"
"v_exp_f32_e32 v18, v18\n"
"v_mfma_f32_16x16x32_bf16 v[28:31], a[152:155], a[24:27], v[28:31]\n"
"ds_read_b128 a[40:43], v5 offset:9216\n"
"v_mul_f32_e32 v20, s44, v20\n"
"v_mfma_f32_16x16x32_bf16 v[28:31], a[156:159], a[28:31], v[28:31]\n"
"s_waitcnt lgkmcnt(10)\n"
"v_exp_f32_e32 v19, v19\n"
"s_add_u32 m0, s48, 16384\n"
"s_add_u32 s47, s45, 16384\n"
"buffer_load_dwordx4 v6, s[24:27], s47 offen lds\n"
"s_add_u32 m0, s48, 17408\n"
"s_add_u32 s47, s45, 16512\n"
"buffer_load_dwordx4 v6, s[24:27], s47 offen lds\n"
"v_mfma_f32_16x16x32_bf16 v[64:67], a[192:195], v[36:39], v[64:67]\n"
"s_waitcnt lgkmcnt(2)\n"
"v_mul_f32_e32 v21, s44, v21\n"
"v_cvt_pk_bf16_f32 v33, v18, v19\n"
"v_mfma_f32_16x16x32_bf16 v[68:71], a[196:199], v[36:39], v[68:71]\n"
"v_exp_f32_e32 v20, v20\n"
"v_mfma_f32_16x16x32_bf16 v[72:75], a[200:203], v[36:39], v[72:75]\n"
"ds_read_b128 a[44:47], v5 offset:9728\n"
"v_mul_f32_e32 v22, s44, v22\n"
"v_mfma_f32_16x16x32_bf16 v[76:79], a[204:207], v[36:39], v[76:79]\n"
"v_exp_f32_e32 v21, v21\n"
"v_mfma_f32_16x16x32_bf16 v[80:83], a[208:211], v[36:39], v[80:83]\n"
"v_mul_f32_e32 v23, s44, v23\n"
"v_cvt_pk_bf16_f32 v34, v20, v21\n"
"v_mfma_f32_16x16x32_bf16 v[84:87], a[212:215], v[36:39], v[84:87]\n"
"v_exp_f32_e32 v22, v22\n"
"v_mfma_f32_16x16x32_bf16 v[88:91], a[216:219], v[36:39], v[88:91]\n"
"ds_read_b128 a[32:35], v5 offset:8192\n"
"v_mfma_f32_16x16x32_bf16 v[92:95], a[220:223], v[36:39], v[92:95]\n"
"v_exp_f32_e32 v23, v23\n"
"v_mfma_f32_16x16x32_bf16 a[228:231], a[224:227], v[36:39], a[228:231]\n"
"s_waitcnt lgkmcnt(0)\n"
"v_cvt_pk_bf16_f32 v35, v22, v23\n"
"v_mfma_f32_16x16x32_bf16 v[16:19], a[96:99], a[32:35], 0\n"
"v_mul_f32_e32 v24, s44, v24\n"
"v_mul_f32_e32 v25, s44, v25\n"
"v_mfma_f32_16x16x32_bf16 v[16:19], a[100:103], a[36:39], v[16:19]\n"
"s_barrier\n"
"v_exp_f32_e32 v24, v24\n"
"v_mfma_f32_16x16x32_bf16 v[16:19], a[104:107], a[40:43], v[16:19]\n"
"v_mul_f32_e32 v26, s44, v26\n"
"v_mfma_f32_16x16x32_bf16 v[16:19], a[108:111], a[44:47], v[16:19]\n"
"v_exp_f32_e32 v25, v25\n"
"v_mfma_f32_16x16x32_bf16 v[20:23], a[112:115], a[32:35], 0\n"
"v_mul_f32_e32 v27, s44, v27\n"
"v_cvt_pk_bf16_f32 v36, v24, v25\n"
"v_mfma_f32_16x16x32_bf16 v[20:23], a[116:119], a[36:39], v[20:23]\n"
"v_exp_f32_e32 v26, v26\n"
"v_mfma_f32_16x16x32_bf16 v[20:23], a[120:123], a[40:43], v[20:23]\n"
"v_mul_f32_e32 v28, s44, v28\n"
"v_mfma_f32_16x16x32_bf16 v[20:23], a[124:127], a[44:47], v[20:23]\n"
"v_exp_f32_e32 v27, v27\n"
"s_add_u32 m0, s48, 49152\n"
"s_add_u32 s76, s45, 16384\n"
"buffer_load_dwordx4 v6, s[28:31], s76 offen lds\n"
"s_add_u32 m0, s48, 50176\n"
"s_add_u32 s76, s45, 16512\n"
"buffer_load_dwordx4 v6, s[28:31], s76 offen lds\n"
"v_mfma_f32_16x16x32_bf16 v[96:99], a[160:163], v[32:35], 0\n"
"v_mul_f32_e32 v29, s44, v29\n"
"v_cvt_pk_bf16_f32 v37, v26, v27\n"
"v_mfma_f32_16x16x32_bf16 v[100:103], a[164:167], v[32:35], 0\n"
"v_exp_f32_e32 v28, v28\n"
"v_mfma_f32_16x16x32_bf16 v[104:107], a[168:171], v[32:35], 0\n"
"v_mul_f32_e32 v30, s44, v30\n"
"v_mfma_f32_16x16x32_bf16 v[108:111], a[172:175], v[32:35], 0\n"
"v_exp_f32_e32 v29, v29\n"
"v_mfma_f32_16x16x32_bf16 v[112:115], a[176:179], v[32:35], 0\n"
"v_mul_f32_e32 v31, s44, v31\n"
"v_cvt_pk_bf16_f32 v38, v28, v29\n"
"v_mfma_f32_16x16x32_bf16 v[116:119], a[180:183], v[32:35], 0\n"
"v_exp_f32_e32 v30, v30\n"
"v_mfma_f32_16x16x32_bf16 v[120:123], a[184:187], v[32:35], 0\n"
"v_mfma_f32_16x16x32_bf16 v[124:127], a[188:191], v[32:35], 0\n"
"v_exp_f32_e32 v31, v31\n"
"v_mfma_f32_16x16x32_bf16 a[232:235], a[224:227], v[32:35], 0\n"
"v_cvt_pk_bf16_f32 v39, v30, v31\n"
"s_add_u32 s72, s50, 1\n"
"s_mul_i32 s70, s72, 3014656\n"
"s_cmp_gt_u32 s63, s72\n"
"s_cselect_b32 s71, 0, 16384\n"
"s_add_u32 s70, s70, s71\n"
"s_add_u32 s36, s66, s70\n"
"s_addc_u32 s37, s67, 0\n"
"s_cmp_eq_u32 s63, s72\n"
"s_cselect_b32 s71, 5, 6\n"
"s_mul_i32 s38, s71, 16384\n"
"s_mul_i32 s58, s64, s71\n"
"s_mul_i32 s58, s58, 4096\n"
"s_cselect_b32 s59, 1073741824, s58\n"
"s_cmp_gt_u32 s72, 3\n"
"s_cselect_b32 s38, 0, s38\n"
"v_mfma_f32_16x16x32_bf16 v[24:27], a[128:131], a[32:35], 0\n"
"v_mul_f32_e32 v16, s44, v16\n"
"v_mul_f32_e32 v17, s44, v17\n"
"v_mfma_f32_16x16x32_bf16 v[24:27], a[132:135], a[36:39], v[24:27]\n"
"v_exp_f32_e32 v16, v16\n"
"v_mfma_f32_16x16x32_bf16 v[24:27], a[136:139], a[40:43], v[24:27]\n"
"s_waitcnt vmcnt(9)\n"
"ds_read_b128 a[48:51], v5 offset:12288\n"
"v_mul_f32_e32 v18, s44, v18\n"
"v_mfma_f32_16x16x32_bf16 v[24:27], a[140:143], a[44:47], v[24:27]\n"
"v_exp_f32_e32 v17, v17\n"
"v_mfma_f32_16x16x32_bf16 v[28:31], a[144:147], a[32:35], 0\n"
"v_mul_f32_e32 v19, s44, v19\n"
"v_cvt_pk_bf16_f32 v32, v16, v17\n"
"v_mfma_f32_16x16x32_bf16 v[28:31], a[148:151], a[36:39], v[28:31]\n"
"v_exp_f32_e32 v18, v18\n"
"v_mfma_f32_16x16x32_bf16 v[28:31], a[152:155], a[40:43], v[28:31]\n"
"ds_read_b128 a[52:55], v5 offset:12800\n"
"v_mul_f32_e32 v20, s44, v20\n"
"v_mfma_f32_16x16x32_bf16 v[28:31], a[156:159], a[44:47], v[28:31]\n"
"v_exp_f32_e32 v19, v19\n"
"s_add_u32 m0, s48, 24576\n"
"s_add_u32 s47, s45, 24576\n"
"buffer_load_dwordx4 v6, s[24:27], s47 offen lds\n"
"s_add_u32 m0, s48, 25600\n"
"s_add_u32 s47, s45, 24704\n"
"buffer_load_dwordx4 v6, s[24:27], s47 offen lds\n"
"v_mfma_f32_16x16x32_bf16 v[96:99], a[192:195], v[36:39], v[96:99]\n"
"v_mul_f32_e32 v21, s44, v21\n"
"v_cvt_pk_bf16_f32 v33, v18, v19\n"
"v_mfma_f32_16x16x32_bf16 v[100:103], a[196:199], v[36:39], v[100:103]\n"
"v_exp_f32_e32 v20, v20\n"
"v_mfma_f32_16x16x32_bf16 v[104:107], a[200:203], v[36:39], v[104:107]\n"
"ds_read_b128 a[56:59], v5 offset:13312\n"
"v_mul_f32_e32 v22, s44, v22\n"
"v_mfma_f32_16x16x32_bf16 v[108:111], a[204:207], v[36:39], v[108:111]\n"
"v_exp_f32_e32 v21, v21\n"
"v_mfma_f32_16x16x32_bf16 v[112:115], a[208:211], v[36:39], v[112:115]\n"
"v_mul_f32_e32 v23, s44, v23\n"
"v_cvt_pk_bf16_f32 v34, v20, v21\n"
"v_mfma_f32_16x16x32_bf16 v[116:119], a[212:215], v[36:39], v[116:119]\n"
"s_mov_b32 s88, L_L5%=+8192-L_code%=\n"
"v_exp_f32_e32 v22, v22\n"
"v_mfma_f32_16x16x32_bf16 v[120:123], a[216:219], v[36:39], v[120:123]\n"
"buffer_load_dword v63, v62, s[84:87], s88 offen\n"
"v_mfma_f32_16x16x32_bf16 v[124:127], a[220:223], v[36:39], v[124:127]\n"
"v_exp_f32_e32 v23, v23\n"
"v_mfma_f32_16x16x32_bf16 a[232:235], a[224:227], v[36:39], a[232:235]\n"
"s_waitcnt lgkmcnt(2)\n"
"ds_read_b128 a[60:63], v5 offset:13824\n"
"v_cvt_pk_bf16_f32 v35, v22, v23\n"
"v_mfma_f32_16x16x32_bf16 v[16:19], a[96:99], a[48:51], 0\n"
"s_waitcnt lgkmcnt(2)\n"
"v_mul_f32_e32 v24, s44, v24\n"
"v_mul_f32_e32 v25, s44, v25\n"
"v_mfma_f32_16x16x32_bf16 v[16:19], a[100:103], a[52:55], v[16:19]\n"
"s_waitcnt lgkmcnt(1)\n"
"v_exp_f32_e32 v24, v24\n"
"v_mfma_f32_16x16x32_bf16 v[16:19], a[104:107], a[56:59], v[16:19]\n"
"s_waitcnt lgkmcnt(0)\n"
"v_mul_f32_e32 v26, s44, v26\n"
"v_mfma_f32_16x16x32_bf16 v[16:19], a[108:111], a[60:63], v[16:19]\n"
"v_exp_f32_e32 v25, v25\n"
"v_mfma_f32_16x16x32_bf16 v[20:23], a[112:115], a[48:51], 0\n"
"v_mul_f32_e32 v27, s44, v27\n"
"v_cvt_pk_bf16_f32 v36, v24, v25\n"
"v_mfma_f32_16x16x32_bf16 v[20:23], a[116:119], a[52:55], v[20:23]\n"
"v_exp_f32_e32 v26, v26\n"
"v_mfma_f32_16x16x32_bf16 v[20:23], a[120:123], a[56:59], v[20:23]\n"
"v_mul_f32_e32 v28, s44, v28\n"
"v_mfma_f32_16x16x32_bf16 v[20:23], a[124:127], a[60:63], v[20:23]\n"
"v_exp_f32_e32 v27, v27\n"
"s_add_u32 m0, s48, 57344\n"
"s_add_u32 s76, s45, 24576\n"
"buffer_load_dwordx4 v6, s[28:31], s76 offen lds\n"
"s_add_u32 m0, s48, 58368\n"
"s_add_u32 s76, s45, 24704\n"
"buffer_load_dwordx4 v6, s[28:31], s76 offen lds\n"
"v_mfma_f32_16x16x32_bf16 v[128:131], a[160:163], v[32:35], 0\n"
"s_add_u32 m0, s48, 1024\n"
"v_mul_f32_e32 v29, s44, v29\n"
"v_cvt_pk_bf16_f32 v37, v26, v27\n"
"v_mfma_f32_16x16x32_bf16 v[132:135], a[164:167], v[32:35], 0\n"
"s_add_u32 s47, s45, 32896\n"
"v_exp_f32_e32 v28, v28\n"
"v_mfma_f32_16x16x32_bf16 v[136:139], a[168:171], v[32:35], 0\n"
"buffer_load_dwordx4 v6, s[24:27], s47 offen lds\n"
"v_mul_f32_e32 v30, s44, v30\n"
"v_mfma_f32_16x16x32_bf16 v[140:143], a[172:175], v[32:35], 0\n"
"v_exp_f32_e32 v29, v29\n"
"v_mfma_f32_16x16x32_bf16 v[144:147], a[176:179], v[32:35], 0\n"
"s_add_u32 m0, s48, 0\n"
"v_mul_f32_e32 v31, s44, v31\n"
"v_cvt_pk_bf16_f32 v38, v28, v29\n"
"v_mfma_f32_16x16x32_bf16 v[148:151], a[180:183], v[32:35], 0\n"
"s_add_u32 s47, s45, 32768\n"
"v_exp_f32_e32 v30, v30\n"
"v_mfma_f32_16x16x32_bf16 v[152:155], a[184:187], v[32:35], 0\n"
"buffer_load_dwordx4 v6, s[24:27], s47 offen lds\n"
"v_mfma_f32_16x16x32_bf16 v[156:159], a[188:191], v[32:35], 0\n"
"v_exp_f32_e32 v31, v31\n"
"v_mfma_f32_16x16x32_bf16 a[236:239], a[224:227], v[32:35], 0\n"
"v_cvt_pk_bf16_f32 v39, v30, v31\n"
"v_mfma_f32_16x16x32_bf16 v[24:27], a[128:131], a[48:51], 0\n"
"s_waitcnt vmcnt(12)\n"
"ds_read_b128 a[76:79], v5 offset:17920\n"
"v_mul_f32_e32 v16, s44, v16\n"
"v_mul_f32_e32 v17, s44, v17\n"
"v_mfma_f32_16x16x32_bf16 v[24:27], a[132:135], a[52:55], v[24:27]\n"
"v_exp_f32_e32 v16, v16\n"
"v_mfma_f32_16x16x32_bf16 v[24:27], a[136:139], a[56:59], v[24:27]\n"
"ds_read_b128 a[64:67], v5 offset:16384\n"
"v_mul_f32_e32 v18, s44, v18\n"
"v_mfma_f32_16x16x32_bf16 v[24:27], a[140:143], a[60:63], v[24:27]\n"
"v_exp_f32_e32 v17, v17\n"
"v_mfma_f32_16x16x32_bf16 v[28:31], a[144:147], a[48:51], 0\n"
"v_mul_f32_e32 v19, s44, v19\n"
"v_cvt_pk_bf16_f32 v32, v16, v17\n"
"v_mfma_f32_16x16x32_bf16 v[28:31], a[148:151], a[52:55], v[28:31]\n"
"v_exp_f32_e32 v18, v18\n"
"v_mfma_f32_16x16x32_bf16 v[28:31], a[152:155], a[56:59], v[28:31]\n"
"ds_read_b128 a[68:71], v5 offset:16896\n"
"v_mul_f32_e32 v20, s44, v20\n"
"v_mfma_f32_16x16x32_bf16 v[28:31], a[156:159], a[60:63], v[28:31]\n"
"v_exp_f32_e32 v19, v19\n"
"v_mfma_f32_16x16x32_bf16 v[128:131], a[192:195], v[36:39], v[128:131]\n"
"s_add_u32 m0, s48, 33792\n"
"v_mul_f32_e32 v21, s44, v21\n"
"v_cvt_pk_bf16_f32 v33, v18, v19\n"
"v_mfma_f32_16x16x32_bf16 v[132:135], a[196:199], v[36:39], v[132:135]\n"
"s_add_u32 s76, s45, 32896\n"
"v_exp_f32_e32 v20, v20\n"
"v_mfma_f32_16x16x32_bf16 v[136:139], a[200:203], v[36:39], v[136:139]\n"
"buffer_load_dwordx4 v6, s[28:31], s76 offen lds\n"
"v_mul_f32_e32 v22, s44, v22\n"
"v_mfma_f32_16x16x32_bf16 v[140:143], a[204:207], v[36:39], v[140:143]\n"
"v_exp_f32_e32 v21, v21\n"
"v_mfma_f32_16x16x32_bf16 v[144:147], a[208:211], v[36:39], v[144:147]\n"
"s_add_u32 m0, s48, 32768\n"
"v_mul_f32_e32 v23, s44, v23\n"
"v_cvt_pk_bf16_f32 v34, v20, v21\n"
"v_mfma_f32_16x16x32_bf16 v[148:151], a[212:215], v[36:39], v[148:151]\n"
"s_add_u32 s76, s45, 32768\n"
"v_exp_f32_e32 v22, v22\n"
"v_mfma_f32_16x16x32_bf16 v[152:155], a[216:219], v[36:39], v[152:155]\n"
"buffer_load_dwordx4 v6, s[28:31], s76 offen lds\n"
"v_mfma_f32_16x16x32_bf16 v[156:159], a[220:223], v[36:39], v[156:159]\n"
"v_exp_f32_e32 v23, v23\n"
"v_mfma_f32_16x16x32_bf16 a[236:239], a[224:227], v[36:39], a[236:239]\n"
"s_waitcnt lgkmcnt(1)\n"
"ds_read_b128 a[72:75], v5 offset:17408\n"
"v_cvt_pk_bf16_f32 v35, v22, v23\n"
"v_mfma_f32_16x16x32_bf16 v[16:19], a[96:99], a[64:67], 0\n"
"s_waitcnt lgkmcnt(1)\n"
"v_mul_f32_e32 v24, s44, v24\n"
"v_mul_f32_e32 v25, s44, v25\n"
"v_mfma_f32_16x16x32_bf16 v[16:19], a[100:103], a[68:71], v[16:19]\n"
"s_waitcnt lgkmcnt(0)\n"
"v_exp_f32_e32 v24, v24\n"
"v_mfma_f32_16x16x32_bf16 v[16:19], a[104:107], a[72:75], v[16:19]\n"
"v_mul_f32_e32 v26, s44, v26\n"
"v_mfma_f32_16x16x32_bf16 v[16:19], a[108:111], a[76:79], v[16:19]\n"
"v_exp_f32_e32 v25, v25\n"
"v_mfma_f32_16x16x32_bf16 v[20:23], a[112:115], a[64:67], 0\n"
"v_mul_f32_e32 v27, s44, v27\n"
"v_cvt_pk_bf16_f32 v36, v24, v25\n"
"v_mfma_f32_16x16x32_bf16 v[20:23], a[116:119], a[68:71], v[20:23]\n"
"v_exp_f32_e32 v26, v26\n"
"v_mfma_f32_16x16x32_bf16 v[20:23], a[120:123], a[72:75], v[20:23]\n"
"v_mul_f32_e32 v28, s44, v28\n"
"v_mfma_f32_16x16x32_bf16 v[20:23], a[124:127], a[76:79], v[20:23]\n"
"s_waitcnt vmcnt(11)\n"
"s_barrier\n"
"v_exp_f32_e32 v27, v27\n"
"v_mfma_f32_16x16x32_bf16 v[160:163], a[160:163], v[32:35], 0\n"
"s_add_u32 m0, s48, 9216\n"
"ds_read_b128 a[96:99], v1 offset:16384\n"
"v_mul_f32_e32 v29, s44, v29\n"
"v_cvt_pk_bf16_f32 v37, v26, v27\n"
"v_mfma_f32_16x16x32_bf16 v[164:167], a[164:167], v[32:35], 0\n"
"s_add_u32 s47, s45, 41088\n"
"ds_read_b128 a[100:103], v1 offset:16896\n"
"v_exp_f32_e32 v28, v28\n"
"v_mfma_f32_16x16x32_bf16 v[168:171], a[168:171], v[32:35], 0\n"
"buffer_load_dwordx4 v6, s[24:27], s47 offen lds\n"
"v_mul_f32_e32 v30, s44, v30\n"
"v_mfma_f32_16x16x32_bf16 v[172:175], a[172:175], v[32:35], 0\n"
"ds_read_b128 a[104:107], v1 offset:17408\n"
"v_exp_f32_e32 v29, v29\n"
"v_mfma_f32_16x16x32_bf16 v[176:179], a[176:179], v[32:35], 0\n"
"ds_read_b128 a[108:111], v1 offset:17920\n"
"v_mul_f32_e32 v31, s44, v31\n"
"v_cvt_pk_bf16_f32 v38, v28, v29\n"
"v_mfma_f32_16x16x32_bf16 v[180:183], a[180:183], v[32:35], 0\n"
"s_mov_b32 s88, L_L5%=+16384-L_code%=\n"
"v_exp_f32_e32 v30, v30\n"
"v_mfma_f32_16x16x32_bf16 v[184:187], a[184:187], v[32:35], 0\n"
"s_add_u32 m0, s48, 8192\n"
"buffer_load_dword v63, v62, s[84:87], s88 offen\n"
"v_mfma_f32_16x16x32_bf16 v[188:191], a[188:191], v[32:35], 0\n"
"s_add_u32 s47, s45, 40960\n"
"v_exp_f32_e32 v31, v31\n"
"v_mfma_f32_16x16x32_bf16 a[240:243], a[224:227], v[32:35], 0\n"
"buffer_load_dwordx4 v6, s[24:27], s47 offen lds\n"
"v_cvt_pk_bf16_f32 v39, v30, v31\n"
"v_mfma_f32_16x16x32_bf16 v[24:27], a[128:131], a[64:67], 0\n"
"ds_read_b128 a[120:123], v2 offset:17408\n"
"v_mul_f32_e32 v16, s44, v16\n"
"v_mul_f32_e32 v17, s44, v17\n"
"v_mfma_f32_16x16x32_bf16 v[24:27], a[132:135], a[68:71], v[24:27]\n"
"ds_read_b128 a[124:127], v2 offset:17920\n"
"v_exp_f32_e32 v16, v16\n"
"v_mfma_f32_16x16x32_bf16 v[24:27], a[136:139], a[72:75], v[24:27]\n"
"ds_read_b128 a[112:115], v2 offset:16384\n"
"v_mul_f32_e32 v18, s44, v18\n"
"v_mfma_f32_16x16x32_bf16 v[24:27], a[140:143], a[76:79], v[24:27]\n"
"v_exp_f32_e32 v17, v17\n"
"v_mfma_f32_16x16x32_bf16 v[28:31], a[144:147], a[64:67], 0\n"
"v_mul_f32_e32 v19, s44, v19\n"
"v_cvt_pk_bf16_f32 v32, v16, v17\n"
"v_mfma_f32_16x16x32_bf16 v[28:31], a[148:151], a[68:71], v[28:31]\n"
"v_exp_f32_e32 v18, v18\n"
"v_mfma_f32_16x16x32_bf16 v[28:31], a[152:155], a[72:75], v[28:31]\n"
"ds_read_b128 a[116:119], v2 offset:16896\n"
"v_mul_f32_e32 v20, s44, v20\n"
"v_mfma_f32_16x16x32_bf16 v[28:31], a[156:159], a[76:79], v[28:31]\n"
"v_exp_f32_e32 v19, v19\n"
"v_mfma_f32_16x16x32_bf16 v[160:163], a[192:195], v[36:39], v[160:163]\n"
"s_add_u32 m0, s48, 41984\n"
"v_mul_f32_e32 v21, s44, v21\n"
"v_cvt_pk_bf16_f32 v33, v18, v19\n"
"v_mfma_f32_16x16x32_bf16 v[164:167], a[196:199], v[36:39], v[164:167]\n"
"s_add_u32 s76, s45, 41088\n"
"v_exp_f32_e32 v20, v20\n"
"v_mfma_f32_16x16x32_bf16 v[168:171], a[200:203], v[36:39], v[168:171]\n"
"buffer_load_dwordx4 v6, s[28:31], s76 offen lds\n"
"v_mul_f32_e32 v22, s44, v22\n"
"v_mfma_f32_16x16x32_bf16 v[172:175], a[204:207], v[36:39], v[172:175]\n"
"v_exp_f32_e32 v21, v21\n"
"v_mfma_f32_16x16x32_bf16 v[176:179], a[208:211], v[36:39], v[176:179]\n"
"s_add_u32 m0, s48, 40960\n"
"v_mul_f32_e32 v23, s44, v23\n"
"v_cvt_pk_bf16_f32 v34, v20, v21\n"
"v_mfma_f32_16x16x32_bf16 v[180:183], a[212:215], v[36:39], v[180:183]\n"
"s_add_u32 s76, s45, 40960\n"
"v_exp_f32_e32 v22, v22\n"
"v_mfma_f32_16x16x32_bf16 v[184:187], a[216:219], v[36:39], v[184:187]\n"
"buffer_load_dwordx4 v6, s[28:31], s76 offen lds\n"
"v_mfma_f32_16x16x32_bf16 v[188:191], a[220:223], v[36:39], v[188:191]\n"
"v_exp_f32_e32 v23, v23\n"
"v_mfma_f32_16x16x32_bf16 a[240:243], a[224:227], v[36:39], a[240:243]\n"
"s_waitcnt lgkmcnt(7)\n"
"v_cvt_pk_bf16_f32 v35, v22, v23\n"
"v_mfma_f32_16x16x32_bf16 v[16:19], a[96:99], a[0:3], 0\n"
"s_waitcnt lgkmcnt(6)\n"
"v_mul_f32_e32 v24, s44, v24\n"
"v_mul_f32_e32 v25, s44, v25\n"
"v_mfma_f32_16x16x32_bf16 v[16:19], a[100:103], a[4:7], v[16:19]\n"
"s_waitcnt lgkmcnt(5)\n"
"v_exp_f32_e32 v24, v24\n"
"v_mfma_f32_16x16x32_bf16 v[16:19], a[104:107], a[8:11], v[16:19]\n"
"s_waitcnt lgkmcnt(4)\n"
"v_mul_f32_e32 v26, s44, v26\n"
"v_mfma_f32_16x16x32_bf16 v[16:19], a[108:111], a[12:15], v[16:19]\n"
"s_waitcnt lgkmcnt(1)\n"
"v_exp_f32_e32 v25, v25\n"
"v_mfma_f32_16x16x32_bf16 v[20:23], a[112:115], a[0:3], 0\n"
"s_waitcnt lgkmcnt(0)\n"
"v_mul_f32_e32 v27, s44, v27\n"
"v_cvt_pk_bf16_f32 v36, v24, v25\n"
"v_mfma_f32_16x16x32_bf16 v[20:23], a[116:119], a[4:7], v[20:23]\n"
"s_waitcnt vmcnt(9)\n"
"s_barrier\n"
"v_exp_f32_e32 v26, v26\n"
"v_mfma_f32_16x16x32_bf16 v[20:23], a[120:123], a[8:11], v[20:23]\n"
"ds_read_b128 a[128:131], v1 offset:24576\n"
"ds_read_b128 a[140:143], v1 offset:26112\n"
"v_mul_f32_e32 v28, s44, v28\n"
"v_mfma_f32_16x16x32_bf16 v[20:23], a[124:127], a[12:15], v[20:23]\n"
"v_exp_f32_e32 v27, v27\n"
"v_mfma_f32_16x16x32_bf16 v[192:195], a[160:163], v[32:35], 0\n"
"v_mul_f32_e32 v29, s44, v29\n"
"v_cvt_pk_bf16_f32 v37, v26, v27\n"
"v_mfma_f32_16x16x32_bf16 v[196:199], a[164:167], v[32:35], 0\n"
"ds_read_b64_tr_b16 a[160:161], v3 offset:16384\n"
"v_exp_f32_e32 v28, v28\n"
"v_mfma_f32_16x16x32_bf16 v[200:203], a[168:171], v[32:35], 0\n"
"ds_read_b128 a[132:135], v1 offset:25088\n"
"ds_read_b64_tr_b16 a[162:163], v4 offset:16384\n"
"v_mul_f32_e32 v30, s44, v30\n"
"v_mfma_f32_16x16x32_bf16 v[204:207], a[172:175], v[32:35], 0\n"
"ds_read_b64_tr_b16 a[164:165], v11 offset:16384\n"
"v_exp_f32_e32 v29, v29\n"
"v_mfma_f32_16x16x32_bf16 v[208:211], a[176:179], v[32:35], 0\n"
"s_add_u32 m0, s48, 16384\n"
"ds_read_b64_tr_b16 a[166:167], v14 offset:16384\n"
"v_mul_f32_e32 v31, s44, v31\n"
"v_cvt_pk_bf16_f32 v38, v28, v29\n"
"v_mfma_f32_16x16x32_bf16 v[212:215], a[180:183], v[32:35], 0\n"
"s_add_u32 s47, s45, 49152\n"
"ds_read_b64_tr_b16 a[168:169], v3 offset:16896\n"
"v_exp_f32_e32 v30, v30\n"
"v_mfma_f32_16x16x32_bf16 v[216:219], a[184:187], v[32:35], 0\n"
"buffer_load_dwordx4 v6, s[24:27], s47 offen lds\n"
"ds_read_b64_tr_b16 a[170:171], v4 offset:16896\n"
"v_mfma_f32_16x16x32_bf16 v[220:223], a[188:191], v[32:35], 0\n"
"v_exp_f32_e32 v31, v31\n"
"v_mfma_f32_16x16x32_bf16 a[244:247], a[224:227], v[32:35], 0\n"
"ds_read_b128 a[136:139], v1 offset:25600\n"
"v_cvt_pk_bf16_f32 v39, v30, v31\n"
"v_mfma_f32_16x16x32_bf16 v[24:27], a[96:99], a[16:19], 0\n"
"ds_read_b128 a[156:159], v2 offset:26112\n"
"v_mul_f32_e32 v16, s44, v16\n"
"v_mul_f32_e32 v17, s44, v17\n"
"v_mfma_f32_16x16x32_bf16 v[24:27], a[100:103], a[20:23], v[24:27]\n"
"ds_read_b64_tr_b16 a[172:173], v11 offset:16896\n"
"v_exp_f32_e32 v16, v16\n"
"v_mfma_f32_16x16x32_bf16 v[24:27], a[104:107], a[24:27], v[24:27]\n"
"ds_read_b128 a[144:147], v2 offset:24576\n"
"ds_read_b64_tr_b16 a[174:175], v14 offset:16896\n"
"v_mul_f32_e32 v18, s44, v18\n"
"v_mfma_f32_16x16x32_bf16 v[24:27], a[108:111], a[28:31], v[24:27]\n"
"ds_read_b64_tr_b16 a[176:177], v3 offset:17408\n"
"v_exp_f32_e32 v17, v17\n"
"v_mfma_f32_16x16x32_bf16 v[28:31], a[112:115], a[16:19], 0\n"
"ds_read_b64_tr_b16 a[178:179], v4 offset:17408\n"
"v_mul_f32_e32 v19, s44, v19\n"
"v_cvt_pk_bf16_f32 v32, v16, v17\n"
"v_mfma_f32_16x16x32_bf16 v[28:31], a[116:119], a[20:23], v[28:31]\n"
"ds_read_b64_tr_b16 a[180:181], v11 offset:17408\n"
"v_exp_f32_e32 v18, v18\n"
"v_mfma_f32_16x16x32_bf16 v[28:31], a[120:123], a[24:27], v[28:31]\n"
"ds_read_b128 a[148:151], v2 offset:25088\n"
"ds_read_b64_tr_b16 a[182:183], v14 offset:17408\n"
"v_mul_f32_e32 v20, s44, v20\n"
"v_mfma_f32_16x16x32_bf16 v[28:31], a[124:127], a[28:31], v[28:31]\n"
"ds_read_b64_tr_b16 a[184:185], v3 offset:17920\n"
"v_exp_f32_e32 v19, v19\n"
"v_mfma_f32_16x16x32_bf16 v[192:195], a[192:195], v[36:39], v[192:195]\n"
"s_add_u32 m0, s49, 0\n"
"ds_read_b64_tr_b16 a[186:187], v4 offset:17920\n"
"v_mul_f32_e32 v21, s44, v21\n"
"v_cvt_pk_bf16_f32 v33, v18, v19\n"
"v_mfma_f32_16x16x32_bf16 v[196:199], a[196:199], v[36:39], v[196:199]\n"
"s_add_u32 s61, s58, 0\n"
"ds_read_b64_tr_b16 a[188:189], v11 offset:17920\n"
"v_exp_f32_e32 v20, v20\n"
"v_mfma_f32_16x16x32_bf16 v[200:203], a[200:203], v[36:39], v[200:203]\n"
"buffer_load_dwordx4 v7, s[36:39], s61 offen  nt lds\n"
"v_mul_f32_e32 v22, s44, v22\n"
"v_mfma_f32_16x16x32_bf16 v[204:207], a[204:207], v[36:39], v[204:207]\n"
"ds_read_b64_tr_b16 a[190:191], v14 offset:17920\n"
"v_exp_f32_e32 v21, v21\n"
"v_mfma_f32_16x16x32_bf16 v[208:211], a[208:211], v[36:39], v[208:211]\n"
"s_add_u32 m0, s48, 17408\n"
"v_mul_f32_e32 v23, s44, v23\n"
"v_cvt_pk_bf16_f32 v34, v20, v21\n"
"v_mfma_f32_16x16x32_bf16 v[212:215], a[212:215], v[36:39], v[212:215]\n"
"s_add_u32 s47, s45, 49280\n"
"v_exp_f32_e32 v22, v22\n"
"v_mfma_f32_16x16x32_bf16 v[216:219], a[216:219], v[36:39], v[216:219]\n"
"buffer_load_dwordx4 v6, s[24:27], s47 offen lds\n"
"v_mfma_f32_16x16x32_bf16 v[220:223], a[220:223], v[36:39], v[220:223]\n"
"v_exp_f32_e32 v23, v23\n"
"v_mfma_f32_16x16x32_bf16 a[244:247], a[224:227], v[36:39], a[244:247]\n"
"ds_read_b128 a[152:155], v2 offset:25600\n"
"v_cvt_pk_bf16_f32 v35, v22, v23\n"
"v_mfma_f32_16x16x32_bf16 v[16:19], a[96:99], a[32:35], 0\n"
"ds_read_b64_tr_b16 a[198:199], v14 offset:24576\n"
"v_mul_f32_e32 v24, s44, v24\n"
"v_mul_f32_e32 v25, s44, v25\n"
"v_mfma_f32_16x16x32_bf16 v[16:19], a[100:103], a[36:39], v[16:19]\n"
"ds_read_b64_tr_b16 a[200:201], v3 offset:25088\n"
"v_exp_f32_e32 v24, v24\n"
"v_mfma_f32_16x16x32_bf16 v[16:19], a[104:107], a[40:43], v[16:19]\n"
"ds_read_b64_tr_b16 a[192:193], v3 offset:24576\n"
"ds_read_b64_tr_b16 a[202:203], v4 offset:25088\n"
"v_mul_f32_e32 v26, s44, v26\n"
"v_mfma_f32_16x16x32_bf16 v[16:19], a[108:111], a[44:47], v[16:19]\n"
"v_exp_f32_e32 v25, v25\n"
"v_mfma_f32_16x16x32_bf16 v[20:23], a[112:115], a[32:35], 0\n"
"v_mul_f32_e32 v27, s44, v27\n"
"v_cvt_pk_bf16_f32 v36, v24, v25\n"
"v_mfma_f32_16x16x32_bf16 v[20:23], a[116:119], a[36:39], v[20:23]\n"
"s_waitcnt lgkmcnt(5)\n"
"s_barrier\n"
"v_exp_f32_e32 v26, v26\n"
"v_mfma_f32_16x16x32_bf16 v[20:23], a[120:123], a[40:43], v[20:23]\n"
"ds_read_b64_tr_b16 a[194:195], v4 offset:24576\n"
"v_mul_f32_e32 v28, s44, v28\n"
"v_mfma_f32_16x16x32_bf16 v[20:23], a[124:127], a[44:47], v[20:23]\n"
"v_exp_f32_e32 v27, v27\n"
"v_mfma_f32_16x16x32_bf16 v[64:67], a[160:163], v[32:35], v[64:67]\n"
"s_add_u32 m0, s49, 1024\n"
"v_mul_f32_e32 v29, s44, v29\n"
"v_cvt_pk_bf16_f32 v37, v26, v27\n"
"v_mfma_f32_16x16x32_bf16 v[68:71], a[164:167], v[32:35], v[68:71]\n"
"s_add_u32 s61, s58, 128\n"
"v_exp_f32_e32 v28, v28\n"
"v_mfma_f32_16x16x32_bf16 v[72:75], a[168:171], v[32:35], v[72:75]\n"
"buffer_load_dwordx4 v7, s[36:39], s61 offen  nt lds\n"
"v_mul_f32_e32 v30, s44, v30\n"
"v_mfma_f32_16x16x32_bf16 v[76:79], a[172:175], v[32:35], v[76:79]\n"
"v_exp_f32_e32 v29, v29\n"
"v_mfma_f32_16x16x32_bf16 v[80:83], a[176:179], v[32:35], v[80:83]\n"
"s_add_u32 m0, s48, 49152\n"
"v_mul_f32_e32 v31, s44, v31\n"
"v_cvt_pk_bf16_f32 v38, v28, v29\n"
"v_mfma_f32_16x16x32_bf16 v[84:87], a[180:183], v[32:35], v[84:87]\n"
"s_add_u32 s76, s45, 49152\n"
"v_exp_f32_e32 v30, v30\n"
"v_mfma_f32_16x16x32_bf16 v[88:91], a[184:187], v[32:35], v[88:91]\n"
"buffer_load_dwordx4 v6, s[28:31], s76 offen lds\n"
"v_mfma_f32_16x16x32_bf16 v[92:95], a[188:191], v[32:35], v[92:95]\n"
"v_exp_f32_e32 v31, v31\n"
"v_mfma_f32_16x16x32_bf16 a[228:231], a[224:227], v[32:35], a[228:231]\n"
"ds_read_b64_tr_b16 a[196:197], v11 offset:24576\n"
"v_cvt_pk_bf16_f32 v39, v30, v31\n"
"v_mfma_f32_16x16x32_bf16 v[24:27], a[96:99], a[48:51], 0\n"
"ds_read_b64_tr_b16 a[210:211], v4 offset:25600\n"
"v_mul_f32_e32 v16, s44, v16\n"
"v_mul_f32_e32 v17, s44, v17\n"
"v_mfma_f32_16x16x32_bf16 v[24:27], a[100:103], a[52:55], v[24:27]\n"
"ds_read_b64_tr_b16 a[212:213], v11 offset:25600\n"
"v_exp_f32_e32 v16, v16\n"
"v_mfma_f32_16x16x32_bf16 v[24:27], a[104:107], a[56:59], v[24:27]\n"
"ds_read_b64_tr_b16 a[204:205], v11 offset:25088\n"
"ds_read_b64_tr_b16 a[214:215], v14 offset:25600\n"
"v_mul_f32_e32 v18, s44, v18\n"
"v_mfma_f32_16x16x32_bf16 v[24:27], a[108:111], a[60:63], v[24:27]\n"
"v_exp_f32_e32 v17, v17\n"
"v_mfma_f32_16x16x32_bf16 v[28:31], a[112:115], a[48:51], 0\n"
"v_mul_f32_e32 v19, s44, v19\n"
"v_cvt_pk_bf16_f32 v32, v16, v17\n"
"v_mfma_f32_16x16x32_bf16 v[28:31], a[116:119], a[52:55], v[28:31]\n"
"v_exp_f32_e32 v18, v18\n"
"v_mfma_f32_16x16x32_bf16 v[28:31], a[120:123], a[56:59], v[28:31]\n"
"ds_read_b64_tr_b16 a[206:207], v14 offset:25088\n"
"v_mul_f32_e32 v20, s44, v20\n"
"v_mfma_f32_16x16x32_bf16 v[28:31], a[124:127], a[60:63], v[28:31]\n"
"v_exp_f32_e32 v19, v19\n"
"v_mfma_f32_16x16x32_bf16 v[96:99], a[160:163], v[36:39], v[96:99]\n"
"s_add_u32 m0, s49, 2048\n"
"v_mul_f32_e32 v21, s44, v21\n"
"v_cvt_pk_bf16_f32 v33, v18, v19\n"
"v_mfma_f32_16x16x32_bf16 v[100:103], a[164:167], v[36:39], v[100:103]\n"
"s_add_u32 s61, s58, 0\n"
"v_exp_f32_e32 v20, v20\n"
"v_mfma_f32_16x16x32_bf16 v[104:107], a[168:171], v[36:39], v[104:107]\n"
"buffer_load_dwordx4 v8, s[36:39], s61 offen  nt lds\n"
"v_mul_f32_e32 v22, s44, v22\n"
"v_mfma_f32_16x16x32_bf16 v[108:111], a[172:175], v[36:39], v[108:111]\n"
"v_exp_f32_e32 v21, v21\n"
"v_mfma_f32_16x16x32_bf16 v[112:115], a[176:179], v[36:39], v[112:115]\n"
"s_add_u32 m0, s48, 50176\n"
"v_mul_f32_e32 v23, s44, v23\n"
"v_cvt_pk_bf16_f32 v34, v20, v21\n"
"v_mfma_f32_16x16x32_bf16 v[116:119], a[180:183], v[36:39], v[116:119]\n"
"s_add_u32 s76, s45, 49280\n"
"v_exp_f32_e32 v22, v22\n"
"v_mfma_f32_16x16x32_bf16 v[120:123], a[184:187], v[36:39], v[120:123]\n"
"buffer_load_dwordx4 v6, s[28:31], s76 offen lds\n"
"v_mfma_f32_16x16x32_bf16 v[124:127], a[188:191], v[36:39], v[124:127]\n"
"v_exp_f32_e32 v23, v23\n"
"v_mfma_f32_16x16x32_bf16 a[232:235], a[224:227], v[36:39], a[232:235]\n"
"ds_read_b64_tr_b16 a[208:209], v3 offset:25600\n"
"v_cvt_pk_bf16_f32 v35, v22, v23\n"
"v_mfma_f32_16x16x32_bf16 v[16:19], a[96:99], a[64:67], 0\n"
"ds_read_b64_tr_b16 a[222:223], v14 offset:26112\n"
"v_mul_f32_e32 v24, s44, v24\n"
"v_mul_f32_e32 v25, s44, v25\n"
"v_mfma_f32_16x16x32_bf16 v[16:19], a[100:103], a[68:71], v[16:19]\n"
"v_exp_f32_e32 v24, v24\n"
"v_mfma_f32_16x16x32_bf16 v[16:19], a[104:107], a[72:75], v[16:19]\n"
"ds_read_b64_tr_b16 a[216:217], v3 offset:26112\n"
"v_mul_f32_e32 v26, s44, v26\n"
"v_mfma_f32_16x16x32_bf16 v[16:19], a[108:111], a[76:79], v[16:19]\n"
"v_exp_f32_e32 v25, v25\n"
"v_mfma_f32_16x16x32_bf16 v[20:23], a[112:115], a[64:67], 0\n"
"v_mul_f32_e32 v27, s44, v27\n"
"v_cvt_pk_bf16_f32 v36, v24, v25\n"
"v_mfma_f32_16x16x32_bf16 v[20:23], a[116:119], a[68:71], v[20:23]\n"
"v_exp_f32_e32 v26, v26\n"
"v_mfma_f32_16x16x32_bf16 v[20:23], a[120:123], a[72:75], v[20:23]\n"
"ds_read_b64_tr_b16 a[218:219], v4 offset:26112\n"
"v_mul_f32_e32 v28, s44, v28\n"
"v_mfma_f32_16x16x32_bf16 v[20:23], a[124:127], a[76:79], v[20:23]\n"
"v_exp_f32_e32 v27, v27\n"
"v_mfma_f32_16x16x32_bf16 v[128:131], a[160:163], v[32:35], v[128:131]\n"
"v_mul_f32_e32 v29, s44, v29\n"
"v_cvt_pk_bf16_f32 v37, v26, v27\n"
"v_mfma_f32_16x16x32_bf16 v[132:135], a[164:167], v[32:35], v[132:135]\n"
"v_exp_f32_e32 v28, v28\n"
"v_mfma_f32_16x16x32_bf16 v[136:139], a[168:171], v[32:35], v[136:139]\n"
"ds_read_b64_tr_b16 a[220:221], v11 offset:26112\n"
"v_mul_f32_e32 v30, s44, v30\n"
"v_mfma_f32_16x16x32_bf16 v[140:143], a[172:175], v[32:35], v[140:143]\n"
"v_exp_f32_e32 v29, v29\n"
"v_mfma_f32_16x16x32_bf16 v[144:147], a[176:179], v[32:35], v[144:147]\n"
"s_add_u32 m0, s49, 3072\n"
"v_mul_f32_e32 v31, s44, v31\n"
"v_cvt_pk_bf16_f32 v38, v28, v29\n"
"v_mfma_f32_16x16x32_bf16 v[148:151], a[180:183], v[32:35], v[148:151]\n"
"s_add_u32 s61, s58, 128\n"
"v_exp_f32_e32 v30, v30\n"
"v_mfma_f32_16x16x32_bf16 v[152:155], a[184:187], v[32:35], v[152:155]\n"
"buffer_load_dwordx4 v8, s[36:39], s61 offen  nt lds\n"
"v_mfma_f32_16x16x32_bf16 v[156:159], a[188:191], v[32:35], v[156:159]\n"
"v_exp_f32_e32 v31, v31\n"
"v_mfma_f32_16x16x32_bf16 a[236:239], a[224:227], v[32:35], a[236:239]\n"
"v_cvt_pk_bf16_f32 v39, v30, v31\n"
"s_branch L_L5%=\n"
"L_E5%=:\n"
"v_mfma_f32_16x16x32_bf16 v[16:19], a[96:99], a[0:3], 0\n"
"v_mul_f32_e32 v24, s44, v24\n"
"v_mul_f32_e32 v25, s44, v25\n"
"v_mfma_f32_16x16x32_bf16 v[16:19], a[100:103], a[4:7], v[16:19]\n"
"s_waitcnt vmcnt(11)\n"
"s_barrier\n"
"v_exp_f32_e32 v24, v24\n"
"v_mfma_f32_16x16x32_bf16 v[16:19], a[104:107], a[8:11], v[16:19]\n"
"ds_read_b128 a[128:131], v1 offset:24576\n"
"ds_read_b128 a[140:143], v1 offset:26112\n"
"v_mul_f32_e32 v26, s44, v26\n"
"v_mfma_f32_16x16x32_bf16 v[16:19], a[108:111], a[12:15], v[16:19]\n"
"ds_read_b128 a[144:147], v2 offset:24576\n"
"v_exp_f32_e32 v25, v25\n"
"v_mfma_f32_16x16x32_bf16 v[20:23], a[112:115], a[0:3], 0\n"
"ds_read_b128 a[148:151], v2 offset:25088\n"
"v_mul_f32_e32 v27, s44, v27\n"
"v_cvt_pk_bf16_f32 v36, v24, v25\n"
"v_mfma_f32_16x16x32_bf16 v[20:23], a[116:119], a[4:7], v[20:23]\n"
"ds_read_b128 a[152:155], v2 offset:25600\n"
"v_exp_f32_e32 v26, v26\n"
"v_mfma_f32_16x16x32_bf16 v[20:23], a[120:123], a[8:11], v[20:23]\n"
"ds_read_b128 a[132:135], v1 offset:25088\n"
"ds_read_b128 a[156:159], v2 offset:26112\n"
"v_mul_f32_e32 v28, s44, v28\n"
"v_mfma_f32_16x16x32_bf16 v[20:23], a[124:127], a[12:15], v[20:23]\n"
"v_exp_f32_e32 v27, v27\n"
"v_mfma_f32_16x16x32_bf16 v[160:163], a[192:195], v[32:35], v[160:163]\n"
"v_mul_f32_e32 v29, s44, v29\n"
"v_cvt_pk_bf16_f32 v37, v26, v27\n"
"v_mfma_f32_16x16x32_bf16 v[164:167], a[196:199], v[32:35], v[164:167]\n"
"v_exp_f32_e32 v28, v28\n"
"v_mfma_f32_16x16x32_bf16 v[168:171], a[200:203], v[32:35], v[168:171]\n"
"ds_read_b128 a[136:139], v1 offset:25600\n"
"v_mul_f32_e32 v30, s44, v30\n"
"v_mfma_f32_16x16x32_bf16 v[172:175], a[204:207], v[32:35], v[172:175]\n"
"v_exp_f32_e32 v29, v29\n"
"v_mfma_f32_16x16x32_bf16 v[176:179], a[208:211], v[32:35], v[176:179]\n"
"v_mul_f32_e32 v31, s44, v31\n"
"v_cvt_pk_bf16_f32 v38, v28, v29\n"
"v_mfma_f32_16x16x32_bf16 v[180:183], a[212:215], v[32:35], v[180:183]\n"
"v_exp_f32_e32 v30, v30\n"
"v_mfma_f32_16x16x32_bf16 v[184:187], a[216:219], v[32:35], v[184:187]\n"
"v_mfma_f32_16x16x32_bf16 v[188:191], a[220:223], v[32:35], v[188:191]\n"
"v_exp_f32_e32 v31, v31\n"
"v_mfma_f32_16x16x32_bf16 a[240:243], a[224:227], v[32:35], a[240:243]\n"
"s_waitcnt lgkmcnt(7)\n"
"v_cvt_pk_bf16_f32 v39, v30, v31\n"
"v_mfma_f32_16x16x32_bf16 v[24:27], a[128:131], a[0:3], 0\n"
"s_waitcnt lgkmcnt(2)\n"
"v_mul_f32_e32 v16, s44, v16\n"
"v_mul_f32_e32 v17, s44, v17\n"
"v_mfma_f32_16x16x32_bf16 v[24:27], a[132:135], a[4:7], v[24:27]\n"
"s_waitcnt lgkmcnt(0)\n"
"v_exp_f32_e32 v16, v16\n"
"v_mfma_f32_16x16x32_bf16 v[24:27], a[136:139], a[8:11], v[24:27]\n"
"v_mul_f32_e32 v18, s44, v18\n"
"v_mfma_f32_16x16x32_bf16 v[24:27], a[140:143], a[12:15], v[24:27]\n"
"v_exp_f32_e32 v17, v17\n"
"v_mfma_f32_16x16x32_bf16 v[28:31], a[144:147], a[0:3], 0\n"
"v_mul_f32_e32 v19, s44, v19\n"
"v_cvt_pk_bf16_f32 v32, v16, v17\n"
"v_mfma_f32_16x16x32_bf16 v[28:31], a[148:151], a[4:7], v[28:31]\n"
"v_exp_f32_e32 v18, v18\n"
"v_mfma_f32_16x16x32_bf16 v[28:31], a[152:155], a[8:11], v[28:31]\n"
"v_mul_f32_e32 v20, s44, v20\n"
"v_mfma_f32_16x16x32_bf16 v[28:31], a[156:159], a[12:15], v[28:31]\n"
"v_exp_f32_e32 v19, v19\n"
"v_mfma_f32_16x16x32_bf16 v[192:195], a[192:195], v[36:39], v[192:195]\n"
"v_mul_f32_e32 v21, s44, v21\n"
"v_cvt_pk_bf16_f32 v33, v18, v19\n"
"v_mfma_f32_16x16x32_bf16 v[196:199], a[196:199], v[36:39], v[196:199]\n"
"v_exp_f32_e32 v20, v20\n"
"v_mfma_f32_16x16x32_bf16 v[200:203], a[200:203], v[36:39], v[200:203]\n"
"ds_read_b64_tr_b16 a[194:195], v4 offset:24576\n"
"v_mul_f32_e32 v22, s44, v22\n"
"v_mfma_f32_16x16x32_bf16 v[204:207], a[204:207], v[36:39], v[204:207]\n"
"ds_read_b64_tr_b16 a[200:201], v3 offset:25088\n"
"v_exp_f32_e32 v21, v21\n"
"v_mfma_f32_16x16x32_bf16 v[208:211], a[208:211], v[36:39], v[208:211]\n"
"ds_read_b64_tr_b16 a[202:203], v4 offset:25088\n"
"v_mul_f32_e32 v23, s44, v23\n"
"v_cvt_pk_bf16_f32 v34, v20, v21\n"
"v_mfma_f32_16x16x32_bf16 v[212:215], a[212:215], v[36:39], v[212:215]\n"
"v_exp_f32_e32 v22, v22\n"
"v_mfma_f32_16x16x32_bf16 v[216:219], a[216:219], v[36:39], v[216:219]\n"
"ds_read_b64_tr_b16 a[192:193], v3 offset:24576\n"
"ds_read_b64_tr_b16 a[196:197], v11 offset:24576\n"
"v_mfma_f32_16x16x32_bf16 v[220:223], a[220:223], v[36:39], v[220:223]\n"
"v_exp_f32_e32 v23, v23\n"
"v_mfma_f32_16x16x32_bf16 a[244:247], a[224:227], v[36:39], a[244:247]\n"
"ds_read_b64_tr_b16 a[198:199], v14 offset:24576\n"
"v_cvt_pk_bf16_f32 v35, v22, v23\n"
"v_mfma_f32_16x16x32_bf16 v[16:19], a[96:99], a[16:19], 0\n"
"ds_read_b64_tr_b16 a[216:217], v3 offset:26112\n"
"v_mul_f32_e32 v24, s44, v24\n"
"v_mul_f32_e32 v25, s44, v25\n"
"v_mfma_f32_16x16x32_bf16 v[16:19], a[100:103], a[20:23], v[16:19]\n"
"ds_read_b64_tr_b16 a[218:219], v4 offset:26112\n"
"v_exp_f32_e32 v24, v24\n"
"v_mfma_f32_16x16x32_bf16 v[16:19], a[104:107], a[24:27], v[16:19]\n"
"ds_read_b64_tr_b16 a[206:207], v14 offset:25088\n"
"ds_read_b64_tr_b16 a[220:221], v11 offset:26112\n"
"v_mul_f32_e32 v26, s44, v26\n"
"v_mfma_f32_16x16x32_bf16 v[16:19], a[108:111], a[28:31], v[16:19]\n"
"ds_read_b64_tr_b16 a[222:223], v14 offset:26112\n"
"v_exp_f32_e32 v25, v25\n"
"v_mfma_f32_16x16x32_bf16 v[20:23], a[112:115], a[16:19], 0\n"
"v_mul_f32_e32 v27, s44, v27\n"
"v_cvt_pk_bf16_f32 v36, v24, v25\n"
"v_mfma_f32_16x16x32_bf16 v[20:23], a[116:119], a[20:23], v[20:23]\n"
"v_exp_f32_e32 v26, v26\n"
"v_mfma_f32_16x16x32_bf16 v[20:23], a[120:123], a[24:27], v[20:23]\n"
"ds_read_b64_tr_b16 a[208:209], v3 offset:25600\n"
"v_mul_f32_e32 v28, s44, v28\n"
"v_mfma_f32_16x16x32_bf16 v[20:23], a[124:127], a[28:31], v[20:23]\n"
"v_exp_f32_e32 v27, v27\n"
"v_mfma_f32_16x16x32_bf16 v[64:67], a[160:163], v[32:35], v[64:67]\n"
"v_mul_f32_e32 v29, s44, v29\n"
"v_cvt_pk_bf16_f32 v37, v26, v27\n"
"v_mfma_f32_16x16x32_bf16 v[68:71], a[164:167], v[32:35], v[68:71]\n"
"v_exp_f32_e32 v28, v28\n"
"v_mfma_f32_16x16x32_bf16 v[72:75], a[168:171], v[32:35], v[72:75]\n"
"ds_read_b64_tr_b16 a[210:211], v4 offset:25600\n"
"v_mul_f32_e32 v30, s44, v30\n"
"v_mfma_f32_16x16x32_bf16 v[76:79], a[172:175], v[32:35], v[76:79]\n"
"v_exp_f32_e32 v29, v29\n"
"v_mfma_f32_16x16x32_bf16 v[80:83], a[176:179], v[32:35], v[80:83]\n"
"v_mul_f32_e32 v31, s44, v31\n"
"v_cvt_pk_bf16_f32 v38, v28, v29\n"
"v_mfma_f32_16x16x32_bf16 v[84:87], a[180:183], v[32:35], v[84:87]\n"
"v_exp_f32_e32 v30, v30\n"
"v_mfma_f32_16x16x32_bf16 v[88:91], a[184:187], v[32:35], v[88:91]\n"
"ds_read_b64_tr_b16 a[204:205], v11 offset:25088\n"
"ds_read_b64_tr_b16 a[212:213], v11 offset:25600\n"
"v_mfma_f32_16x16x32_bf16 v[92:95], a[188:191], v[32:35], v[92:95]\n"
"v_exp_f32_e32 v31, v31\n"
"v_mfma_f32_16x16x32_bf16 a[228:231], a[224:227], v[32:35], a[228:231]\n"
"ds_read_b64_tr_b16 a[214:215], v14 offset:25600\n"
"v_cvt_pk_bf16_f32 v39, v30, v31\n"
"v_mfma_f32_16x16x32_bf16 v[24:27], a[128:131], a[16:19], 0\n"
"v_mul_f32_e32 v16, s44, v16\n"
"v_mul_f32_e32 v17, s44, v17\n"
"v_mfma_f32_16x16x32_bf16 v[24:27], a[132:135], a[20:23], v[24:27]\n"
"v_exp_f32_e32 v16, v16\n"
"v_mfma_f32_16x16x32_bf16 v[24:27], a[136:139], a[24:27], v[24:27]\n"
"v_mul_f32_e32 v18, s44, v18\n"
"v_mfma_f32_16x16x32_bf16 v[24:27], a[140:143], a[28:31], v[24:27]\n"
"v_exp_f32_e32 v17, v17\n"
"v_mfma_f32_16x16x32_bf16 v[28:31], a[144:147], a[16:19], 0\n"
"v_mul_f32_e32 v19, s44, v19\n"
"v_cvt_pk_bf16_f32 v32, v16, v17\n"
"v_mfma_f32_16x16x32_bf16 v[28:31], a[148:151], a[20:23], v[28:31]\n"
"v_exp_f32_e32 v18, v18\n"
"v_mfma_f32_16x16x32_bf16 v[28:31], a[152:155], a[24:27], v[28:31]\n"
"v_mul_f32_e32 v20, s44, v20\n"
"v_mfma_f32_16x16x32_bf16 v[28:31], a[156:159], a[28:31], v[28:31]\n"
"s_waitcnt lgkmcnt(12)\n"
"v_exp_f32_e32 v19, v19\n"
"v_mfma_f32_16x16x32_bf16 v[64:67], a[192:195], v[36:39], v[64:67]\n"
"s_waitcnt lgkmcnt(10)\n"
"v_mul_f32_e32 v21, s44, v21\n"
"v_cvt_pk_bf16_f32 v33, v18, v19\n"
"v_mfma_f32_16x16x32_bf16 v[68:71], a[196:199], v[36:39], v[68:71]\n"
"v_exp_f32_e32 v20, v20\n"
"v_mfma_f32_16x16x32_bf16 v[72:75], a[200:203], v[36:39], v[72:75]\n"
"s_waitcnt lgkmcnt(2)\n"
"v_mul_f32_e32 v22, s44, v22\n"
"v_mfma_f32_16x16x32_bf16 v[76:79], a[204:207], v[36:39], v[76:79]\n"
"v_exp_f32_e32 v21, v21\n"
"v_mfma_f32_16x16x32_bf16 v[80:83], a[208:211], v[36:39], v[80:83]\n"
"s_waitcnt lgkmcnt(0)\n"
"v_mul_f32_e32 v23, s44, v23\n"
"v_cvt_pk_bf16_f32 v34, v20, v21\n"
"v_mfma_f32_16x16x32_bf16 v[84:87], a[212:215], v[36:39], v[84:87]\n"
"v_exp_f32_e32 v22, v22\n"
"v_mfma_f32_16x16x32_bf16 v[88:91], a[216:219], v[36:39], v[88:91]\n"
"v_mfma_f32_16x16x32_bf16 v[92:95], a[220:223], v[36:39], v[92:95]\n"
"v_exp_f32_e32 v23, v23\n"
"v_mfma_f32_16x16x32_bf16 a[228:231], a[224:227], v[36:39], a[228:231]\n"
"v_cvt_pk_bf16_f32 v35, v22, v23\n"
"v_mfma_f32_16x16x32_bf16 v[16:19], a[96:99], a[32:35], 0\n"
"v_mul_f32_e32 v24, s44, v24\n"
"v_mul_f32_e32 v25, s44, v25\n"
"v_mfma_f32_16x16x32_bf16 v[16:19], a[100:103], a[36:39], v[16:19]\n"
"v_exp_f32_e32 v24, v24\n"
"v_mfma_f32_16x16x32_bf16 v[16:19], a[104:107], a[40:43], v[16:19]\n"
"v_mul_f32_e32 v26, s44, v26\n"
"v_mfma_f32_16x16x32_bf16 v[16:19], a[108:111], a[44:47], v[16:19]\n"
"v_exp_f32_e32 v25, v25\n"
"v_mfma_f32_16x16x32_bf16 v[20:23], a[112:115], a[32:35], 0\n"
"v_mul_f32_e32 v27, s44, v27\n"
"v_cvt_pk_bf16_f32 v36, v24, v25\n"
"v_accvgpr_read_b32 v10, a228\n"
"v_subrev_u32_e32 v13, s51, v10\n"
"v_rcp_f32_e32 v12, v10\n"
"v_cmp_le_u32_e32 vcc, s52, v13\n"
"v_pk_mul_f32 v[64:65], v[64:65], v[12:13] op_sel_hi:[1,0]\n"
"v_pk_mul_f32 v[66:67], v[66:67], v[12:13] op_sel_hi:[1,0]\n"
"v_pk_mul_f32 v[68:69], v[68:69], v[12:13] op_sel_hi:[1,0]\n"
"v_pk_mul_f32 v[70:71], v[70:71], v[12:13] op_sel_hi:[1,0]\n"
"v_pk_mul_f32 v[72:73], v[72:73], v[12:13] op_sel_hi:[1,0]\n"
"v_pk_mul_f32 v[74:75], v[74:75], v[12:13] op_sel_hi:[1,0]\n"
"v_pk_mul_f32 v[76:77], v[76:77], v[12:13] op_sel_hi:[1,0]\n"
"v_pk_mul_f32 v[78:79], v[78:79], v[12:13] op_sel_hi:[1,0]\n"
"v_pk_mul_f32 v[80:81], v[80:81], v[12:13] op_sel_hi:[1,0]\n"
"v_pk_mul_f32 v[82:83], v[82:83], v[12:13] op_sel_hi:[1,0]\n"
"v_pk_mul_f32 v[84:85], v[84:85], v[12:13] op_sel_hi:[1,0]\n"
"v_pk_mul_f32 v[86:87], v[86:87], v[12:13] op_sel_hi:[1,0]\n"
"v_pk_mul_f32 v[88:89], v[88:89], v[12:13] op_sel_hi:[1,0]\n"
"v_pk_mul_f32 v[90:91], v[90:91], v[12:13] op_sel_hi:[1,0]\n"
"v_pk_mul_f32 v[92:93], v[92:93], v[12:13] op_sel_hi:[1,0]\n"
"v_pk_mul_f32 v[94:95], v[94:95], v[12:13] op_sel_hi:[1,0]\n"
"s_or_b64 s[54:55], s[54:55], vcc\n"
"v_mfma_f32_16x16x32_bf16 v[20:23], a[116:119], a[36:39], v[20:23]\n"
"v_exp_f32_e32 v26, v26\n"
"v_cvt_pk_bf16_f32 v40, v64, v65\n"
"v_cvt_pk_bf16_f32 v41, v66, v67\n"
"v_cvt_pk_bf16_f32 v42, v68, v69\n"
"v_cvt_pk_bf16_f32 v43, v70, v71\n"
"ds_write_b64 v56, v[40:41] offset:0\n"
"ds_write_b64 v57, v[42:43] offset:0\n"
"v_mfma_f32_16x16x32_bf16 v[20:23], a[120:123], a[40:43], v[20:23]\n"
"v_mul_f32_e32 v28, s44, v28\n"
"v_mfma_f32_16x16x32_bf16 v[20:23], a[124:127], a[44:47], v[20:23]\n"
"v_exp_f32_e32 v27, v27\n"
"v_cvt_pk_bf16_f32 v44, v72, v73\n"
"v_cvt_pk_bf16_f32 v45, v74, v75\n"
"v_cvt_pk_bf16_f32 v46, v76, v77\n"
"v_cvt_pk_bf16_f32 v47, v78, v79\n"
"ds_write_b64 v15, v[44:45] offset:0\n"
"ds_write_b64 v60, v[46:47] offset:0\n"
"v_mfma_f32_16x16x32_bf16 v[96:99], a[160:163], v[32:35], v[96:99]\n"
"v_mul_f32_e32 v29, s44, v29\n"
"v_cvt_pk_bf16_f32 v37, v26, v27\n"
"v_mfma_f32_16x16x32_bf16 v[100:103], a[164:167], v[32:35], v[100:103]\n"
"v_exp_f32_e32 v28, v28\n"
"v_cvt_pk_bf16_f32 v48, v80, v81\n"
"v_cvt_pk_bf16_f32 v49, v82, v83\n"
"v_cvt_pk_bf16_f32 v50, v84, v85\n"
"v_cvt_pk_bf16_f32 v51, v86, v87\n"
"ds_write_b64 v56, v[48:49] offset:128\n"
"ds_write_b64 v57, v[50:51] offset:128\n"
"v_mfma_f32_16x16x32_bf16 v[104:107], a[168:171], v[32:35], v[104:107]\n"
"v_mul_f32_e32 v30, s44, v30\n"
"v_mfma_f32_16x16x32_bf16 v[108:111], a[172:175], v[32:35], v[108:111]\n"
"v_exp_f32_e32 v29, v29\n"
"v_cvt_pk_bf16_f32 v52, v88, v89\n"
"v_cvt_pk_bf16_f32 v53, v90, v91\n"
"v_cvt_pk_bf16_f32 v54, v92, v93\n"
"v_cvt_pk_bf16_f32 v55, v94, v95\n"
"ds_write_b64 v15, v[52:53] offset:128\n"
"ds_write_b64 v60, v[54:55] offset:128\n"
"v_mfma_f32_16x16x32_bf16 v[112:115], a[176:179], v[32:35], v[112:115]\n"
"v_mul_f32_e32 v31, s44, v31\n"
"v_cvt_pk_bf16_f32 v38, v28, v29\n"
"v_mfma_f32_16x16x32_bf16 v[116:119], a[180:183], v[32:35], v[116:119]\n"
"v_exp_f32_e32 v30, v30\n"
"ds_read_b128 v[40:43], v58 offset:0\n"
"ds_read_b128 v[44:47], v59 offset:1024\n"
"ds_read_b128 v[48:51], v58 offset:2048\n"
"ds_read_b128 v[52:55], v59 offset:3072\n"
"v_mfma_f32_16x16x32_bf16 v[120:123], a[184:187], v[32:35], v[120:123]\n"
"v_mfma_f32_16x16x32_bf16 v[124:127], a[188:191], v[32:35], v[124:127]\n"
"v_exp_f32_e32 v31, v31\n"
"v_mfma_f32_16x16x32_bf16 a[232:235], a[224:227], v[32:35], a[232:235]\n"
"v_cvt_pk_bf16_f32 v39, v30, v31\n"
"v_mfma_f32_16x16x32_bf16 v[24:27], a[128:131], a[32:35], 0\n"
"v_mul_f32_e32 v16, s44, v16\n"
"v_mul_f32_e32 v17, s44, v17\n"
"v_mfma_f32_16x16x32_bf16 v[24:27], a[132:135], a[36:39], v[24:27]\n"
"v_exp_f32_e32 v16, v16\n"
"v_mfma_f32_16x16x32_bf16 v[24:27], a[136:139], a[40:43], v[24:27]\n"
"v_mul_f32_e32 v18, s44, v18\n"
"s_add_u32 s77, s60, 0\n"
"v_mfma_f32_16x16x32_bf16 v[24:27], a[140:143], a[44:47], v[24:27]\n"
"v_exp_f32_e32 v17, v17\n"
"s_waitcnt lgkmcnt(3)\n"
"buffer_store_dwordx4 v[40:43], v9, s[40:43], s77 offen offset:0  sc1\n"
"v_mfma_f32_16x16x32_bf16 v[28:31], a[144:147], a[32:35], 0\n"
"v_mul_f32_e32 v19, s44, v19\n"
"v_cvt_pk_bf16_f32 v32, v16, v17\n"
"v_mfma_f32_16x16x32_bf16 v[28:31], a[148:151], a[36:39], v[28:31]\n"
"v_exp_f32_e32 v18, v18\n"
"s_waitcnt lgkmcnt(2)\n"
"buffer_store_dwordx4 v[44:47], v9, s[40:43], s77 offen offset:1024  sc1\n"
"v_mfma_f32_16x16x32_bf16 v[28:31], a[152:155], a[40:43], v[28:31]\n"
"v_mul_f32_e32 v20, s44, v20\n"
"v_mfma_f32_16x16x32_bf16 v[28:31], a[156:159], a[44:47], v[28:31]\n"
"v_exp_f32_e32 v19, v19\n"
"s_waitcnt lgkmcnt(1)\n"
"buffer_store_dwordx4 v[48:51], v9, s[40:43], s77 offen offset:2048  sc1\n"
"v_mfma_f32_16x16x32_bf16 v[96:99], a[192:195], v[36:39], v[96:99]\n"
"v_mul_f32_e32 v21, s44, v21\n"
"v_cvt_pk_bf16_f32 v33, v18, v19\n"
"v_mfma_f32_16x16x32_bf16 v[100:103], a[196:199], v[36:39], v[100:103]\n"
"v_exp_f32_e32 v20, v20\n"
"s_waitcnt lgkmcnt(0)\n"
"buffer_store_dwordx4 v[52:55], v9, s[40:43], s77 offen offset:3072  sc1\n"
"v_mfma_f32_16x16x32_bf16 v[104:107], a[200:203], v[36:39], v[104:107]\n"
"v_mul_f32_e32 v22, s44, v22\n"
"v_mfma_f32_16x16x32_bf16 v[108:111], a[204:207], v[36:39], v[108:111]\n"
"v_exp_f32_e32 v21, v21\n"
"v_mfma_f32_16x16x32_bf16 v[112:115], a[208:211], v[36:39], v[112:115]\n"
"v_mul_f32_e32 v23, s44, v23\n"
"v_cvt_pk_bf16_f32 v34, v20, v21\n"
"v_mfma_f32_16x16x32_bf16 v[116:119], a[212:215], v[36:39], v[116:119]\n"
"v_exp_f32_e32 v22, v22\n"
"v_mfma_f32_16x16x32_bf16 v[120:123], a[216:219], v[36:39], v[120:123]\n"
"v_mfma_f32_16x16x32_bf16 v[124:127], a[220:223], v[36:39], v[124:127]\n"
"v_exp_f32_e32 v23, v23\n"
"v_mfma_f32_16x16x32_bf16 a[232:235], a[224:227], v[36:39], a[232:235]\n"
"v_cvt_pk_bf16_f32 v35, v22, v23\n"
"v_mfma_f32_16x16x32_bf16 v[16:19], a[96:99], a[48:51], 0\n"
"v_mul_f32_e32 v24, s44, v24\n"
"v_mul_f32_e32 v25, s44, v25\n"
"v_mfma_f32_16x16x32_bf16 v[16:19], a[100:103], a[52:55], v[16:19]\n"
"v_exp_f32_e32 v24, v24\n"
"v_mfma_f32_16x16x32_bf16 v[16:19], a[104:107], a[56:59], v[16:19]\n"
"v_mul_f32_e32 v26, s44, v26\n"
"v_mfma_f32_16x16x32_bf16 v[16:19], a[108:111], a[60:63], v[16:19]\n"
"v_exp_f32_e32 v25, v25\n"
"v_mfma_f32_16x16x32_bf16 v[20:23], a[112:115], a[48:51], 0\n"
"v_mul_f32_e32 v27, s44, v27\n"
"v_cvt_pk_bf16_f32 v36, v24, v25\n"
"v_accvgpr_read_b32 v10, a232\n"
"v_subrev_u32_e32 v13, s51, v10\n"
"v_rcp_f32_e32 v12, v10\n"
"v_cmp_le_u32_e32 vcc, s52, v13\n"
"v_pk_mul_f32 v[96:97], v[96:97], v[12:13] op_sel_hi:[1,0]\n"
"v_pk_mul_f32 v[98:99], v[98:99], v[12:13] op_sel_hi:[1,0]\n"
"v_pk_mul_f32 v[100:101], v[100:101], v[12:13] op_sel_hi:[1,0]\n"
"v_pk_mul_f32 v[102:103], v[102:103], v[12:13] op_sel_hi:[1,0]\n"
"v_pk_mul_f32 v[104:105], v[104:105], v[12:13] op_sel_hi:[1,0]\n"
"v_pk_mul_f32 v[106:107], v[106:107], v[12:13] op_sel_hi:[1,0]\n"
"v_pk_mul_f32 v[108:109], v[108:109], v[12:13] op_sel_hi:[1,0]\n"
"v_pk_mul_f32 v[110:111], v[110:111], v[12:13] op_sel_hi:[1,0]\n"
"v_pk_mul_f32 v[112:113], v[112:113], v[12:13] op_sel_hi:[1,0]\n"
"v_pk_mul_f32 v[114:115], v[114:115], v[12:13] op_sel_hi:[1,0]\n"
"v_pk_mul_f32 v[116:117], v[116:117], v[12:13] op_sel_hi:[1,0]\n"
"v_pk_mul_f32 v[118:119], v[118:119], v[12:13] op_sel_hi:[1,0]\n"
"v_pk_mul_f32 v[120:121], v[120:121], v[12:13] op_sel_hi:[1,0]\n"
"v_pk_mul_f32 v[122:123], v[122:123], v[12:13] op_sel_hi:[1,0]\n"
"v_pk_mul_f32 v[124:125], v[124:125], v[12:13] op_sel_hi:[1,0]\n"
"v_pk_mul_f32 v[126:127], v[126:127], v[12:13] op_sel_hi:[1,0]\n"
"s_or_b64 s[54:55], s[54:55], vcc\n"
"v_mfma_f32_16x16x32_bf16 v[20:23], a[116:119], a[52:55], v[20:23]\n"
"v_exp_f32_e32 v26, v26\n"
"v_cvt_pk_bf16_f32 v40, v96, v97\n"
"v_cvt_pk_bf16_f32 v41, v98, v99\n"
"v_cvt_pk_bf16_f32 v42, v100, v101\n"
"v_cvt_pk_bf16_f32 v43, v102, v103\n"
"ds_write_b64 v56, v[40:41] offset:4096\n"
"ds_write_b64 v57, v[42:43] offset:4096\n"
"v_mfma_f32_16x16x32_bf16 v[20:23], a[120:123], a[56:59], v[20:23]\n"
"v_mul_f32_e32 v28, s44, v28\n"
"v_mfma_f32_16x16x32_bf16 v[20:23], a[124:127], a[60:63], v[20:23]\n"
"v_exp_f32_e32 v27, v27\n"
"v_cvt_pk_bf16_f32 v44, v104, v105\n"
"v_cvt_pk_bf16_f32 v45, v106, v107\n"
"v_cvt_pk_bf16_f32 v46, v108, v109\n"
"v_cvt_pk_bf16_f32 v47, v110, v111\n"
"ds_write_b64 v15, v[44:45] offset:4096\n"
"ds_write_b64 v60, v[46:47] offset:4096\n"
"v_mfma_f32_16x16x32_bf16 v[128:131], a[160:163], v[32:35], v[128:131]\n"
"v_mul_f32_e32 v29, s44, v29\n"
"v_cvt_pk_bf16_f32 v37, v26, v27\n"
"v_mfma_f32_16x16x32_bf16 v[132:135], a[164:167], v[32:35], v[132:135]\n"
"v_exp_f32_e32 v28, v28\n"
"v_cvt_pk_bf16_f32 v48, v112, v113\n"
"v_cvt_pk_bf16_f32 v49, v114, v115\n"
"v_cvt_pk_bf16_f32 v50, v116, v117\n"
"v_cvt_pk_bf16_f32 v51, v118, v119\n"
"ds_write_b64 v56, v[48:49] offset:4224\n"
"ds_write_b64 v57, v[50:51] offset:4224\n"
"v_mfma_f32_16x16x32_bf16 v[136:139], a[168:171], v[32:35], v[136:139]\n"
"v_mul_f32_e32 v30, s44, v30\n"
"v_mfma_f32_16x16x32_bf16 v[140:143], a[172:175], v[32:35], v[140:143]\n"
"v_exp_f32_e32 v29, v29\n"
"v_cvt_pk_bf16_f32 v52, v120, v121\n"
"v_cvt_pk_bf16_f32 v53, v122, v123\n"
"v_cvt_pk_bf16_f32 v54, v124, v125\n"
"v_cvt_pk_bf16_f32 v55, v126, v127\n"
"ds_write_b64 v15, v[52:53] offset:4224\n"
"ds_write_b64 v60, v[54:55] offset:4224\n"
"v_mfma_f32_16x16x32_bf16 v[144:147], a[176:179], v[32:35], v[144:147]\n"
"v_mul_f32_e32 v31, s44, v31\n"
"v_cvt_pk_bf16_f32 v38, v28, v29\n"
"v_mfma_f32_16x16x32_bf16 v[148:151], a[180:183], v[32:35], v[148:151]\n"
"v_exp_f32_e32 v30, v30\n"
"ds_read_b128 v[40:43], v58 offset:4096\n"
"ds_read_b128 v[44:47], v59 offset:5120\n"
"ds_read_b128 v[48:51], v58 offset:6144\n"
"ds_read_b128 v[52:55], v59 offset:7168\n"
"v_mfma_f32_16x16x32_bf16 v[152:155], a[184:187], v[32:35], v[152:155]\n"
"v_mfma_f32_16x16x32_bf16 v[156:159], a[188:191], v[32:35], v[156:159]\n"
"v_exp_f32_e32 v31, v31\n"
"v_mfma_f32_16x16x32_bf16 a[236:239], a[224:227], v[32:35], a[236:239]\n"
"v_cvt_pk_bf16_f32 v39, v30, v31\n"
"v_mfma_f32_16x16x32_bf16 v[24:27], a[128:131], a[48:51], 0\n"
"v_mul_f32_e32 v16, s44, v16\n"
"v_mul_f32_e32 v17, s44, v17\n"
"v_mfma_f32_16x16x32_bf16 v[24:27], a[132:135], a[52:55], v[24:27]\n"
"v_exp_f32_e32 v16, v16\n"
"v_mfma_f32_16x16x32_bf16 v[24:27], a[136:139], a[56:59], v[24:27]\n"
"v_mul_f32_e32 v18, s44, v18\n"
"s_add_u32 s77, s60, 4096\n"
"v_mfma_f32_16x16x32_bf16 v[24:27], a[140:143], a[60:63], v[24:27]\n"
"v_exp_f32_e32 v17, v17\n"
"s_waitcnt lgkmcnt(3)\n"
"buffer_store_dwordx4 v[40:43], v9, s[40:43], s77 offen offset:0  sc1\n"
"v_mfma_f32_16x16x32_bf16 v[28:31], a[144:147], a[48:51], 0\n"
"v_mul_f32_e32 v19, s44, v19\n"
"v_cvt_pk_bf16_f32 v32, v16, v17\n"
"v_mfma_f32_16x16x32_bf16 v[28:31], a[148:151], a[52:55], v[28:31]\n"
"v_exp_f32_e32 v18, v18\n"
"s_waitcnt lgkmcnt(2)\n"
"buffer_store_dwordx4 v[44:47], v9, s[40:43], s77 offen offset:1024  sc1\n"
"v_mfma_f32_16x16x32_bf16 v[28:31], a[152:155], a[56:59], v[28:31]\n"
"v_mul_f32_e32 v20, s44, v20\n"
"v_mfma_f32_16x16x32_bf16 v[28:31], a[156:159], a[60:63], v[28:31]\n"
"v_exp_f32_e32 v19, v19\n"
"s_waitcnt lgkmcnt(1)\n"
"buffer_store_dwordx4 v[48:51], v9, s[40:43], s77 offen offset:2048  sc1\n"
"v_mfma_f32_16x16x32_bf16 v[128:131], a[192:195], v[36:39], v[128:131]\n"
"v_mul_f32_e32 v21, s44, v21\n"
"v_cvt_pk_bf16_f32 v33, v18, v19\n"
"v_mfma_f32_16x16x32_bf16 v[132:135], a[196:199], v[36:39], v[132:135]\n"
"v_exp_f32_e32 v20, v20\n"
"s_waitcnt lgkmcnt(0)\n"
"buffer_store_dwordx4 v[52:55], v9, s[40:43], s77 offen offset:3072  sc1\n"
"v_mfma_f32_16x16x32_bf16 v[136:139], a[200:203], v[36:39], v[136:139]\n"
"v_mul_f32_e32 v22, s44, v22\n"
"v_mfma_f32_16x16x32_bf16 v[140:143], a[204:207], v[36:39], v[140:143]\n"
"v_exp_f32_e32 v21, v21\n"
"v_mfma_f32_16x16x32_bf16 v[144:147], a[208:211], v[36:39], v[144:147]\n"
"v_mul_f32_e32 v23, s44, v23\n"
"v_cvt_pk_bf16_f32 v34, v20, v21\n"
"v_mfma_f32_16x16x32_bf16 v[148:151], a[212:215], v[36:39], v[148:151]\n"
"v_exp_f32_e32 v22, v22\n"
"v_mfma_f32_16x16x32_bf16 v[152:155], a[216:219], v[36:39], v[152:155]\n"
"v_mfma_f32_16x16x32_bf16 v[156:159], a[220:223], v[36:39], v[156:159]\n"
"v_exp_f32_e32 v23, v23\n"
"v_mfma_f32_16x16x32_bf16 a[236:239], a[224:227], v[36:39], a[236:239]\n"
"v_cvt_pk_bf16_f32 v35, v22, v23\n"
"v_mfma_f32_16x16x32_bf16 v[16:19], a[96:99], a[64:67], 0\n"
"v_mul_f32_e32 v24, s44, v24\n"
"v_mul_f32_e32 v25, s44, v25\n"
"v_mfma_f32_16x16x32_bf16 v[16:19], a[100:103], a[68:71], v[16:19]\n"
"v_exp_f32_e32 v24, v24\n"
"v_mfma_f32_16x16x32_bf16 v[16:19], a[104:107], a[72:75], v[16:19]\n"
"v_mul_f32_e32 v26, s44, v26\n"
"v_mfma_f32_16x16x32_bf16 v[16:19], a[108:111], a[76:79], v[16:19]\n"
"v_exp_f32_e32 v25, v25\n"
"v_mfma_f32_16x16x32_bf16 v[20:23], a[112:115], a[64:67], 0\n"
"v_mul_f32_e32 v27, s44, v27\n"
"v_cvt_pk_bf16_f32 v36, v24, v25\n"
"v_accvgpr_read_b32 v10, a236\n"
"v_subrev_u32_e32 v13, s51, v10\n"
"v_rcp_f32_e32 v12, v10\n"
"v_cmp_le_u32_e32 vcc, s52, v13\n"
"v_pk_mul_f32 v[128:129], v[128:129], v[12:13] op_sel_hi:[1,0]\n"
"v_pk_mul_f32 v[130:131], v[130:131], v[12:13] op_sel_hi:[1,0]\n"
"v_pk_mul_f32 v[132:133], v[132:133], v[12:13] op_sel_hi:[1,0]\n"
"v_pk_mul_f32 v[134:135], v[134:135], v[12:13] op_sel_hi:[1,0]\n"
"v_pk_mul_f32 v[136:137], v[136:137], v[12:13] op_sel_hi:[1,0]\n"
"v_pk_mul_f32 v[138:139], v[138:139], v[12:13] op_sel_hi:[1,0]\n"
"v_pk_mul_f32 v[140:141], v[140:141], v[12:13] op_sel_hi:[1,0]\n"
"v_pk_mul_f32 v[142:143], v[142:143], v[12:13] op_sel_hi:[1,0]\n"
"v_pk_mul_f32 v[144:145], v[144:145], v[12:13] op_sel_hi:[1,0]\n"
"v_pk_mul_f32 v[146:147], v[146:147], v[12:13] op_sel_hi:[1,0]\n"
"v_pk_mul_f32 v[148:149], v[148:149], v[12:13] op_sel_hi:[1,0]\n"
"v_pk_mul_f32 v[150:151], v[150:151], v[12:13] op_sel_hi:[1,0]\n"
"v_pk_mul_f32 v[152:153], v[152:153], v[12:13] op_sel_hi:[1,0]\n"
"v_pk_mul_f32 v[154:155], v[154:155], v[12:13] op_sel_hi:[1,0]\n"
"v_pk_mul_f32 v[156:157], v[156:157], v[12:13] op_sel_hi:[1,0]\n"
"v_pk_mul_f32 v[158:159], v[158:159], v[12:13] op_sel_hi:[1,0]\n"
"s_or_b64 s[54:55], s[54:55], vcc\n"
"v_mfma_f32_16x16x32_bf16 v[20:23], a[116:119], a[68:71], v[20:23]\n"
"v_exp_f32_e32 v26, v26\n"
"v_cvt_pk_bf16_f32 v40, v128, v129\n"
"v_cvt_pk_bf16_f32 v41, v130, v131\n"
"v_cvt_pk_bf16_f32 v42, v132, v133\n"
"v_cvt_pk_bf16_f32 v43, v134, v135\n"
"ds_write_b64 v56, v[40:41] offset:8192\n"
"ds_write_b64 v57, v[42:43] offset:8192\n"
"v_mfma_f32_16x16x32_bf16 v[20:23], a[120:123], a[72:75], v[20:23]\n"
"v_mul_f32_e32 v28, s44, v28\n"
"v_mfma_f32_16x16x32_bf16 v[20:23], a[124:127], a[76:79], v[20:23]\n"
"v_exp_f32_e32 v27, v27\n"
"v_cvt_pk_bf16_f32 v44, v136, v137\n"
"v_cvt_pk_bf16_f32 v45, v138, v139\n"
"v_cvt_pk_bf16_f32 v46, v140, v141\n"
"v_cvt_pk_bf16_f32 v47, v142, v143\n"
"ds_write_b64 v15, v[44:45] offset:8192\n"
"ds_write_b64 v60, v[46:47] offset:8192\n"
"v_mfma_f32_16x16x32_bf16 v[160:163], a[160:163], v[32:35], v[160:163]\n"
"v_mul_f32_e32 v29, s44, v29\n"
"v_cvt_pk_bf16_f32 v37, v26, v27\n"
"v_mfma_f32_16x16x32_bf16 v[164:167], a[164:167], v[32:35], v[164:167]\n"
"v_exp_f32_e32 v28, v28\n"
"v_cvt_pk_bf16_f32 v48, v144, v145\n"
"v_cvt_pk_bf16_f32 v49, v146, v147\n"
"v_cvt_pk_bf16_f32 v50, v148, v149\n"
"v_cvt_pk_bf16_f32 v51, v150, v151\n"
"ds_write_b64 v56, v[48:49] offset:8320\n"
"ds_write_b64 v57, v[50:51] offset:8320\n"
"v_mfma_f32_16x16x32_bf16 v[168:171], a[168:171], v[32:35], v[168:171]\n"
"v_mul_f32_e32 v30, s44, v30\n"
"v_mfma_f32_16x16x32_bf16 v[172:175], a[172:175], v[32:35], v[172:175]\n"
"v_exp_f32_e32 v29, v29\n"
"v_cvt_pk_bf16_f32 v52, v152, v153\n"
"v_cvt_pk_bf16_f32 v53, v154, v155\n"
"v_cvt_pk_bf16_f32 v54, v156, v157\n"
"v_cvt_pk_bf16_f32 v55, v158, v159\n"
"ds_write_b64 v15, v[52:53] offset:8320\n"
"ds_write_b64 v60, v[54:55] offset:8320\n"
"v_mfma_f32_16x16x32_bf16 v[176:179], a[176:179], v[32:35], v[176:179]\n"
"v_mul_f32_e32 v31, s44, v31\n"
"v_cvt_pk_bf16_f32 v38, v28, v29\n"
"v_mfma_f32_16x16x32_bf16 v[180:183], a[180:183], v[32:35], v[180:183]\n"
"v_exp_f32_e32 v30, v30\n"
"ds_read_b128 v[40:43], v58 offset:8192\n"
"ds_read_b128 v[44:47], v59 offset:9216\n"
"ds_read_b128 v[48:51], v58 offset:10240\n"
"ds_read_b128 v[52:55], v59 offset:11264\n"
"v_mfma_f32_16x16x32_bf16 v[184:187], a[184:187], v[32:35], v[184:187]\n"
"v_mfma_f32_16x16x32_bf16 v[188:191], a[188:191], v[32:35], v[188:191]\n"
"v_exp_f32_e32 v31, v31\n"
"v_mfma_f32_16x16x32_bf16 a[240:243], a[224:227], v[32:35], a[240:243]\n"
"v_cvt_pk_bf16_f32 v39, v30, v31\n"
"v_mfma_f32_16x16x32_bf16 v[24:27], a[128:131], a[64:67], 0\n"
"v_mul_f32_e32 v16, s44, v16\n"
"v_mul_f32_e32 v17, s44, v17\n"
"v_mfma_f32_16x16x32_bf16 v[24:27], a[132:135], a[68:71], v[24:27]\n"
"v_exp_f32_e32 v16, v16\n"
"v_mfma_f32_16x16x32_bf16 v[24:27], a[136:139], a[72:75], v[24:27]\n"
"v_mul_f32_e32 v18, s44, v18\n"
"s_add_u32 s77, s60, 8192\n"
"v_mfma_f32_16x16x32_bf16 v[24:27], a[140:143], a[76:79], v[24:27]\n"
"v_exp_f32_e32 v17, v17\n"
"s_waitcnt lgkmcnt(3)\n"
"buffer_store_dwordx4 v[40:43], v9, s[40:43], s77 offen offset:0  sc1\n"
"v_mfma_f32_16x16x32_bf16 v[28:31], a[144:147], a[64:67], 0\n"
"v_mul_f32_e32 v19, s44, v19\n"
"v_cvt_pk_bf16_f32 v32, v16, v17\n"
"v_mfma_f32_16x16x32_bf16 v[28:31], a[148:151], a[68:71], v[28:31]\n"
"v_exp_f32_e32 v18, v18\n"
"s_waitcnt lgkmcnt(2)\n"
"buffer_store_dwordx4 v[44:47], v9, s[40:43], s77 offen offset:1024  sc1\n"
"v_mfma_f32_16x16x32_bf16 v[28:31], a[152:155], a[72:75], v[28:31]\n"
"v_mul_f32_e32 v20, s44, v20\n"
"v_mfma_f32_16x16x32_bf16 v[28:31], a[156:159], a[76:79], v[28:31]\n"
"v_exp_f32_e32 v19, v19\n"
"s_waitcnt lgkmcnt(1)\n"
"buffer_store_dwordx4 v[48:51], v9, s[40:43], s77 offen offset:2048  sc1\n"
"v_mfma_f32_16x16x32_bf16 v[160:163], a[192:195], v[36:39], v[160:163]\n"
"v_mul_f32_e32 v21, s44, v21\n"
"v_cvt_pk_bf16_f32 v33, v18, v19\n"
"v_mfma_f32_16x16x32_bf16 v[164:167], a[196:199], v[36:39], v[164:167]\n"
"v_exp_f32_e32 v20, v20\n"
"s_waitcnt lgkmcnt(0)\n"
"buffer_store_dwordx4 v[52:55], v9, s[40:43], s77 offen offset:3072  sc1\n"
"v_mfma_f32_16x16x32_bf16 v[168:171], a[200:203], v[36:39], v[168:171]\n"
"v_mul_f32_e32 v22, s44, v22\n"
"v_mfma_f32_16x16x32_bf16 v[172:175], a[204:207], v[36:39], v[172:175]\n"
"v_exp_f32_e32 v21, v21\n"
"v_mfma_f32_16x16x32_bf16 v[176:179], a[208:211], v[36:39], v[176:179]\n"
"v_mul_f32_e32 v23, s44, v23\n"
"v_cvt_pk_bf16_f32 v34, v20, v21\n"
"v_mfma_f32_16x16x32_bf16 v[180:183], a[212:215], v[36:39], v[180:183]\n"
"v_exp_f32_e32 v22, v22\n"
"v_mfma_f32_16x16x32_bf16 v[184:187], a[216:219], v[36:39], v[184:187]\n"
"v_mfma_f32_16x16x32_bf16 v[188:191], a[220:223], v[36:39], v[188:191]\n"
"v_exp_f32_e32 v23, v23\n"
"v_mfma_f32_16x16x32_bf16 a[240:243], a[224:227], v[36:39], a[240:243]\n"
"v_cvt_pk_bf16_f32 v35, v22, v23\n"
"v_mul_f32_e32 v24, s44, v24\n"
"v_mul_f32_e32 v25, s44, v25\n"
"v_exp_f32_e32 v24, v24\n"
"v_mul_f32_e32 v26, s44, v26\n"
"v_exp_f32_e32 v25, v25\n"
"v_mul_f32_e32 v27, s44, v27\n"
"v_cvt_pk_bf16_f32 v36, v24, v25\n"
"v_accvgpr_read_b32 v10, a240\n"
"v_subrev_u32_e32 v13, s51, v10\n"
"v_rcp_f32_e32 v12, v10\n"
"v_cmp_le_u32_e32 vcc, s52, v13\n"
"v_pk_mul_f32 v[160:161], v[160:161], v[12:13] op_sel_hi:[1,0]\n"
"v_pk_mul_f32 v[162:163], v[162:163], v[12:13] op_sel_hi:[1,0]\n"
"v_pk_mul_f32 v[164:165], v[164:165], v[12:13] op_sel_hi:[1,0]\n"
"v_pk_mul_f32 v[166:167], v[166:167], v[12:13] op_sel_hi:[1,0]\n"
"v_pk_mul_f32 v[168:169], v[168:169], v[12:13] op_sel_hi:[1,0]\n"
"v_pk_mul_f32 v[170:171], v[170:171], v[12:13] op_sel_hi:[1,0]\n"
"v_pk_mul_f32 v[172:173], v[172:173], v[12:13] op_sel_hi:[1,0]\n"
"v_pk_mul_f32 v[174:175], v[174:175], v[12:13] op_sel_hi:[1,0]\n"
"v_pk_mul_f32 v[176:177], v[176:177], v[12:13] op_sel_hi:[1,0]\n"
"v_pk_mul_f32 v[178:179], v[178:179], v[12:13] op_sel_hi:[1,0]\n"
"v_pk_mul_f32 v[180:181], v[180:181], v[12:13] op_sel_hi:[1,0]\n"
"v_pk_mul_f32 v[182:183], v[182:183], v[12:13] op_sel_hi:[1,0]\n"
"v_pk_mul_f32 v[184:185], v[184:185], v[12:13] op_sel_hi:[1,0]\n"
"v_pk_mul_f32 v[186:187], v[186:187], v[12:13] op_sel_hi:[1,0]\n"
"v_pk_mul_f32 v[188:189], v[188:189], v[12:13] op_sel_hi:[1,0]\n"
"v_pk_mul_f32 v[190:191], v[190:191], v[12:13] op_sel_hi:[1,0]\n"
"s_or_b64 s[54:55], s[54:55], vcc\n"
"v_exp_f32_e32 v26, v26\n"
"v_cvt_pk_bf16_f32 v40, v160, v161\n"
"v_cvt_pk_bf16_f32 v41, v162, v163\n"
"v_cvt_pk_bf16_f32 v42, v164, v165\n"
"v_cvt_pk_bf16_f32 v43, v166, v167\n"
"ds_write_b64 v56, v[40:41] offset:12288\n"
"ds_write_b64 v57, v[42:43] offset:12288\n"
"v_mul_f32_e32 v28, s44, v28\n"
"v_exp_f32_e32 v27, v27\n"
"v_cvt_pk_bf16_f32 v44, v168, v169\n"
"v_cvt_pk_bf16_f32 v45, v170, v171\n"
"v_cvt_pk_bf16_f32 v46, v172, v173\n"
"v_cvt_pk_bf16_f32 v47, v174, v175\n"
"ds_write_b64 v15, v[44:45] offset:12288\n"
"ds_write_b64 v60, v[46:47] offset:12288\n"
"v_mfma_f32_16x16x32_bf16 v[192:195], a[160:163], v[32:35], v[192:195]\n"
"v_mul_f32_e32 v29, s44, v29\n"
"v_cvt_pk_bf16_f32 v37, v26, v27\n"
"v_mfma_f32_16x16x32_bf16 v[196:199], a[164:167], v[32:35], v[196:199]\n"
"v_exp_f32_e32 v28, v28\n"
"v_cvt_pk_bf16_f32 v48, v176, v177\n"
"v_cvt_pk_bf16_f32 v49, v178, v179\n"
"v_cvt_pk_bf16_f32 v50, v180, v181\n"
"v_cvt_pk_bf16_f32 v51, v182, v183\n"
"ds_write_b64 v56, v[48:49] offset:12416\n"
"ds_write_b64 v57, v[50:51] offset:12416\n"
"v_mfma_f32_16x16x32_bf16 v[200:203], a[168:171], v[32:35], v[200:203]\n"
"v_mul_f32_e32 v30, s44, v30\n"
"v_mfma_f32_16x16x32_bf16 v[204:207], a[172:175], v[32:35], v[204:207]\n"
"v_exp_f32_e32 v29, v29\n"
"v_cvt_pk_bf16_f32 v52, v184, v185\n"
"v_cvt_pk_bf16_f32 v53, v186, v187\n"
"v_cvt_pk_bf16_f32 v54, v188, v189\n"
"v_cvt_pk_bf16_f32 v55, v190, v191\n"
"ds_write_b64 v15, v[52:53] offset:12416\n"
"ds_write_b64 v60, v[54:55] offset:12416\n"
"v_mfma_f32_16x16x32_bf16 v[208:211], a[176:179], v[32:35], v[208:211]\n"
"v_mul_f32_e32 v31, s44, v31\n"
"v_cvt_pk_bf16_f32 v38, v28, v29\n"
"v_mfma_f32_16x16x32_bf16 v[212:215], a[180:183], v[32:35], v[212:215]\n"
"v_exp_f32_e32 v30, v30\n"
"ds_read_b128 v[40:43], v58 offset:12288\n"
"ds_read_b128 v[44:47], v59 offset:13312\n"
"ds_read_b128 v[48:51], v58 offset:14336\n"
"ds_read_b128 v[52:55], v59 offset:15360\n"
"v_mfma_f32_16x16x32_bf16 v[216:219], a[184:187], v[32:35], v[216:219]\n"
"v_mfma_f32_16x16x32_bf16 v[220:223], a[188:191], v[32:35], v[220:223]\n"
"v_exp_f32_e32 v31, v31\n"
"v_mfma_f32_16x16x32_bf16 a[244:247], a[224:227], v[32:35], a[244:247]\n"
"v_cvt_pk_bf16_f32 v39, v30, v31\n"
"s_add_u32 s77, s60, 12288\n"
"s_waitcnt lgkmcnt(3)\n"
"buffer_store_dwordx4 v[40:43], v9, s[40:43], s77 offen offset:0  sc1\n"
"s_waitcnt lgkmcnt(2)\n"
"buffer_store_dwordx4 v[44:47], v9, s[40:43], s77 offen offset:1024  sc1\n"
"s_waitcnt lgkmcnt(1)\n"
"buffer_store_dwordx4 v[48:51], v9, s[40:43], s77 offen offset:2048  sc1\n"
"v_mfma_f32_16x16x32_bf16 v[192:195], a[192:195], v[36:39], v[192:195]\n"
"v_mfma_f32_16x16x32_bf16 v[196:199], a[196:199], v[36:39], v[196:199]\n"
"s_waitcnt lgkmcnt(0)\n"
"buffer_store_dwordx4 v[52:55], v9, s[40:43], s77 offen offset:3072  sc1\n"
"v_mfma_f32_16x16x32_bf16 v[200:203], a[200:203], v[36:39], v[200:203]\n"
"v_mfma_f32_16x16x32_bf16 v[204:207], a[204:207], v[36:39], v[204:207]\n"
"v_mfma_f32_16x16x32_bf16 v[208:211], a[208:211], v[36:39], v[208:211]\n"
"v_mfma_f32_16x16x32_bf16 v[212:215], a[212:215], v[36:39], v[212:215]\n"
"v_mfma_f32_16x16x32_bf16 v[216:219], a[216:219], v[36:39], v[216:219]\n"
"v_mfma_f32_16x16x32_bf16 v[220:223], a[220:223], v[36:39], v[220:223]\n"
"v_mfma_f32_16x16x32_bf16 a[244:247], a[224:227], v[36:39], a[244:247]\n"
"s_nop 7\n"
"v_accvgpr_read_b32 v10, a244\n"
"v_subrev_u32_e32 v13, s51, v10\n"
"v_rcp_f32_e32 v12, v10\n"
"v_cmp_le_u32_e32 vcc, s52, v13\n"
"v_pk_mul_f32 v[192:193], v[192:193], v[12:13] op_sel_hi:[1,0]\n"
"v_pk_mul_f32 v[194:195], v[194:195], v[12:13] op_sel_hi:[1,0]\n"
"v_pk_mul_f32 v[196:197], v[196:197], v[12:13] op_sel_hi:[1,0]\n"
"v_pk_mul_f32 v[198:199], v[198:199], v[12:13] op_sel_hi:[1,0]\n"
"v_pk_mul_f32 v[200:201], v[200:201], v[12:13] op_sel_hi:[1,0]\n"
"v_pk_mul_f32 v[202:203], v[202:203], v[12:13] op_sel_hi:[1,0]\n"
"v_pk_mul_f32 v[204:205], v[204:205], v[12:13] op_sel_hi:[1,0]\n"
"v_pk_mul_f32 v[206:207], v[206:207], v[12:13] op_sel_hi:[1,0]\n"
"v_pk_mul_f32 v[208:209], v[208:209], v[12:13] op_sel_hi:[1,0]\n"
"v_pk_mul_f32 v[210:211], v[210:211], v[12:13] op_sel_hi:[1,0]\n"
"v_pk_mul_f32 v[212:213], v[212:213], v[12:13] op_sel_hi:[1,0]\n"
"v_pk_mul_f32 v[214:215], v[214:215], v[12:13] op_sel_hi:[1,0]\n"
"v_pk_mul_f32 v[216:217], v[216:217], v[12:13] op_sel_hi:[1,0]\n"
"v_pk_mul_f32 v[218:219], v[218:219], v[12:13] op_sel_hi:[1,0]\n"
"v_pk_mul_f32 v[220:221], v[220:221], v[12:13] op_sel_hi:[1,0]\n"
"v_pk_mul_f32 v[222:223], v[222:223], v[12:13] op_sel_hi:[1,0]\n"
"s_or_b64 s[54:55], s[54:55], vcc\n"
"v_cvt_pk_bf16_f32 v40, v192, v193\n"
"v_cvt_pk_bf16_f32 v41, v194, v195\n"
"v_cvt_pk_bf16_f32 v42, v196, v197\n"
"v_cvt_pk_bf16_f32 v43, v198, v199\n"
"ds_write_b64 v56, v[40:41] offset:16384\n"
"ds_write_b64 v57, v[42:43] offset:16384\n"
"v_cvt_pk_bf16_f32 v44, v200, v201\n"
"v_cvt_pk_bf16_f32 v45, v202, v203\n"
"v_cvt_pk_bf16_f32 v46, v204, v205\n"
"v_cvt_pk_bf16_f32 v47, v206, v207\n"
"ds_write_b64 v15, v[44:45] offset:16384\n"
"ds_write_b64 v60, v[46:47] offset:16384\n"
"v_cvt_pk_bf16_f32 v48, v208, v209\n"
"v_cvt_pk_bf16_f32 v49, v210, v211\n"
"v_cvt_pk_bf16_f32 v50, v212, v213\n"
"v_cvt_pk_bf16_f32 v51, v214, v215\n"
"ds_write_b64 v56, v[48:49] offset:16512\n"
"ds_write_b64 v57, v[50:51] offset:16512\n"
"v_cvt_pk_bf16_f32 v52, v216, v217\n"
"v_cvt_pk_bf16_f32 v53, v218, v219\n"
"v_cvt_pk_bf16_f32 v54, v220, v221\n"
"v_cvt_pk_bf16_f32 v55, v222, v223\n"
"ds_write_b64 v15, v[52:53] offset:16512\n"
"ds_write_b64 v60, v[54:55] offset:16512\n"
"ds_read_b128 v[40:43], v58 offset:16384\n"
"ds_read_b128 v[44:47], v59 offset:17408\n"
"ds_read_b128 v[48:51], v58 offset:18432\n"
"ds_read_b128 v[52:55], v59 offset:19456\n"
"s_add_u32 s77, s60, 16384\n"
"s_waitcnt lgkmcnt(3)\n"
"buffer_store_dwordx4 v[40:43], v9, s[40:43], s77 offen offset:0  sc1\n"
"s_waitcnt lgkmcnt(2)\n"
"buffer_store_dwordx4 v[44:47], v9, s[40:43], s77 offen offset:1024  sc1\n"
"s_waitcnt lgkmcnt(1)\n"
"buffer_store_dwordx4 v[48:51], v9, s[40:43], s77 offen offset:2048  sc1\n"
"s_waitcnt lgkmcnt(0)\n"
"buffer_store_dwordx4 v[52:55], v9, s[40:43], s77 offen offset:3072  sc1\n"
"s_cmp_lg_u64 s[54:55], 0\n"
"s_cselect_b32 s57, s56, 0\n"
"s_or_b32 s53, s53, s57\n"
"s_lshl_b32 s56, s56, 1\n"
"s_mov_b64 s[54:55], 0\n"
"s_branch L_end%=\n"
"L_E6%=:\n"
"v_mfma_f32_16x16x32_bf16 v[16:19], a[96:99], a[0:3], 0\n"
"v_mul_f32_e32 v24, s44, v24\n"
"v_mul_f32_e32 v25, s44, v25\n"
"v_mfma_f32_16x16x32_bf16 v[16:19], a[100:103], a[4:7], v[16:19]\n"
"s_waitcnt vmcnt(13)\n"
"s_barrier\n"
"v_exp_f32_e32 v24, v24\n"
"v_mfma_f32_16x16x32_bf16 v[16:19], a[104:107], a[8:11], v[16:19]\n"
"ds_read_b128 a[128:131], v1 offset:24576\n"
"ds_read_b128 a[140:143], v1 offset:26112\n"
"v_mul_f32_e32 v26, s44, v26\n"
"v_mfma_f32_16x16x32_bf16 v[16:19], a[108:111], a[12:15], v[16:19]\n"
"ds_read_b128 a[144:147], v2 offset:24576\n"
"v_exp_f32_e32 v25, v25\n"
"v_mfma_f32_16x16x32_bf16 v[20:23], a[112:115], a[0:3], 0\n"
"ds_read_b128 a[148:151], v2 offset:25088\n"
"v_mul_f32_e32 v27, s44, v27\n"
"v_cvt_pk_bf16_f32 v36, v24, v25\n"
"v_mfma_f32_16x16x32_bf16 v[20:23], a[116:119], a[4:7], v[20:23]\n"
"ds_read_b128 a[152:155], v2 offset:25600\n"
"v_exp_f32_e32 v26, v26\n"
"v_mfma_f32_16x16x32_bf16 v[20:23], a[120:123], a[8:11], v[20:23]\n"
"ds_read_b128 a[132:135], v1 offset:25088\n"
"ds_read_b128 a[156:159], v2 offset:26112\n"
"v_mul_f32_e32 v28, s44, v28\n"
"v_mfma_f32_16x16x32_bf16 v[20:23], a[124:127], a[12:15], v[20:23]\n"
"v_exp_f32_e32 v27, v27\n"
"v_mfma_f32_16x16x32_bf16 v[192:195], a[192:195], v[32:35], v[192:195]\n"
"v_mul_f32_e32 v29, s44, v29\n"
"v_cvt_pk_bf16_f32 v37, v26, v27\n"
"v_mfma_f32_16x16x32_bf16 v[196:199], a[196:199], v[32:35], v[196:199]\n"
"v_exp_f32_e32 v28, v28\n"
"v_mfma_f32_16x16x32_bf16 v[200:203], a[200:203], v[32:35], v[200:203]\n"
"ds_read_b128 a[136:139], v1 offset:25600\n"
"v_mul_f32_e32 v30, s44, v30\n"
"v_mfma_f32_16x16x32_bf16 v[204:207], a[204:207], v[32:35], v[204:207]\n"
"v_exp_f32_e32 v29, v29\n"
"v_mfma_f32_16x16x32_bf16 v[208:211], a[208:211], v[32:35], v[208:211]\n"
"v_mul_f32_e32 v31, s44, v31\n"
"v_cvt_pk_bf16_f32 v38, v28, v29\n"
"v_mfma_f32_16x16x32_bf16 v[212:215], a[212:215], v[32:35], v[212:215]\n"
"v_exp_f32_e32 v30, v30\n"
"v_mfma_f32_16x16x32_bf16 v[216:219], a[216:219], v[32:35], v[216:219]\n"
"v_mfma_f32_16x16x32_bf16 v[220:223], a[220:223], v[32:35], v[220:223]\n"
"v_exp_f32_e32 v31, v31\n"
"v_mfma_f32_16x16x32_bf16 a[244:247], a[224:227], v[32:35], a[244:247]\n"
"s_waitcnt lgkmcnt(7)\n"
"v_cvt_pk_bf16_f32 v39, v30, v31\n"
"v_mfma_f32_16x16x32_bf16 v[24:27], a[128:131], a[0:3], 0\n"
"s_waitcnt lgkmcnt(2)\n"
"v_mul_f32_e32 v16, s44, v16\n"
"v_mul_f32_e32 v17, s44, v17\n"
"v_mfma_f32_16x16x32_bf16 v[24:27], a[132:135], a[4:7], v[24:27]\n"
"s_waitcnt lgkmcnt(0)\n"
"v_exp_f32_e32 v16, v16\n"
"v_mfma_f32_16x16x32_bf16 v[24:27], a[136:139], a[8:11], v[24:27]\n"
"v_mul_f32_e32 v18, s44, v18\n"
"v_mfma_f32_16x16x32_bf16 v[24:27], a[140:143], a[12:15], v[24:27]\n"
"v_exp_f32_e32 v17, v17\n"
"v_mfma_f32_16x16x32_bf16 v[28:31], a[144:147], a[0:3], 0\n"
"v_mul_f32_e32 v19, s44, v19\n"
"v_cvt_pk_bf16_f32 v32, v16, v17\n"
"v_mfma_f32_16x16x32_bf16 v[28:31], a[148:151], a[4:7], v[28:31]\n"
"v_exp_f32_e32 v18, v18\n"
"v_mfma_f32_16x16x32_bf16 v[28:31], a[152:155], a[8:11], v[28:31]\n"
"v_mul_f32_e32 v20, s44, v20\n"
"v_mfma_f32_16x16x32_bf16 v[28:31], a[156:159], a[12:15], v[28:31]\n"
"v_exp_f32_e32 v19, v19\n"
"v_mfma_f32_16x16x32_bf16 v[224:227], a[192:195], v[36:39], v[224:227]\n"
"v_mul_f32_e32 v21, s44, v21\n"
"v_cvt_pk_bf16_f32 v33, v18, v19\n"
"v_mfma_f32_16x16x32_bf16 v[228:231], a[196:199], v[36:39], v[228:231]\n"
"v_exp_f32_e32 v20, v20\n"
"v_mfma_f32_16x16x32_bf16 v[232:235], a[200:203], v[36:39], v[232:235]\n"
"ds_read_b64_tr_b16 a[194:195], v4 offset:24576\n"
"v_mul_f32_e32 v22, s44, v22\n"
"v_mfma_f32_16x16x32_bf16 v[236:239], a[204:207], v[36:39], v[236:239]\n"
"ds_read_b64_tr_b16 a[200:201], v3 offset:25088\n"
"v_exp_f32_e32 v21, v21\n"
"v_mfma_f32_16x16x32_bf16 v[240:243], a[208:211], v[36:39], v[240:243]\n"
"ds_read_b64_tr_b16 a[202:203], v4 offset:25088\n"
"v_mul_f32_e32 v23, s44, v23\n"
"v_cvt_pk_bf16_f32 v34, v20, v21\n"
"v_mfma_f32_16x16x32_bf16 v[244:247], a[212:215], v[36:39], v[244:247]\n"
"v_exp_f32_e32 v22, v22\n"
"v_mfma_f32_16x16x32_bf16 v[248:251], a[216:219], v[36:39], v[248:251]\n"
"ds_read_b64_tr_b16 a[192:193], v3 offset:24576\n"
"ds_read_b64_tr_b16 a[196:197], v11 offset:24576\n"
"v_mfma_f32_16x16x32_bf16 v[252:255], a[220:223], v[36:39], v[252:255]\n"
"v_exp_f32_e32 v23, v23\n"
"v_mfma_f32_16x16x32_bf16 a[248:251], a[224:227], v[36:39], a[248:251]\n"
"ds_read_b64_tr_b16 a[198:199], v14 offset:24576\n"
"v_cvt_pk_bf16_f32 v35, v22, v23\n"
"v_mfma_f32_16x16x32_bf16 v[16:19], a[96:99], a[16:19], 0\n"
"ds_read_b64_tr_b16 a[216:217], v3 offset:26112\n"
"v_mul_f32_e32 v24, s44, v24\n"
"v_mul_f32_e32 v25, s44, v25\n"
"v_mfma_f32_16x16x32_bf16 v[16:19], a[100:103], a[20:23], v[16:19]\n"
"ds_read_b64_tr_b16 a[218:219], v4 offset:26112\n"
"v_exp_f32_e32 v24, v24\n"
"v_mfma_f32_16x16x32_bf16 v[16:19], a[104:107], a[24:27], v[16:19]\n"
"ds_read_b64_tr_b16 a[206:207], v14 offset:25088\n"
"ds_read_b64_tr_b16 a[220:221], v11 offset:26112\n"
"v_mul_f32_e32 v26, s44, v26\n"
"v_mfma_f32_16x16x32_bf16 v[16:19], a[108:111], a[28:31], v[16:19]\n"
"ds_read_b64_tr_b16 a[222:223], v14 offset:26112\n"
"v_exp_f32_e32 v25, v25\n"
"v_mfma_f32_16x16x32_bf16 v[20:23], a[112:115], a[16:19], 0\n"
"v_mul_f32_e32 v27, s44, v27\n"
"v_cvt_pk_bf16_f32 v36, v24, v25\n"
"v_mfma_f32_16x16x32_bf16 v[20:23], a[116:119], a[20:23], v[20:23]\n"
"v_exp_f32_e32 v26, v26\n"
"v_mfma_f32_16x16x32_bf16 v[20:23], a[120:123], a[24:27], v[20:23]\n"
"ds_read_b64_tr_b16 a[208:209], v3 offset:25600\n"
"v_mul_f32_e32 v28, s44, v28\n"
"v_mfma_f32_16x16x32_bf16 v[20:23], a[124:127], a[28:31], v[20:23]\n"
"v_exp_f32_e32 v27, v27\n"
"v_mfma_f32_16x16x32_bf16 v[64:67], a[160:163], v[32:35], v[64:67]\n"
"v_mul_f32_e32 v29, s44, v29\n"
"v_cvt_pk_bf16_f32 v37, v26, v27\n"
"v_mfma_f32_16x16x32_bf16 v[68:71], a[164:167], v[32:35], v[68:71]\n"
"v_exp_f32_e32 v28, v28\n"
"v_mfma_f32_16x16x32_bf16 v[72:75], a[168:171], v[32:35], v[72:75]\n"
"ds_read_b64_tr_b16 a[210:211], v4 offset:25600\n"
"v_mul_f32_e32 v30, s44, v30\n"
"v_mfma_f32_16x16x32_bf16 v[76:79], a[172:175], v[32:35], v[76:79]\n"
"v_exp_f32_e32 v29, v29\n"
"v_mfma_f32_16x16x32_bf16 v[80:83], a[176:179], v[32:35], v[80:83]\n"
"v_mul_f32_e32 v31, s44, v31\n"
"v_cvt_pk_bf16_f32 v38, v28, v29\n"
"v_mfma_f32_16x16x32_bf16 v[84:87], a[180:183], v[32:35], v[84:87]\n"
"v_exp_f32_e32 v30, v30\n"
"v_mfma_f32_16x16x32_bf16 v[88:91], a[184:187], v[32:35], v[88:91]\n"
"ds_read_b64_tr_b16 a[204:205], v11 offset:25088\n"
"ds_read_b64_tr_b16 a[212:213], v11 offset:25600\n"
"v_mfma_f32_16x16x32_bf16 v[92:95], a[188:191], v[32:35], v[92:95]\n"
"v_exp_f32_e32 v31, v31\n"
"v_mfma_f32_16x16x32_bf16 a[228:231], a[224:227], v[32:35], a[228:231]\n"
"ds_read_b64_tr_b16 a[214:215], v14 offset:25600\n"
"v_cvt_pk_bf16_f32 v39, v30, v31\n"
"v_mfma_f32_16x16x32_bf16 v[24:27], a[128:131], a[16:19], 0\n"
"v_mul_f32_e32 v16, s44, v16\n"
"v_mul_f32_e32 v17, s44, v17\n"
"v_mfma_f32_16x16x32_bf16 v[24:27], a[132:135], a[20:23], v[24:27]\n"
"v_exp_f32_e32 v16, v16\n"
"v_mfma_f32_16x16x32_bf16 v[24:27], a[136:139], a[24:27], v[24:27]\n"
"v_mul_f32_e32 v18, s44, v18\n"
"v_mfma_f32_16x16x32_bf16 v[24:27], a[140:143], a[28:31], v[24:27]\n"
"v_exp_f32_e32 v17, v17\n"
"v_mfma_f32_16x16x32_bf16 v[28:31], a[144:147], a[16:19], 0\n"
"v_mul_f32_e32 v19, s44, v19\n"
"v_cvt_pk_bf16_f32 v32, v16, v17\n"
"v_mfma_f32_16x16x32_bf16 v[28:31], a[148:151], a[20:23], v[28:31]\n"
"v_exp_f32_e32 v18, v18\n"
"v_mfma_f32_16x16x32_bf16 v[28:31], a[152:155], a[24:27], v[28:31]\n"
"v_mul_f32_e32 v20, s44, v20\n"
"v_mfma_f32_16x16x32_bf16 v[28:31], a[156:159], a[28:31], v[28:31]\n"
"s_waitcnt lgkmcnt(12)\n"
"v_exp_f32_e32 v19, v19\n"
"v_mfma_f32_16x16x32_bf16 v[64:67], a[192:195], v[36:39], v[64:67]\n"
"s_waitcnt lgkmcnt(10)\n"
"v_mul_f32_e32 v21, s44, v21\n"
"v_cvt_pk_bf16_f32 v33, v18, v19\n"
"v_mfma_f32_16x16x32_bf16 v[68:71], a[196:199], v[36:39], v[68:71]\n"
"v_exp_f32_e32 v20, v20\n"
"v_mfma_f32_16x16x32_bf16 v[72:75], a[200:203], v[36:39], v[72:75]\n"
"s_waitcnt lgkmcnt(2)\n"
"v_mul_f32_e32 v22, s44, v22\n"
"v_mfma_f32_16x16x32_bf16 v[76:79], a[204:207], v[36:39], v[76:79]\n"
"v_exp_f32_e32 v21, v21\n"
"v_mfma_f32_16x16x32_bf16 v[80:83], a[208:211], v[36:39], v[80:83]\n"
"s_waitcnt lgkmcnt(0)\n"
"v_mul_f32_e32 v23, s44, v23\n"
"v_cvt_pk_bf16_f32 v34, v20, v21\n"
"v_mfma_f32_16x16x32_bf16 v[84:87], a[212:215], v[36:39], v[84:87]\n"
"v_exp_f32_e32 v22, v22\n"
"v_mfma_f32_16x16x32_bf16 v[88:91], a[216:219], v[36:39], v[88:91]\n"
"v_mfma_f32_16x16x32_bf16 v[92:95], a[220:223], v[36:39], v[92:95]\n"
"v_exp_f32_e32 v23, v23\n"
"v_mfma_f32_16x16x32_bf16 a[228:231], a[224:227], v[36:39], a[228:231]\n"
"v_cvt_pk_bf16_f32 v35, v22, v23\n"
"v_mfma_f32_16x16x32_bf16 v[16:19], a[96:99], a[32:35], 0\n"
"v_mul_f32_e32 v24, s44, v24\n"
"v_mul_f32_e32 v25, s44, v25\n"
"v_mfma_f32_16x16x32_bf16 v[16:19], a[100:103], a[36:39], v[16:19]\n"
"v_exp_f32_e32 v24, v24\n"
"v_mfma_f32_16x16x32_bf16 v[16:19], a[104:107], a[40:43], v[16:19]\n"
"v_mul_f32_e32 v26, s44, v26\n"
"v_mfma_f32_16x16x32_bf16 v[16:19], a[108:111], a[44:47], v[16:19]\n"
"v_exp_f32_e32 v25, v25\n"
"v_mfma_f32_16x16x32_bf16 v[20:23], a[112:115], a[32:35], 0\n"
"v_mul_f32_e32 v27, s44, v27\n"
"v_cvt_pk_bf16_f32 v36, v24, v25\n"
"v_accvgpr_read_b32 v10, a228\n"
"v_subrev_u32_e32 v13, s51, v10\n"
"v_rcp_f32_e32 v12, v10\n"
"v_cmp_le_u32_e32 vcc, s52, v13\n"
"v_pk_mul_f32 v[64:65], v[64:65], v[12:13] op_sel_hi:[1,0]\n"
"v_pk_mul_f32 v[66:67], v[66:67], v[12:13] op_sel_hi:[1,0]\n"
"v_pk_mul_f32 v[68:69], v[68:69], v[12:13] op_sel_hi:[1,0]\n"
"v_pk_mul_f32 v[70:71], v[70:71], v[12:13] op_sel_hi:[1,0]\n"
"v_pk_mul_f32 v[72:73], v[72:73], v[12:13] op_sel_hi:[1,0]\n"
"v_pk_mul_f32 v[74:75], v[74:75], v[12:13] op_sel_hi:[1,0]\n"
"v_pk_mul_f32 v[76:77], v[76:77], v[12:13] op_sel_hi:[1,0]\n"
"v_pk_mul_f32 v[78:79], v[78:79], v[12:13] op_sel_hi:[1,0]\n"
"v_pk_mul_f32 v[80:81], v[80:81], v[12:13] op_sel_hi:[1,0]\n"
"v_pk_mul_f32 v[82:83], v[82:83], v[12:13] op_sel_hi:[1,0]\n"
"v_pk_mul_f32 v[84:85], v[84:85], v[12:13] op_sel_hi:[1,0]\n"
"v_pk_mul_f32 v[86:87], v[86:87], v[12:13] op_sel_hi:[1,0]\n"
"v_pk_mul_f32 v[88:89], v[88:89], v[12:13] op_sel_hi:[1,0]\n"
"v_pk_mul_f32 v[90:91], v[90:91], v[12:13] op_sel_hi:[1,0]\n"
"v_pk_mul_f32 v[92:93], v[92:93], v[12:13] op_sel_hi:[1,0]\n"
"v_pk_mul_f32 v[94:95], v[94:95], v[12:13] op_sel_hi:[1,0]\n"
"s_or_b64 s[54:55], s[54:55], vcc\n"
"v_mfma_f32_16x16x32_bf16 v[20:23], a[116:119], a[36:39], v[20:23]\n"
"v_exp_f32_e32 v26, v26\n"
"v_cvt_pk_bf16_f32 v40, v64, v65\n"
"v_cvt_pk_bf16_f32 v41, v66, v67\n"
"v_cvt_pk_bf16_f32 v42, v68, v69\n"
"v_cvt_pk_bf16_f32 v43, v70, v71\n"
"ds_write_b64 v56, v[40:41] offset:0\n"
"ds_write_b64 v57, v[42:43] offset:0\n"
"v_mfma_f32_16x16x32_bf16 v[20:23], a[120:123], a[40:43], v[20:23]\n"
"v_mul_f32_e32 v28, s44, v28\n"
"v_mfma_f32_16x16x32_bf16 v[20:23], a[124:127], a[44:47], v[20:23]\n"
"v_exp_f32_e32 v27, v27\n"
"v_cvt_pk_bf16_f32 v44, v72, v73\n"
"v_cvt_pk_bf16_f32 v45, v74, v75\n"
"v_cvt_pk_bf16_f32 v46, v76, v77\n"
"v_cvt_pk_bf16_f32 v47, v78, v79\n"
"ds_write_b64 v15, v[44:45] offset:0\n"
"ds_write_b64 v60, v[46:47] offset:0\n"
"v_mfma_f32_16x16x32_bf16 v[96:99], a[160:163], v[32:35], v[96:99]\n"
"v_mul_f32_e32 v29, s44, v29\n"
"v_cvt_pk_bf16_f32 v37, v26, v27\n"
"v_mfma_f32_16x16x32_bf16 v[100:103], a[164:167], v[32:35], v[100:103]\n"
"v_exp_f32_e32 v28, v28\n"
"v_cvt_pk_bf16_f32 v48, v80, v81\n"
"v_cvt_pk_bf16_f32 v49, v82, v83\n"
"v_cvt_pk_bf16_f32 v50, v84, v85\n"
"v_cvt_pk_bf16_f32 v51, v86, v87\n"
"ds_write_b64 v56, v[48:49] offset:128\n"
"ds_write_b64 v57, v[50:51] offset:128\n"
"v_mfma_f32_16x16x32_bf16 v[104:107], a[168:171], v[32:35], v[104:107]\n"
"v_mul_f32_e32 v30, s44, v30\n"
"v_mfma_f32_16x16x32_bf16 v[108:111], a[172:175], v[32:35], v[108:111]\n"
"v_exp_f32_e32 v29, v29\n"
"v_cvt_pk_bf16_f32 v52, v88, v89\n"
"v_cvt_pk_bf16_f32 v53, v90, v91\n"
"v_cvt_pk_bf16_f32 v54, v92, v93\n"
"v_cvt_pk_bf16_f32 v55, v94, v95\n"
"ds_write_b64 v15, v[52:53] offset:128\n"
"ds_write_b64 v60, v[54:55] offset:128\n"
"v_mfma_f32_16x16x32_bf16 v[112:115], a[176:179], v[32:35], v[112:115]\n"
"v_mul_f32_e32 v31, s44, v31\n"
"v_cvt_pk_bf16_f32 v38, v28, v29\n"
"v_mfma_f32_16x16x32_bf16 v[116:119], a[180:183], v[32:35], v[116:119]\n"
"v_exp_f32_e32 v30, v30\n"
"ds_read_b128 v[40:43], v58 offset:0\n"
"ds_read_b128 v[44:47], v59 offset:1024\n"
"ds_read_b128 v[48:51], v58 offset:2048\n"
"ds_read_b128 v[52:55], v59 offset:3072\n"
"v_mfma_f32_16x16x32_bf16 v[120:123], a[184:187], v[32:35], v[120:123]\n"
"v_mfma_f32_16x16x32_bf16 v[124:127], a[188:191], v[32:35], v[124:127]\n"
"v_exp_f32_e32 v31, v31\n"
"v_mfma_f32_16x16x32_bf16 a[232:235], a[224:227], v[32:35], a[232:235]\n"
"v_cvt_pk_bf16_f32 v39, v30, v31\n"
"v_mfma_f32_16x16x32_bf16 v[24:27], a[128:131], a[32:35], 0\n"
"v_mul_f32_e32 v16, s44, v16\n"
"v_mul_f32_e32 v17, s44, v17\n"
"v_mfma_f32_16x16x32_bf16 v[24:27], a[132:135], a[36:39], v[24:27]\n"
"v_exp_f32_e32 v16, v16\n"
"v_mfma_f32_16x16x32_bf16 v[24:27], a[136:139], a[40:43], v[24:27]\n"
"v_mul_f32_e32 v18, s44, v18\n"
"s_add_u32 s77, s60, 0\n"
"v_mfma_f32_16x16x32_bf16 v[24:27], a[140:143], a[44:47], v[24:27]\n"
"v_exp_f32_e32 v17, v17\n"
"s_waitcnt lgkmcnt(3)\n"
"buffer_store_dwordx4 v[40:43], v9, s[40:43], s77 offen offset:0  sc1\n"
"v_mfma_f32_16x16x32_bf16 v[28:31], a[144:147], a[32:35], 0\n"
"v_mul_f32_e32 v19, s44, v19\n"
"v_cvt_pk_bf16_f32 v32, v16, v17\n"
"v_mfma_f32_16x16x32_bf16 v[28:31], a[148:151], a[36:39], v[28:31]\n"
"v_exp_f32_e32 v18, v18\n"
"s_waitcnt lgkmcnt(2)\n"
"buffer_store_dwordx4 v[44:47], v9, s[40:43], s77 offen offset:1024  sc1\n"
"v_mfma_f32_16x16x32_bf16 v[28:31], a[152:155], a[40:43], v[28:31]\n"
"v_mul_f32_e32 v20, s44, v20\n"
"v_mfma_f32_16x16x32_bf16 v[28:31], a[156:159], a[44:47], v[28:31]\n"
"v_exp_f32_e32 v19, v19\n"
"s_waitcnt lgkmcnt(1)\n"
"buffer_store_dwordx4 v[48:51], v9, s[40:43], s77 offen offset:2048  sc1\n"
"v_mfma_f32_16x16x32_bf16 v[96:99], a[192:195], v[36:39], v[96:99]\n"
"v_mul_f32_e32 v21, s44, v21\n"
"v_cvt_pk_bf16_f32 v33, v18, v19\n"
"v_mfma_f32_16x16x32_bf16 v[100:103], a[196:199], v[36:39], v[100:103]\n"
"v_exp_f32_e32 v20, v20\n"
"s_waitcnt lgkmcnt(0)\n"
"buffer_store_dwordx4 v[52:55], v9, s[40:43], s77 offen offset:3072  sc1\n"
"v_mfma_f32_16x16x32_bf16 v[104:107], a[200:203], v[36:39], v[104:107]\n"
"v_mul_f32_e32 v22, s44, v22\n"
"v_mfma_f32_16x16x32_bf16 v[108:111], a[204:207], v[36:39], v[108:111]\n"
"v_exp_f32_e32 v21, v21\n"
"v_mfma_f32_16x16x32_bf16 v[112:115], a[208:211], v[36:39], v[112:115]\n"
"v_mul_f32_e32 v23, s44, v23\n"
"v_cvt_pk_bf16_f32 v34, v20, v21\n"
"v_mfma_f32_16x16x32_bf16 v[116:119], a[212:215], v[36:39], v[116:119]\n"
"v_exp_f32_e32 v22, v22\n"
"v_mfma_f32_16x16x32_bf16 v[120:123], a[216:219], v[36:39], v[120:123]\n"
"v_mfma_f32_16x16x32_bf16 v[124:127], a[220:223], v[36:39], v[124:127]\n"
"v_exp_f32_e32 v23, v23\n"
"v_mfma_f32_16x16x32_bf16 a[232:235], a[224:227], v[36:39], a[232:235]\n"
"v_cvt_pk_bf16_f32 v35, v22, v23\n"
"v_mfma_f32_16x16x32_bf16 v[16:19], a[96:99], a[48:51], 0\n"
"v_mul_f32_e32 v24, s44, v24\n"
"v_mul_f32_e32 v25, s44, v25\n"
"v_mfma_f32_16x16x32_bf16 v[16:19], a[100:103], a[52:55], v[16:19]\n"
"v_exp_f32_e32 v24, v24\n"
"v_mfma_f32_16x16x32_bf16 v[16:19], a[104:107], a[56:59], v[16:19]\n"
"v_mul_f32_e32 v26, s44, v26\n"
"v_mfma_f32_16x16x32_bf16 v[16:19], a[108:111], a[60:63], v[16:19]\n"
"v_exp_f32_e32 v25, v25\n"
"v_mfma_f32_16x16x32_bf16 v[20:23], a[112:115], a[48:51], 0\n"
"v_mul_f32_e32 v27, s44, v27\n"
"v_cvt_pk_bf16_f32 v36, v24, v25\n"
"v_accvgpr_read_b32 v10, a232\n"
"v_subrev_u32_e32 v13, s51, v10\n"
"v_rcp_f32_e32 v12, v10\n"
"v_cmp_le_u32_e32 vcc, s52, v13\n"
"v_pk_mul_f32 v[96:97], v[96:97], v[12:13] op_sel_hi:[1,0]\n"
"v_pk_mul_f32 v[98:99], v[98:99], v[12:13] op_sel_hi:[1,0]\n"
"v_pk_mul_f32 v[100:101], v[100:101], v[12:13] op_sel_hi:[1,0]\n"
"v_pk_mul_f32 v[102:103], v[102:103], v[12:13] op_sel_hi:[1,0]\n"
"v_pk_mul_f32 v[104:105], v[104:105], v[12:13] op_sel_hi:[1,0]\n"
"v_pk_mul_f32 v[106:107], v[106:107], v[12:13] op_sel_hi:[1,0]\n"
"v_pk_mul_f32 v[108:109], v[108:109], v[12:13] op_sel_hi:[1,0]\n"
"v_pk_mul_f32 v[110:111], v[110:111], v[12:13] op_sel_hi:[1,0]\n"
"v_pk_mul_f32 v[112:113], v[112:113], v[12:13] op_sel_hi:[1,0]\n"
"v_pk_mul_f32 v[114:115], v[114:115], v[12:13] op_sel_hi:[1,0]\n"
"v_pk_mul_f32 v[116:117], v[116:117], v[12:13] op_sel_hi:[1,0]\n"
"v_pk_mul_f32 v[118:119], v[118:119], v[12:13] op_sel_hi:[1,0]\n"
"v_pk_mul_f32 v[120:121], v[120:121], v[12:13] op_sel_hi:[1,0]\n"
"v_pk_mul_f32 v[122:123], v[122:123], v[12:13] op_sel_hi:[1,0]\n"
"v_pk_mul_f32 v[124:125], v[124:125], v[12:13] op_sel_hi:[1,0]\n"
"v_pk_mul_f32 v[126:127], v[126:127], v[12:13] op_sel_hi:[1,0]\n"
"s_or_b64 s[54:55], s[54:55], vcc\n"
"v_mfma_f32_16x16x32_bf16 v[20:23], a[116:119], a[52:55], v[20:23]\n"
"v_exp_f32_e32 v26, v26\n"
"v_cvt_pk_bf16_f32 v40, v96, v97\n"
"v_cvt_pk_bf16_f32 v41, v98, v99\n"
"v_cvt_pk_bf16_f32 v42, v100, v101\n"
"v_cvt_pk_bf16_f32 v43, v102, v103\n"
"ds_write_b64 v56, v[40:41] offset:4096\n"
"ds_write_b64 v57, v[42:43] offset:4096\n"
"v_mfma_f32_16x16x32_bf16 v[20:23], a[120:123], a[56:59], v[20:23]\n"
"v_mul_f32_e32 v28, s44, v28\n"
"v_mfma_f32_16x16x32_bf16 v[20:23], a[124:127], a[60:63], v[20:23]\n"
"v_exp_f32_e32 v27, v27\n"
"v_cvt_pk_bf16_f32 v44, v104, v105\n"
"v_cvt_pk_bf16_f32 v45, v106, v107\n"
"v_cvt_pk_bf16_f32 v46, v108, v109\n"
"v_cvt_pk_bf16_f32 v47, v110, v111\n"
"ds_write_b64 v15, v[44:45] offset:4096\n"
"ds_write_b64 v60, v[46:47] offset:4096\n"
"v_mfma_f32_16x16x32_bf16 v[128:131], a[160:163], v[32:35], v[128:131]\n"
"v_mul_f32_e32 v29, s44, v29\n"
"v_cvt_pk_bf16_f32 v37, v26, v27\n"
"v_mfma_f32_16x16x32_bf16 v[132:135], a[164:167], v[32:35], v[132:135]\n"
"v_exp_f32_e32 v28, v28\n"
"v_cvt_pk_bf16_f32 v48, v112, v113\n"
"v_cvt_pk_bf16_f32 v49, v114, v115\n"
"v_cvt_pk_bf16_f32 v50, v116, v117\n"
"v_cvt_pk_bf16_f32 v51, v118, v119\n"
"ds_write_b64 v56, v[48:49] offset:4224\n"
"ds_write_b64 v57, v[50:51] offset:4224\n"
"v_mfma_f32_16x16x32_bf16 v[136:139], a[168:171], v[32:35], v[136:139]\n"
"v_mul_f32_e32 v30, s44, v30\n"
"v_mfma_f32_16x16x32_bf16 v[140:143], a[172:175], v[32:35], v[140:143]\n"
"v_exp_f32_e32 v29, v29\n"
"v_cvt_pk_bf16_f32 v52, v120, v121\n"
"v_cvt_pk_bf16_f32 v53, v122, v123\n"
"v_cvt_pk_bf16_f32 v54, v124, v125\n"
"v_cvt_pk_bf16_f32 v55, v126, v127\n"
"ds_write_b64 v15, v[52:53] offset:4224\n"
"ds_write_b64 v60, v[54:55] offset:4224\n"
"v_mfma_f32_16x16x32_bf16 v[144:147], a[176:179], v[32:35], v[144:147]\n"
"v_mul_f32_e32 v31, s44, v31\n"
"v_cvt_pk_bf16_f32 v38, v28, v29\n"
"v_mfma_f32_16x16x32_bf16 v[148:151], a[180:183], v[32:35], v[148:151]\n"
"v_exp_f32_e32 v30, v30\n"
"ds_read_b128 v[40:43], v58 offset:4096\n"
"ds_read_b128 v[44:47], v59 offset:5120\n"
"ds_read_b128 v[48:51], v58 offset:6144\n"
"ds_read_b128 v[52:55], v59 offset:7168\n"
"v_mfma_f32_16x16x32_bf16 v[152:155], a[184:187], v[32:35], v[152:155]\n"
"v_mfma_f32_16x16x32_bf16 v[156:159], a[188:191], v[32:35], v[156:159]\n"
"v_exp_f32_e32 v31, v31\n"
"v_mfma_f32_16x16x32_bf16 a[236:239], a[224:227], v[32:35], a[236:239]\n"
"v_cvt_pk_bf16_f32 v39, v30, v31\n"
"v_mfma_f32_16x16x32_bf16 v[24:27], a[128:131], a[48:51], 0\n"
"v_mul_f32_e32 v16, s44, v16\n"
"v_mul_f32_e32 v17, s44, v17\n"
"v_mfma_f32_16x16x32_bf16 v[24:27], a[132:135], a[52:55], v[24:27]\n"
"v_exp_f32_e32 v16, v16\n"
"v_mfma_f32_16x16x32_bf16 v[24:27], a[136:139], a[56:59], v[24:27]\n"
"v_mul_f32_e32 v18, s44, v18\n"
"s_add_u32 s77, s60, 4096\n"
"v_mfma_f32_16x16x32_bf16 v[24:27], a[140:143], a[60:63], v[24:27]\n"
"v_exp_f32_e32 v17, v17\n"
"s_waitcnt lgkmcnt(3)\n"
"buffer_store_dwordx4 v[40:43], v9, s[40:43], s77 offen offset:0  sc1\n"
"v_mfma_f32_16x16x32_bf16 v[28:31], a[144:147], a[48:51], 0\n"
"v_mul_f32_e32 v19, s44, v19\n"
"v_cvt_pk_bf16_f32 v32, v16, v17\n"
"v_mfma_f32_16x16x32_bf16 v[28:31], a[148:151], a[52:55], v[28:31]\n"
"v_exp_f32_e32 v18, v18\n"
"s_waitcnt lgkmcnt(2)\n"
"buffer_store_dwordx4 v[44:47], v9, s[40:43], s77 offen offset:1024  sc1\n"
"v_mfma_f32_16x16x32_bf16 v[28:31], a[152:155], a[56:59], v[28:31]\n"
"v_mul_f32_e32 v20, s44, v20\n"
"v_mfma_f32_16x16x32_bf16 v[28:31], a[156:159], a[60:63], v[28:31]\n"
"v_exp_f32_e32 v19, v19\n"
"s_waitcnt lgkmcnt(1)\n"
"buffer_store_dwordx4 v[48:51], v9, s[40:43], s77 offen offset:2048  sc1\n"
"v_mfma_f32_16x16x32_bf16 v[128:131], a[192:195], v[36:39], v[128:131]\n"
"v_mul_f32_e32 v21, s44, v21\n"
"v_cvt_pk_bf16_f32 v33, v18, v19\n"
"v_mfma_f32_16x16x32_bf16 v[132:135], a[196:199], v[36:39], v[132:135]\n"
"v_exp_f32_e32 v20, v20\n"
"s_waitcnt lgkmcnt(0)\n"
"buffer_store_dwordx4 v[52:55], v9, s[40:43], s77 offen offset:3072  sc1\n"
"v_mfma_f32_16x16x32_bf16 v[136:139], a[200:203], v[36:39], v[136:139]\n"
"v_mul_f32_e32 v22, s44, v22\n"
"v_mfma_f32_16x16x32_bf16 v[140:143], a[204:207], v[36:39], v[140:143]\n"
"v_exp_f32_e32 v21, v21\n"
"v_mfma_f32_16x16x32_bf16 v[144:147], a[208:211], v[36:39], v[144:147]\n"
"v_mul_f32_e32 v23, s44, v23\n"
"v_cvt_pk_bf16_f32 v34, v20, v21\n"
"v_mfma_f32_16x16x32_bf16 v[148:151], a[212:215], v[36:39], v[148:151]\n"
"v_exp_f32_e32 v22, v22\n"
"v_mfma_f32_16x16x32_bf16 v[152:155], a[216:219], v[36:39], v[152:155]\n"
"v_mfma_f32_16x16x32_bf16 v[156:159], a[220:223], v[36:39], v[156:159]\n"
"v_exp_f32_e32 v23, v23\n"
"v_mfma_f32_16x16x32_bf16 a[236:239], a[224:227], v[36:39], a[236:239]\n"
"v_cvt_pk_bf16_f32 v35, v22, v23\n"
"v_mfma_f32_16x16x32_bf16 v[16:19], a[96:99], a[64:67], 0\n"
"v_mul_f32_e32 v24, s44, v24\n"
"v_mul_f32_e32 v25, s44, v25\n"
"v_mfma_f32_16x16x32_bf16 v[16:19], a[100:103], a[68:71], v[16:19]\n"
"v_exp_f32_e32 v24, v24\n"
"v_mfma_f32_16x16x32_bf16 v[16:19], a[104:107], a[72:75], v[16:19]\n"
"v_mul_f32_e32 v26, s44, v26\n"
"v_mfma_f32_16x16x32_bf16 v[16:19], a[108:111], a[76:79], v[16:19]\n"
"v_exp_f32_e32 v25, v25\n"
"v_mfma_f32_16x16x32_bf16 v[20:23], a[112:115], a[64:67], 0\n"
"v_mul_f32_e32 v27, s44, v27\n"
"v_cvt_pk_bf16_f32 v36, v24, v25\n"
"v_accvgpr_read_b32 v10, a236\n"
"v_subrev_u32_e32 v13, s51, v10\n"
"v_rcp_f32_e32 v12, v10\n"
"v_cmp_le_u32_e32 vcc, s52, v13\n"
"v_pk_mul_f32 v[128:129], v[128:129], v[12:13] op_sel_hi:[1,0]\n"
"v_pk_mul_f32 v[130:131], v[130:131], v[12:13] op_sel_hi:[1,0]\n"
"v_pk_mul_f32 v[132:133], v[132:133], v[12:13] op_sel_hi:[1,0]\n"
"v_pk_mul_f32 v[134:135], v[134:135], v[12:13] op_sel_hi:[1,0]\n"
"v_pk_mul_f32 v[136:137], v[136:137], v[12:13] op_sel_hi:[1,0]\n"
"v_pk_mul_f32 v[138:139], v[138:139], v[12:13] op_sel_hi:[1,0]\n"
"v_pk_mul_f32 v[140:141], v[140:141], v[12:13] op_sel_hi:[1,0]\n"
"v_pk_mul_f32 v[142:143], v[142:143], v[12:13] op_sel_hi:[1,0]\n"
"v_pk_mul_f32 v[144:145], v[144:145], v[12:13] op_sel_hi:[1,0]\n"
"v_pk_mul_f32 v[146:147], v[146:147], v[12:13] op_sel_hi:[1,0]\n"
"v_pk_mul_f32 v[148:149], v[148:149], v[12:13] op_sel_hi:[1,0]\n"
"v_pk_mul_f32 v[150:151], v[150:151], v[12:13] op_sel_hi:[1,0]\n"
"v_pk_mul_f32 v[152:153], v[152:153], v[12:13] op_sel_hi:[1,0]\n"
"v_pk_mul_f32 v[154:155], v[154:155], v[12:13] op_sel_hi:[1,0]\n"
"v_pk_mul_f32 v[156:157], v[156:157], v[12:13] op_sel_hi:[1,0]\n"
"v_pk_mul_f32 v[158:159], v[158:159], v[12:13] op_sel_hi:[1,0]\n"
"s_or_b64 s[54:55], s[54:55], vcc\n"
"v_mfma_f32_16x16x32_bf16 v[20:23], a[116:119], a[68:71], v[20:23]\n"
"v_exp_f32_e32 v26, v26\n"
"v_cvt_pk_bf16_f32 v40, v128, v129\n"
"v_cvt_pk_bf16_f32 v41, v130, v131\n"
"v_cvt_pk_bf16_f32 v42, v132, v133\n"
"v_cvt_pk_bf16_f32 v43, v134, v135\n"
"ds_write_b64 v56, v[40:41] offset:8192\n"
"ds_write_b64 v57, v[42:43] offset:8192\n"
"v_mfma_f32_16x16x32_bf16 v[20:23], a[120:123], a[72:75], v[20:23]\n"
"v_mul_f32_e32 v28, s44, v28\n"
"v_mfma_f32_16x16x32_bf16 v[20:23], a[124:127], a[76:79], v[20:23]\n"
"v_exp_f32_e32 v27, v27\n"
"v_cvt_pk_bf16_f32 v44, v136, v137\n"
"v_cvt_pk_bf16_f32 v45, v138, v139\n"
"v_cvt_pk_bf16_f32 v46, v140, v141\n"
"v_cvt_pk_bf16_f32 v47, v142, v143\n"
"ds_write_b64 v15, v[44:45] offset:8192\n"
"ds_write_b64 v60, v[46:47] offset:8192\n"
"v_mfma_f32_16x16x32_bf16 v[160:163], a[160:163], v[32:35], v[160:163]\n"
"v_mul_f32_e32 v29, s44, v29\n"
"v_cvt_pk_bf16_f32 v37, v26, v27\n"
"v_mfma_f32_16x16x32_bf16 v[164:167], a[164:167], v[32:35], v[164:167]\n"
"v_exp_f32_e32 v28, v28\n"
"v_cvt_pk_bf16_f32 v48, v144, v145\n"
"v_cvt_pk_bf16_f32 v49, v146, v147\n"
"v_cvt_pk_bf16_f32 v50, v148, v149\n"
"v_cvt_pk_bf16_f32 v51, v150, v151\n"
"ds_write_b64 v56, v[48:49] offset:8320\n"
"ds_write_b64 v57, v[50:51] offset:8320\n"
"v_mfma_f32_16x16x32_bf16 v[168:171], a[168:171], v[32:35], v[168:171]\n"
"v_mul_f32_e32 v30, s44, v30\n"
"v_mfma_f32_16x16x32_bf16 v[172:175], a[172:175], v[32:35], v[172:175]\n"
"v_exp_f32_e32 v29, v29\n"
"v_cvt_pk_bf16_f32 v52, v152, v153\n"
"v_cvt_pk_bf16_f32 v53, v154, v155\n"
"v_cvt_pk_bf16_f32 v54, v156, v157\n"
"v_cvt_pk_bf16_f32 v55, v158, v159\n"
"ds_write_b64 v15, v[52:53] offset:8320\n"
"ds_write_b64 v60, v[54:55] offset:8320\n"
"v_mfma_f32_16x16x32_bf16 v[176:179], a[176:179], v[32:35], v[176:179]\n"
"v_mul_f32_e32 v31, s44, v31\n"
"v_cvt_pk_bf16_f32 v38, v28, v29\n"
"v_mfma_f32_16x16x32_bf16 v[180:183], a[180:183], v[32:35], v[180:183]\n"
"v_exp_f32_e32 v30, v30\n"
"ds_read_b128 v[40:43], v58 offset:8192\n"
"ds_read_b128 v[44:47], v59 offset:9216\n"
"ds_read_b128 v[48:51], v58 offset:10240\n"
"ds_read_b128 v[52:55], v59 offset:11264\n"
"v_mfma_f32_16x16x32_bf16 v[184:187], a[184:187], v[32:35], v[184:187]\n"
"v_mfma_f32_16x16x32_bf16 v[188:191], a[188:191], v[32:35], v[188:191]\n"
"v_exp_f32_e32 v31, v31\n"
"v_mfma_f32_16x16x32_bf16 a[240:243], a[224:227], v[32:35], a[240:243]\n"
"v_cvt_pk_bf16_f32 v39, v30, v31\n"
"v_mfma_f32_16x16x32_bf16 v[24:27], a[128:131], a[64:67], 0\n"
"v_mul_f32_e32 v16, s44, v16\n"
"v_mul_f32_e32 v17, s44, v17\n"
"v_mfma_f32_16x16x32_bf16 v[24:27], a[132:135], a[68:71], v[24:27]\n"
"v_exp_f32_e32 v16, v16\n"
"v_mfma_f32_16x16x32_bf16 v[24:27], a[136:139], a[72:75], v[24:27]\n"
"v_mul_f32_e32 v18, s44, v18\n"
"s_add_u32 s77, s60, 8192\n"
"v_mfma_f32_16x16x32_bf16 v[24:27], a[140:143], a[76:79], v[24:27]\n"
"v_exp_f32_e32 v17, v17\n"
"s_waitcnt lgkmcnt(3)\n"
"buffer_store_dwordx4 v[40:43], v9, s[40:43], s77 offen offset:0  sc1\n"
"v_mfma_f32_16x16x32_bf16 v[28:31], a[144:147], a[64:67], 0\n"
"v_mul_f32_e32 v19, s44, v19\n"
"v_cvt_pk_bf16_f32 v32, v16, v17\n"
"v_mfma_f32_16x16x32_bf16 v[28:31], a[148:151], a[68:71], v[28:31]\n"
"v_exp_f32_e32 v18, v18\n"
"s_waitcnt lgkmcnt(2)\n"
"buffer_store_dwordx4 v[44:47], v9, s[40:43], s77 offen offset:1024  sc1\n"
"v_mfma_f32_16x16x32_bf16 v[28:31], a[152:155], a[72:75], v[28:31]\n"
"v_mul_f32_e32 v20, s44, v20\n"
"v_mfma_f32_16x16x32_bf16 v[28:31], a[156:159], a[76:79], v[28:31]\n"
"v_exp_f32_e32 v19, v19\n"
"s_waitcnt lgkmcnt(1)\n"
"buffer_store_dwordx4 v[48:51], v9, s[40:43], s77 offen offset:2048  sc1\n"
"v_mfma_f32_16x16x32_bf16 v[160:163], a[192:195], v[36:39], v[160:163]\n"
"v_mul_f32_e32 v21, s44, v21\n"
"v_cvt_pk_bf16_f32 v33, v18, v19\n"
"v_mfma_f32_16x16x32_bf16 v[164:167], a[196:199], v[36:39], v[164:167]\n"
"v_exp_f32_e32 v20, v20\n"
"s_waitcnt lgkmcnt(0)\n"
"buffer_store_dwordx4 v[52:55], v9, s[40:43], s77 offen offset:3072  sc1\n"
"v_mfma_f32_16x16x32_bf16 v[168:171], a[200:203], v[36:39], v[168:171]\n"
"v_mul_f32_e32 v22, s44, v22\n"
"v_mfma_f32_16x16x32_bf16 v[172:175], a[204:207], v[36:39], v[172:175]\n"
"v_exp_f32_e32 v21, v21\n"
"v_mfma_f32_16x16x32_bf16 v[176:179], a[208:211], v[36:39], v[176:179]\n"
"v_mul_f32_e32 v23, s44, v23\n"
"v_cvt_pk_bf16_f32 v34, v20, v21\n"
"v_mfma_f32_16x16x32_bf16 v[180:183], a[212:215], v[36:39], v[180:183]\n"
"v_exp_f32_e32 v22, v22\n"
"v_mfma_f32_16x16x32_bf16 v[184:187], a[216:219], v[36:39], v[184:187]\n"
"v_mfma_f32_16x16x32_bf16 v[188:191], a[220:223], v[36:39], v[188:191]\n"
"v_exp_f32_e32 v23, v23\n"
"v_mfma_f32_16x16x32_bf16 a[240:243], a[224:227], v[36:39], a[240:243]\n"
"v_cvt_pk_bf16_f32 v35, v22, v23\n"
"v_mfma_f32_16x16x32_bf16 v[16:19], a[96:99], a[80:83], 0\n"
"v_mul_f32_e32 v24, s44, v24\n"
"v_mul_f32_e32 v25, s44, v25\n"
"v_mfma_f32_16x16x32_bf16 v[16:19], a[100:103], a[84:87], v[16:19]\n"
"v_exp_f32_e32 v24, v24\n"
"v_mfma_f32_16x16x32_bf16 v[16:19], a[104:107], a[88:91], v[16:19]\n"
"v_mul_f32_e32 v26, s44, v26\n"
"v_mfma_f32_16x16x32_bf16 v[16:19], a[108:111], a[92:95], v[16:19]\n"
"v_exp_f32_e32 v25, v25\n"
"v_mfma_f32_16x16x32_bf16 v[20:23], a[112:115], a[80:83], 0\n"
"v_mul_f32_e32 v27, s44, v27\n"
"v_cvt_pk_bf16_f32 v36, v24, v25\n"
"v_accvgpr_read_b32 v10, a240\n"
"v_subrev_u32_e32 v13, s51, v10\n"
"v_rcp_f32_e32 v12, v10\n"
"v_cmp_le_u32_e32 vcc, s52, v13\n"
"v_pk_mul_f32 v[160:161], v[160:161], v[12:13] op_sel_hi:[1,0]\n"
"v_pk_mul_f32 v[162:163], v[162:163], v[12:13] op_sel_hi:[1,0]\n"
"v_pk_mul_f32 v[164:165], v[164:165], v[12:13] op_sel_hi:[1,0]\n"
"v_pk_mul_f32 v[166:167], v[166:167], v[12:13] op_sel_hi:[1,0]\n"
"v_pk_mul_f32 v[168:169], v[168:169], v[12:13] op_sel_hi:[1,0]\n"
"v_pk_mul_f32 v[170:171], v[170:171], v[12:13] op_sel_hi:[1,0]\n"
"v_pk_mul_f32 v[172:173], v[172:173], v[12:13] op_sel_hi:[1,0]\n"
"v_pk_mul_f32 v[174:175], v[174:175], v[12:13] op_sel_hi:[1,0]\n"
"v_pk_mul_f32 v[176:177], v[176:177], v[12:13] op_sel_hi:[1,0]\n"
"v_pk_mul_f32 v[178:179], v[178:179], v[12:13] op_sel_hi:[1,0]\n"
"v_pk_mul_f32 v[180:181], v[180:181], v[12:13] op_sel_hi:[1,0]\n"
"v_pk_mul_f32 v[182:183], v[182:183], v[12:13] op_sel_hi:[1,0]\n"
"v_pk_mul_f32 v[184:185], v[184:185], v[12:13] op_sel_hi:[1,0]\n"
"v_pk_mul_f32 v[186:187], v[186:187], v[12:13] op_sel_hi:[1,0]\n"
"v_pk_mul_f32 v[188:189], v[188:189], v[12:13] op_sel_hi:[1,0]\n"
"v_pk_mul_f32 v[190:191], v[190:191], v[12:13] op_sel_hi:[1,0]\n"
"s_or_b64 s[54:55], s[54:55], vcc\n"
"v_mfma_f32_16x16x32_bf16 v[20:23], a[116:119], a[84:87], v[20:23]\n"
"v_exp_f32_e32 v26, v26\n"
"v_cvt_pk_bf16_f32 v40, v160, v161\n"
"v_cvt_pk_bf16_f32 v41, v162, v163\n"
"v_cvt_pk_bf16_f32 v42, v164, v165\n"
"v_cvt_pk_bf16_f32 v43, v166, v167\n"
"ds_write_b64 v56, v[40:41] offset:12288\n"
"ds_write_b64 v57, v[42:43] offset:12288\n"
"v_mfma_f32_16x16x32_bf16 v[20:23], a[120:123], a[88:91], v[20:23]\n"
"v_mul_f32_e32 v28, s44, v28\n"
"v_mfma_f32_16x16x32_bf16 v[20:23], a[124:127], a[92:95], v[20:23]\n"
"v_exp_f32_e32 v27, v27\n"
"v_cvt_pk_bf16_f32 v44, v168, v169\n"
"v_cvt_pk_bf16_f32 v45, v170, v171\n"
"v_cvt_pk_bf16_f32 v46, v172, v173\n"
"v_cvt_pk_bf16_f32 v47, v174, v175\n"
"ds_write_b64 v15, v[44:45] offset:12288\n"
"ds_write_b64 v60, v[46:47] offset:12288\n"
"v_mfma_f32_16x16x32_bf16 v[192:195], a[160:163], v[32:35], v[192:195]\n"
"v_mul_f32_e32 v29, s44, v29\n"
"v_cvt_pk_bf16_f32 v37, v26, v27\n"
"v_mfma_f32_16x16x32_bf16 v[196:199], a[164:167], v[32:35], v[196:199]\n"
"v_exp_f32_e32 v28, v28\n"
"v_cvt_pk_bf16_f32 v48, v176, v177\n"
"v_cvt_pk_bf16_f32 v49, v178, v179\n"
"v_cvt_pk_bf16_f32 v50, v180, v181\n"
"v_cvt_pk_bf16_f32 v51, v182, v183\n"
"ds_write_b64 v56, v[48:49] offset:12416\n"
"ds_write_b64 v57, v[50:51] offset:12416\n"
"v_mfma_f32_16x16x32_bf16 v[200:203], a[168:171], v[32:35], v[200:203]\n"
"v_mul_f32_e32 v30, s44, v30\n"
"v_mfma_f32_16x16x32_bf16 v[204:207], a[172:175], v[32:35], v[204:207]\n"
"v_exp_f32_e32 v29, v29\n"
"v_cvt_pk_bf16_f32 v52, v184, v185\n"
"v_cvt_pk_bf16_f32 v53, v186, v187\n"
"v_cvt_pk_bf16_f32 v54, v188, v189\n"
"v_cvt_pk_bf16_f32 v55, v190, v191\n"
"ds_write_b64 v15, v[52:53] offset:12416\n"
"ds_write_b64 v60, v[54:55] offset:12416\n"
"v_mfma_f32_16x16x32_bf16 v[208:211], a[176:179], v[32:35], v[208:211]\n"
"v_mul_f32_e32 v31, s44, v31\n"
"v_cvt_pk_bf16_f32 v38, v28, v29\n"
"v_mfma_f32_16x16x32_bf16 v[212:215], a[180:183], v[32:35], v[212:215]\n"
"v_exp_f32_e32 v30, v30\n"
"ds_read_b128 v[40:43], v58 offset:12288\n"
"ds_read_b128 v[44:47], v59 offset:13312\n"
"ds_read_b128 v[48:51], v58 offset:14336\n"
"ds_read_b128 v[52:55], v59 offset:15360\n"
"v_mfma_f32_16x16x32_bf16 v[216:219], a[184:187], v[32:35], v[216:219]\n"
"v_mfma_f32_16x16x32_bf16 v[220:223], a[188:191], v[32:35], v[220:223]\n"
"v_exp_f32_e32 v31, v31\n"
"v_mfma_f32_16x16x32_bf16 a[244:247], a[224:227], v[32:35], a[244:247]\n"
"v_cvt_pk_bf16_f32 v39, v30, v31\n"
"v_mfma_f32_16x16x32_bf16 v[24:27], a[128:131], a[80:83], 0\n"
"v_mul_f32_e32 v16, s44, v16\n"
"v_mul_f32_e32 v17, s44, v17\n"
"v_mfma_f32_16x16x32_bf16 v[24:27], a[132:135], a[84:87], v[24:27]\n"
"v_exp_f32_e32 v16, v16\n"
"v_mfma_f32_16x16x32_bf16 v[24:27], a[136:139], a[88:91], v[24:27]\n"
"v_mul_f32_e32 v18, s44, v18\n"
"s_add_u32 s77, s60, 12288\n"
"v_mfma_f32_16x16x32_bf16 v[24:27], a[140:143], a[92:95], v[24:27]\n"
"v_exp_f32_e32 v17, v17\n"
"s_waitcnt lgkmcnt(3)\n"
"buffer_store_dwordx4 v[40:43], v9, s[40:43], s77 offen offset:0  sc1\n"
"v_mfma_f32_16x16x32_bf16 v[28:31], a[144:147], a[80:83], 0\n"
"v_mul_f32_e32 v19, s44, v19\n"
"v_cvt_pk_bf16_f32 v32, v16, v17\n"
"v_mfma_f32_16x16x32_bf16 v[28:31], a[148:151], a[84:87], v[28:31]\n"
"v_exp_f32_e32 v18, v18\n"
"s_waitcnt lgkmcnt(2)\n"
"buffer_store_dwordx4 v[44:47], v9, s[40:43], s77 offen offset:1024  sc1\n"
"v_mfma_f32_16x16x32_bf16 v[28:31], a[152:155], a[88:91], v[28:31]\n"
"v_mul_f32_e32 v20, s44, v20\n"
"v_mfma_f32_16x16x32_bf16 v[28:31], a[156:159], a[92:95], v[28:31]\n"
"v_exp_f32_e32 v19, v19\n"
"s_waitcnt lgkmcnt(1)\n"
"buffer_store_dwordx4 v[48:51], v9, s[40:43], s77 offen offset:2048  sc1\n"
"v_mfma_f32_16x16x32_bf16 v[192:195], a[192:195], v[36:39], v[192:195]\n"
"v_mul_f32_e32 v21, s44, v21\n"
"v_cvt_pk_bf16_f32 v33, v18, v19\n"
"v_mfma_f32_16x16x32_bf16 v[196:199], a[196:199], v[36:39], v[196:199]\n"
"v_exp_f32_e32 v20, v20\n"
"s_waitcnt lgkmcnt(0)\n"
"buffer_store_dwordx4 v[52:55], v9, s[40:43], s77 offen offset:3072  sc1\n"
"v_mfma_f32_16x16x32_bf16 v[200:203], a[200:203], v[36:39], v[200:203]\n"
"v_mul_f32_e32 v22, s44, v22\n"
"v_mfma_f32_16x16x32_bf16 v[204:207], a[204:207], v[36:39], v[204:207]\n"
"v_exp_f32_e32 v21, v21\n"
"v_mfma_f32_16x16x32_bf16 v[208:211], a[208:211], v[36:39], v[208:211]\n"
"v_mul_f32_e32 v23, s44, v23\n"
"v_cvt_pk_bf16_f32 v34, v20, v21\n"
"v_mfma_f32_16x16x32_bf16 v[212:215], a[212:215], v[36:39], v[212:215]\n"
"v_exp_f32_e32 v22, v22\n"
"v_mfma_f32_16x16x32_bf16 v[216:219], a[216:219], v[36:39], v[216:219]\n"
"v_mfma_f32_16x16x32_bf16 v[220:223], a[220:223], v[36:39], v[220:223]\n"
"v_exp_f32_e32 v23, v23\n"
"v_mfma_f32_16x16x32_bf16 a[244:247], a[224:227], v[36:39], a[244:247]\n"
"v_cvt_pk_bf16_f32 v35, v22, v23\n"
"v_mul_f32_e32 v24, s44, v24\n"
"v_mul_f32_e32 v25, s44, v25\n"
"v_exp_f32_e32 v24, v24\n"
"v_mul_f32_e32 v26, s44, v26\n"
"v_exp_f32_e32 v25, v25\n"
"v_mul_f32_e32 v27, s44, v27\n"
"v_cvt_pk_bf16_f32 v36, v24, v25\n"
"v_accvgpr_read_b32 v10, a244\n"
"v_subrev_u32_e32 v13, s51, v10\n"
"v_rcp_f32_e32 v12, v10\n"
"v_cmp_le_u32_e32 vcc, s52, v13\n"
"v_pk_mul_f32 v[192:193], v[192:193], v[12:13] op_sel_hi:[1,0]\n"
"v_pk_mul_f32 v[194:195], v[194:195], v[12:13] op_sel_hi:[1,0]\n"
"v_pk_mul_f32 v[196:197], v[196:197], v[12:13] op_sel_hi:[1,0]\n"
"v_pk_mul_f32 v[198:199], v[198:199], v[12:13] op_sel_hi:[1,0]\n"
"v_pk_mul_f32 v[200:201], v[200:201], v[12:13] op_sel_hi:[1,0]\n"
"v_pk_mul_f32 v[202:203], v[202:203], v[12:13] op_sel_hi:[1,0]\n"
"v_pk_mul_f32 v[204:205], v[204:205], v[12:13] op_sel_hi:[1,0]\n"
"v_pk_mul_f32 v[206:207], v[206:207], v[12:13] op_sel_hi:[1,0]\n"
"v_pk_mul_f32 v[208:209], v[208:209], v[12:13] op_sel_hi:[1,0]\n"
"v_pk_mul_f32 v[210:211], v[210:211], v[12:13] op_sel_hi:[1,0]\n"
"v_pk_mul_f32 v[212:213], v[212:213], v[12:13] op_sel_hi:[1,0]\n"
"v_pk_mul_f32 v[214:215], v[214:215], v[12:13] op_sel_hi:[1,0]\n"
"v_pk_mul_f32 v[216:217], v[216:217], v[12:13] op_sel_hi:[1,0]\n"
"v_pk_mul_f32 v[218:219], v[218:219], v[12:13] op_sel_hi:[1,0]\n"
"v_pk_mul_f32 v[220:221], v[220:221], v[12:13] op_sel_hi:[1,0]\n"
"v_pk_mul_f32 v[222:223], v[222:223], v[12:13] op_sel_hi:[1,0]\n"
"s_or_b64 s[54:55], s[54:55], vcc\n"
"v_exp_f32_e32 v26, v26\n"
"v_cvt_pk_bf16_f32 v40, v192, v193\n"
"v_cvt_pk_bf16_f32 v41, v194, v195\n"
"v_cvt_pk_bf16_f32 v42, v196, v197\n"
"v_cvt_pk_bf16_f32 v43, v198, v199\n"
"ds_write_b64 v56, v[40:41] offset:16384\n"
"ds_write_b64 v57, v[42:43] offset:16384\n"
"v_mul_f32_e32 v28, s44, v28\n"
"v_exp_f32_e32 v27, v27\n"
"v_cvt_pk_bf16_f32 v44, v200, v201\n"
"v_cvt_pk_bf16_f32 v45, v202, v203\n"
"v_cvt_pk_bf16_f32 v46, v204, v205\n"
"v_cvt_pk_bf16_f32 v47, v206, v207\n"
"ds_write_b64 v15, v[44:45] offset:16384\n"
"ds_write_b64 v60, v[46:47] offset:16384\n"
"v_mfma_f32_16x16x32_bf16 v[224:227], a[160:163], v[32:35], v[224:227]\n"
"v_mul_f32_e32 v29, s44, v29\n"
"v_cvt_pk_bf16_f32 v37, v26, v27\n"
"v_mfma_f32_16x16x32_bf16 v[228:231], a[164:167], v[32:35], v[228:231]\n"
"v_exp_f32_e32 v28, v28\n"
"v_cvt_pk_bf16_f32 v48, v208, v209\n"
"v_cvt_pk_bf16_f32 v49, v210, v211\n"
"v_cvt_pk_bf16_f32 v50, v212, v213\n"
"v_cvt_pk_bf16_f32 v51, v214, v215\n"
"ds_write_b64 v56, v[48:49] offset:16512\n"
"ds_write_b64 v57, v[50:51] offset:16512\n"
"v_mfma_f32_16x16x32_bf16 v[232:235], a[168:171], v[32:35], v[232:235]\n"
"v_mul_f32_e32 v30, s44, v30\n"
"v_mfma_f32_16x16x32_bf16 v[236:239], a[172:175], v[32:35], v[236:239]\n"
"v_exp_f32_e32 v29, v29\n"
"v_cvt_pk_bf16_f32 v52, v216, v217\n"
"v_cvt_pk_bf16_f32 v53, v218, v219\n"
"v_cvt_pk_bf16_f32 v54, v220, v221\n"
"v_cvt_pk_bf16_f32 v55, v222, v223\n"
"ds_write_b64 v15, v[52:53] offset:16512\n"
"ds_write_b64 v60, v[54:55] offset:16512\n"
"v_mfma_f32_16x16x32_bf16 v[240:243], a[176:179], v[32:35], v[240:243]\n"
"v_mul_f32_e32 v31, s44, v31\n"
"v_cvt_pk_bf16_f32 v38, v28, v29\n"
"v_mfma_f32_16x16x32_bf16 v[244:247], a[180:183], v[32:35], v[244:247]\n"
"v_exp_f32_e32 v30, v30\n"
"ds_read_b128 v[40:43], v58 offset:16384\n"
"ds_read_b128 v[44:47], v59 offset:17408\n"
"ds_read_b128 v[48:51], v58 offset:18432\n"
"ds_read_b128 v[52:55], v59 offset:19456\n"
"v_mfma_f32_16x16x32_bf16 v[248:251], a[184:187], v[32:35], v[248:251]\n"
"v_mfma_f32_16x16x32_bf16 v[252:255], a[188:191], v[32:35], v[252:255]\n"
"v_exp_f32_e32 v31, v31\n"
"v_mfma_f32_16x16x32_bf16 a[248:251], a[224:227], v[32:35], a[248:251]\n"
"v_cvt_pk_bf16_f32 v39, v30, v31\n"
"s_add_u32 s77, s60, 16384\n"
"s_waitcnt lgkmcnt(3)\n"
"buffer_store_dwordx4 v[40:43], v9, s[40:43], s77 offen offset:0  sc1\n"
"s_waitcnt lgkmcnt(2)\n"
"buffer_store_dwordx4 v[44:47], v9, s[40:43], s77 offen offset:1024  sc1\n"
"s_waitcnt lgkmcnt(1)\n"
"buffer_store_dwordx4 v[48:51], v9, s[40:43], s77 offen offset:2048  sc1\n"
"v_mfma_f32_16x16x32_bf16 v[224:227], a[192:195], v[36:39], v[224:227]\n"
"v_mfma_f32_16x16x32_bf16 v[228:231], a[196:199], v[36:39], v[228:231]\n"
"s_waitcnt lgkmcnt(0)\n"
"buffer_store_dwordx4 v[52:55], v9, s[40:43], s77 offen offset:3072  sc1\n"
"v_mfma_f32_16x16x32_bf16 v[232:235], a[200:203], v[36:39], v[232:235]\n"
"v_mfma_f32_16x16x32_bf16 v[236:239], a[204:207], v[36:39], v[236:239]\n"
"v_mfma_f32_16x16x32_bf16 v[240:243], a[208:211], v[36:39], v[240:243]\n"
"v_mfma_f32_16x16x32_bf16 v[244:247], a[212:215], v[36:39], v[244:247]\n"
"v_mfma_f32_16x16x32_bf16 v[248:251], a[216:219], v[36:39], v[248:251]\n"
"v_mfma_f32_16x16x32_bf16 v[252:255], a[220:223], v[36:39], v[252:255]\n"
"v_mfma_f32_16x16x32_bf16 a[248:251], a[224:227], v[36:39], a[248:251]\n"
"s_nop 7\n"
"v_accvgpr_read_b32 v10, a248\n"
"v_subrev_u32_e32 v13, s51, v10\n"
"v_rcp_f32_e32 v12, v10\n"
"v_cmp_le_u32_e32 vcc, s52, v13\n"
"v_pk_mul_f32 v[224:225], v[224:225], v[12:13] op_sel_hi:[1,0]\n"
"v_pk_mul_f32 v[226:227], v[226:227], v[12:13] op_sel_hi:[1,0]\n"
"v_pk_mul_f32 v[228:229], v[228:229], v[12:13] op_sel_hi:[1,0]\n"
"v_pk_mul_f32 v[230:231], v[230:231], v[12:13] op_sel_hi:[1,0]\n"
"v_pk_mul_f32 v[232:233], v[232:233], v[12:13] op_sel_hi:[1,0]\n"
"v_pk_mul_f32 v[234:235], v[234:235], v[12:13] op_sel_hi:[1,0]\n"
"v_pk_mul_f32 v[236:237], v[236:237], v[12:13] op_sel_hi:[1,0]\n"
"v_pk_mul_f32 v[238:239], v[238:239], v[12:13] op_sel_hi:[1,0]\n"
"v_pk_mul_f32 v[240:241], v[240:241], v[12:13] op_sel_hi:[1,0]\n"
"v_pk_mul_f32 v[242:243], v[242:243], v[12:13] op_sel_hi:[1,0]\n"
"v_pk_mul_f32 v[244:245], v[244:245], v[12:13] op_sel_hi:[1,0]\n"
"v_pk_mul_f32 v[246:247], v[246:247], v[12:13] op_sel_hi:[1,0]\n"
"v_pk_mul_f32 v[248:249], v[248:249], v[12:13] op_sel_hi:[1,0]\n"
"v_pk_mul_f32 v[250:251], v[250:251], v[12:13] op_sel_hi:[1,0]\n"
"v_pk_mul_f32 v[252:253], v[252:253], v[12:13] op_sel_hi:[1,0]\n"
"v_pk_mul_f32 v[254:255], v[254:255], v[12:13] op_sel_hi:[1,0]\n"
"s_or_b64 s[54:55], s[54:55], vcc\n"
"v_cvt_pk_bf16_f32 v40, v224, v225\n"
"v_cvt_pk_bf16_f32 v41, v226, v227\n"
"v_cvt_pk_bf16_f32 v42, v228, v229\n"
"v_cvt_pk_bf16_f32 v43, v230, v231\n"
"s_waitcnt vmcnt(24)\n"
"ds_write_b64 v56, v[40:41] offset:20480\n"
"ds_write_b64 v57, v[42:43] offset:20480\n"
"v_cvt_pk_bf16_f32 v44, v232, v233\n"
"v_cvt_pk_bf16_f32 v45, v234, v235\n"
"v_cvt_pk_bf16_f32 v46, v236, v237\n"
"v_cvt_pk_bf16_f32 v47, v238, v239\n"
"ds_write_b64 v15, v[44:45] offset:20480\n"
"ds_write_b64 v60, v[46:47] offset:20480\n"
"v_cvt_pk_bf16_f32 v48, v240, v241\n"
"v_cvt_pk_bf16_f32 v49, v242, v243\n"
"v_cvt_pk_bf16_f32 v50, v244, v245\n"
"v_cvt_pk_bf16_f32 v51, v246, v247\n"
"ds_write_b64 v56, v[48:49] offset:20608\n"
"ds_write_b64 v57, v[50:51] offset:20608\n"
"v_cvt_pk_bf16_f32 v52, v248, v249\n"
"v_cvt_pk_bf16_f32 v53, v250, v251\n"
"v_cvt_pk_bf16_f32 v54, v252, v253\n"
"v_cvt_pk_bf16_f32 v55, v254, v255\n"
"ds_write_b64 v15, v[52:53] offset:20608\n"
"ds_write_b64 v60, v[54:55] offset:20608\n"
"ds_read_b128 v[40:43], v58 offset:20480\n"
"ds_read_b128 v[44:47], v59 offset:21504\n"
"ds_read_b128 v[48:51], v58 offset:22528\n"
"ds_read_b128 v[52:55], v59 offset:23552\n"
"s_add_u32 s77, s62, 20480\n"
"s_waitcnt lgkmcnt(3)\n"
"buffer_store_dwordx4 v[40:43], v9, s[40:43], s77 offen offset:0  sc1\n"
"s_waitcnt lgkmcnt(2)\n"
"buffer_store_dwordx4 v[44:47], v9, s[40:43], s77 offen offset:1024  sc1\n"
"s_waitcnt lgkmcnt(1)\n"
"buffer_store_dwordx4 v[48:51], v9, s[40:43], s77 offen offset:2048  sc1\n"
"s_waitcnt lgkmcnt(0)\n"
"buffer_store_dwordx4 v[52:55], v9, s[40:43], s77 offen offset:3072  sc1\n"
"s_cmp_lg_u64 s[54:55], 0\n"
"s_cselect_b32 s57, s56, 0\n"
"s_or_b32 s53, s53, s57\n"
"s_lshl_b32 s56, s56, 1\n"
"s_mov_b64 s[54:55], 0\n"
"L_end%=:\n"
"s_waitcnt vmcnt(0) lgkmcnt(0)\n"
"s_mov_b32 %0, s53\n"
"s_mov_b32 %1, s64\n"
    : "=s"(mask), "=s"(wave)
    : "v"(threadIdx.x), "s"(bid), "s"(ka), "s"(0), "s"((unsigned)(size_t)lds)
    : "v1", "v2", "v3", "v4", "v5", "v6", "v7", "v8", "v9", "v10", "v11", "v12", "v13", "v14", "v15", "v16", "v17", "v18", "v19", "v20", "v21", "v22",
      "v23", "v24", "v25", "v26", "v27", "v28", "v29", "v30", "v31", "v32", "v33", "v34", "v35", "v36", "v37", "v38", "v39", "v40", "v41", "v42",
      "v43", "v44", "v45", "v46", "v47", "v48", "v49", "v50", "v51", "v52", "v53", "v54", "v55", "v56", "v57", "v58", "v59", "v60", "v61", "v62",
      "v63", "v64", "v65", "v66", "v67", "v68", "v69", "v70", "v71", "v72", "v73", "v74", "v75", "v76", "v77", "v78", "v79", "v80", "v81", "v82",
      "v83", "v84", "v85", "v86", "v87", "v88", "v89", "v90", "v91", "v92", "v93", "v94", "v95", "v96", "v97", "v98", "v99", "v100", "v101", "v102",
      "v103", "v104", "v105", "v106", "v107", "v108", "v109", "v110", "v111", "v112", "v113", "v114", "v115", "v116", "v117", "v118", "v119", "v120",
      "v121", "v122", "v123", "v124", "v125", "v126", "v127", "v128", "v129", "v130", "v131", "v132", "v133", "v134", "v135", "v136", "v137", "v138",
      "v139", "v140", "v141", "v142", "v143", "v144", "v145", "v146", "v147", "v148", "v149", "v150", "v151", "v152", "v153", "v154", "v155", "v156",
      "v157", "v158", "v159", "v160", "v161", "v162", "v163", "v164", "v165", "v166", "v167", "v168", "v169", "v170", "v171", "v172", "v173", "v174",
      "v175", "v176", "v177", "v178", "v179", "v180", "v181", "v182", "v183", "v184", "v185", "v186", "v187", "v188", "v189", "v190", "v191", "v192",
      "v193", "v194", "v195", "v196", "v197", "v198", "v199", "v200", "v201", "v202", "v203", "v204", "v205", "v206", "v207", "v208", "v209", "v210",
      "v211", "v212", "v213", "v214", "v215", "v216", "v217", "v218", "v219", "v220", "v221", "v222", "v223", "v224", "v225", "v226", "v227", "v228",
      "v229", "v230", "v231", "v232", "v233", "v234", "v235", "v236", "v237", "v238", "v239", "v240", "v241", "v242", "v243", "v244", "v245", "v246",
      "v247", "v248", "v249", "v250", "v251", "v252", "v253", "v254", "v255", "a0", "a1", "a2", "a3", "a4", "a5", "a6", "a7", "a8", "a9", "a10", "a11",
      "a12", "a13", "a14", "a15", "a16", "a17", "a18", "a19", "a20", "a21", "a22", "a23", "a24", "a25", "a26", "a27", "a28", "a29", "a30", "a31",
      "a32", "a33", "a34", "a35", "a36", "a37", "a38", "a39", "a40", "a41", "a42", "a43", "a44", "a45", "a46", "a47", "a48", "a49", "a50", "a51",
      "a52", "a53", "a54", "a55", "a56", "a57", "a58", "a59", "a60", "a61", "a62", "a63", "a64", "a65", "a66", "a67", "a68", "a69", "a70", "a71",
      "a72", "a73", "a74", "a75", "a76", "a77", "a78", "a79", "a80", "a81", "a82", "a83", "a84", "a85", "a86", "a87", "a88", "a89", "a90", "a91",
      "a92", "a93", "a94", "a95", "a96", "a97", "a98", "a99", "a100", "a101", "a102", "a103", "a104", "a105", "a106", "a107", "a108", "a109", "a110",
      "a111", "a112", "a113", "a114", "a115", "a116", "a117", "a118", "a119", "a120", "a121", "a122", "a123", "a124", "a125", "a126", "a127", "a128",
      "a129", "a130", "a131", "a132", "a133", "a134", "a135", "a136", "a137", "a138", "a139", "a140", "a141", "a142", "a143", "a144", "a145", "a146",
      "a147", "a148", "a149", "a150", "a151", "a152", "a153", "a154", "a155", "a156", "a157", "a158", "a159", "a160", "a161", "a162", "a163", "a164",
      "a165", "a166", "a167", "a168", "a169", "a170", "a171", "a172", "a173", "a174", "a175", "a176", "a177", "a178", "a179", "a180", "a181", "a182",
      "a183", "a184", "a185", "a186", "a187", "a188", "a189", "a190", "a191", "a192", "a193", "a194", "a195", "a196", "a197", "a198", "a199", "a200",
      "a201", "a202", "a203", "a204", "a205", "a206", "a207", "a208", "a209", "a210", "a211", "a212", "a213", "a214", "a215", "a216", "a217", "a218",
      "a219", "a220", "a221", "a222", "a223", "a224", "a225", "a226", "a227", "a228", "a229", "a230", "a231", "a232", "a233", "a234", "a235", "a236",
      "a237", "a238", "a239", "a240", "a241", "a242", "a243", "a244", "a245", "a246", "a247", "a248", "a249", "a250", "a251", "a252", "a253", "a254",
      "a255", "s24", "s25", "s26", "s27", "s28", "s29", "s30", "s31", "s36", "s37", "s38", "s39", "s40", "s41", "s42", "s43", "s44", "s45", "s46",
      "s47", "s48", "s49", "s50", "s51", "s52", "s53", "s54", "s55", "s56", "s57", "s58", "s59", "s60", "s61", "s62", "s63", "s64", "s65", "s66",
      "s67", "s68", "s69", "s70", "s71", "s72", "s73", "s74", "s75", "s76", "s77", "s78", "s79", "s80", "s81", "s82", "s83", "s84", "s85", "s86",
      "s87", "s88", "s89", "s90", "s91", "s92", "s93", "s94", "s95", "vcc", "scc", "memory");
  if (mask) {
    unsigned long long kb = ka;
    asm volatile("" : "+s"(kb));
    const unsigned long long* kp = (const unsigned long long*)kb;
    q = (const u16*)kp[0]; k = (const u16*)kp[1]; v = (const u16*)kp[2]; o = (__hip_bfloat16*)kp[3];
    const int lane = __builtin_amdgcn_mbcnt_hi(~0u, __builtin_amdgcn_mbcnt_lo(~0u, 0u));
    const int kv0 = (bid & 7) * 32 + (bid >> 5), member = (bid >> 3) & 3;
    for (int p = 0; p < 4; ++p) {
      if (!(mask >> p & 1)) continue;
      const int n = p == member ? 5 : 6, b0 = 24 * member - (member > p ? 4 : 0);
      exact_rows(q, k, v, o, kv0 + 8 * p, (b0 + n * (int)wave) * 16, n * 16, lane);
    }
  }
}

extern "C" void kernel_launch(void* const* d_in, const int* in_sizes, int n_in, void* d_out, int out_size, void* d_ws, size_t ws_size,
                              hipStream_t stream) {
  gqa_attn_kernel<<<256, 256, 0, stream>>>((const u16*)d_in[0], (const u16*)d_in[1], (const u16*)d_in[2], (__hip_bfloat16*)d_out);
}
```
